```python
import torch

def _softmax(x, axis=-1):
    return torch.softmax(x, dim=axis)
def _gelu(x):
    return torch.nn.functional.gelu(x, approximate="tanh")
def _silu(x):
    return torch.nn.functional.silu(x)
def _sigmoid(x):
    return torch.sigmoid(x)
def _layernorm(x, axis=-1, eps=1e-5):
    m = x.mean(dim=axis, keepdim=True)
    v = x.var(dim=axis, keepdim=True, unbiased=False)
    return (x - m) / torch.sqrt(v + eps)
def _scatter_add(tgt, idx, src, axis=0):
    return tgt.clone().index_add_(axis, idx, src)
def _segment_sum(src, seg, n):
    out = torch.zeros((n,) + src.shape[1:], dtype=src.dtype)
    return out.index_add_(0, seg, src)
def _pad(x, pw):
    flat = tuple(v for pair in reversed(pw) for v in pair)
    return torch.nn.functional.pad(x, flat)

kernel_name = 'concat_heads__gqa_block_cached__gqa_block_cached__near_ridge'

def setup_inputs():

    _w = (lambda a: (lambda t: t.to(torch.bfloat16) if t.is_floating_point() else t)(torch.as_tensor(a)))
    def _A_setup():
        B, Sq, D = 8, 256, 640
        Hq, Hkv, Sk, Dh = 32, 32, 1024, 192
        assert Hq % Hkv == 0, (Hq, Hkv)
        return {
            "x": _w((lambda *s: torch.randn(*s, dtype=torch.bfloat16) * 0.8)(B, Sq, D)),
            "k_cache": _w((lambda *s: torch.randn(*s, dtype=torch.bfloat16) * 0.8)(B, Hkv, Sk, Dh)),
            "v_cache": _w((lambda *s: torch.randn(*s, dtype=torch.bfloat16) * 0.05)(B, Hkv, Sk, Dh)),
            "wq": _w((lambda *s: torch.randn(*s, dtype=torch.bfloat16) * 0.8)(D, Hq * Dh) * (D ** -0.5)),
            "wo": _w((lambda *s: torch.randn(*s, dtype=torch.bfloat16) * 0.05)(Hq * Dh, D) * ((Hq * Dh) ** -0.5)),
        }
    _A_in = _A_setup()

    def _B_setup():
        B, Sq, D = 8, 256, 640
        Hq, Hkv, Sk, Dh = 32, 32, 1024, 192
        assert Hq % Hkv == 0, (Hq, Hkv)
        return {
            "x": _w((lambda *s: torch.randn(*s, dtype=torch.bfloat16) * 0.8)(B, Sq, D)),
            "k_cache": _w((lambda *s: torch.randn(*s, dtype=torch.bfloat16) * 0.8)(B, Hkv, Sk, Dh)),
            "v_cache": _w((lambda *s: torch.randn(*s, dtype=torch.bfloat16) * 0.05)(B, Hkv, Sk, Dh)),
            "wq": _w((lambda *s: torch.randn(*s, dtype=torch.bfloat16) * 0.8)(D, Hq * Dh) * (D ** -0.5)),
            "wo": _w((lambda *s: torch.randn(*s, dtype=torch.bfloat16) * 0.05)(Hq * Dh, D) * ((Hq * Dh) ** -0.5)),
        }
    _B_in = _B_setup()

    return {
        "A_x": _A_in["x"],
        "A_k_cache": _A_in["k_cache"],
        "A_v_cache": _A_in["v_cache"],
        "A_wq": _A_in["wq"],
        "A_wo": _A_in["wo"],
        "B_x": _B_in["x"],
        "B_k_cache": _B_in["k_cache"],
        "B_v_cache": _B_in["v_cache"],
        "B_wq": _B_in["wq"],
        "B_wo": _B_in["wo"],
    }

def reference(A_x, A_k_cache, A_v_cache, A_wq, A_wo, B_x, B_k_cache, B_v_cache, B_wq, B_wo):
    def _A_ref(x, k_cache, v_cache, wq, wo):
        B, Sq, D = x.shape
        Hq, Dh = 32, 192
        Hkv = k_cache.shape[1]
        rep = Hq // Hkv
        q = (lambda x, *d: x.permute(*d))((x @ wq).reshape(B, Sq, Hq, Dh), 0, 2, 1, 3)
        k = (lambda x, n, axis: torch.repeat_interleave(x, n, dim=axis))(k_cache, rep, axis=1)
        v = (lambda x, n, axis: torch.repeat_interleave(x, n, dim=axis))(v_cache, rep, axis=1)
        scores = torch.einsum("bhqd,bhkd->bhqk", q, k) * (Dh ** -0.5)
        p = _softmax(scores, axis=-1)
        o = torch.einsum("bhqk,bhkd->bhqd", p, v)
        return (lambda x, *d: x.permute(*d))(o, 0, 2, 1, 3).reshape(B, Sq, Hq * Dh) @ wo

    def _B_ref(x, k_cache, v_cache, wq, wo):
        B, Sq, D = x.shape
        Hq, Dh = 32, 192
        Hkv = k_cache.shape[1]
        rep = Hq // Hkv
        q = (lambda x, *d: x.permute(*d))((x @ wq).reshape(B, Sq, Hq, Dh), 0, 2, 1, 3)
        k = (lambda x, n, axis: torch.repeat_interleave(x, n, dim=axis))(k_cache, rep, axis=1)
        v = (lambda x, n, axis: torch.repeat_interleave(x, n, dim=axis))(v_cache, rep, axis=1)
        scores = torch.einsum("bhqd,bhkd->bhqk", q, k) * (Dh ** -0.5)
        p = _softmax(scores, axis=-1)
        o = torch.einsum("bhqk,bhkd->bhqd", p, v)
        return (lambda x, *d: x.permute(*d))(o, 0, 2, 1, 3).reshape(B, Sq, Hq * Dh) @ wo

    a = _A_ref(A_x, A_k_cache, A_v_cache, A_wq, A_wo)
    b = _B_ref(B_x, B_k_cache, B_v_cache, B_wq, B_wo)
    return torch.concatenate([a, b], axis=-1)
```

```cpp
#include <hip/hip_runtime.h>
#include <hip/hip_bf16.h>
#include <cstdio>

typedef __attribute__((ext_vector_type(8))) short bf16x8;
typedef __attribute__((ext_vector_type(4))) short s16x4;
typedef __attribute__((ext_vector_type(16))) float f32x16;
typedef __attribute__((ext_vector_type(4))) unsigned u32x4;
typedef __attribute__((ext_vector_type(2))) float f32x2;
typedef __attribute__((ext_vector_type(2))) __bf16 bf16x2;
typedef __attribute__((address_space(3))) char* lds_ptr;
typedef __attribute__((address_space(3))) const char* lds_cptr;
typedef short v4i16_t __attribute__((ext_vector_type(4)));

constexpr int NB = 8, SQ = 256, DM = 640, NH = 32, SK = 1024, DH = 192, HD = NH * DH;
#define DI __device__ __forceinline__
#define MFMA(a, b, c) __builtin_amdgcn_mfma_f32_32x32x16_bf16((a), (b), (c), 0, 0, 0)

DI unsigned cvtpk(float lo, float hi) { f32x2 v = {lo, hi}; bf16x2 b = __builtin_convertvector(v, bf16x2); return __builtin_bit_cast(unsigned, b); }
DI s16x4 trrd(lds_cptr p) { return __builtin_bit_cast(s16x4, __builtin_amdgcn_ds_read_tr16_b64_v4i16((__attribute__((address_space(3))) v4i16_t*)p)); }
DI bf16x8 ldsr128(lds_cptr p) { return *(const __attribute__((address_space(3))) bf16x8*)p; }
DI bf16x8 cat(s16x4 a, s16x4 b) { return __builtin_shufflevector(a, b, 0, 1, 2, 3, 4, 5, 6, 7); }
DI void glds16(const void* g, lds_ptr l) { __builtin_amdgcn_global_load_lds((const __attribute__((address_space(1))) void*)g, (__attribute__((address_space(3))) void*)l, 16, 0, 0); }
DI bf16x8 packs(const f32x16& x, int s, float sc) {
  u32x4 w; for (int i = 0; i < 4; ++i) w[i] = cvtpk(x[8 * s + 2 * i] * sc, x[8 * s + 2 * i + 1] * sc); return __builtin_bit_cast(bf16x8, w); }

struct Ptrs { const unsigned short* x[2]; const unsigned short* k[2]; const unsigned short* v[2]; const unsigned short* wq[2]; const unsigned short* wo[2]; };

__global__ __launch_bounds__(256, 1) void k1(Ptrs P, u32x4* __restrict__ ofrag) {
  extern __shared__ __attribute__((aligned(16))) char smem[];
  lds_ptr lds = (lds_ptr)smem;
  const int tid = threadIdx.x, lane = tid & 63, wid = __builtin_amdgcn_readfirstlane(tid >> 6), r = lane & 31, h = lane >> 5;
  const int item = blockIdx.x; const int blk = item >> 8, b = (item >> 5) & 7, hd = item & 31;
  const unsigned short* x = P.x[blk] + (size_t)b * SQ * DM;
  const unsigned short* wq = P.wq[blk] + hd * DH;
  const unsigned short* kc = P.k[blk] + (size_t)(b * NH + hd) * SK * DH;
  const unsigned short* vc = P.v[blk] + (size_t)(b * NH + hd) * SK * DH;
  const int g = lane >> 4, q4 = (lane & 15) >> 2, p4 = lane & 3;
  f32x16 acc[6][2];
  for (int i = 0; i < 6; ++i) for (int j = 0; j < 2; ++j) acc[i][j] = f32x16{};
  lds_ptr XS = lds;
  lds_ptr WS = lds + 32768;
  for (int kt = 0; kt < DM / 64; ++kt) {
    __syncthreads();
    for (int j = 0; j < 8; ++j) {
      int row = wid * 64 + j * 8 + (lane >> 3), p = lane & 7, c = p ^ ((row >> 1) & 7);
      glds16(x + (size_t)row * DM + kt * 64 + c * 8, XS + (wid * 8 + j) * 1024);
    }
    for (int j = 0; j < 6; ++j) {
      int n = (wid * 6 + j) * 64 + lane, row = n / 24, p = n % 24, c = p ^ (((row >> 1) & 1) << 2);
      glds16(wq + (size_t)(kt * 64 + row) * HD + c * 8, WS + (wid * 6 + j) * 1024);
    }
    asm volatile("s_waitcnt vmcnt(0)" ::: "memory");
    __syncthreads();
#pragma unroll
    for (int ks = 0; ks < 4; ++ks) {
      bf16x8 xb[2];
      for (int qb = 0; qb < 2; ++qb) { int row = wid * 64 + qb * 32 + r; xb[qb] = ldsr128(XS + row * 128 + 16 * ((2 * ks + h) ^ ((row >> 1) & 7))); }
#pragma unroll
      for (int db = 0; db < 6; ++db) {
        s16x4 t[2];
        for (int tt = 0; tt < 2; ++tt) {
          int krow = 16 * ks + 8 * h + 4 * tt + q4;
          int col = 32 * db + 16 * (g & 1) + 8 * (p4 & 1) + 4 * (p4 >> 1);
          int c = col >> 3, ph = c ^ (((krow >> 1) & 1) << 2);
          t[tt] = trrd(WS + krow * 384 + ph * 16 + (col & 7) * 2);
        }
        bf16x8 a = cat(t[0], t[1]);
        acc[db][0] = MFMA(a, xb[0], acc[db][0]);
        acc[db][1] = MFMA(a, xb[1], acc[db][1]);
      }
    }
  }
  const float C = 0.07216878364870322f * 1.4426950408889634f;
  bf16x8 qf[12][2];
#pragma unroll
  for (int db = 0; db < 6; ++db) for (int s = 0; s < 2; ++s) for (int qb = 0; qb < 2; ++qb) qf[2 * db + s][qb] = packs(acc[db][qb], s, C);
  for (int i = 0; i < 6; ++i) for (int j = 0; j < 2; ++j) acc[i][j] = f32x16{};
  float m[2] = {-1e30f, -1e30f}, l[2] = {0.f, 0.f};
  lds_ptr KS = lds, VS = lds + 12288;
  for (int u = 0; u < SK / 32; ++u) {
    __syncthreads();
    for (int j = 0; j < 3; ++j) {
      int n = (wid * 3 + j) * 64 + lane, row = n / 24, p = n % 24;
      int ck = p ^ ((row >> 1) & 7), cv = p ^ (((row >> 1) & 1) << 2);
      glds16(kc + (size_t)(u * 32 + row) * DH + ck * 8, KS + (wid * 3 + j) * 1024);
      glds16(vc + (size_t)(u * 32 + row) * DH + cv * 8, VS + (wid * 3 + j) * 1024);
    }
    asm volatile("s_waitcnt vmcnt(0)" ::: "memory");
    __syncthreads();
    f32x16 s[2] = {f32x16{}, f32x16{}};
#pragma unroll
    for (int ks = 0; ks < 12; ++ks) {
      int c = 2 * ks + h;
      bf16x8 a = ldsr128(KS + r * 384 + 16 * (c ^ ((r >> 1) & 7)));
      s[0] = MFMA(a, qf[ks][0], s[0]);
      s[1] = MFMA(a, qf[ks][1], s[1]);
    }
    bf16x8 pf[2][2];
#pragma unroll
    for (int qb = 0; qb < 2; ++qb) {
      float mx = s[qb][0];
      for (int i = 1; i < 16; ++i) mx = fmaxf(mx, s[qb][i]);
      mx = fmaxf(mx, __shfl_xor(mx, 32));
      float mn = fmaxf(m[qb], mx), al = exp2f(m[qb] - mn);
      m[qb] = mn; float rs = 0.f;
      for (int i = 0; i < 16; ++i) { s[qb][i] = exp2f(s[qb][i] - mn); rs += s[qb][i]; }
      l[qb] = l[qb] * al + rs;
      for (int db = 0; db < 6; ++db) for (int i = 0; i < 16; ++i) acc[db][qb][i] *= al;
      pf[0][qb] = packs(s[qb], 0, 1.f); pf[1][qb] = packs(s[qb], 1, 1.f);
    }
#pragma unroll
    for (int ks = 0; ks < 2; ++ks)
#pragma unroll
      for (int db = 0; db < 6; ++db) {
        s16x4 t[2];
        for (int tt = 0; tt < 2; ++tt) {
          int key = 16 * ks + 8 * tt + 4 * h + q4;
          int col = 32 * db + 16 * (g & 1) + 8 * (p4 & 1) + 4 * (p4 >> 1);
          int c = col >> 3, ph = c ^ (((key >> 1) & 1) << 2);
          t[tt] = trrd(VS + key * 384 + ph * 16 + (col & 7) * 2);
        }
        bf16x8 a = cat(t[0], t[1]);
        acc[db][0] = MFMA(a, pf[ks][0], acc[db][0]);
        acc[db][1] = MFMA(a, pf[ks][1], acc[db][1]);
      }
  }
#pragma unroll
  for (int qb = 0; qb < 2; ++qb) {
    float lt = l[qb] + __shfl_xor(l[qb], 32);
    float il = 1.f / lt;
    size_t base = ((size_t)((blk * 8 + b) * 8 + wid * 2 + qb) * 384 + hd * 12) * 64 + lane;
#pragma unroll
    for (int db = 0; db < 6; ++db) for (int s = 0; s < 2; ++s)
      ofrag[base + (2 * db + s) * 64] = __builtin_bit_cast(u32x4, packs(acc[db][qb], s, il));
  }
}

__global__ __launch_bounds__(256, 1) void k2(Ptrs P, const u32x4* __restrict__ ofrag, unsigned short* __restrict__ out) {
  __shared__ __attribute__((aligned(16))) char smem[64 * 320];
  lds_ptr WS = (lds_ptr)smem;
  const int tid = threadIdx.x, lane = tid & 63, wid = __builtin_amdgcn_readfirstlane(tid >> 6), r = lane & 31, h = lane >> 5;
  const int g = lane >> 4, q4 = (lane & 15) >> 2, p4 = lane & 3;
  const int nt = blockIdx.x & 3, mt = blockIdx.x >> 2;
  const int blk = mt >> 4;
  const int qblk = mt * 4 + wid;
  const unsigned short* wo = P.wo[blk] + nt * 160;
  f32x16 acc[5];
  for (int i = 0; i < 5; ++i) acc[i] = f32x16{};
  for (int kt = 0; kt < HD / 64; ++kt) {
    __syncthreads();
    for (int j = 0; j < 5; ++j) {
      int n = (wid * 5 + j) * 64 + lane, row = n / 20, p = n % 20;
      glds16(wo + (size_t)(kt * 64 + row) * DM + p * 8, WS + (wid * 5 + j) * 1024);
    }
    bf16x8 of[4];
    for (int ks = 0; ks < 4; ++ks) of[ks] = __builtin_bit_cast(bf16x8, ofrag[((size_t)qblk * 384 + kt * 4 + ks) * 64 + lane]);
    asm volatile("s_waitcnt vmcnt(0)" ::: "memory");
    __syncthreads();
#pragma unroll
    for (int ks = 0; ks < 4; ++ks)
#pragma unroll
      for (int nb = 0; nb < 5; ++nb) {
        s16x4 t[2];
        for (int tt = 0; tt < 2; ++tt) {
          int krow = 16 * ks + 8 * h + 4 * tt + q4;
          int mm = 16 * (g & 1) + 4 * p4;
          int col = 32 * nb + 16 * ((mm >> 2) & 1) + 4 * (mm >> 3);
          t[tt] = trrd(WS + krow * 320 + col * 2);
        }
        acc[nb] = MFMA(cat(t[0], t[1]), of[ks], acc[nb]);
      }
  }
  const int grow = (qblk & 63) * 32 + r;
  unsigned short* o = out + (size_t)grow * (2 * DM) + blk * DM + nt * 160 + 16 * h;
#pragma unroll
  for (int nb = 0; nb < 5; ++nb) {
    *(u32x4*)(o + 32 * nb) = __builtin_bit_cast(u32x4, packs(acc[nb], 0, 1.f));
    *(u32x4*)(o + 32 * nb + 8) = __builtin_bit_cast(u32x4, packs(acc[nb], 1, 1.f));
  }
}

extern "C" void kernel_launch(void* const* d_in, const int* in_sizes, int n_in, void* d_out, int out_size, void* d_ws, size_t ws_size, hipStream_t stream) {
  Ptrs P;
  for (int i = 0; i < 2; ++i) {
    P.x[i] = (const unsigned short*)d_in[5 * i + 0]; P.k[i] = (const unsigned short*)d_in[5 * i + 1]; P.v[i] = (const unsigned short*)d_in[5 * i + 2];
    P.wq[i] = (const unsigned short*)d_in[5 * i + 3]; P.wo[i] = (const unsigned short*)d_in[5 * i + 4];
  }
  hipFuncSetAttribute((const void*)k1, hipFuncAttributeMaxDynamicSharedMemorySize, 57344);
  hipLaunchKernelGGL(k1, dim3(512), dim3(256), 57344, stream, P, (u32x4*)d_ws);
  hipLaunchKernelGGL(k2, dim3(128), dim3(256), 0, stream, P, (const u32x4*)d_ws, (unsigned short*)d_out);
}
```

```cpp
#include <hip/hip_runtime.h>

typedef __attribute__((ext_vector_type(16))) unsigned u32x16;
typedef __attribute__((ext_vector_type(4))) unsigned u32x4;
typedef __attribute__((ext_vector_type(8))) unsigned u32x8;
typedef __attribute__((ext_vector_type(2))) unsigned u32x2;
typedef unsigned long long u64;
#define DI __device__ __forceinline__
constexpr unsigned WFLG = 51u << 20, WPK = 52u << 20, WST = 88u << 20, WSZ = 98u << 20;

struct Ptrs { const char* x[2]; const char* k[2]; const char* v[2]; const char* wq[2]; const char* wo[2]; };
DI unsigned rfl(unsigned x) { return __builtin_amdgcn_readfirstlane(x); }
DI unsigned lo32(const void* p) { return rfl((unsigned)(u64)p); }
DI unsigned hi16(const void* p) { return rfl((unsigned)((u64)p >> 32) & 0xffffu); }
extern "C" __device__ u64 launch_index() __asm("llvm.amdgcn.dispatch.id");

__global__ __launch_bounds__(256, 1) __attribute__((aligned(4096))) void k1(Ptrs P, char* __restrict__ ows, char* __restrict__ out) {
  extern __shared__ __attribute__((aligned(16))) char smem[];
  asm volatile(
"L_al:\n"
"s_load_dwordx16 s[72:87], s[2:3], 0x0\n"
"s_load_dwordx8 s[88:95], s[2:3], 0x40\n"
"s_memtime s[40:41]\n"
"s_memrealtime s[42:43]\n"
"v_readfirstlane_b32 s7, v0\n"
"v_and_b32 v42, 63, v0\n"
"s_and_b32 s54, s6, 3\n"
"s_lshr_b32 s48, s6, 5\n"
"v_lshlrev_b32 v12, 4, v42\n"
"s_bfe_u32 s56, s6, 0x20003\n"
"s_and_b32 s7, s7, 0xc0\n"
"v_lshrrev_b32 v1, 3, v42\n"
"v_or_b32 v1, s7, v1\n"
"v_xor_b32 v2, v12, v42\n"
"s_lshl3_add_u32 s55, s54, s56\n"
"s_mul_i32 s68, s54, 20480\n"
"v_mul_u32_u24 v1, 0x500, v1\n"
"v_and_b32 v2, 0x70, v2\n"
"s_xor_b32 s69, s54, 1\n"
"s_mul_i32 s69, s69, 20480\n"
"v_or_b32 v32, v1, v2\n"
"s_xor_b32 s70, s54, 2\n"
"s_mul_i32 s70, s70, 20480\n"
"v_add_u32 v33, 0x2400, v32\n"
"s_xor_b32 s71, s54, 3\n"
"s_mul_i32 s71, s71, 20480\n"
"v_xor_b32 v33, 64, v33\n"
"s_lshl_b32 s44, s7, 7\n"
"s_add_u32 s44, s44, 0x4000\n"
"s_mul_i32 s57, s48, 327680\n"
"s_mul_i32 s96, s55, 384\n"
"s_mov_b32 s18, 327680\n"
"s_mov_b32 s19, 0x20000\n"
"s_mul_i32 s52, s7, 1536\n"
"s_and_b32 s53, s7, 64\n"
"s_or_b32 s52, s52, s53\n"
"v_mul_u32_u24 v2, 43, v42\n"
"v_xor_b32 v1, s52, v12\n"
"v_add_u32 v4, 0xac0, v2\n"
"v_add_u32 v6, 0x80, v42\n"
"v_lshrrev_b32 v3, 10, v2\n"
"v_lshrrev_b32 v2, 6, v2\n"
"v_mul_u32_u24 v6, 0xab, v6\n"
"v_and_b32 v2, 32, v2\n"
"v_mul_u32_u24 v3, 0x2e80, v3\n"
"v_lshrrev_b32 v5, 10, v4\n"
"v_xad_u32 v36, v1, v2, v3\n"
"v_lshrrev_b32 v2, 6, v4\n"
"v_and_b32 v2, 32, v2\n"
"v_mul_u32_u24 v3, 0x2e80, v5\n"
"v_lshrrev_b32 v7, 12, v6\n"
"v_xad_u32 v37, v1, v2, v3\n"
"v_lshrrev_b32 v2, 8, v6\n"
"v_and_b32 v2, 32, v2\n"
"v_mul_u32_u24 v3, 0x2e80, v7\n"
"v_xad_u32 v38, v1, v2, v3\n"
"s_mov_b32 s22, 7852416\n"
"s_mov_b32 s23, 0x20000\n"
"s_mul_i32 s38, s7, 48\n"
"s_bitcmp1_b32 s6, 2\n"
"s_waitcnt lgkmcnt(0)\n"
"s_cselect_b64 s[16:17], s[74:75], s[72:73]\n"
"s_cselect_b64 s[20:21], s[86:87], s[84:85]\n"
"s_add_u32 s16, s16, s57\n"
"s_addc_u32 s17, s17, 0\n"
"s_add_u32 s20, s20, s96\n"
"s_addc_u32 s21, s21, 0\n"
"s_add_u32 m0, s44, 0\n"
"s_add_u32 s51, s68, 0\n"
"s_nop 0\n"
"buffer_load_dwordx4 v32, s[16:19], s51 offen offset:0 lds\n"
"s_add_u32 s39, s44, 0x12000\n"
"s_movk_i32 s59, 0x6000\n"
"s_cmp_lt_u32 s7, 0xc0\n"
"s_cselect_b32 s39, s39, 0x2000\n"
"s_cselect_b32 s59, s59, 0xffffe000\n"
"buffer_load_dwordx4 v33, s[16:19], s51 offen offset:1024 lds\n"
"s_add_u32 s58, s39, s59\n"
"v_and_b32 v43, 15, v42\n"
"s_lshl_b32 s52, s6, 3\n"
"s_add_u32 m0, s44, 2048\n"
"s_add_u32 s51, s69, 0\n"
"s_nop 0\n"
"buffer_load_dwordx4 v32, s[16:19], s51 offen offset:0 lds\n"
"s_lshr_b32 s53, s7, 2\n"
"v_lshlrev_b32 v47, 3, v42\n"
"s_and_b32 s52, s52, 0x3c0\n"
"buffer_load_dwordx4 v33, s[16:19], s51 offen offset:1024 lds\n"
"s_or_b32 s52, s52, s53\n"
"v_and_b32 v46, 24, v47\n"
"v_or_b32 v14, s52, v43\n"
"s_add_u32 m0, s44, 4096\n"
"s_add_u32 s51, s70, 0\n"
"s_nop 0\n"
"buffer_load_dwordx4 v32, s[16:19], s51 offen offset:0 lds\n"
"v_and_b32 v47, 0x70, v47\n"
"v_min_u32 v14, 799, v14\n"
"v_and_b32 v49, 48, v42\n"
"buffer_load_dwordx4 v33, s[16:19], s51 offen offset:1024 lds\n"
"v_lshlrev_b32 v14, 7, v14\n"
"v_xor_b32 v47, v47, v49\n"
"v_lshl_or_b32 v40, v43, 7, v47\n"
"s_add_u32 m0, s44, 6144\n"
"s_add_u32 s51, s71, 0\n"
"s_nop 0\n"
"buffer_load_dwordx4 v32, s[16:19], s51 offen offset:0 lds\n"
"v_lshlrev_b32 v48, 2, v42\n"
"v_xor_b32 v41, 64, v40\n"
"v_and_b32 v48, 0x60, v48\n"
"buffer_load_dwordx4 v33, s[16:19], s51 offen offset:1024 lds\n"
"v_add_u32 v34, s58, v40\n"
"v_add_u32 v35, s58, v41\n"
"v_add_u32 v40, s44, v40\n"
"s_add_u32 m0, s38, 65536\n"
"s_mov_b32 s50, 0\n"
"s_nop 0\n"
"buffer_load_dwordx4 v36, s[20:23], s50 offen offset:0 lds\n"
"v_add_u32 v41, s44, v41\n"
"v_bfe_u32 v45, v42, 2, 2\n"
"v_lshrrev_b32 v50, 1, v49\n"
"buffer_load_dwordx4 v37, s[20:23], s50 offen offset:1024 lds\n"
"v_and_b32 v51, 48, v12\n"
"v_or_b32 v46, v46, v48\n"
"v_or_b32 v50, v50, v45\n"
"buffer_load_dwordx4 v38, s[20:23], s50 offen offset:2048 lds\n"
"v_xor_b32 v51, v51, v48\n"
"v_mul_u32_u24 v50, 0x180, v50\n"
"v_or_b32 v50, v50, v51\n"
"s_add_u32 m0, s38, 77824\n"
"s_mov_b32 s50, 393216\n"
"s_nop 0\n"
"buffer_load_dwordx4 v36, s[20:23], s50 offen offset:0 lds\n"
"v_add_u32 v28, 0x10000, v50\n"
"v_xor_b32 v29, 8, v28\n"
"v_xor_b32 v30, 64, v28\n"
"buffer_load_dwordx4 v37, s[20:23], s50 offen offset:1024 lds\n"
"v_xor_b32 v31, 0x48, v28\n"
"buffer_load_dwordx4 v38, s[20:23], s50 offen offset:2048 lds\n"
"s_getpc_b64 s[52:53]\n"
"L_pc0:\n"
"s_sub_u32 s52, s52, L_pc0-L_al\n"
"s_subb_u32 s53, s53, 0\n"
"s_mov_b32 m0, 64512\n"
"s_nop 2\n"
"global_load_lds_dword v14, s[52:53]\n"
"v_readfirstlane_b32 s60, v14\n"
"s_nop 3\n"
"s_min_u32 s60, s60, 100352\n"
"s_add_u32 s60, s60, s52\n"
"s_addc_u32 s61, s53, 0\n"
"s_add_u32 m0, s58, 0\n"
"s_add_u32 s51, s68, 128\n"
"s_nop 0\n"
"buffer_load_dwordx4 v32, s[16:19], s51 offen offset:0 lds\n"
"s_bitcmp1_b32 s6, 2\n"
"s_cselect_b64 s[8:9], s[78:79], s[76:77]\n"
"s_cselect_b64 s[12:13], s[82:83], s[80:81]\n"
"v_mul_u32_u24 v13, 0x180, v43\n"
"s_bitcmp1_b32 s6, 2\n"
"s_cselect_b64 s[72:73], s[90:91], s[88:89]\n"
"s_cselect_b32 s57, 8, 0\n"
"v_lshrrev_b32 v50, 2, v42\n"
"buffer_load_dwordx4 v33, s[16:19], s51 offen offset:1024 lds\n"
"s_mov_b64 s[24:25], s[92:93]\n"
"v_or_b32 v0, v13, v47\n"
"s_mov_b64 s[74:75], s[94:95]\n"
"v_or_b32 v13, v13, v49\n"
"s_add_u32 m0, s58, 2048\n"
"s_add_u32 s51, s69, 128\n"
"s_nop 0\n"
"buffer_load_dwordx4 v32, s[16:19], s51 offen offset:0 lds\n"
"s_mov_b32 s26, 102760448\n"
"v_add_u32 v0, 0x16000, v0\n"
"s_mov_b32 s27, 0x20000\n"
"v_mul_u32_u24 v2, 0x180, v50\n"
"buffer_load_dwordx4 v33, s[16:19], s51 offen offset:1024 lds\n"
"s_lshl_b32 s2, s48, 5\n"
"v_xor_b32 v1, 64, v0\n"
"s_add_u32 s2, s2, s55\n"
"v_or_b32 v2, v2, v46\n"
"s_add_u32 m0, s58, 4096\n"
"s_add_u32 s51, s70, 128\n"
"s_nop 0\n"
"buffer_load_dwordx4 v32, s[16:19], s51 offen offset:0 lds\n"
"s_mul_i32 s3, s2, 393216\n"
"v_add_u32 v2, 0x4000, v2\n"
"s_add_u32 s8, s8, s3\n"
"s_addc_u32 s9, s9, 0\n"
"v_xor_b32 v3, 32, v2\n"
"buffer_load_dwordx4 v33, s[16:19], s51 offen offset:1024 lds\n"
"s_add_u32 s12, s12, s3\n"
"s_addc_u32 s13, s13, 0\n"
"v_xor_b32 v4, 64, v2\n"
"s_sub_u32 s8, s8, 99328\n"
"s_subb_u32 s9, s9, 0\n"
"v_xor_b32 v5, 0x60, v2\n"
"s_add_u32 m0, s58, 6144\n"
"s_add_u32 s51, s71, 128\n"
"s_nop 0\n"
"buffer_load_dwordx4 v32, s[16:19], s51 offen offset:0 lds\n"
"s_sub_u32 s12, s12, 99328\n"
"s_subb_u32 s13, s13, 0\n"
"v_mul_u32_u24 v16, 0x140, v50\n"
"s_add_u32 s28, s8, 1572864\n"
"s_addc_u32 s29, s9, 0\n"
"v_and_b32 v17, 24, v46\n"
"buffer_load_dwordx4 v33, s[16:19], s51 offen offset:1024 lds\n"
"s_add_u32 s30, s12, 1572864\n"
"s_addc_u32 s31, s13, 0\n"
"v_lshrrev_b32 v51, 1, v46\n"
"s_add_u32 s34, s20, 1536\n"
"s_addc_u32 s35, s21, 0\n"
"v_add_u32 v16, v16, v17\n"
"s_add_u32 m0, s38, 139264\n"
"s_mov_b32 s50, 786432\n"
"s_nop 0\n"
"buffer_load_dwordx4 v36, s[20:23], s50 offen offset:0 lds\n"
"s_mov_b32 s10, 492544\n"
"v_and_b32 v51, 32, v51\n"
"s_mov_b32 s11, 0x20000\n"
"v_mul_u32_u24 v23, 0xa00, v43\n"
"buffer_load_dwordx4 v37, s[20:23], s50 offen offset:1024 lds\n"
"s_mov_b32 s14, 492544\n"
"v_xor_b32 v17, 32, v51\n"
"s_mov_b32 s15, 0x20000\n"
"v_or_b32 v23, v23, v49\n"
"buffer_load_dwordx4 v38, s[20:23], s50 offen offset:2048 lds\n"
"s_mul_i32 s52, s56, 320\n"
"v_add_u32 v17, v16, v17\n"
"s_mul_i32 s3, s54, 1966080\n"
"v_add_u32 v16, v16, v51\n"
"s_add_u32 m0, s38, 151552\n"
"s_mov_b32 s50, 1179648\n"
"s_nop 0\n"
"buffer_load_dwordx4 v36, s[20:23], s50 offen offset:0 lds\n"
"s_add_u32 s72, s72, s3\n"
"s_addc_u32 s73, s73, 0\n"
"v_add_u32 v52, s38, v12\n"
"s_mov_b64 s[84:85], s[72:73]\n"
"v_lshrrev_b32 v51, 1, v42\n"
"buffer_load_dwordx4 v37, s[20:23], s50 offen offset:1024 lds\n"
"s_add_u32 s72, s72, s52\n"
"s_addc_u32 s73, s73, 0\n"
"v_and_b32 v6, v42, v51\n"
"s_mul_i32 s53, s7, 2560\n"
"v_and_b32 v7, 16, v51\n"
"buffer_load_dwordx4 v38, s[20:23], s50 offen offset:2048 lds\n"
"s_add_u32 s52, s52, s53\n"
"v_or_b32 v8, v42, v51\n"
"s_mul_i32 s53, s48, 655360\n"
"v_and_b32 v6, 16, v6\n"
"s_mov_b32 s49, 0\n"
"s_mov_b64 exec, 0\n"
"s_cmp_eq_u32 s38, 0\n"
"s_cbranch_scc1 L_pg0\n"
"s_cmp_eq_u32 s38, 3072\n"
"s_cbranch_scc1 L_pg1\n"
"s_cmp_eq_u32 s38, 6144\n"
"s_cbranch_scc1 L_pg2\n"
"s_branch L_pg3\n"
"L_stag:\n"
"s_sub_u32 s44, s44, 6600\n"
"s_max_i32 s44, s44, 0\n"
"s_lshr_b32 s44, s44, 4\n"
"s_min_u32 s44, s44, 300\n"
"s_add_u32 s97, s97, s44\n"
"s_lshr_b32 s47, s6, 3\n"
"s_mul_i32 s47, s47, 7\n"
"L_pws:\n"
"s_sleep 1\n"
"s_sub_u32 s47, s47, 7\n"
"s_cmp_gt_i32 s47, 0\n"
"s_cbranch_scc1 L_pws\n"
"s_memtime s[64:65]\n"
"s_memrealtime s[66:67]\n"
"s_bitcmp1_b32 s6, 0\n"
"s_waitcnt lgkmcnt(0)\n"
"s_cselect_b32 s65, -6, 0\n"
"s_addk_i32 s65, 176\n"
"s_movk_i32 s67, 1630\n"
"s_branch L_prs\n"
"L_pg0:\n"
"L_bk0:\n"
"s_mov_b64 exec, -1\n"
"s_waitcnt vmcnt(21)\n"
"s_memtime s[44:45]\n"
"s_memrealtime s[46:47]\n"
"ds_read_b128 a[192:195], v40 offset:0\n"
"ds_read_b128 a[196:199], v40 offset:2048\n"
"ds_read_b128 a[200:203], v40 offset:4096\n"
"ds_read_b128 a[204:207], v40 offset:6144\n"
"ds_read_b128 a[208:211], v41 offset:0\n"
"ds_read_b128 a[212:215], v41 offset:2048\n"
"ds_read_b128 a[232:235], v41 offset:4096\n"
"ds_read_b128 a[236:239], v41 offset:6144\n"
"s_waitcnt vmcnt(15)\n"
"s_waitcnt lgkmcnt(0)\n"
"s_barrier\n"
"ds_read_b64_tr_b16 a[240:241], v28 offset:0\n"
"ds_read_b64_tr_b16 a[242:243], v28 offset:1536\n"
"ds_read_b64_tr_b16 a[244:245], v28 offset:12288\n"
"ds_read_b64_tr_b16 a[246:247], v28 offset:13824\n"
"ds_read_b64_tr_b16 a[248:249], v29 offset:0\n"
"ds_read_b64_tr_b16 a[250:251], v29 offset:1536\n"
"ds_read_b64_tr_b16 a[252:253], v29 offset:12288\n"
"ds_read_b64_tr_b16 a[254:255], v29 offset:13824\n"
"ds_read_b64_tr_b16 a[216:217], v30 offset:0\n"
"ds_read_b64_tr_b16 a[218:219], v30 offset:1536\n"
"ds_read_b64_tr_b16 a[220:221], v30 offset:12288\n"
"ds_read_b64_tr_b16 a[222:223], v30 offset:13824\n"
"s_sub_u32 s44, s44, s40\n"
"s_sub_u32 s46, s46, s42\n"
"s_mul_i32 s46, s46, 20\n"
"s_max_u32 s46, s46, 3900\n"
"s_cmp_gt_u32 s44, s46\n"
"s_cselect_b32 s99, 191, 0\n"
"s_cselect_b32 s97, 1530, 0\n"
"s_mov_b32 s65, 0\n"
"s_cbranch_scc1 L_stag\n"
"L_prs:\n"
"s_add_u32 s97, s97, s42\n"
"s_lshr_b32 s44, s6, 3\n"
"s_mul_i32 s44, s44, 1\n"
"s_add_u32 s97, s97, s44\n"
"s_waitcnt lgkmcnt(8)\n"
"v_mfma_f32_16x16x32_bf16 v[64:67], a[240:243], a[192:195], 0\n"
"ds_read_b64_tr_b16 a[224:225], v31 offset:0\n"
"v_mfma_f32_16x16x32_bf16 v[64:67], a[244:247], a[208:211], v[64:67]\n"
"ds_read_b64_tr_b16 a[226:227], v31 offset:1536\n"
"v_mfma_f32_16x16x32_bf16 v[68:71], a[240:243], a[196:199], 0\n"
"s_add_u32 s52, s52, s53\n"
"ds_read_b64_tr_b16 a[228:229], v31 offset:12288\n"
"v_add_u32 v7, 16, v7\n"
"v_mfma_f32_16x16x32_bf16 v[68:71], a[244:247], a[212:215], v[68:71]\n"
"s_mul_i32 s53, s57, 160\n"
"ds_read_b64_tr_b16 a[230:231], v31 offset:13824\n"
"v_and_or_b32 v8, v8, 16, 32\n"
"v_mfma_f32_16x16x32_bf16 v[72:75], a[240:243], a[200:203], 0\n"
"s_add_u32 m0, s39, 0\n"
"s_add_u32 s51, s68, 256\n"
"v_lshlrev_b32 v9, 1, v6\n"
"v_mfma_f32_16x16x32_bf16 v[72:75], a[244:247], a[232:235], v[72:75]\n"
"s_add_u32 s52, s52, s53\n"
"buffer_load_dwordx4 v32, s[16:19], s51 offen offset:0 lds\n"
"v_mfma_f32_16x16x32_bf16 v[76:79], a[240:243], a[204:207], 0\n"
"s_add_u32 m0, s39, 0\n"
"s_add_u32 s74, s74, s52\n"
"s_addc_u32 s75, s75, 0\n"
"v_max3_f32 v252, |v64|, |v65|, |v66|\n"
"v_lshlrev_b32 v10, 1, v7\n"
"v_mfma_f32_16x16x32_bf16 v[76:79], a[244:247], a[236:239], v[76:79]\n"
"s_waitcnt lgkmcnt(8)\n"
"s_add_u32 s57, s57, s48\n"
"buffer_load_dwordx4 v33, s[16:19], s51 offen offset:1024 lds\n"
"v_mfma_f32_16x16x32_bf16 v[160:163], a[248:251], a[192:195], 0\n"
"s_lshl_b32 s90, s57, 9\n"
"ds_read_b64_tr_b16 a[240:241], v28 offset:128\n"
"v_max3_f32 v253, |v68|, |v69|, |v70|\n"
"v_mfma_f32_16x16x32_bf16 v[160:163], a[252:255], a[208:211], v[160:163]\n"
"s_add_u32 s90, s90, 53477376\n"
"ds_read_b64_tr_b16 a[242:243], v28 offset:1664\n"
"v_max3_f32 v254, |v72|, |v73|, |v74|\n"
"v_mfma_f32_16x16x32_bf16 v[164:167], a[248:251], a[196:199], 0\n"
"s_lshl4_add_u32 s89, s55, s90\n"
"ds_read_b64_tr_b16 a[244:245], v28 offset:12416\n"
"v_lshlrev_b32 v11, 1, v8\n"
"v_mfma_f32_16x16x32_bf16 v[164:167], a[252:255], a[212:215], v[164:167]\n"
"s_and_b32 s52, s54, 2\n"
"ds_read_b64_tr_b16 a[246:247], v28 offset:13952\n"
"v_max3_f32 v255, |v76|, |v77|, |v78|\n"
"v_mfma_f32_16x16x32_bf16 v[168:171], a[248:251], a[200:203], 0\n"
"s_add_u32 m0, s39, 2048\n"
"s_add_u32 s51, s69, 256\n"
"v_min3_f32 v252, v252, v253, v254\n"
"v_min_f32 v252, v252, v255\n"
"v_mfma_f32_16x16x32_bf16 v[168:171], a[252:255], a[232:235], v[168:171]\n"
"s_lshl_b32 s52, s52, 7\n"
"buffer_load_dwordx4 v32, s[16:19], s51 offen offset:0 lds\n"
"v_mfma_f32_16x16x32_bf16 v[172:175], a[248:251], a[204:207], 0\n"
"s_add_u32 m0, s39, 2048\n"
"s_add_u32 s90, s90, s52\n"
"v_min_f32 v252, 0x71800000, v252\n"
"v_add_u32 v252, 0x1000000, v252\n"
"v_mfma_f32_16x16x32_bf16 v[172:175], a[252:255], a[236:239], v[172:175]\n"
"s_waitcnt lgkmcnt(8)\n"
"s_lshr_b32 s2, s7, 6\n"
"buffer_load_dwordx4 v33, s[16:19], s51 offen offset:1024 lds\n"
"v_mfma_f32_16x16x32_bf16 v[80:83], a[216:219], a[192:195], 0\n"
"s_lshl2_add_u32 s3, s48, s2\n"
"ds_read_b64_tr_b16 a[248:249], v29 offset:128\n"
"v_and_b32 v252, 0x7e000000, v252\n"
"v_mfma_f32_16x16x32_bf16 v[80:83], a[220:223], a[208:211], v[80:83]\n"
"s_mul_i32 s95, s2, 5120\n"
"ds_read_b64_tr_b16 a[250:251], v29 offset:1664\n"
"v_mul_f32 v253, 0x42c00000, v252\n"
"v_mfma_f32_16x16x32_bf16 v[84:87], a[216:219], a[196:199], 0\n"
"s_mul_i32 s52, s3, 61440\n"
"ds_read_b64_tr_b16 a[252:253], v29 offset:12416\n"
"v_mul_f32 v254, 0x42c00000, v252\n"
"v_mfma_f32_16x16x32_bf16 v[84:87], a[220:223], a[212:215], v[84:87]\n"
"s_add_u32 s80, s34, s52\n"
"s_addc_u32 s81, s35, 0\n"
"ds_read_b64_tr_b16 a[254:255], v29 offset:13952\n"
"v_mul_f32 v255, 0x42c00000, v252\n"
"v_mfma_f32_16x16x32_bf16 v[88:91], a[216:219], a[200:203], 0\n"
"s_add_u32 m0, s39, 4096\n"
"s_add_u32 s51, s70, 256\n"
"v_mul_f32 v252, 0x42c00000, v252\n"
"v_mul_f32 v62, 0xbdd53b95, v252\n"
"v_mfma_f32_16x16x32_bf16 v[88:91], a[220:223], a[232:235], v[88:91]\n"
"s_sub_u32 s82, 7852416, s52\n"
"buffer_load_dwordx4 v32, s[16:19], s51 offen offset:0 lds\n"
"v_mfma_f32_16x16x32_bf16 v[92:95], a[216:219], a[204:207], 0\n"
"s_add_u32 m0, s39, 4096\n"
"s_mov_b32 s83, 0x20000\n"
"v_xor_b32 v9, v9, v52\n"
"v_mfma_f32_16x16x32_bf16 v[92:95], a[220:223], a[236:239], v[92:95]\n"
"s_waitcnt lgkmcnt(8)\n"
"s_lshl2_add_u32 s3, s57, s2\n"
"buffer_load_dwordx4 v33, s[16:19], s51 offen offset:1024 lds\n"
"v_mfma_f32_16x16x32_bf16 v[176:179], a[224:227], a[192:195], v[252:255]\n"
"s_mul_i32 s3, s3, 786432\n"
"ds_read_b64_tr_b16 a[216:217], v30 offset:128\n"
"v_xor_b32 v10, v10, v52\n"
"v_mfma_f32_16x16x32_bf16 v[176:179], a[228:231], a[208:211], v[176:179]\n"
"s_mul_i32 s52, s55, 24576\n"
"ds_read_b64_tr_b16 a[218:219], v30 offset:1664\n"
"v_xor_b32 v11, v11, v52\n"
"v_mfma_f32_16x16x32_bf16 v[180:183], a[224:227], a[196:199], v[252:255]\n"
"s_add_u32 s36, s52, s3\n"
"ds_read_b64_tr_b16 a[220:221], v30 offset:12416\n"
"v_mul_u32_u24 v39, 11, v42\n"
"v_mfma_f32_16x16x32_bf16 v[180:183], a[228:231], a[212:215], v[180:183]\n"
"s_add_u32 s37, s36, 98304\n"
"ds_read_b64_tr_b16 a[222:223], v30 offset:13952\n"
"v_lshlrev_b32 v51, 7, v42\n"
"v_mfma_f32_16x16x32_bf16 v[184:187], a[224:227], a[200:203], v[252:255]\n"
"s_add_u32 m0, s39, 6144\n"
"s_add_u32 s51, s71, 256\n"
"v_lshrrev_b32 v39, 5, v39\n"
"v_mfma_f32_16x16x32_bf16 v[184:187], a[228:231], a[232:235], v[184:187]\n"
"s_mul_i32 s52, s54, 196608\n"
"buffer_load_dwordx4 v32, s[16:19], s51 offen offset:0 lds\n"
"v_mfma_f32_16x16x32_bf16 v[188:191], a[224:227], a[204:207], v[252:255]\n"
"s_add_u32 m0, s39, 6144\n"
"s_add_u32 s78, s52, s3\n"
"v_cmp_gt_u32 vcc, 15, v42\n"
"v_mfma_f32_16x16x32_bf16 v[188:191], a[228:231], a[236:239], v[188:191]\n"
"s_waitcnt lgkmcnt(8)\n"
"s_mov_b32 s87, s56\n"
"buffer_load_dwordx4 v33, s[16:19], s51 offen offset:1024 lds\n"
"v_mfma_f32_16x16x32_bf16 v[96:99], a[240:243], a[192:195], v[252:255]\n"
"s_lshr_b32 s52, s6, 3\n"
"ds_read_b64_tr_b16 a[224:225], v31 offset:128\n"
"v_mul_u32_u24 v39, 0x2e80, v39\n"
"v_mfma_f32_16x16x32_bf16 v[96:99], a[244:247], a[208:211], v[96:99]\n"
"s_lshl2_add_u32 s52, s52, s2\n"
"ds_read_b64_tr_b16 a[226:227], v31 offset:1664\n"
"v_mov_b32 v24, 0\n"
"v_mfma_f32_16x16x32_bf16 v[100:103], a[240:243], a[196:199], v[252:255]\n"
"s_mul_i32 s52, s52, 3840\n"
"ds_read_b64_tr_b16 a[228:229], v31 offset:12416\n"
"v_add_u32 v39, v39, v51\n"
"v_mfma_f32_16x16x32_bf16 v[100:103], a[244:247], a[212:215], v[100:103]\n"
"s_waitcnt vmcnt(14)\n"
"s_sub_u32 s86, 1966080, s52\n"
"ds_read_b64_tr_b16 a[230:231], v31 offset:13952\n"
"v_mov_b32 v25, 0\n"
"v_mfma_f32_16x16x32_bf16 v[104:107], a[240:243], a[200:203], v[252:255]\n"
"s_add_u32 m0, s38, 40960\n"
"s_mov_b32 s50, 1572864\n"
"ds_read_b128 a[0:3], v34 offset:0\n"
"v_cndmask_b32 v39, -1, v39, vcc\n"
"v_mfma_f32_16x16x32_bf16 v[104:107], a[244:247], a[232:235], v[104:107]\n"
"s_add_u32 s84, s84, s52\n"
"s_addc_u32 s85, s85, 0\n"
"buffer_load_dwordx4 v36, s[20:23], s50 offen offset:0 lds\n"
"v_mfma_f32_16x16x32_bf16 v[108:111], a[240:243], a[204:207], v[252:255]\n"
"s_add_u32 m0, s38, 40960\n"
"s_lshl2_add_u32 s96, s57, s56\n"
"ds_read_b128 a[16:19], v35 offset:0\n"
"v_mul_u32_u24 v53, 0xccd, v42\n"
"v_mfma_f32_16x16x32_bf16 v[108:111], a[244:247], a[236:239], v[108:111]\n"
"s_waitcnt lgkmcnt(8)\n"
"s_lshl2_add_u32 s96, s96, s2\n"
"buffer_load_dwordx4 v37, s[20:23], s50 offen offset:1024 lds\n"
"v_mfma_f32_16x16x32_bf16 v[192:195], a[248:251], a[192:195], v[252:255]\n"
"s_lshl3_add_u32 s91, s96, s54\n"
"ds_read_b64_tr_b16 a[240:241], v28 offset:256\n"
"v_lshlrev_b32 v60, 6, s54\n"
"v_mfma_f32_16x16x32_bf16 v[192:195], a[252:255], a[208:211], v[192:195]\n"
"s_lshl_b32 s91, s91, 4\n"
"ds_read_b64_tr_b16 a[242:243], v28 offset:1792\n"
"v_lshrrev_b32 v54, 16, v53\n"
"v_mfma_f32_16x16x32_bf16 v[196:199], a[248:251], a[196:199], v[252:255]\n"
"s_add_u32 s91, s91, 53485568\n"
"ds_read_b64_tr_b16 a[244:245], v28 offset:12544\n"
"v_cmp_gt_u16 vcc, 0xc700, v53\n"
"v_mfma_f32_16x16x32_bf16 v[196:199], a[252:255], a[212:215], v[196:199]\n"
"s_mul_i32 s92, s56, 15360\n"
"ds_read_b64_tr_b16 a[246:247], v28 offset:14080\n"
"v_lshrrev_b32 v55, 13, v53\n"
"v_mfma_f32_16x16x32_bf16 v[200:203], a[248:251], a[200:203], v[252:255]\n"
"s_add_u32 m0, s38, 40960\n"
"s_add_u32 s92, s92, s3\n"
"ds_read_b128 a[4:7], v34 offset:2048\n"
"v_add_u32 v28, 0x12000, v28\n"
"v_mfma_f32_16x16x32_bf16 v[200:203], a[252:255], a[232:235], v[200:203]\n"
"s_mov_b32 s93, s54\n"
"buffer_load_dwordx4 v38, s[20:23], s50 offen offset:2048 lds\n"
"v_mfma_f32_16x16x32_bf16 v[204:207], a[248:251], a[204:207], v[252:255]\n"
"s_add_u32 m0, s38, 53248\n"
"s_mov_b32 s50, 1966080\n"
"ds_read_b128 a[20:23], v35 offset:2048\n"
"v_mul_u32_u24 v18, 0x3c0, v54\n"
"v_mfma_f32_16x16x32_bf16 v[204:207], a[252:255], a[236:239], v[204:207]\n"
"s_waitcnt lgkmcnt(8)\n"
"s_lshl2_add_u32 s94, s6, s2\n"
"buffer_load_dwordx4 v36, s[20:23], s50 offen offset:0 lds\n"
"v_mfma_f32_16x16x32_bf16 v[112:115], a[216:219], a[192:195], v[252:255]\n"
"s_lshl_b32 s94, s94, 11\n"
"ds_read_b64_tr_b16 a[248:249], v29 offset:256\n"
"v_cndmask_b32 v61, 0, v60, vcc\n"
"v_mfma_f32_16x16x32_bf16 v[112:115], a[220:223], a[208:211], v[112:115]\n"
"s_add_u32 s94, s94, 92274688\n"
"ds_read_b64_tr_b16 a[250:251], v29 offset:1792\n"
"v_add_u32 v18, v18, v12\n"
"v_mfma_f32_16x16x32_bf16 v[116:119], a[216:219], a[196:199], v[252:255]\n"
"s_lshl1_add_u32 s76, s4, 1\n"
"ds_read_b64_tr_b16 a[252:253], v29 offset:12544\n"
"v_and_or_b32 v55, v55, 32, v61\n"
"v_mfma_f32_16x16x32_bf16 v[116:119], a[220:223], a[212:215], v[116:119]\n"
"s_mul_i32 s76, s76, 0x9E3779B1\n"
"ds_read_b64_tr_b16 a[254:255], v29 offset:14080\n"
"v_add_u32 v53, 0x33340, v53\n"
"v_mfma_f32_16x16x32_bf16 v[120:123], a[216:219], a[200:203], v[252:255]\n"
"s_add_u32 m0, s38, 53248\n"
"s_lshr_b64 s[0:1], s[0:1], 6\n"
"ds_read_b128 a[8:11], v34 offset:4096\n"
"v_add_u32 v29, 0x12000, v29\n"
"v_mfma_f32_16x16x32_bf16 v[120:123], a[220:223], a[232:235], v[120:123]\n"
"s_lshr_b64 s[4:5], s[4:5], 31\n"
"buffer_load_dwordx4 v37, s[20:23], s50 offen offset:1024 lds\n"
"v_mfma_f32_16x16x32_bf16 v[124:127], a[216:219], a[204:207], v[252:255]\n"
"s_add_u32 m0, s38, 53248\n"
"s_add_u32 s77, s0, s4\n"
"ds_read_b128 a[24:27], v35 offset:4096\n"
"v_lshrrev_b32 v54, 16, v53\n"
"v_mfma_f32_16x16x32_bf16 v[124:127], a[220:223], a[236:239], v[124:127]\n"
"s_waitcnt lgkmcnt(8)\n"
"s_mul_i32 s77, s77, 0x85EBCA6B\n"
"buffer_load_dwordx4 v38, s[20:23], s50 offen offset:2048 lds\n"
"v_mfma_f32_16x16x32_bf16 v[208:211], a[224:227], a[192:195], v[252:255]\n"
"s_mul_i32 s52, s76, 0xC2B2AE35\n"
"ds_read_b64_tr_b16 a[216:217], v30 offset:256\n"
"v_cmp_gt_u16 vcc, 0xc700, v53\n"
"v_mfma_f32_16x16x32_bf16 v[208:211], a[228:231], a[208:211], v[208:211]\n"
"s_xor_b32 s77, s77, s52\n"
"ds_read_b64_tr_b16 a[218:219], v30 offset:1792\n"
"v_lshrrev_b32 v56, 13, v53\n"
"v_mfma_f32_16x16x32_bf16 v[212:215], a[224:227], a[196:199], v[252:255]\n"
"s_lshl_b32 s0, s2, 2\n"
"ds_read_b64_tr_b16 a[220:221], v30 offset:12544\n"
"v_mul_u32_u24 v19, 0x3c0, v54\n"
"v_mfma_f32_16x16x32_bf16 v[212:215], a[228:231], a[212:215], v[212:215]\n"
"s_waitcnt lgkmcnt(8)\n"
"s_lshr_b32 s88, 0x1021, s0\n"
"ds_read_b64_tr_b16 a[222:223], v30 offset:14080\n"
"v_cndmask_b32 v61, 0, v60, vcc\n"
"v_mfma_f32_16x16x32_bf16 v[216:219], a[224:227], a[200:203], v[252:255]\n"
"s_add_u32 m0, s58, 0\n"
"s_add_u32 s51, s68, 384\n"
"ds_read_b128 a[12:15], v34 offset:6144\n"
"v_add_u32 v30, 0x12000, v30\n"
"v_mfma_f32_16x16x32_bf16 v[216:219], a[228:231], a[232:235], v[216:219]\n"
"s_lshr_b32 s0, 0x350, s0\n"
"buffer_load_dwordx4 v32, s[16:19], s51 offen offset:0 lds\n"
"v_mfma_f32_16x16x32_bf16 v[220:223], a[224:227], a[204:207], v[252:255]\n"
"s_add_u32 m0, s58, 0\n"
"s_and_b32 s88, s88, 15\n"
"ds_read_b128 a[28:31], v35 offset:6144\n"
"v_sub_u32 v34, v34, s59\n"
"v_mfma_f32_16x16x32_bf16 v[220:223], a[228:231], a[236:239], v[220:223]\n"
"s_waitcnt lgkmcnt(8)\n"
"s_lshl_b32 s88, s88, 10\n"
"buffer_load_dwordx4 v33, s[16:19], s51 offen offset:1024 lds\n"
"v_mfma_f32_16x16x32_bf16 v[128:131], a[240:243], a[192:195], v[252:255]\n"
"s_mul_i32 s63, s48, 3\n"
"ds_read_b64_tr_b16 a[224:225], v31 offset:256\n"
"v_sub_u32 v35, v35, s59\n"
"v_mfma_f32_16x16x32_bf16 v[128:131], a[244:247], a[208:211], v[128:131]\n"
"s_add_u32 s63, s63, s6\n"
"ds_read_b64_tr_b16 a[226:227], v31 offset:1792\n"
"v_add_u32 v19, v19, v12\n"
"v_mfma_f32_16x16x32_bf16 v[132:135], a[240:243], a[196:199], v[252:255]\n"
"s_cmp_lt_u32 s2, 2\n"
"s_cselect_b32 s1, 0, 0\n"
"ds_read_b64_tr_b16 a[228:229], v31 offset:12544\n"
"v_and_or_b32 v56, v56, 32, v61\n"
"v_mfma_f32_16x16x32_bf16 v[132:135], a[244:247], a[212:215], v[132:135]\n"
"s_waitcnt lgkmcnt(3)\n"
"s_add_u32 s1, s1, s63\n"
"ds_read_b64_tr_b16 a[230:231], v31 offset:14080\n"
"v_add_u32 v53, 0x33340, v53\n"
"v_mfma_f32_16x16x32_bf16 v[136:139], a[240:243], a[200:203], v[252:255]\n"
"s_add_u32 m0, s58, 2048\n"
"s_add_u32 s51, s69, 384\n"
"v_add_u32 v31, 0x12000, v31\n"
"v_lshrrev_b32 v54, 16, v53\n"
"v_mfma_f32_16x16x32_bf16 v[136:139], a[244:247], a[232:235], v[136:139]\n"
"s_and_b32 s1, s1, 31\n"
"buffer_load_dwordx4 v32, s[16:19], s51 offen offset:0 lds\n"
"v_mfma_f32_16x16x32_bf16 v[140:143], a[240:243], a[204:207], v[252:255]\n"
"s_add_u32 m0, s58, 2048\n"
"s_mul_i32 s1, s1, 3072\n"
"v_cmp_gt_u16 vcc, 0xc700, v53\n"
"v_mfma_f32_16x16x32_bf16 v[140:143], a[244:247], a[236:239], v[140:143]\n"
"s_waitcnt lgkmcnt(4)\n"
"s_add_u32 s88, s88, s1\n"
"buffer_load_dwordx4 v33, s[16:19], s51 offen offset:1024 lds\n"
"v_mfma_f32_16x16x32_bf16 v[224:227], a[248:251], a[192:195], v[252:255]\n"
"s_lshr_b32 s1, s88, 15\n"
"v_lshrrev_b32 v57, 13, v53\n"
"v_mfma_f32_16x16x32_bf16 v[224:227], a[252:255], a[208:211], v[224:227]\n"
"s_waitcnt vmcnt(18)\n"
"s_mul_i32 s1, s1, 0x18000\n"
"v_mul_u32_u24 v20, 0x3c0, v54\n"
"v_mfma_f32_16x16x32_bf16 v[228:231], a[248:251], a[196:199], v[252:255]\n"
"s_waitcnt lgkmcnt(0)\n"
"s_add_u32 s88, s88, s1\n"
"v_cndmask_b32 v61, 0, v60, vcc\n"
"v_mfma_f32_16x16x32_bf16 v[228:231], a[252:255], a[212:215], v[228:231]\n"
"s_barrier\n"
"s_or_b32 s88, s88, 0x18000\n"
"v_add_u32 v20, v20, v12\n"
"v_mfma_f32_16x16x32_bf16 v[232:235], a[248:251], a[200:203], v[252:255]\n"
"s_and_b32 s0, s0, 15\n"
"ds_read_b64_tr_b16 a[240:241], v28 offset:0\n"
"v_and_or_b32 v57, v57, 32, v61\n"
"v_mfma_f32_16x16x32_bf16 v[232:235], a[252:255], a[232:235], v[232:235]\n"
"s_mul_i32 s0, s0, 384\n"
"ds_read_b64_tr_b16 a[242:243], v28 offset:1536\n"
"v_add_u32 v53, 0x33340, v53\n"
"v_mfma_f32_16x16x32_bf16 v[236:239], a[248:251], a[204:207], v[252:255]\n"
"s_mul_i32 s1, s2, 29696\n"
"ds_read_b64_tr_b16 a[244:245], v28 offset:12288\n"
"v_lshrrev_b32 v54, 16, v53\n"
"v_mfma_f32_16x16x32_bf16 v[236:239], a[252:255], a[236:239], v[236:239]\n"
"s_waitcnt lgkmcnt(3)\n"
"s_add_u32 s1, s1, s0\n"
"ds_read_b64_tr_b16 a[246:247], v28 offset:13824\n"
"v_cmp_gt_u16 vcc, 0xc700, v53\n"
"v_mfma_f32_16x16x32_bf16 v[144:147], a[216:219], a[192:195], v[252:255]\n"
"s_sub_u32 s5, 2048, s0\n"
"L_pg1:\n"
"s_cbranch_execz L_bk0\n"
"ds_read_b64_tr_b16 a[248:249], v29 offset:0\n"
"v_lshrrev_b32 v58, 13, v53\n"
"v_mfma_f32_16x16x32_bf16 v[144:147], a[220:223], a[208:211], v[144:147]\n"
"s_sub_u32 s4, s1, 3072\n"
"ds_read_b64_tr_b16 a[250:251], v29 offset:1536\n"
"v_mul_u32_u24 v21, 0x3c0, v54\n"
"v_mfma_f32_16x16x32_bf16 v[148:151], a[216:219], a[196:199], v[252:255]\n"
"s_cmp_eq_u32 s0, 0\n"
"s_cselect_b32 s4, s1, s4\n"
"ds_read_b64_tr_b16 a[252:253], v29 offset:12288\n"
"v_cndmask_b32 v61, 0, v60, vcc\n"
"v_mfma_f32_16x16x32_bf16 v[148:151], a[220:223], a[212:215], v[148:151]\n"
"s_and_b32 s3, s7, 64\n"
"ds_read_b64_tr_b16 a[254:255], v29 offset:13824\n"
"v_add_u32 v21, v21, v12\n"
"v_mfma_f32_16x16x32_bf16 v[152:155], a[216:219], a[200:203], v[252:255]\n"
"s_add_u32 m0, s58, 4096\n"
"s_add_u32 s51, s70, 384\n"
"v_and_or_b32 v58, v58, 32, v61\n"
"v_mfma_f32_16x16x32_bf16 v[152:155], a[220:223], a[232:235], v[152:155]\n"
"s_mul_i32 s52, s2, 20480\n"
"buffer_load_dwordx4 v32, s[16:19], s51 offen offset:0 lds\n"
"v_mfma_f32_16x16x32_bf16 v[156:159], a[216:219], a[204:207], v[252:255]\n"
"s_add_u32 m0, s58, 4096\n"
"s_cmp_eq_u32 s7, 0\n"
"s_cselect_b32 s53, 6, 2\n"
"v_add_u32 v53, 0x33340, v53\n"
"v_mfma_f32_16x16x32_bf16 v[156:159], a[220:223], a[236:239], v[156:159]\n"
"s_waitcnt lgkmcnt(8)\n"
"buffer_load_dwordx4 v33, s[16:19], s51 offen offset:1024 lds\n"
"v_mfma_f32_16x16x32_bf16 v[240:243], a[224:227], a[192:195], v[252:255]\n"
"ds_read_b64_tr_b16 a[216:217], v30 offset:0\n"
"v_lshrrev_b32 v54, 16, v53\n"
"v_mfma_f32_16x16x32_bf16 v[240:243], a[228:231], a[208:211], v[240:243]\n"
"ds_read_b64_tr_b16 a[218:219], v30 offset:1536\n"
"v_cmp_gt_u16 vcc, 0xc700, v53\n"
"v_mfma_f32_16x16x32_bf16 v[244:247], a[224:227], a[196:199], v[252:255]\n"
"ds_read_b64_tr_b16 a[220:221], v30 offset:12288\n"
"v_lshrrev_b32 v59, 13, v53\n"
"v_mfma_f32_16x16x32_bf16 v[244:247], a[228:231], a[212:215], v[244:247]\n"
"s_waitcnt lgkmcnt(8)\n"
"ds_read_b64_tr_b16 a[222:223], v30 offset:13824\n"
"v_mul_u32_u24 v22, 0x3c0, v54\n"
"v_mfma_f32_16x16x32_bf16 v[248:251], a[224:227], a[200:203], v[252:255]\n"
"s_add_u32 m0, s58, 6144\n"
"s_add_u32 s51, s71, 384\n"
"v_cndmask_b32 v61, 0, v60, vcc\n"
"v_mfma_f32_16x16x32_bf16 v[248:251], a[228:231], a[232:235], v[248:251]\n"
"buffer_load_dwordx4 v32, s[16:19], s51 offen offset:0 lds\n"
"v_mfma_f32_16x16x32_bf16 v[252:255], a[224:227], a[204:207], v[252:255]\n"
"s_add_u32 m0, s58, 6144\n"
"v_add_u32 v22, v22, v12\n"
"v_mfma_f32_16x16x32_bf16 v[252:255], a[228:231], a[236:239], v[252:255]\n"
"s_waitcnt lgkmcnt(8)\n"
"buffer_load_dwordx4 v33, s[16:19], s51 offen offset:1024 lds\n"
"v_mfma_f32_16x16x32_bf16 v[64:67], a[240:243], a[0:3], v[64:67]\n"
"ds_read_b64_tr_b16 a[224:225], v31 offset:0\n"
"v_and_or_b32 v59, v59, 32, v61\n"
"v_mfma_f32_16x16x32_bf16 v[64:67], a[244:247], a[16:19], v[64:67]\n"
"ds_read_b64_tr_b16 a[226:227], v31 offset:1536\n"
"v_add_u32 v22, 0x1000, v22\n"
"v_mfma_f32_16x16x32_bf16 v[68:71], a[240:243], a[4:7], v[68:71]\n"
"ds_read_b64_tr_b16 a[228:229], v31 offset:12288\n"
"v_xor_b32 v52, s3, v52\n"
"v_mfma_f32_16x16x32_bf16 v[68:71], a[244:247], a[20:23], v[68:71]\n"
"ds_read_b64_tr_b16 a[230:231], v31 offset:13824\n"
"v_lshlrev_b32 v51, 6, v42\n"
"v_mfma_f32_16x16x32_bf16 v[72:75], a[240:243], a[8:11], v[72:75]\n"
"v_xor_b32 v6, v6, v52\n"
"v_mfma_f32_16x16x32_bf16 v[72:75], a[244:247], a[24:27], v[72:75]\n"
"v_xor_b32 v7, v7, v52\n"
"v_mfma_f32_16x16x32_bf16 v[76:79], a[240:243], a[12:15], v[76:79]\n"
"s_add_u32 m0, s38, 65536\n"
"s_mov_b32 s50, 2359296\n"
"v_xor_b32 v8, v8, v52\n"
"v_mfma_f32_16x16x32_bf16 v[76:79], a[244:247], a[28:31], v[76:79]\n"
"s_waitcnt lgkmcnt(8)\n"
"buffer_load_dwordx4 v36, s[20:23], s50 offen offset:0 lds\n"
"v_mfma_f32_16x16x32_bf16 v[160:163], a[248:251], a[0:3], v[160:163]\n"
"ds_read_b64_tr_b16 a[240:241], v28 offset:128\n"
"v_and_b32 v51, 64, v51\n"
"v_mfma_f32_16x16x32_bf16 v[160:163], a[252:255], a[16:19], v[160:163]\n"
"ds_read_b64_tr_b16 a[242:243], v28 offset:1664\n"
"v_lshl_add_u32 v44, v42, 6, s91\n"
"v_mfma_f32_16x16x32_bf16 v[164:167], a[248:251], a[4:7], v[164:167]\n"
"ds_read_b64_tr_b16 a[244:245], v28 offset:12416\n"
"v_xor_b32 v51, s89, v51\n"
"v_mfma_f32_16x16x32_bf16 v[164:167], a[252:255], a[20:23], v[164:167]\n"
"s_waitcnt vmcnt(15)\n"
"ds_read_b64_tr_b16 a[246:247], v28 offset:13952\n"
"v_and_b32 v50, 4, v42\n"
"v_mfma_f32_16x16x32_bf16 v[168:171], a[248:251], a[8:11], v[168:171]\n"
"ds_read_b128 a[32:35], v34 offset:0\n"
"v_lshl_or_b32 v51, v50, 1, v51\n"
"v_mfma_f32_16x16x32_bf16 v[168:171], a[252:255], a[24:27], v[168:171]\n"
"ds_read_b128 a[36:39], v34 offset:2048\n"
"v_cmp_gt_u32 vcc, 2, v42\n"
"v_mfma_f32_16x16x32_bf16 v[172:175], a[248:251], a[12:15], v[172:175]\n"
"s_add_u32 m0, s38, 65536\n"
"ds_read_b128 a[40:43], v34 offset:4096\n"
"v_add_u32 v18, s52, v18\n"
"v_mfma_f32_16x16x32_bf16 v[172:175], a[252:255], a[28:31], v[172:175]\n"
"s_waitcnt lgkmcnt(8)\n"
"buffer_load_dwordx4 v37, s[20:23], s50 offen offset:1024 lds\n"
"v_mfma_f32_16x16x32_bf16 v[80:83], a[216:219], a[0:3], v[80:83]\n"
"ds_read_b64_tr_b16 a[248:249], v29 offset:128\n"
"v_add_u32 v19, s52, v19\n"
"v_mfma_f32_16x16x32_bf16 v[80:83], a[220:223], a[16:19], v[80:83]\n"
"ds_read_b64_tr_b16 a[250:251], v29 offset:1664\n"
"v_add_u32 v20, s52, v20\n"
"v_mfma_f32_16x16x32_bf16 v[84:87], a[216:219], a[4:7], v[84:87]\n"
"ds_read_b64_tr_b16 a[252:253], v29 offset:12416\n"
"v_cndmask_b32 v44, v51, v44, vcc\n"
"v_mfma_f32_16x16x32_bf16 v[84:87], a[220:223], a[20:23], v[84:87]\n"
"ds_read_b64_tr_b16 a[254:255], v29 offset:13952\n"
"v_cmp_gt_u32 vcc, s53, v42\n"
"v_mfma_f32_16x16x32_bf16 v[88:91], a[216:219], a[8:11], v[88:91]\n"
"ds_read_b128 a[44:47], v34 offset:6144\n"
"v_add_u32 v34, v34, s59\n"
"v_mfma_f32_16x16x32_bf16 v[88:91], a[220:223], a[24:27], v[88:91]\n"
"ds_read_b128 a[48:51], v35 offset:0\n"
"v_add_u32 v21, s52, v21\n"
"v_mfma_f32_16x16x32_bf16 v[92:95], a[216:219], a[12:15], v[92:95]\n"
"s_add_u32 m0, s38, 65536\n"
"ds_read_b128 a[52:55], v35 offset:2048\n"
"v_add_u32 v22, s52, v22\n"
"v_mfma_f32_16x16x32_bf16 v[92:95], a[220:223], a[28:31], v[92:95]\n"
"s_waitcnt lgkmcnt(8)\n"
"buffer_load_dwordx4 v38, s[20:23], s50 offen offset:2048 lds\n"
"v_mfma_f32_16x16x32_bf16 v[176:179], a[224:227], a[0:3], v[176:179]\n"
"ds_read_b64_tr_b16 a[216:217], v30 offset:128\n"
"v_xor_b32 v18, v18, v55\n"
"v_mfma_f32_16x16x32_bf16 v[176:179], a[228:231], a[16:19], v[176:179]\n"
"ds_read_b64_tr_b16 a[218:219], v30 offset:1664\n"
"v_cndmask_b32 v44, -1, v44, vcc\n"
"v_mfma_f32_16x16x32_bf16 v[180:183], a[224:227], a[4:7], v[180:183]\n"
"ds_read_b64_tr_b16 a[220:221], v30 offset:12416\n"
"v_xor_b32 v19, v19, v56\n"
"v_mfma_f32_16x16x32_bf16 v[180:183], a[228:231], a[20:23], v[180:183]\n"
"ds_read_b64_tr_b16 a[222:223], v30 offset:13952\n"
"v_xor_b32 v20, v20, v57\n"
"v_mfma_f32_16x16x32_bf16 v[184:187], a[224:227], a[8:11], v[184:187]\n"
"ds_read_b128 a[56:59], v35 offset:4096\n"
"v_xor_b32 v21, v21, v58\n"
"v_mfma_f32_16x16x32_bf16 v[184:187], a[228:231], a[24:27], v[184:187]\n"
"ds_read_b128 a[60:63], v35 offset:6144\n"
"v_add_u32 v35, v35, s59\n"
"v_mfma_f32_16x16x32_bf16 v[188:191], a[224:227], a[12:15], v[188:191]\n"
"s_add_u32 m0, s38, 77824\n"
"s_mov_b32 s50, 2752512\n"
"v_xor_b32 v22, v22, v59\n"
"v_mfma_f32_16x16x32_bf16 v[188:191], a[228:231], a[28:31], v[188:191]\n"
"s_waitcnt lgkmcnt(8)\n"
"buffer_load_dwordx4 v36, s[20:23], s50 offen offset:0 lds\n"
"v_mfma_f32_16x16x32_bf16 v[96:99], a[240:243], a[0:3], v[96:99]\n"
"ds_read_b64_tr_b16 a[224:225], v31 offset:128\n"
"v_cmp_gt_u32 vcc, s5, v12\n"
"v_mfma_f32_16x16x32_bf16 v[96:99], a[244:247], a[16:19], v[96:99]\n"
"ds_read_b64_tr_b16 a[226:227], v31 offset:1664\n"
"v_add_u32 v6, s1, v6\n"
"v_mfma_f32_16x16x32_bf16 v[100:103], a[240:243], a[4:7], v[100:103]\n"
"ds_read_b64_tr_b16 a[228:229], v31 offset:12416\n"
"v_add_u32 v9, s1, v9\n"
"v_mfma_f32_16x16x32_bf16 v[100:103], a[244:247], a[20:23], v[100:103]\n"
"s_waitcnt lgkmcnt(3)\n"
"ds_read_b64_tr_b16 a[230:231], v31 offset:13952\n"
"v_cndmask_b32 v54, -1, v24, vcc\n"
"v_mfma_f32_16x16x32_bf16 v[104:107], a[240:243], a[8:11], v[104:107]\n"
"s_add_u32 m0, s39, 0\n"
"s_add_u32 s51, s68, 512\n"
"v_add_u32 v8, s4, v8\n"
"v_mfma_f32_16x16x32_bf16 v[104:107], a[244:247], a[24:27], v[104:107]\n"
"buffer_load_dwordx4 v32, s[16:19], s51 offen offset:0 lds\n"
"v_mfma_f32_16x16x32_bf16 v[108:111], a[240:243], a[12:15], v[108:111]\n"
"s_add_u32 m0, s38, 77824\n"
"v_and_b32 v54, 0xfffff400, v54\n"
"v_mfma_f32_16x16x32_bf16 v[108:111], a[244:247], a[28:31], v[108:111]\n"
"s_waitcnt lgkmcnt(4)\n"
"buffer_load_dwordx4 v37, s[20:23], s50 offen offset:1024 lds\n"
"v_mfma_f32_16x16x32_bf16 v[192:195], a[248:251], a[0:3], v[192:195]\n"
"ds_read_b64_tr_b16 a[240:241], v28 offset:256\n"
"v_add_u32 v11, s4, v11\n"
"v_mfma_f32_16x16x32_bf16 v[192:195], a[252:255], a[16:19], v[192:195]\n"
"ds_read_b64_tr_b16 a[242:243], v28 offset:1792\n"
"v_add3_u32 v7, v7, v54, s1\n"
"v_mfma_f32_16x16x32_bf16 v[196:199], a[248:251], a[4:7], v[196:199]\n"
"ds_read_b64_tr_b16 a[244:245], v28 offset:12544\n"
"v_add3_u32 v10, v10, v54, s1\n"
"v_mfma_f32_16x16x32_bf16 v[196:199], a[252:255], a[20:23], v[196:199]\n"
"ds_read_b64_tr_b16 a[246:247], v28 offset:14080\n"
"v_mfma_f32_16x16x32_bf16 v[200:203], a[248:251], a[8:11], v[200:203]\n"
"s_add_u32 m0, s39, 0\n"
"v_add_u32 v28, 0xfffe8000, v28\n"
"v_mfma_f32_16x16x32_bf16 v[200:203], a[252:255], a[24:27], v[200:203]\n"
"buffer_load_dwordx4 v33, s[16:19], s51 offen offset:1024 lds\n"
"v_mfma_f32_16x16x32_bf16 v[204:207], a[248:251], a[12:15], v[204:207]\n"
"s_add_u32 m0, s38, 77824\n"
"v_mfma_f32_16x16x32_bf16 v[204:207], a[252:255], a[28:31], v[204:207]\n"
"s_waitcnt lgkmcnt(8)\n"
"buffer_load_dwordx4 v38, s[20:23], s50 offen offset:2048 lds\n"
"v_mfma_f32_16x16x32_bf16 v[112:115], a[216:219], a[0:3], v[112:115]\n"
"ds_read_b64_tr_b16 a[248:249], v29 offset:256\n"
"v_mfma_f32_16x16x32_bf16 v[112:115], a[220:223], a[16:19], v[112:115]\n"
"ds_read_b64_tr_b16 a[250:251], v29 offset:1792\n"
"v_mfma_f32_16x16x32_bf16 v[116:119], a[216:219], a[4:7], v[116:119]\n"
"ds_read_b64_tr_b16 a[252:253], v29 offset:12544\n"
"v_mfma_f32_16x16x32_bf16 v[116:119], a[220:223], a[20:23], v[116:119]\n"
"ds_read_b64_tr_b16 a[254:255], v29 offset:14080\n"
"v_mfma_f32_16x16x32_bf16 v[120:123], a[216:219], a[8:11], v[120:123]\n"
"s_add_u32 m0, s39, 2048\n"
"s_add_u32 s51, s69, 512\n"
"v_add_u32 v29, 0xfffe8000, v29\n"
"v_mfma_f32_16x16x32_bf16 v[120:123], a[220:223], a[24:27], v[120:123]\n"
"buffer_load_dwordx4 v32, s[16:19], s51 offen offset:0 lds\n"
"v_mfma_f32_16x16x32_bf16 v[124:127], a[216:219], a[12:15], v[124:127]\n"
"s_add_u32 m0, s39, 2048\n"
"v_mfma_f32_16x16x32_bf16 v[124:127], a[220:223], a[28:31], v[124:127]\n"
"s_waitcnt lgkmcnt(8)\n"
"buffer_load_dwordx4 v33, s[16:19], s51 offen offset:1024 lds\n"
"v_mfma_f32_16x16x32_bf16 v[208:211], a[224:227], a[0:3], v[208:211]\n"
"ds_read_b64_tr_b16 a[216:217], v30 offset:256\n"
"v_mfma_f32_16x16x32_bf16 v[208:211], a[228:231], a[16:19], v[208:211]\n"
"ds_read_b64_tr_b16 a[218:219], v30 offset:1792\n"
"v_mfma_f32_16x16x32_bf16 v[212:215], a[224:227], a[4:7], v[212:215]\n"
"ds_read_b64_tr_b16 a[220:221], v30 offset:12544\n"
"v_mfma_f32_16x16x32_bf16 v[212:215], a[228:231], a[20:23], v[212:215]\n"
"ds_read_b64_tr_b16 a[222:223], v30 offset:14080\n"
"v_mfma_f32_16x16x32_bf16 v[216:219], a[224:227], a[8:11], v[216:219]\n"
"s_add_u32 m0, s39, 4096\n"
"s_add_u32 s51, s70, 512\n"
"v_add_u32 v30, 0xfffe8000, v30\n"
"v_mfma_f32_16x16x32_bf16 v[216:219], a[228:231], a[24:27], v[216:219]\n"
"buffer_load_dwordx4 v32, s[16:19], s51 offen offset:0 lds\n"
"v_mfma_f32_16x16x32_bf16 v[220:223], a[224:227], a[12:15], v[220:223]\n"
"s_add_u32 m0, s39, 4096\n"
"v_mfma_f32_16x16x32_bf16 v[220:223], a[228:231], a[28:31], v[220:223]\n"
"s_waitcnt lgkmcnt(8)\n"
"buffer_load_dwordx4 v33, s[16:19], s51 offen offset:1024 lds\n"
"v_mfma_f32_16x16x32_bf16 v[128:131], a[240:243], a[0:3], v[128:131]\n"
"ds_read_b64_tr_b16 a[224:225], v31 offset:256\n"
"v_mfma_f32_16x16x32_bf16 v[128:131], a[244:247], a[16:19], v[128:131]\n"
"ds_read_b64_tr_b16 a[226:227], v31 offset:1792\n"
"v_mfma_f32_16x16x32_bf16 v[132:135], a[240:243], a[4:7], v[132:135]\n"
"ds_read_b64_tr_b16 a[228:229], v31 offset:12544\n"
"v_mfma_f32_16x16x32_bf16 v[132:135], a[244:247], a[20:23], v[132:135]\n"
"ds_read_b64_tr_b16 a[230:231], v31 offset:14080\n"
"v_mfma_f32_16x16x32_bf16 v[136:139], a[240:243], a[8:11], v[136:139]\n"
"s_add_u32 m0, s39, 6144\n"
"s_add_u32 s51, s71, 512\n"
"v_add_u32 v31, 0xfffe8000, v31\n"
"v_mfma_f32_16x16x32_bf16 v[136:139], a[244:247], a[24:27], v[136:139]\n"
"buffer_load_dwordx4 v32, s[16:19], s51 offen offset:0 lds\n"
"v_mfma_f32_16x16x32_bf16 v[140:143], a[240:243], a[12:15], v[140:143]\n"
"s_add_u32 m0, s39, 6144\n"
"v_mfma_f32_16x16x32_bf16 v[140:143], a[244:247], a[28:31], v[140:143]\n"
"s_waitcnt lgkmcnt(8)\n"
"buffer_load_dwordx4 v33, s[16:19], s51 offen offset:1024 lds\n"
"v_mfma_f32_16x16x32_bf16 v[224:227], a[248:251], a[0:3], v[224:227]\n"
"v_mfma_f32_16x16x32_bf16 v[224:227], a[252:255], a[16:19], v[224:227]\n"
"s_waitcnt vmcnt(22)\n"
"v_mfma_f32_16x16x32_bf16 v[228:231], a[248:251], a[4:7], v[228:231]\n"
"s_waitcnt lgkmcnt(0)\n"
"v_mfma_f32_16x16x32_bf16 v[228:231], a[252:255], a[20:23], v[228:231]\n"
"s_barrier\n"
"v_mfma_f32_16x16x32_bf16 v[232:235], a[248:251], a[8:11], v[232:235]\n"
"ds_read_b64_tr_b16 a[240:241], v28 offset:0\n"
"v_mfma_f32_16x16x32_bf16 v[232:235], a[252:255], a[24:27], v[232:235]\n"
"ds_read_b64_tr_b16 a[242:243], v28 offset:1536\n"
"v_mfma_f32_16x16x32_bf16 v[236:239], a[248:251], a[12:15], v[236:239]\n"
"ds_read_b64_tr_b16 a[244:245], v28 offset:12288\n"
"v_mfma_f32_16x16x32_bf16 v[236:239], a[252:255], a[28:31], v[236:239]\n"
"s_waitcnt lgkmcnt(3)\n"
"ds_read_b64_tr_b16 a[246:247], v28 offset:13824\n"
"v_mfma_f32_16x16x32_bf16 v[144:147], a[216:219], a[0:3], v[144:147]\n"
"ds_read_b64_tr_b16 a[248:249], v29 offset:0\n"
"v_mfma_f32_16x16x32_bf16 v[144:147], a[220:223], a[16:19], v[144:147]\n"
"ds_read_b64_tr_b16 a[250:251], v29 offset:1536\n"
"v_mfma_f32_16x16x32_bf16 v[148:151], a[216:219], a[4:7], v[148:151]\n"
"ds_read_b64_tr_b16 a[252:253], v29 offset:12288\n"
"v_mfma_f32_16x16x32_bf16 v[148:151], a[220:223], a[20:23], v[148:151]\n"
"s_waitcnt vmcnt(14)\n"
"ds_read_b64_tr_b16 a[254:255], v29 offset:13824\n"
"v_mfma_f32_16x16x32_bf16 v[152:155], a[216:219], a[8:11], v[152:155]\n"
"ds_read_b128 a[192:195], v34 offset:0\n"
"v_mfma_f32_16x16x32_bf16 v[152:155], a[220:223], a[24:27], v[152:155]\n"
"ds_read_b128 a[196:199], v34 offset:2048\n"
"v_mfma_f32_16x16x32_bf16 v[156:159], a[216:219], a[12:15], v[156:159]\n"
"ds_read_b128 a[200:203], v34 offset:4096\n"
"v_mfma_f32_16x16x32_bf16 v[156:159], a[220:223], a[28:31], v[156:159]\n"
"s_waitcnt lgkmcnt(8)\n"
"ds_read_b128 a[204:207], v34 offset:6144\n"
"v_sub_u32 v34, v34, s59\n"
"v_mfma_f32_16x16x32_bf16 v[240:243], a[224:227], a[0:3], v[240:243]\n"
"ds_read_b64_tr_b16 a[216:217], v30 offset:0\n"
"v_mfma_f32_16x16x32_bf16 v[240:243], a[228:231], a[16:19], v[240:243]\n"
"ds_read_b64_tr_b16 a[218:219], v30 offset:1536\n"
"v_mfma_f32_16x16x32_bf16 v[244:247], a[224:227], a[4:7], v[244:247]\n"
"ds_read_b64_tr_b16 a[220:221], v30 offset:12288\n"
"v_mfma_f32_16x16x32_bf16 v[244:247], a[228:231], a[20:23], v[244:247]\n"
"s_waitcnt lgkmcnt(8)\n"
"ds_read_b64_tr_b16 a[222:223], v30 offset:13824\n"
"v_mfma_f32_16x16x32_bf16 v[248:251], a[224:227], a[8:11], v[248:251]\n"
"ds_read_b128 a[208:211], v35 offset:0\n"
"v_mfma_f32_16x16x32_bf16 v[248:251], a[228:231], a[24:27], v[248:251]\n"
"ds_read_b128 a[212:215], v35 offset:2048\n"
"v_mfma_f32_16x16x32_bf16 v[252:255], a[224:227], a[12:15], v[252:255]\n"
"ds_read_b128 a[232:235], v35 offset:4096\n"
"v_mfma_f32_16x16x32_bf16 v[252:255], a[228:231], a[28:31], v[252:255]\n"
"s_waitcnt lgkmcnt(8)\n"
"ds_read_b128 a[236:239], v35 offset:6144\n"
"v_sub_u32 v35, v35, s59\n"
"v_mfma_f32_16x16x32_bf16 v[64:67], a[240:243], a[32:35], v[64:67]\n"
"ds_read_b64_tr_b16 a[224:225], v31 offset:0\n"
"v_mfma_f32_16x16x32_bf16 v[64:67], a[244:247], a[48:51], v[64:67]\n"
"ds_read_b64_tr_b16 a[226:227], v31 offset:1536\n"
"v_mfma_f32_16x16x32_bf16 v[68:71], a[240:243], a[36:39], v[68:71]\n"
"ds_read_b64_tr_b16 a[228:229], v31 offset:12288\n"
"v_mfma_f32_16x16x32_bf16 v[68:71], a[244:247], a[52:55], v[68:71]\n"
"s_waitcnt lgkmcnt(3)\n"
"ds_read_b64_tr_b16 a[230:231], v31 offset:13824\n"
"v_mfma_f32_16x16x32_bf16 v[72:75], a[240:243], a[40:43], v[72:75]\n"
"s_add_u32 m0, s58, 0\n"
"s_add_u32 s51, s68, 640\n"
"v_mfma_f32_16x16x32_bf16 v[72:75], a[244:247], a[56:59], v[72:75]\n"
"buffer_load_dwordx4 v32, s[16:19], s51 offen offset:0 lds\n"
"v_mfma_f32_16x16x32_bf16 v[76:79], a[240:243], a[44:47], v[76:79]\n"
"s_add_u32 m0, s38, 139264\n"
"s_mov_b32 s50, 3145728\n"
"v_mfma_f32_16x16x32_bf16 v[76:79], a[244:247], a[60:63], v[76:79]\n"
"s_waitcnt lgkmcnt(4)\n"
"buffer_load_dwordx4 v36, s[20:23], s50 offen offset:0 lds\n"
"v_mfma_f32_16x16x32_bf16 v[160:163], a[248:251], a[32:35], v[160:163]\n"
"ds_read_b64_tr_b16 a[240:241], v28 offset:128\n"
"v_mfma_f32_16x16x32_bf16 v[160:163], a[252:255], a[48:51], v[160:163]\n"
"ds_read_b64_tr_b16 a[242:243], v28 offset:1664\n"
"v_mfma_f32_16x16x32_bf16 v[164:167], a[248:251], a[36:39], v[164:167]\n"
"ds_read_b64_tr_b16 a[244:245], v28 offset:12416\n"
"v_mfma_f32_16x16x32_bf16 v[164:167], a[252:255], a[52:55], v[164:167]\n"
"ds_read_b64_tr_b16 a[246:247], v28 offset:13952\n"
"v_mfma_f32_16x16x32_bf16 v[168:171], a[248:251], a[40:43], v[168:171]\n"
"s_add_u32 m0, s58, 0\n"
"v_mfma_f32_16x16x32_bf16 v[168:171], a[252:255], a[56:59], v[168:171]\n"
"buffer_load_dwordx4 v33, s[16:19], s51 offen offset:1024 lds\n"
"v_mfma_f32_16x16x32_bf16 v[172:175], a[248:251], a[44:47], v[172:175]\n"
"s_add_u32 m0, s38, 139264\n"
"v_mfma_f32_16x16x32_bf16 v[172:175], a[252:255], a[60:63], v[172:175]\n"
"s_waitcnt lgkmcnt(8)\n"
"buffer_load_dwordx4 v37, s[20:23], s50 offen offset:1024 lds\n"
"v_mfma_f32_16x16x32_bf16 v[80:83], a[216:219], a[32:35], v[80:83]\n"
"ds_read_b64_tr_b16 a[248:249], v29 offset:128\n"
"v_mfma_f32_16x16x32_bf16 v[80:83], a[220:223], a[48:51], v[80:83]\n"
"ds_read_b64_tr_b16 a[250:251], v29 offset:1664\n"
"v_mfma_f32_16x16x32_bf16 v[84:87], a[216:219], a[36:39], v[84:87]\n"
"ds_read_b64_tr_b16 a[252:253], v29 offset:12416\n"
"v_mfma_f32_16x16x32_bf16 v[84:87], a[220:223], a[52:55], v[84:87]\n"
"ds_read_b64_tr_b16 a[254:255], v29 offset:13952\n"
"v_mfma_f32_16x16x32_bf16 v[88:91], a[216:219], a[40:43], v[88:91]\n"
"s_add_u32 m0, s58, 2048\n"
"s_add_u32 s51, s69, 640\n"
"v_mfma_f32_16x16x32_bf16 v[88:91], a[220:223], a[56:59], v[88:91]\n"
"buffer_load_dwordx4 v32, s[16:19], s51 offen offset:0 lds\n"
"v_mfma_f32_16x16x32_bf16 v[92:95], a[216:219], a[44:47], v[92:95]\n"
"s_add_u32 m0, s38, 139264\n"
"v_mfma_f32_16x16x32_bf16 v[92:95], a[220:223], a[60:63], v[92:95]\n"
"s_waitcnt lgkmcnt(8)\n"
"buffer_load_dwordx4 v38, s[20:23], s50 offen offset:2048 lds\n"
"v_mfma_f32_16x16x32_bf16 v[176:179], a[224:227], a[32:35], v[176:179]\n"
"ds_read_b64_tr_b16 a[216:217], v30 offset:128\n"
"v_mfma_f32_16x16x32_bf16 v[176:179], a[228:231], a[48:51], v[176:179]\n"
"ds_read_b64_tr_b16 a[218:219], v30 offset:1664\n"
"v_mfma_f32_16x16x32_bf16 v[180:183], a[224:227], a[36:39], v[180:183]\n"
"ds_read_b64_tr_b16 a[220:221], v30 offset:12416\n"
"v_mfma_f32_16x16x32_bf16 v[180:183], a[228:231], a[52:55], v[180:183]\n"
"ds_read_b64_tr_b16 a[222:223], v30 offset:13952\n"
"v_mfma_f32_16x16x32_bf16 v[184:187], a[224:227], a[40:43], v[184:187]\n"
"s_add_u32 m0, s58, 2048\n"
"v_mfma_f32_16x16x32_bf16 v[184:187], a[228:231], a[56:59], v[184:187]\n"
"buffer_load_dwordx4 v33, s[16:19], s51 offen offset:1024 lds\n"
"v_mfma_f32_16x16x32_bf16 v[188:191], a[224:227], a[44:47], v[188:191]\n"
"s_add_u32 m0, s38, 151552\n"
"s_mov_b32 s50, 3538944\n"
"v_mfma_f32_16x16x32_bf16 v[188:191], a[228:231], a[60:63], v[188:191]\n"
"s_waitcnt lgkmcnt(8)\n"
"buffer_load_dwordx4 v36, s[20:23], s50 offen offset:0 lds\n"
"v_mfma_f32_16x16x32_bf16 v[96:99], a[240:243], a[32:35], v[96:99]\n"
"ds_read_b64_tr_b16 a[224:225], v31 offset:128\n"
"v_mfma_f32_16x16x32_bf16 v[96:99], a[244:247], a[48:51], v[96:99]\n"
"ds_read_b64_tr_b16 a[226:227], v31 offset:1664\n"
"v_mfma_f32_16x16x32_bf16 v[100:103], a[240:243], a[36:39], v[100:103]\n"
"ds_read_b64_tr_b16 a[228:229], v31 offset:12416\n"
"v_mfma_f32_16x16x32_bf16 v[100:103], a[244:247], a[52:55], v[100:103]\n"
"ds_read_b64_tr_b16 a[230:231], v31 offset:13952\n"
"v_mfma_f32_16x16x32_bf16 v[104:107], a[240:243], a[40:43], v[104:107]\n"
"s_add_u32 m0, s58, 4096\n"
"s_add_u32 s51, s70, 640\n"
"v_mfma_f32_16x16x32_bf16 v[104:107], a[244:247], a[56:59], v[104:107]\n"
"buffer_load_dwordx4 v32, s[16:19], s51 offen offset:0 lds\n"
"v_mfma_f32_16x16x32_bf16 v[108:111], a[240:243], a[44:47], v[108:111]\n"
"s_add_u32 m0, s38, 151552\n"
"v_mfma_f32_16x16x32_bf16 v[108:111], a[244:247], a[60:63], v[108:111]\n"
"s_waitcnt lgkmcnt(8)\n"
"buffer_load_dwordx4 v37, s[20:23], s50 offen offset:1024 lds\n"
"v_mfma_f32_16x16x32_bf16 v[192:195], a[248:251], a[32:35], v[192:195]\n"
"ds_read_b64_tr_b16 a[240:241], v28 offset:256\n"
"v_mfma_f32_16x16x32_bf16 v[192:195], a[252:255], a[48:51], v[192:195]\n"
"ds_read_b64_tr_b16 a[242:243], v28 offset:1792\n"
"v_mfma_f32_16x16x32_bf16 v[196:199], a[248:251], a[36:39], v[196:199]\n"
"ds_read_b64_tr_b16 a[244:245], v28 offset:12544\n"
"v_mfma_f32_16x16x32_bf16 v[196:199], a[252:255], a[52:55], v[196:199]\n"
"ds_read_b64_tr_b16 a[246:247], v28 offset:14080\n"
"v_mfma_f32_16x16x32_bf16 v[200:203], a[248:251], a[40:43], v[200:203]\n"
"s_add_u32 m0, s58, 4096\n"
"v_add_u32 v28, 0x6000, v28\n"
"v_mfma_f32_16x16x32_bf16 v[200:203], a[252:255], a[56:59], v[200:203]\n"
"buffer_load_dwordx4 v33, s[16:19], s51 offen offset:1024 lds\n"
"v_mfma_f32_16x16x32_bf16 v[204:207], a[248:251], a[44:47], v[204:207]\n"
"s_add_u32 m0, s38, 151552\n"
"v_mfma_f32_16x16x32_bf16 v[204:207], a[252:255], a[60:63], v[204:207]\n"
"s_waitcnt lgkmcnt(8)\n"
"buffer_load_dwordx4 v38, s[20:23], s50 offen offset:2048 lds\n"
"v_mfma_f32_16x16x32_bf16 v[112:115], a[216:219], a[32:35], v[112:115]\n"
"ds_read_b64_tr_b16 a[248:249], v29 offset:256\n"
"v_mfma_f32_16x16x32_bf16 v[112:115], a[220:223], a[48:51], v[112:115]\n"
"ds_read_b64_tr_b16 a[250:251], v29 offset:1792\n"
"v_mfma_f32_16x16x32_bf16 v[116:119], a[216:219], a[36:39], v[116:119]\n"
"ds_read_b64_tr_b16 a[252:253], v29 offset:12544\n"
"v_mfma_f32_16x16x32_bf16 v[116:119], a[220:223], a[52:55], v[116:119]\n"
"ds_read_b64_tr_b16 a[254:255], v29 offset:14080\n"
"v_mfma_f32_16x16x32_bf16 v[120:123], a[216:219], a[40:43], v[120:123]\n"
"s_add_u32 m0, s58, 6144\n"
"s_add_u32 s51, s71, 640\n"
"v_add_u32 v29, 0x6000, v29\n"
"v_mfma_f32_16x16x32_bf16 v[120:123], a[220:223], a[56:59], v[120:123]\n"
"buffer_load_dwordx4 v32, s[16:19], s51 offen offset:0 lds\n"
"v_mfma_f32_16x16x32_bf16 v[124:127], a[216:219], a[44:47], v[124:127]\n"
"v_mfma_f32_16x16x32_bf16 v[124:127], a[220:223], a[60:63], v[124:127]\n"
"s_waitcnt lgkmcnt(8)\n"
"v_mfma_f32_16x16x32_bf16 v[208:211], a[224:227], a[32:35], v[208:211]\n"
"ds_read_b64_tr_b16 a[216:217], v30 offset:256\n"
"v_mfma_f32_16x16x32_bf16 v[208:211], a[228:231], a[48:51], v[208:211]\n"
"ds_read_b64_tr_b16 a[218:219], v30 offset:1792\n"
"v_mfma_f32_16x16x32_bf16 v[212:215], a[224:227], a[36:39], v[212:215]\n"
"ds_read_b64_tr_b16 a[220:221], v30 offset:12544\n"
"v_mfma_f32_16x16x32_bf16 v[212:215], a[228:231], a[52:55], v[212:215]\n"
"ds_read_b64_tr_b16 a[222:223], v30 offset:14080\n"
"v_mfma_f32_16x16x32_bf16 v[216:219], a[224:227], a[40:43], v[216:219]\n"
"s_add_u32 m0, s58, 6144\n"
"v_add_u32 v30, 0x6000, v30\n"
"v_mfma_f32_16x16x32_bf16 v[216:219], a[228:231], a[56:59], v[216:219]\n"
"buffer_load_dwordx4 v33, s[16:19], s51 offen offset:1024 lds\n"
"v_mfma_f32_16x16x32_bf16 v[220:223], a[224:227], a[44:47], v[220:223]\n"
"v_mfma_f32_16x16x32_bf16 v[220:223], a[228:231], a[60:63], v[220:223]\n"
"s_waitcnt lgkmcnt(8)\n"
"v_mfma_f32_16x16x32_bf16 v[128:131], a[240:243], a[32:35], v[128:131]\n"
"ds_read_b64_tr_b16 a[224:225], v31 offset:256\n"
"v_mfma_f32_16x16x32_bf16 v[128:131], a[244:247], a[48:51], v[128:131]\n"
"ds_read_b64_tr_b16 a[226:227], v31 offset:1792\n"
"v_mfma_f32_16x16x32_bf16 v[132:135], a[240:243], a[36:39], v[132:135]\n"
"ds_read_b64_tr_b16 a[228:229], v31 offset:12544\n"
"v_mfma_f32_16x16x32_bf16 v[132:135], a[244:247], a[52:55], v[132:135]\n"
"ds_read_b64_tr_b16 a[230:231], v31 offset:14080\n"
"v_mfma_f32_16x16x32_bf16 v[136:139], a[240:243], a[40:43], v[136:139]\n"
"v_add_u32 v31, 0x6000, v31\n"
"v_mfma_f32_16x16x32_bf16 v[136:139], a[244:247], a[56:59], v[136:139]\n"
"v_mfma_f32_16x16x32_bf16 v[140:143], a[240:243], a[44:47], v[140:143]\n"
"v_mfma_f32_16x16x32_bf16 v[140:143], a[244:247], a[60:63], v[140:143]\n"
"s_waitcnt lgkmcnt(8)\n"
"v_mfma_f32_16x16x32_bf16 v[224:227], a[248:251], a[32:35], v[224:227]\n"
"v_mfma_f32_16x16x32_bf16 v[224:227], a[252:255], a[48:51], v[224:227]\n"
"s_waitcnt vmcnt(20)\n"
"v_mfma_f32_16x16x32_bf16 v[228:231], a[248:251], a[36:39], v[228:231]\n"
"s_waitcnt lgkmcnt(0)\n"
"v_mfma_f32_16x16x32_bf16 v[228:231], a[252:255], a[52:55], v[228:231]\n"
"s_barrier\n"
"v_mfma_f32_16x16x32_bf16 v[232:235], a[248:251], a[40:43], v[232:235]\n"
"ds_read_b64_tr_b16 a[240:241], v28 offset:0\n"
"v_mfma_f32_16x16x32_bf16 v[232:235], a[252:255], a[56:59], v[232:235]\n"
"ds_read_b64_tr_b16 a[242:243], v28 offset:1536\n"
"v_mfma_f32_16x16x32_bf16 v[236:239], a[248:251], a[44:47], v[236:239]\n"
"ds_read_b64_tr_b16 a[244:245], v28 offset:12288\n"
"v_mfma_f32_16x16x32_bf16 v[236:239], a[252:255], a[60:63], v[236:239]\n"
"s_waitcnt lgkmcnt(3)\n"
"ds_read_b64_tr_b16 a[246:247], v28 offset:13824\n"
"v_mfma_f32_16x16x32_bf16 v[144:147], a[216:219], a[32:35], v[144:147]\n"
"ds_read_b64_tr_b16 a[248:249], v29 offset:0\n"
"v_mfma_f32_16x16x32_bf16 v[144:147], a[220:223], a[48:51], v[144:147]\n"
"ds_read_b64_tr_b16 a[250:251], v29 offset:1536\n"
"v_mfma_f32_16x16x32_bf16 v[148:151], a[216:219], a[36:39], v[148:151]\n"
"ds_read_b64_tr_b16 a[252:253], v29 offset:12288\n"
"v_mfma_f32_16x16x32_bf16 v[148:151], a[220:223], a[52:55], v[148:151]\n"
"s_waitcnt vmcnt(14)\n"
"ds_read_b64_tr_b16 a[254:255], v29 offset:13824\n"
"v_mfma_f32_16x16x32_bf16 v[152:155], a[216:219], a[40:43], v[152:155]\n"
"ds_read_b128 a[0:3], v34 offset:0\n"
"v_mfma_f32_16x16x32_bf16 v[152:155], a[220:223], a[56:59], v[152:155]\n"
"ds_read_b128 a[4:7], v34 offset:2048\n"
"v_mfma_f32_16x16x32_bf16 v[156:159], a[216:219], a[44:47], v[156:159]\n"
"ds_read_b128 a[8:11], v34 offset:4096\n"
"v_mfma_f32_16x16x32_bf16 v[156:159], a[220:223], a[60:63], v[156:159]\n"
"s_waitcnt lgkmcnt(8)\n"
"ds_read_b128 a[12:15], v34 offset:6144\n"
"v_add_u32 v34, v34, s59\n"
"v_mfma_f32_16x16x32_bf16 v[240:243], a[224:227], a[32:35], v[240:243]\n"
"ds_read_b64_tr_b16 a[216:217], v30 offset:0\n"
"v_mfma_f32_16x16x32_bf16 v[240:243], a[228:231], a[48:51], v[240:243]\n"
"ds_read_b64_tr_b16 a[218:219], v30 offset:1536\n"
"v_mfma_f32_16x16x32_bf16 v[244:247], a[224:227], a[36:39], v[244:247]\n"
"ds_read_b64_tr_b16 a[220:221], v30 offset:12288\n"
"v_mfma_f32_16x16x32_bf16 v[244:247], a[228:231], a[52:55], v[244:247]\n"
"s_waitcnt lgkmcnt(8)\n"
"ds_read_b64_tr_b16 a[222:223], v30 offset:13824\n"
"v_mfma_f32_16x16x32_bf16 v[248:251], a[224:227], a[40:43], v[248:251]\n"
"ds_read_b128 a[16:19], v35 offset:0\n"
"v_mfma_f32_16x16x32_bf16 v[248:251], a[228:231], a[56:59], v[248:251]\n"
"ds_read_b128 a[20:23], v35 offset:2048\n"
"v_mfma_f32_16x16x32_bf16 v[252:255], a[224:227], a[44:47], v[252:255]\n"
"ds_read_b128 a[24:27], v35 offset:4096\n"
"v_mfma_f32_16x16x32_bf16 v[252:255], a[228:231], a[60:63], v[252:255]\n"
"s_waitcnt lgkmcnt(8)\n"
"ds_read_b128 a[28:31], v35 offset:6144\n"
"v_add_u32 v35, v35, s59\n"
"v_mfma_f32_16x16x32_bf16 v[64:67], a[240:243], a[192:195], v[64:67]\n"
"ds_read_b64_tr_b16 a[224:225], v31 offset:0\n"
"v_mfma_f32_16x16x32_bf16 v[64:67], a[244:247], a[208:211], v[64:67]\n"
"ds_read_b64_tr_b16 a[226:227], v31 offset:1536\n"
"v_mfma_f32_16x16x32_bf16 v[68:71], a[240:243], a[196:199], v[68:71]\n"
"s_mov_b32 s56, 0x3dd53b95\n"
"ds_read_b64_tr_b16 a[228:229], v31 offset:12288\n"
"v_mfma_f32_16x16x32_bf16 v[68:71], a[244:247], a[212:215], v[68:71]\n"
"s_waitcnt lgkmcnt(3)\n"
"ds_read_b64_tr_b16 a[230:231], v31 offset:13824\n"
"v_mfma_f32_16x16x32_bf16 v[72:75], a[240:243], a[200:203], v[72:75]\n"
"s_add_u32 m0, s39, 0\n"
"s_add_u32 s51, s68, 768\n"
"v_mfma_f32_16x16x32_bf16 v[72:75], a[244:247], a[232:235], v[72:75]\n"
"buffer_load_dwordx4 v32, s[16:19], s51 offen offset:0 lds\n"
"v_mfma_f32_16x16x32_bf16 v[76:79], a[240:243], a[204:207], v[76:79]\n"
"s_add_u32 m0, s38, 40960\n"
"s_mov_b32 s50, 3932160\n"
"v_mfma_f32_16x16x32_bf16 v[76:79], a[244:247], a[236:239], v[76:79]\n"
"s_waitcnt lgkmcnt(4)\n"
"buffer_load_dwordx4 v36, s[20:23], s50 offen offset:0 lds\n"
"v_mfma_f32_16x16x32_bf16 v[160:163], a[248:251], a[192:195], v[160:163]\n"
"ds_read_b64_tr_b16 a[240:241], v28 offset:128\n"
"v_mfma_f32_16x16x32_bf16 v[160:163], a[252:255], a[208:211], v[160:163]\n"
"ds_read_b64_tr_b16 a[242:243], v28 offset:1664\n"
"v_mfma_f32_16x16x32_bf16 v[164:167], a[248:251], a[196:199], v[164:167]\n"
"s_mov_b32 s57, 0x3dd53b95\n"
"ds_read_b64_tr_b16 a[244:245], v28 offset:12416\n"
"v_mfma_f32_16x16x32_bf16 v[164:167], a[252:255], a[212:215], v[164:167]\n"
"ds_read_b64_tr_b16 a[246:247], v28 offset:13952\n"
"v_mfma_f32_16x16x32_bf16 v[168:171], a[248:251], a[200:203], v[168:171]\n"
"s_add_u32 m0, s39, 0\n"
"v_mfma_f32_16x16x32_bf16 v[168:171], a[252:255], a[232:235], v[168:171]\n"
"buffer_load_dwordx4 v33, s[16:19], s51 offen offset:1024 lds\n"
"L_pg2:\n"
"s_cbranch_execz L_bk0\n"
"v_mfma_f32_16x16x32_bf16 v[172:175], a[248:251], a[204:207], v[172:175]\n"
"s_add_u32 m0, s38, 40960\n"
"v_mfma_f32_16x16x32_bf16 v[172:175], a[252:255], a[236:239], v[172:175]\n"
"s_waitcnt lgkmcnt(8)\n"
"buffer_load_dwordx4 v37, s[20:23], s50 offen offset:1024 lds\n"
"v_mfma_f32_16x16x32_bf16 v[80:83], a[216:219], a[192:195], v[80:83]\n"
"ds_read_b64_tr_b16 a[248:249], v29 offset:128\n"
"v_mfma_f32_16x16x32_bf16 v[80:83], a[220:223], a[208:211], v[80:83]\n"
"ds_read_b64_tr_b16 a[250:251], v29 offset:1664\n"
"v_mfma_f32_16x16x32_bf16 v[84:87], a[216:219], a[196:199], v[84:87]\n"
"s_add_u32 s2, s36, 0\n"
"ds_read_b64_tr_b16 a[252:253], v29 offset:12416\n"
"v_mfma_f32_16x16x32_bf16 v[84:87], a[220:223], a[212:215], v[84:87]\n"
"ds_read_b64_tr_b16 a[254:255], v29 offset:13952\n"
"v_mfma_f32_16x16x32_bf16 v[88:91], a[216:219], a[200:203], v[88:91]\n"
"s_add_u32 m0, s39, 2048\n"
"s_add_u32 s51, s69, 768\n"
"v_mfma_f32_16x16x32_bf16 v[88:91], a[220:223], a[232:235], v[88:91]\n"
"buffer_load_dwordx4 v32, s[16:19], s51 offen offset:0 lds\n"
"v_mfma_f32_16x16x32_bf16 v[92:95], a[216:219], a[204:207], v[92:95]\n"
"s_add_u32 m0, s38, 40960\n"
"v_mfma_f32_16x16x32_bf16 v[92:95], a[220:223], a[236:239], v[92:95]\n"
"s_waitcnt lgkmcnt(8)\n"
"buffer_load_dwordx4 v38, s[20:23], s50 offen offset:2048 lds\n"
"v_mfma_f32_16x16x32_bf16 v[176:179], a[224:227], a[192:195], v[176:179]\n"
"ds_read_b64_tr_b16 a[216:217], v30 offset:128\n"
"v_mfma_f32_16x16x32_bf16 v[176:179], a[228:231], a[208:211], v[176:179]\n"
"ds_read_b64_tr_b16 a[218:219], v30 offset:1664\n"
"v_mfma_f32_16x16x32_bf16 v[180:183], a[224:227], a[196:199], v[180:183]\n"
"s_add_u32 s3, s36, 4096\n"
"ds_read_b64_tr_b16 a[220:221], v30 offset:12416\n"
"v_mfma_f32_16x16x32_bf16 v[180:183], a[228:231], a[212:215], v[180:183]\n"
"ds_read_b64_tr_b16 a[222:223], v30 offset:13952\n"
"v_mfma_f32_16x16x32_bf16 v[184:187], a[224:227], a[200:203], v[184:187]\n"
"s_add_u32 m0, s39, 2048\n"
"v_mfma_f32_16x16x32_bf16 v[184:187], a[228:231], a[232:235], v[184:187]\n"
"buffer_load_dwordx4 v33, s[16:19], s51 offen offset:1024 lds\n"
"v_mfma_f32_16x16x32_bf16 v[188:191], a[224:227], a[204:207], v[188:191]\n"
"s_add_u32 m0, s38, 53248\n"
"s_mov_b32 s50, 4325376\n"
"v_mfma_f32_16x16x32_bf16 v[188:191], a[228:231], a[236:239], v[188:191]\n"
"s_waitcnt lgkmcnt(8)\n"
"buffer_load_dwordx4 v36, s[20:23], s50 offen offset:0 lds\n"
"v_mfma_f32_16x16x32_bf16 v[96:99], a[240:243], a[192:195], v[96:99]\n"
"ds_read_b64_tr_b16 a[224:225], v31 offset:128\n"
"v_mfma_f32_16x16x32_bf16 v[96:99], a[244:247], a[208:211], v[96:99]\n"
"ds_read_b64_tr_b16 a[226:227], v31 offset:1664\n"
"v_mfma_f32_16x16x32_bf16 v[100:103], a[240:243], a[196:199], v[100:103]\n"
"s_add_u32 s4, s36, 8192\n"
"ds_read_b64_tr_b16 a[228:229], v31 offset:12416\n"
"v_mfma_f32_16x16x32_bf16 v[100:103], a[244:247], a[212:215], v[100:103]\n"
"ds_read_b64_tr_b16 a[230:231], v31 offset:13952\n"
"v_mfma_f32_16x16x32_bf16 v[104:107], a[240:243], a[200:203], v[104:107]\n"
"s_add_u32 m0, s39, 4096\n"
"s_add_u32 s51, s70, 768\n"
"v_mfma_f32_16x16x32_bf16 v[104:107], a[244:247], a[232:235], v[104:107]\n"
"buffer_load_dwordx4 v32, s[16:19], s51 offen offset:0 lds\n"
"v_mfma_f32_16x16x32_bf16 v[108:111], a[240:243], a[204:207], v[108:111]\n"
"s_add_u32 m0, s38, 53248\n"
"v_mfma_f32_16x16x32_bf16 v[108:111], a[244:247], a[236:239], v[108:111]\n"
"s_waitcnt lgkmcnt(8)\n"
"buffer_load_dwordx4 v37, s[20:23], s50 offen offset:1024 lds\n"
"v_mfma_f32_16x16x32_bf16 v[192:195], a[248:251], a[192:195], v[192:195]\n"
"ds_read_b64_tr_b16 a[240:241], v28 offset:256\n"
"v_mfma_f32_16x16x32_bf16 v[192:195], a[252:255], a[208:211], v[192:195]\n"
"ds_read_b64_tr_b16 a[242:243], v28 offset:1792\n"
"v_mfma_f32_16x16x32_bf16 v[196:199], a[248:251], a[196:199], v[196:199]\n"
"s_add_u32 s5, s36, 12288\n"
"ds_read_b64_tr_b16 a[244:245], v28 offset:12544\n"
"v_mfma_f32_16x16x32_bf16 v[196:199], a[252:255], a[212:215], v[196:199]\n"
"ds_read_b64_tr_b16 a[246:247], v28 offset:14080\n"
"v_mfma_f32_16x16x32_bf16 v[200:203], a[248:251], a[200:203], v[200:203]\n"
"s_add_u32 m0, s39, 4096\n"
"v_add_u32 v28, 0x12000, v28\n"
"v_mfma_f32_16x16x32_bf16 v[200:203], a[252:255], a[232:235], v[200:203]\n"
"buffer_load_dwordx4 v33, s[16:19], s51 offen offset:1024 lds\n"
"v_mfma_f32_16x16x32_bf16 v[204:207], a[248:251], a[204:207], v[204:207]\n"
"s_add_u32 m0, s38, 53248\n"
"v_mfma_f32_16x16x32_bf16 v[204:207], a[252:255], a[236:239], v[204:207]\n"
"s_waitcnt lgkmcnt(8)\n"
"buffer_load_dwordx4 v38, s[20:23], s50 offen offset:2048 lds\n"
"v_mfma_f32_16x16x32_bf16 v[112:115], a[216:219], a[192:195], v[112:115]\n"
"ds_read_b64_tr_b16 a[248:249], v29 offset:256\n"
"v_mfma_f32_16x16x32_bf16 v[112:115], a[220:223], a[208:211], v[112:115]\n"
"ds_read_b64_tr_b16 a[250:251], v29 offset:1792\n"
"v_mfma_f32_16x16x32_bf16 v[116:119], a[216:219], a[196:199], v[116:119]\n"
"s_add_u32 s6, s36, 16384\n"
"ds_read_b64_tr_b16 a[252:253], v29 offset:12544\n"
"v_mfma_f32_16x16x32_bf16 v[116:119], a[220:223], a[212:215], v[116:119]\n"
"ds_read_b64_tr_b16 a[254:255], v29 offset:14080\n"
"v_mfma_f32_16x16x32_bf16 v[120:123], a[216:219], a[200:203], v[120:123]\n"
"s_add_u32 m0, s39, 6144\n"
"s_add_u32 s51, s71, 768\n"
"v_add_u32 v29, 0x12000, v29\n"
"v_mfma_f32_16x16x32_bf16 v[120:123], a[220:223], a[232:235], v[120:123]\n"
"buffer_load_dwordx4 v32, s[16:19], s51 offen offset:0 lds\n"
"v_mfma_f32_16x16x32_bf16 v[124:127], a[216:219], a[204:207], v[124:127]\n"
"v_mfma_f32_16x16x32_bf16 v[124:127], a[220:223], a[236:239], v[124:127]\n"
"s_waitcnt lgkmcnt(8)\n"
"buffer_store_dwordx2 v[24:25], v44, s[24:27], 0 offen offset:0 sc1\n"
"v_mfma_f32_16x16x32_bf16 v[208:211], a[224:227], a[192:195], v[208:211]\n"
"ds_read_b64_tr_b16 a[216:217], v30 offset:256\n"
"v_mfma_f32_16x16x32_bf16 v[208:211], a[228:231], a[208:211], v[208:211]\n"
"ds_read_b64_tr_b16 a[218:219], v30 offset:1792\n"
"v_mfma_f32_16x16x32_bf16 v[212:215], a[224:227], a[196:199], v[212:215]\n"
"s_add_u32 s7, s36, 20480\n"
"ds_read_b64_tr_b16 a[220:221], v30 offset:12544\n"
"v_mfma_f32_16x16x32_bf16 v[212:215], a[228:231], a[212:215], v[212:215]\n"
"ds_read_b64_tr_b16 a[222:223], v30 offset:14080\n"
"v_mfma_f32_16x16x32_bf16 v[216:219], a[224:227], a[200:203], v[216:219]\n"
"s_add_u32 m0, s39, 6144\n"
"v_add_u32 v30, 0x12000, v30\n"
"v_mfma_f32_16x16x32_bf16 v[216:219], a[228:231], a[232:235], v[216:219]\n"
"buffer_load_dwordx4 v33, s[16:19], s51 offen offset:1024 lds\n"
"v_mfma_f32_16x16x32_bf16 v[220:223], a[224:227], a[204:207], v[220:223]\n"
"v_mfma_f32_16x16x32_bf16 v[220:223], a[228:231], a[236:239], v[220:223]\n"
"s_waitcnt lgkmcnt(8)\n"
"buffer_store_dwordx4 v[16:19], v12, s[24:27], s94 offen offset:0\n"
"v_mfma_f32_16x16x32_bf16 v[128:131], a[240:243], a[192:195], v[128:131]\n"
"ds_read_b64_tr_b16 a[224:225], v31 offset:256\n"
"v_mfma_f32_16x16x32_bf16 v[128:131], a[244:247], a[208:211], v[128:131]\n"
"ds_read_b64_tr_b16 a[226:227], v31 offset:1792\n"
"v_mfma_f32_16x16x32_bf16 v[132:135], a[240:243], a[196:199], v[132:135]\n"
"s_mov_b32 s96, 0\n"
"ds_read_b64_tr_b16 a[228:229], v31 offset:12544\n"
"v_mfma_f32_16x16x32_bf16 v[132:135], a[244:247], a[212:215], v[132:135]\n"
"ds_read_b64_tr_b16 a[230:231], v31 offset:14080\n"
"v_mfma_f32_16x16x32_bf16 v[136:139], a[240:243], a[200:203], v[136:139]\n"
"v_add_u32 v31, 0x12000, v31\n"
"v_mfma_f32_16x16x32_bf16 v[136:139], a[244:247], a[232:235], v[136:139]\n"
"v_mfma_f32_16x16x32_bf16 v[140:143], a[240:243], a[204:207], v[140:143]\n"
"v_mfma_f32_16x16x32_bf16 v[140:143], a[244:247], a[236:239], v[140:143]\n"
"s_waitcnt lgkmcnt(8)\n"
"buffer_store_dwordx4 v[20:23], v12, s[24:27], s94 offen offset:1024\n"
"v_mfma_f32_16x16x32_bf16 v[224:227], a[248:251], a[192:195], v[224:227]\n"
"v_mfma_f32_16x16x32_bf16 v[224:227], a[252:255], a[208:211], v[224:227]\n"
"s_waitcnt vmcnt(19)\n"
"v_mfma_f32_16x16x32_bf16 v[228:231], a[248:251], a[196:199], v[228:231]\n"
"s_waitcnt lgkmcnt(0)\n"
"s_mov_b32 s36, 1966080\n"
"v_mfma_f32_16x16x32_bf16 v[228:231], a[252:255], a[212:215], v[228:231]\n"
"s_barrier\n"
"v_mfma_f32_16x16x32_bf16 v[232:235], a[248:251], a[200:203], v[232:235]\n"
"ds_read_b64_tr_b16 a[240:241], v28 offset:0\n"
"v_mfma_f32_16x16x32_bf16 v[232:235], a[252:255], a[232:235], v[232:235]\n"
"ds_read_b64_tr_b16 a[242:243], v28 offset:1536\n"
"v_mfma_f32_16x16x32_bf16 v[236:239], a[248:251], a[204:207], v[236:239]\n"
"ds_read_b64_tr_b16 a[244:245], v28 offset:12288\n"
"v_mfma_f32_16x16x32_bf16 v[236:239], a[252:255], a[236:239], v[236:239]\n"
"s_waitcnt lgkmcnt(3)\n"
"ds_read_b64_tr_b16 a[246:247], v28 offset:13824\n"
"v_mfma_f32_16x16x32_bf16 v[144:147], a[216:219], a[192:195], v[144:147]\n"
"ds_read_b64_tr_b16 a[248:249], v29 offset:0\n"
"v_mfma_f32_16x16x32_bf16 v[144:147], a[220:223], a[208:211], v[144:147]\n"
"ds_read_b64_tr_b16 a[250:251], v29 offset:1536\n"
"v_mfma_f32_16x16x32_bf16 v[148:151], a[216:219], a[196:199], v[148:151]\n"
"s_mov_b32 s33, 0\n"
"ds_read_b64_tr_b16 a[252:253], v29 offset:12288\n"
"v_mfma_f32_16x16x32_bf16 v[148:151], a[220:223], a[212:215], v[148:151]\n"
"s_waitcnt vmcnt(17)\n"
"ds_read_b64_tr_b16 a[254:255], v29 offset:13824\n"
"v_mfma_f32_16x16x32_bf16 v[152:155], a[216:219], a[200:203], v[152:155]\n"
"ds_read_b128 a[32:35], v34 offset:0\n"
"v_mfma_f32_16x16x32_bf16 v[152:155], a[220:223], a[232:235], v[152:155]\n"
"ds_read_b128 a[36:39], v34 offset:2048\n"
"v_mfma_f32_16x16x32_bf16 v[156:159], a[216:219], a[204:207], v[156:159]\n"
"ds_read_b128 a[40:43], v34 offset:4096\n"
"v_mfma_f32_16x16x32_bf16 v[156:159], a[220:223], a[236:239], v[156:159]\n"
"s_waitcnt lgkmcnt(8)\n"
"ds_read_b128 a[44:47], v34 offset:6144\n"
"v_sub_u32 v34, v34, s59\n"
"v_mfma_f32_16x16x32_bf16 v[240:243], a[224:227], a[192:195], v[240:243]\n"
"ds_read_b64_tr_b16 a[216:217], v30 offset:0\n"
"v_mfma_f32_16x16x32_bf16 v[240:243], a[228:231], a[208:211], v[240:243]\n"
"ds_read_b64_tr_b16 a[218:219], v30 offset:1536\n"
"v_mfma_f32_16x16x32_bf16 v[244:247], a[224:227], a[196:199], v[244:247]\n"
"ds_read_b64_tr_b16 a[220:221], v30 offset:12288\n"
"v_mfma_f32_16x16x32_bf16 v[244:247], a[228:231], a[212:215], v[244:247]\n"
"s_waitcnt lgkmcnt(8)\n"
"ds_read_b64_tr_b16 a[222:223], v30 offset:13824\n"
"v_mfma_f32_16x16x32_bf16 v[248:251], a[224:227], a[200:203], v[248:251]\n"
"ds_read_b128 a[48:51], v35 offset:0\n"
"v_mfma_f32_16x16x32_bf16 v[248:251], a[228:231], a[232:235], v[248:251]\n"
"ds_read_b128 a[52:55], v35 offset:2048\n"
"v_mfma_f32_16x16x32_bf16 v[252:255], a[224:227], a[204:207], v[252:255]\n"
"ds_read_b128 a[56:59], v35 offset:4096\n"
"v_mfma_f32_16x16x32_bf16 v[252:255], a[228:231], a[236:239], v[252:255]\n"
"s_waitcnt lgkmcnt(8)\n"
"ds_read_b128 a[60:63], v35 offset:6144\n"
"v_sub_u32 v35, v35, s59\n"
"v_mfma_f32_16x16x32_bf16 v[64:67], a[240:243], a[0:3], v[64:67]\n"
"ds_read_b64_tr_b16 a[224:225], v31 offset:0\n"
"v_mfma_f32_16x16x32_bf16 v[64:67], a[244:247], a[16:19], v[64:67]\n"
"ds_read_b64_tr_b16 a[226:227], v31 offset:1536\n"
"v_mfma_f32_16x16x32_bf16 v[68:71], a[240:243], a[4:7], v[68:71]\n"
"s_mov_b32 s42, s88\n"
"ds_read_b64_tr_b16 a[228:229], v31 offset:12288\n"
"v_mfma_f32_16x16x32_bf16 v[68:71], a[244:247], a[20:23], v[68:71]\n"
"s_waitcnt lgkmcnt(3)\n"
"ds_read_b64_tr_b16 a[230:231], v31 offset:13824\n"
"v_mfma_f32_16x16x32_bf16 v[72:75], a[240:243], a[8:11], v[72:75]\n"
"s_add_u32 m0, s58, 0\n"
"s_add_u32 s51, s68, 896\n"
"v_mfma_f32_16x16x32_bf16 v[72:75], a[244:247], a[24:27], v[72:75]\n"
"buffer_load_dwordx4 v32, s[16:19], s51 offen offset:0 lds\n"
"v_mfma_f32_16x16x32_bf16 v[76:79], a[240:243], a[12:15], v[76:79]\n"
"s_add_u32 m0, s38, 65536\n"
"s_mov_b32 s50, 4718592\n"
"v_mfma_f32_16x16x32_bf16 v[76:79], a[244:247], a[28:31], v[76:79]\n"
"s_waitcnt lgkmcnt(4)\n"
"buffer_load_dwordx4 v36, s[20:23], s50 offen offset:0 lds\n"
"v_mfma_f32_16x16x32_bf16 v[160:163], a[248:251], a[0:3], v[160:163]\n"
"ds_read_b64_tr_b16 a[240:241], v28 offset:128\n"
"v_mfma_f32_16x16x32_bf16 v[160:163], a[252:255], a[16:19], v[160:163]\n"
"ds_read_b64_tr_b16 a[242:243], v28 offset:1664\n"
"v_mfma_f32_16x16x32_bf16 v[164:167], a[248:251], a[4:7], v[164:167]\n"
"s_mov_b32 s43, s88\n"
"ds_read_b64_tr_b16 a[244:245], v28 offset:12416\n"
"v_mfma_f32_16x16x32_bf16 v[164:167], a[252:255], a[20:23], v[164:167]\n"
"ds_read_b64_tr_b16 a[246:247], v28 offset:13952\n"
"v_mfma_f32_16x16x32_bf16 v[168:171], a[248:251], a[8:11], v[168:171]\n"
"s_add_u32 m0, s58, 0\n"
"v_mfma_f32_16x16x32_bf16 v[168:171], a[252:255], a[24:27], v[168:171]\n"
"buffer_load_dwordx4 v33, s[16:19], s51 offen offset:1024 lds\n"
"v_mfma_f32_16x16x32_bf16 v[172:175], a[248:251], a[12:15], v[172:175]\n"
"s_add_u32 m0, s38, 65536\n"
"v_mfma_f32_16x16x32_bf16 v[172:175], a[252:255], a[28:31], v[172:175]\n"
"s_waitcnt lgkmcnt(8)\n"
"buffer_load_dwordx4 v37, s[20:23], s50 offen offset:1024 lds\n"
"v_mfma_f32_16x16x32_bf16 v[80:83], a[216:219], a[0:3], v[80:83]\n"
"ds_read_b64_tr_b16 a[248:249], v29 offset:128\n"
"v_mfma_f32_16x16x32_bf16 v[80:83], a[220:223], a[16:19], v[80:83]\n"
"ds_read_b64_tr_b16 a[250:251], v29 offset:1664\n"
"v_mfma_f32_16x16x32_bf16 v[84:87], a[216:219], a[4:7], v[84:87]\n"
"s_mov_b32 s55, 0\n"
"ds_read_b64_tr_b16 a[252:253], v29 offset:12416\n"
"v_mfma_f32_16x16x32_bf16 v[84:87], a[220:223], a[20:23], v[84:87]\n"
"ds_read_b64_tr_b16 a[254:255], v29 offset:13952\n"
"v_mfma_f32_16x16x32_bf16 v[88:91], a[216:219], a[8:11], v[88:91]\n"
"s_add_u32 m0, s58, 2048\n"
"s_add_u32 s51, s69, 896\n"
"v_mov_b32 v22, v62\n"
"v_mov_b32 v23, v62\n"
"v_mfma_f32_16x16x32_bf16 v[88:91], a[220:223], a[24:27], v[88:91]\n"
"buffer_load_dwordx4 v32, s[16:19], s51 offen offset:0 lds\n"
"v_mfma_f32_16x16x32_bf16 v[92:95], a[216:219], a[12:15], v[92:95]\n"
"s_add_u32 m0, s38, 65536\n"
"v_mfma_f32_16x16x32_bf16 v[92:95], a[220:223], a[28:31], v[92:95]\n"
"s_waitcnt lgkmcnt(8)\n"
"buffer_load_dwordx4 v38, s[20:23], s50 offen offset:2048 lds\n"
"v_mfma_f32_16x16x32_bf16 v[176:179], a[224:227], a[0:3], v[176:179]\n"
"ds_read_b64_tr_b16 a[216:217], v30 offset:128\n"
"v_mfma_f32_16x16x32_bf16 v[176:179], a[228:231], a[16:19], v[176:179]\n"
"ds_read_b64_tr_b16 a[218:219], v30 offset:1664\n"
"v_mfma_f32_16x16x32_bf16 v[180:183], a[224:227], a[4:7], v[180:183]\n"
"s_add_u32 s40, s38, 90112\n"
"ds_read_b64_tr_b16 a[220:221], v30 offset:12416\n"
"v_mfma_f32_16x16x32_bf16 v[180:183], a[228:231], a[20:23], v[180:183]\n"
"ds_read_b64_tr_b16 a[222:223], v30 offset:13952\n"
"v_mfma_f32_16x16x32_bf16 v[184:187], a[224:227], a[8:11], v[184:187]\n"
"s_add_u32 m0, s58, 2048\n"
"v_mfma_f32_16x16x32_bf16 v[184:187], a[228:231], a[24:27], v[184:187]\n"
"buffer_load_dwordx4 v33, s[16:19], s51 offen offset:1024 lds\n"
"v_mfma_f32_16x16x32_bf16 v[188:191], a[224:227], a[12:15], v[188:191]\n"
"s_add_u32 m0, s38, 77824\n"
"s_mov_b32 s50, 5111808\n"
"v_mfma_f32_16x16x32_bf16 v[188:191], a[228:231], a[28:31], v[188:191]\n"
"s_waitcnt lgkmcnt(8)\n"
"buffer_load_dwordx4 v36, s[20:23], s50 offen offset:0 lds\n"
"v_mfma_f32_16x16x32_bf16 v[96:99], a[240:243], a[0:3], v[96:99]\n"
"ds_read_b64_tr_b16 a[224:225], v31 offset:128\n"
"v_mfma_f32_16x16x32_bf16 v[96:99], a[244:247], a[16:19], v[96:99]\n"
"ds_read_b64_tr_b16 a[226:227], v31 offset:1664\n"
"v_mfma_f32_16x16x32_bf16 v[100:103], a[240:243], a[4:7], v[100:103]\n"
"s_add_u32 s41, s38, 65536\n"
"ds_read_b64_tr_b16 a[228:229], v31 offset:12416\n"
"v_mfma_f32_16x16x32_bf16 v[100:103], a[244:247], a[20:23], v[100:103]\n"
"ds_read_b64_tr_b16 a[230:231], v31 offset:13952\n"
"v_mfma_f32_16x16x32_bf16 v[104:107], a[240:243], a[8:11], v[104:107]\n"
"s_add_u32 m0, s58, 4096\n"
"s_add_u32 s51, s70, 896\n"
"v_mfma_f32_16x16x32_bf16 v[104:107], a[244:247], a[24:27], v[104:107]\n"
"buffer_load_dwordx4 v32, s[16:19], s51 offen offset:0 lds\n"
"v_mfma_f32_16x16x32_bf16 v[108:111], a[240:243], a[12:15], v[108:111]\n"
"s_add_u32 m0, s38, 77824\n"
"v_mfma_f32_16x16x32_bf16 v[108:111], a[244:247], a[28:31], v[108:111]\n"
"s_waitcnt lgkmcnt(8)\n"
"buffer_load_dwordx4 v37, s[20:23], s50 offen offset:1024 lds\n"
"v_mfma_f32_16x16x32_bf16 v[192:195], a[248:251], a[0:3], v[192:195]\n"
"ds_read_b64_tr_b16 a[240:241], v28 offset:256\n"
"v_mfma_f32_16x16x32_bf16 v[192:195], a[252:255], a[16:19], v[192:195]\n"
"ds_read_b64_tr_b16 a[242:243], v28 offset:1792\n"
"v_mfma_f32_16x16x32_bf16 v[196:199], a[248:251], a[4:7], v[196:199]\n"
"s_mov_b32 s44, 24576\n"
"ds_read_b64_tr_b16 a[244:245], v28 offset:12544\n"
"v_mfma_f32_16x16x32_bf16 v[196:199], a[252:255], a[20:23], v[196:199]\n"
"ds_read_b64_tr_b16 a[246:247], v28 offset:14080\n"
"v_mfma_f32_16x16x32_bf16 v[200:203], a[248:251], a[8:11], v[200:203]\n"
"s_add_u32 m0, s58, 4096\n"
"v_add_u32 v28, 0xfffe8000, v28\n"
"v_mfma_f32_16x16x32_bf16 v[200:203], a[252:255], a[24:27], v[200:203]\n"
"buffer_load_dwordx4 v33, s[16:19], s51 offen offset:1024 lds\n"
"v_mfma_f32_16x16x32_bf16 v[204:207], a[248:251], a[12:15], v[204:207]\n"
"s_add_u32 m0, s38, 77824\n"
"v_mfma_f32_16x16x32_bf16 v[204:207], a[252:255], a[28:31], v[204:207]\n"
"s_waitcnt lgkmcnt(8)\n"
"buffer_load_dwordx4 v38, s[20:23], s50 offen offset:2048 lds\n"
"v_mfma_f32_16x16x32_bf16 v[112:115], a[216:219], a[0:3], v[112:115]\n"
"ds_read_b64_tr_b16 a[248:249], v29 offset:256\n"
"v_mfma_f32_16x16x32_bf16 v[112:115], a[220:223], a[16:19], v[112:115]\n"
"ds_read_b64_tr_b16 a[250:251], v29 offset:1792\n"
"v_mfma_f32_16x16x32_bf16 v[116:119], a[216:219], a[4:7], v[116:119]\n"
"s_mov_b32 s45, 24576\n"
"ds_read_b64_tr_b16 a[252:253], v29 offset:12544\n"
"v_mfma_f32_16x16x32_bf16 v[116:119], a[220:223], a[20:23], v[116:119]\n"
"ds_read_b64_tr_b16 a[254:255], v29 offset:14080\n"
"v_mfma_f32_16x16x32_bf16 v[120:123], a[216:219], a[8:11], v[120:123]\n"
"s_add_u32 m0, s58, 6144\n"
"s_add_u32 s51, s71, 896\n"
"v_add_u32 v29, 0xfffe8000, v29\n"
"v_mfma_f32_16x16x32_bf16 v[120:123], a[220:223], a[24:27], v[120:123]\n"
"buffer_load_dwordx4 v32, s[16:19], s51 offen offset:0 lds\n"
"v_mfma_f32_16x16x32_bf16 v[124:127], a[216:219], a[12:15], v[124:127]\n"
"v_mfma_f32_16x16x32_bf16 v[124:127], a[220:223], a[28:31], v[124:127]\n"
"s_waitcnt lgkmcnt(8)\n"
"v_mfma_f32_16x16x32_bf16 v[208:211], a[224:227], a[0:3], v[208:211]\n"
"ds_read_b64_tr_b16 a[216:217], v30 offset:256\n"
"v_mfma_f32_16x16x32_bf16 v[208:211], a[228:231], a[16:19], v[208:211]\n"
"ds_read_b64_tr_b16 a[218:219], v30 offset:1792\n"
"v_mfma_f32_16x16x32_bf16 v[212:215], a[224:227], a[4:7], v[212:215]\n"
"s_mov_b32 s46, -49152\n"
"ds_read_b64_tr_b16 a[220:221], v30 offset:12544\n"
"v_mfma_f32_16x16x32_bf16 v[212:215], a[228:231], a[20:23], v[212:215]\n"
"ds_read_b64_tr_b16 a[222:223], v30 offset:14080\n"
"v_mfma_f32_16x16x32_bf16 v[216:219], a[224:227], a[8:11], v[216:219]\n"
"s_add_u32 m0, s58, 6144\n"
"v_add_u32 v30, 0xfffe8000, v30\n"
"v_mfma_f32_16x16x32_bf16 v[216:219], a[228:231], a[24:27], v[216:219]\n"
"buffer_load_dwordx4 v33, s[16:19], s51 offen offset:1024 lds\n"
"v_mfma_f32_16x16x32_bf16 v[220:223], a[224:227], a[12:15], v[220:223]\n"
"v_mfma_f32_16x16x32_bf16 v[220:223], a[228:231], a[28:31], v[220:223]\n"
"s_waitcnt lgkmcnt(8)\n"
"v_mfma_f32_16x16x32_bf16 v[128:131], a[240:243], a[0:3], v[128:131]\n"
"ds_read_b64_tr_b16 a[224:225], v31 offset:256\n"
"v_mfma_f32_16x16x32_bf16 v[128:131], a[244:247], a[16:19], v[128:131]\n"
"ds_read_b64_tr_b16 a[226:227], v31 offset:1792\n"
"v_lshlrev_b32 v26, 3, v12\n"
"v_mfma_f32_16x16x32_bf16 v[132:135], a[240:243], a[4:7], v[132:135]\n"
"s_movk_i32 s98, 0xb0\n"
"ds_read_b64_tr_b16 a[228:229], v31 offset:12544\n"
"v_mfma_f32_16x16x32_bf16 v[132:135], a[244:247], a[20:23], v[132:135]\n"
"ds_read_b64_tr_b16 a[230:231], v31 offset:14080\n"
"v_and_b32 v26, 0x780, v26\n"
"v_mfma_f32_16x16x32_bf16 v[136:139], a[240:243], a[8:11], v[136:139]\n"
"v_add_u32 v31, 0xfffe8000, v31\n"
"v_mfma_f32_16x16x32_bf16 v[136:139], a[244:247], a[24:27], v[136:139]\n"
"global_load_dword v27, v26, s[60:61]\n"
"v_mfma_f32_16x16x32_bf16 v[140:143], a[240:243], a[12:15], v[140:143]\n"
"v_mfma_f32_16x16x32_bf16 v[140:143], a[244:247], a[28:31], v[140:143]\n"
"s_waitcnt lgkmcnt(8)\n"
"v_mfma_f32_16x16x32_bf16 v[224:227], a[248:251], a[0:3], v[224:227]\n"
"v_mfma_f32_16x16x32_bf16 v[224:227], a[252:255], a[16:19], v[224:227]\n"
"s_waitcnt vmcnt(20)\n"
"v_mfma_f32_16x16x32_bf16 v[228:231], a[248:251], a[4:7], v[228:231]\n"
"s_waitcnt lgkmcnt(0)\n"
"s_min_u32 s79, s99, 32\n"
"v_mfma_f32_16x16x32_bf16 v[228:231], a[252:255], a[20:23], v[228:231]\n"
"s_barrier\n"
"v_mfma_f32_16x16x32_bf16 v[232:235], a[248:251], a[8:11], v[232:235]\n"
"ds_read_b64_tr_b16 a[240:241], v28 offset:0\n"
"v_mfma_f32_16x16x32_bf16 v[232:235], a[252:255], a[24:27], v[232:235]\n"
"ds_read_b64_tr_b16 a[242:243], v28 offset:1536\n"
"v_mfma_f32_16x16x32_bf16 v[236:239], a[248:251], a[12:15], v[236:239]\n"
"ds_read_b64_tr_b16 a[244:245], v28 offset:12288\n"
"v_mfma_f32_16x16x32_bf16 v[236:239], a[252:255], a[28:31], v[236:239]\n"
"s_waitcnt lgkmcnt(3)\n"
"ds_read_b64_tr_b16 a[246:247], v28 offset:13824\n"
"v_mfma_f32_16x16x32_bf16 v[144:147], a[216:219], a[0:3], v[144:147]\n"
"ds_read_b64_tr_b16 a[248:249], v29 offset:0\n"
"v_mfma_f32_16x16x32_bf16 v[144:147], a[220:223], a[16:19], v[144:147]\n"
"ds_read_b64_tr_b16 a[250:251], v29 offset:1536\n"
"v_mfma_f32_16x16x32_bf16 v[148:151], a[216:219], a[4:7], v[148:151]\n"
"s_movk_i32 s32, -300\n"
"ds_read_b64_tr_b16 a[252:253], v29 offset:12288\n"
"v_mfma_f32_16x16x32_bf16 v[148:151], a[220:223], a[20:23], v[148:151]\n"
"s_waitcnt vmcnt(17)\n"
"ds_read_b64_tr_b16 a[254:255], v29 offset:13824\n"
"v_mfma_f32_16x16x32_bf16 v[152:155], a[216:219], a[8:11], v[152:155]\n"
"ds_read_b128 a[192:195], v34 offset:0\n"
"v_mfma_f32_16x16x32_bf16 v[152:155], a[220:223], a[24:27], v[152:155]\n"
"ds_read_b128 a[196:199], v34 offset:2048\n"
"v_mfma_f32_16x16x32_bf16 v[156:159], a[216:219], a[12:15], v[156:159]\n"
"ds_read_b128 a[200:203], v34 offset:4096\n"
"v_mfma_f32_16x16x32_bf16 v[156:159], a[220:223], a[28:31], v[156:159]\n"
"s_waitcnt lgkmcnt(8)\n"
"ds_read_b128 a[204:207], v34 offset:6144\n"
"v_add_u32 v34, v34, s59\n"
"v_mfma_f32_16x16x32_bf16 v[240:243], a[224:227], a[0:3], v[240:243]\n"
"ds_read_b64_tr_b16 a[216:217], v30 offset:0\n"
"v_mfma_f32_16x16x32_bf16 v[240:243], a[228:231], a[16:19], v[240:243]\n"
"ds_read_b64_tr_b16 a[218:219], v30 offset:1536\n"
"v_mfma_f32_16x16x32_bf16 v[244:247], a[224:227], a[4:7], v[244:247]\n"
"ds_read_b64_tr_b16 a[220:221], v30 offset:12288\n"
"v_mfma_f32_16x16x32_bf16 v[244:247], a[228:231], a[20:23], v[244:247]\n"
"s_waitcnt lgkmcnt(8)\n"
"ds_read_b64_tr_b16 a[222:223], v30 offset:13824\n"
"v_mfma_f32_16x16x32_bf16 v[248:251], a[224:227], a[8:11], v[248:251]\n"
"ds_read_b128 a[208:211], v35 offset:0\n"
"v_mfma_f32_16x16x32_bf16 v[248:251], a[228:231], a[24:27], v[248:251]\n"
"ds_read_b128 a[212:215], v35 offset:2048\n"
"v_mfma_f32_16x16x32_bf16 v[252:255], a[224:227], a[12:15], v[252:255]\n"
"ds_read_b128 a[232:235], v35 offset:4096\n"
"v_mfma_f32_16x16x32_bf16 v[252:255], a[228:231], a[28:31], v[252:255]\n"
"s_waitcnt lgkmcnt(8)\n"
"ds_read_b128 a[236:239], v35 offset:6144\n"
"v_add_u32 v35, v35, s59\n"
"L_q4:\n"
"v_mfma_f32_16x16x32_bf16 v[64:67], a[240:243], a[32:35], v[64:67]\n"
"ds_read_b64_tr_b16 a[224:225], v31 offset:0\n"
"v_mfma_f32_16x16x32_bf16 v[64:67], a[244:247], a[48:51], v[64:67]\n"
"ds_read_b64_tr_b16 a[226:227], v31 offset:1536\n"
"v_mfma_f32_16x16x32_bf16 v[68:71], a[240:243], a[36:39], v[68:71]\n"
"ds_read_b64_tr_b16 a[228:229], v31 offset:12288\n"
"v_mfma_f32_16x16x32_bf16 v[68:71], a[244:247], a[52:55], v[68:71]\n"
"s_waitcnt lgkmcnt(3)\n"
"ds_read_b64_tr_b16 a[230:231], v31 offset:13824\n"
"v_mfma_f32_16x16x32_bf16 v[72:75], a[240:243], a[40:43], v[72:75]\n"
"s_add_u32 m0, s39, 0\n"
"s_add_u32 s51, s68, 1024\n"
"v_mfma_f32_16x16x32_bf16 v[72:75], a[244:247], a[56:59], v[72:75]\n"
"buffer_load_dwordx4 v32, s[16:19], s51 offen offset:0 lds\n"
"v_mfma_f32_16x16x32_bf16 v[76:79], a[240:243], a[44:47], v[76:79]\n"
"s_add_u32 m0, s38, 139264\n"
"s_mov_b32 s50, 5505024\n"
"v_mfma_f32_16x16x32_bf16 v[76:79], a[244:247], a[60:63], v[76:79]\n"
"s_waitcnt lgkmcnt(4)\n"
"buffer_load_dwordx4 v36, s[20:23], s50 offen offset:0 lds\n"
"v_mfma_f32_16x16x32_bf16 v[160:163], a[248:251], a[32:35], v[160:163]\n"
"ds_read_b64_tr_b16 a[240:241], v28 offset:128\n"
"v_mfma_f32_16x16x32_bf16 v[160:163], a[252:255], a[48:51], v[160:163]\n"
"ds_read_b64_tr_b16 a[242:243], v28 offset:1664\n"
"v_mfma_f32_16x16x32_bf16 v[164:167], a[248:251], a[36:39], v[164:167]\n"
"ds_read_b64_tr_b16 a[244:245], v28 offset:12416\n"
"v_mfma_f32_16x16x32_bf16 v[164:167], a[252:255], a[52:55], v[164:167]\n"
"ds_read_b64_tr_b16 a[246:247], v28 offset:13952\n"
"v_mfma_f32_16x16x32_bf16 v[168:171], a[248:251], a[40:43], v[168:171]\n"
"s_add_u32 m0, s39, 0\n"
"v_mfma_f32_16x16x32_bf16 v[168:171], a[252:255], a[56:59], v[168:171]\n"
"buffer_load_dwordx4 v33, s[16:19], s51 offen offset:1024 lds\n"
"v_mfma_f32_16x16x32_bf16 v[172:175], a[248:251], a[44:47], v[172:175]\n"
"s_add_u32 m0, s38, 139264\n"
"v_mfma_f32_16x16x32_bf16 v[172:175], a[252:255], a[60:63], v[172:175]\n"
"s_waitcnt lgkmcnt(8)\n"
"buffer_load_dwordx4 v37, s[20:23], s50 offen offset:1024 lds\n"
"v_mfma_f32_16x16x32_bf16 v[80:83], a[216:219], a[32:35], v[80:83]\n"
"ds_read_b64_tr_b16 a[248:249], v29 offset:128\n"
"v_mfma_f32_16x16x32_bf16 v[80:83], a[220:223], a[48:51], v[80:83]\n"
"ds_read_b64_tr_b16 a[250:251], v29 offset:1664\n"
"v_mfma_f32_16x16x32_bf16 v[84:87], a[216:219], a[36:39], v[84:87]\n"
"ds_read_b64_tr_b16 a[252:253], v29 offset:12416\n"
"v_mfma_f32_16x16x32_bf16 v[84:87], a[220:223], a[52:55], v[84:87]\n"
"ds_read_b64_tr_b16 a[254:255], v29 offset:13952\n"
"v_mfma_f32_16x16x32_bf16 v[88:91], a[216:219], a[40:43], v[88:91]\n"
"s_add_u32 m0, s39, 2048\n"
"s_add_u32 s51, s69, 1024\n"
"v_mfma_f32_16x16x32_bf16 v[88:91], a[220:223], a[56:59], v[88:91]\n"
"buffer_load_dwordx4 v32, s[16:19], s51 offen offset:0 lds\n"
"v_mfma_f32_16x16x32_bf16 v[92:95], a[216:219], a[44:47], v[92:95]\n"
"s_add_u32 m0, s38, 139264\n"
"v_mfma_f32_16x16x32_bf16 v[92:95], a[220:223], a[60:63], v[92:95]\n"
"s_waitcnt lgkmcnt(8)\n"
"buffer_load_dwordx4 v38, s[20:23], s50 offen offset:2048 lds\n"
"v_mfma_f32_16x16x32_bf16 v[176:179], a[224:227], a[32:35], v[176:179]\n"
"ds_read_b64_tr_b16 a[216:217], v30 offset:128\n"
"v_mfma_f32_16x16x32_bf16 v[176:179], a[228:231], a[48:51], v[176:179]\n"
"ds_read_b64_tr_b16 a[218:219], v30 offset:1664\n"
"v_mfma_f32_16x16x32_bf16 v[180:183], a[224:227], a[36:39], v[180:183]\n"
"ds_read_b64_tr_b16 a[220:221], v30 offset:12416\n"
"v_mfma_f32_16x16x32_bf16 v[180:183], a[228:231], a[52:55], v[180:183]\n"
"ds_read_b64_tr_b16 a[222:223], v30 offset:13952\n"
"v_mfma_f32_16x16x32_bf16 v[184:187], a[224:227], a[40:43], v[184:187]\n"
"s_add_u32 m0, s39, 2048\n"
"v_mfma_f32_16x16x32_bf16 v[184:187], a[228:231], a[56:59], v[184:187]\n"
"buffer_load_dwordx4 v33, s[16:19], s51 offen offset:1024 lds\n"
"v_mfma_f32_16x16x32_bf16 v[188:191], a[224:227], a[44:47], v[188:191]\n"
"s_add_u32 m0, s38, 151552\n"
"s_mov_b32 s50, 5898240\n"
"v_mfma_f32_16x16x32_bf16 v[188:191], a[228:231], a[60:63], v[188:191]\n"
"s_waitcnt lgkmcnt(8)\n"
"buffer_load_dwordx4 v36, s[20:23], s50 offen offset:0 lds\n"
"v_mfma_f32_16x16x32_bf16 v[96:99], a[240:243], a[32:35], v[96:99]\n"
"ds_read_b64_tr_b16 a[224:225], v31 offset:128\n"
"v_mfma_f32_16x16x32_bf16 v[96:99], a[244:247], a[48:51], v[96:99]\n"
"ds_read_b64_tr_b16 a[226:227], v31 offset:1664\n"
"v_mfma_f32_16x16x32_bf16 v[100:103], a[240:243], a[36:39], v[100:103]\n"
"ds_read_b64_tr_b16 a[228:229], v31 offset:12416\n"
"v_mfma_f32_16x16x32_bf16 v[100:103], a[244:247], a[52:55], v[100:103]\n"
"ds_read_b64_tr_b16 a[230:231], v31 offset:13952\n"
"v_mfma_f32_16x16x32_bf16 v[104:107], a[240:243], a[40:43], v[104:107]\n"
"s_add_u32 m0, s39, 4096\n"
"s_add_u32 s51, s70, 1024\n"
"v_mfma_f32_16x16x32_bf16 v[104:107], a[244:247], a[56:59], v[104:107]\n"
"buffer_load_dwordx4 v32, s[16:19], s51 offen offset:0 lds\n"
"v_mfma_f32_16x16x32_bf16 v[108:111], a[240:243], a[44:47], v[108:111]\n"
"s_add_u32 m0, s38, 151552\n"
"v_mfma_f32_16x16x32_bf16 v[108:111], a[244:247], a[60:63], v[108:111]\n"
"s_waitcnt lgkmcnt(8)\n"
"buffer_load_dwordx4 v37, s[20:23], s50 offen offset:1024 lds\n"
"v_mfma_f32_16x16x32_bf16 v[192:195], a[248:251], a[32:35], v[192:195]\n"
"ds_read_b64_tr_b16 a[240:241], v28 offset:256\n"
"v_mfma_f32_16x16x32_bf16 v[192:195], a[252:255], a[48:51], v[192:195]\n"
"ds_read_b64_tr_b16 a[242:243], v28 offset:1792\n"
"v_mfma_f32_16x16x32_bf16 v[196:199], a[248:251], a[36:39], v[196:199]\n"
"ds_read_b64_tr_b16 a[244:245], v28 offset:12544\n"
"v_mfma_f32_16x16x32_bf16 v[196:199], a[252:255], a[52:55], v[196:199]\n"
"ds_read_b64_tr_b16 a[246:247], v28 offset:14080\n"
"v_mfma_f32_16x16x32_bf16 v[200:203], a[248:251], a[40:43], v[200:203]\n"
"s_add_u32 m0, s39, 4096\n"
"v_add_u32 v28, 0x6000, v28\n"
"v_mfma_f32_16x16x32_bf16 v[200:203], a[252:255], a[56:59], v[200:203]\n"
"buffer_load_dwordx4 v33, s[16:19], s51 offen offset:1024 lds\n"
"v_mfma_f32_16x16x32_bf16 v[204:207], a[248:251], a[44:47], v[204:207]\n"
"s_add_u32 m0, s38, 151552\n"
"v_mfma_f32_16x16x32_bf16 v[204:207], a[252:255], a[60:63], v[204:207]\n"
"s_waitcnt lgkmcnt(8)\n"
"buffer_load_dwordx4 v38, s[20:23], s50 offen offset:2048 lds\n"
"v_mfma_f32_16x16x32_bf16 v[112:115], a[216:219], a[32:35], v[112:115]\n"
"ds_read_b64_tr_b16 a[248:249], v29 offset:256\n"
"v_mfma_f32_16x16x32_bf16 v[112:115], a[220:223], a[48:51], v[112:115]\n"
"ds_read_b64_tr_b16 a[250:251], v29 offset:1792\n"
"v_mfma_f32_16x16x32_bf16 v[116:119], a[216:219], a[36:39], v[116:119]\n"
"ds_read_b64_tr_b16 a[252:253], v29 offset:12544\n"
"v_mfma_f32_16x16x32_bf16 v[116:119], a[220:223], a[52:55], v[116:119]\n"
"ds_read_b64_tr_b16 a[254:255], v29 offset:14080\n"
"v_mfma_f32_16x16x32_bf16 v[120:123], a[216:219], a[40:43], v[120:123]\n"
"s_add_u32 m0, s39, 6144\n"
"s_add_u32 s51, s71, 1024\n"
"v_add_u32 v29, 0x6000, v29\n"
"v_mfma_f32_16x16x32_bf16 v[120:123], a[220:223], a[56:59], v[120:123]\n"
"buffer_load_dwordx4 v32, s[16:19], s51 offen offset:0 lds\n"
"v_mfma_f32_16x16x32_bf16 v[124:127], a[216:219], a[44:47], v[124:127]\n"
"L_pg3:\n"
"s_cbranch_execz L_bk0\n"
"v_mfma_f32_16x16x32_bf16 v[124:127], a[220:223], a[60:63], v[124:127]\n"
"s_waitcnt lgkmcnt(8)\n"
"v_mfma_f32_16x16x32_bf16 v[208:211], a[224:227], a[32:35], v[208:211]\n"
"ds_read_b64_tr_b16 a[216:217], v30 offset:256\n"
"v_mfma_f32_16x16x32_bf16 v[208:211], a[228:231], a[48:51], v[208:211]\n"
"ds_read_b64_tr_b16 a[218:219], v30 offset:1792\n"
"v_mfma_f32_16x16x32_bf16 v[212:215], a[224:227], a[36:39], v[212:215]\n"
"ds_read_b64_tr_b16 a[220:221], v30 offset:12544\n"
"v_mfma_f32_16x16x32_bf16 v[212:215], a[228:231], a[52:55], v[212:215]\n"
"ds_read_b64_tr_b16 a[222:223], v30 offset:14080\n"
"v_mfma_f32_16x16x32_bf16 v[216:219], a[224:227], a[40:43], v[216:219]\n"
"s_add_u32 m0, s39, 6144\n"
"v_add_u32 v30, 0x6000, v30\n"
"v_mfma_f32_16x16x32_bf16 v[216:219], a[228:231], a[56:59], v[216:219]\n"
"buffer_load_dwordx4 v33, s[16:19], s51 offen offset:1024 lds\n"
"v_mfma_f32_16x16x32_bf16 v[220:223], a[224:227], a[44:47], v[220:223]\n"
"v_mfma_f32_16x16x32_bf16 v[220:223], a[228:231], a[60:63], v[220:223]\n"
"s_waitcnt lgkmcnt(8)\n"
"v_mfma_f32_16x16x32_bf16 v[128:131], a[240:243], a[32:35], v[128:131]\n"
"ds_read_b64_tr_b16 a[224:225], v31 offset:256\n"
"v_mfma_f32_16x16x32_bf16 v[128:131], a[244:247], a[48:51], v[128:131]\n"
"ds_read_b64_tr_b16 a[226:227], v31 offset:1792\n"
"v_mfma_f32_16x16x32_bf16 v[132:135], a[240:243], a[36:39], v[132:135]\n"
"ds_read_b64_tr_b16 a[228:229], v31 offset:12544\n"
"v_mfma_f32_16x16x32_bf16 v[132:135], a[244:247], a[52:55], v[132:135]\n"
"ds_read_b64_tr_b16 a[230:231], v31 offset:14080\n"
"v_mfma_f32_16x16x32_bf16 v[136:139], a[240:243], a[40:43], v[136:139]\n"
"v_add_u32 v31, 0x6000, v31\n"
"v_mfma_f32_16x16x32_bf16 v[136:139], a[244:247], a[56:59], v[136:139]\n"
"v_mfma_f32_16x16x32_bf16 v[140:143], a[240:243], a[44:47], v[140:143]\n"
"v_mfma_f32_16x16x32_bf16 v[140:143], a[244:247], a[60:63], v[140:143]\n"
"s_waitcnt lgkmcnt(8)\n"
"v_mfma_f32_16x16x32_bf16 v[224:227], a[248:251], a[32:35], v[224:227]\n"
"v_mfma_f32_16x16x32_bf16 v[224:227], a[252:255], a[48:51], v[224:227]\n"
"s_waitcnt vmcnt(17)\n"
"v_mfma_f32_16x16x32_bf16 v[228:231], a[248:251], a[36:39], v[228:231]\n"
"s_waitcnt lgkmcnt(0)\n"
"v_mfma_f32_16x16x32_bf16 v[228:231], a[252:255], a[52:55], v[228:231]\n"
"s_barrier\n"
"v_mfma_f32_16x16x32_bf16 v[232:235], a[248:251], a[40:43], v[232:235]\n"
"ds_read_b64_tr_b16 a[240:241], v28 offset:0\n"
"v_mfma_f32_16x16x32_bf16 v[232:235], a[252:255], a[56:59], v[232:235]\n"
"ds_read_b64_tr_b16 a[242:243], v28 offset:1536\n"
"v_mfma_f32_16x16x32_bf16 v[236:239], a[248:251], a[44:47], v[236:239]\n"
"ds_read_b64_tr_b16 a[244:245], v28 offset:12288\n"
"v_mfma_f32_16x16x32_bf16 v[236:239], a[252:255], a[60:63], v[236:239]\n"
"s_waitcnt lgkmcnt(3)\n"
"ds_read_b64_tr_b16 a[246:247], v28 offset:13824\n"
"v_mfma_f32_16x16x32_bf16 v[144:147], a[216:219], a[32:35], v[144:147]\n"
"ds_read_b64_tr_b16 a[248:249], v29 offset:0\n"
"v_mfma_f32_16x16x32_bf16 v[144:147], a[220:223], a[48:51], v[144:147]\n"
"ds_read_b64_tr_b16 a[250:251], v29 offset:1536\n"
"v_mfma_f32_16x16x32_bf16 v[148:151], a[216:219], a[36:39], v[148:151]\n"
"ds_read_b64_tr_b16 a[252:253], v29 offset:12288\n"
"v_mfma_f32_16x16x32_bf16 v[148:151], a[220:223], a[52:55], v[148:151]\n"
"s_waitcnt vmcnt(15)\n"
"ds_read_b64_tr_b16 a[254:255], v29 offset:13824\n"
"v_mfma_f32_16x16x32_bf16 v[152:155], a[216:219], a[40:43], v[152:155]\n"
"ds_read_b128 a[0:3], v34 offset:0\n"
"v_mfma_f32_16x16x32_bf16 v[152:155], a[220:223], a[56:59], v[152:155]\n"
"ds_read_b128 a[4:7], v34 offset:2048\n"
"v_mfma_f32_16x16x32_bf16 v[156:159], a[216:219], a[44:47], v[156:159]\n"
"ds_read_b128 a[8:11], v34 offset:4096\n"
"v_mfma_f32_16x16x32_bf16 v[156:159], a[220:223], a[60:63], v[156:159]\n"
"s_waitcnt lgkmcnt(8)\n"
"ds_read_b128 a[12:15], v34 offset:6144\n"
"v_sub_u32 v34, v34, s59\n"
"v_mfma_f32_16x16x32_bf16 v[240:243], a[224:227], a[32:35], v[240:243]\n"
"ds_read_b64_tr_b16 a[216:217], v30 offset:0\n"
"v_mfma_f32_16x16x32_bf16 v[240:243], a[228:231], a[48:51], v[240:243]\n"
"ds_read_b64_tr_b16 a[218:219], v30 offset:1536\n"
"v_mfma_f32_16x16x32_bf16 v[244:247], a[224:227], a[36:39], v[244:247]\n"
"ds_read_b64_tr_b16 a[220:221], v30 offset:12288\n"
"v_mfma_f32_16x16x32_bf16 v[244:247], a[228:231], a[52:55], v[244:247]\n"
"s_waitcnt lgkmcnt(8)\n"
"ds_read_b64_tr_b16 a[222:223], v30 offset:13824\n"
"v_mfma_f32_16x16x32_bf16 v[248:251], a[224:227], a[40:43], v[248:251]\n"
"ds_read_b128 a[16:19], v35 offset:0\n"
"v_mfma_f32_16x16x32_bf16 v[248:251], a[228:231], a[56:59], v[248:251]\n"
"ds_read_b128 a[20:23], v35 offset:2048\n"
"v_mfma_f32_16x16x32_bf16 v[252:255], a[224:227], a[44:47], v[252:255]\n"
"ds_read_b128 a[24:27], v35 offset:4096\n"
"v_mfma_f32_16x16x32_bf16 v[252:255], a[228:231], a[60:63], v[252:255]\n"
"s_waitcnt lgkmcnt(8)\n"
"ds_read_b128 a[28:31], v35 offset:6144\n"
"v_sub_u32 v35, v35, s59\n"
"v_mfma_f32_16x16x32_bf16 v[64:67], a[240:243], a[192:195], v[64:67]\n"
"ds_read_b64_tr_b16 a[224:225], v31 offset:0\n"
"v_mfma_f32_16x16x32_bf16 v[64:67], a[244:247], a[208:211], v[64:67]\n"
"ds_read_b64_tr_b16 a[226:227], v31 offset:1536\n"
"v_mfma_f32_16x16x32_bf16 v[68:71], a[240:243], a[196:199], v[68:71]\n"
"ds_read_b64_tr_b16 a[228:229], v31 offset:12288\n"
"v_mfma_f32_16x16x32_bf16 v[68:71], a[244:247], a[212:215], v[68:71]\n"
"s_waitcnt lgkmcnt(3)\n"
"ds_read_b64_tr_b16 a[230:231], v31 offset:13824\n"
"v_mfma_f32_16x16x32_bf16 v[72:75], a[240:243], a[200:203], v[72:75]\n"
"s_add_u32 m0, s58, 0\n"
"s_add_u32 s51, s68, 1152\n"
"v_mfma_f32_16x16x32_bf16 v[72:75], a[244:247], a[232:235], v[72:75]\n"
"buffer_load_dwordx4 v32, s[16:19], s51 offen offset:0 lds\n"
"v_mfma_f32_16x16x32_bf16 v[76:79], a[240:243], a[204:207], v[76:79]\n"
"s_add_u32 m0, s38, 40960\n"
"s_mov_b32 s50, 6291456\n"
"v_mfma_f32_16x16x32_bf16 v[76:79], a[244:247], a[236:239], v[76:79]\n"
"s_waitcnt lgkmcnt(4)\n"
"buffer_load_dwordx4 v36, s[20:23], s50 offen offset:0 lds\n"
"v_mfma_f32_16x16x32_bf16 v[160:163], a[248:251], a[192:195], v[160:163]\n"
"ds_read_b64_tr_b16 a[240:241], v28 offset:128\n"
"v_mfma_f32_16x16x32_bf16 v[160:163], a[252:255], a[208:211], v[160:163]\n"
"ds_read_b64_tr_b16 a[242:243], v28 offset:1664\n"
"v_mfma_f32_16x16x32_bf16 v[164:167], a[248:251], a[196:199], v[164:167]\n"
"ds_read_b64_tr_b16 a[244:245], v28 offset:12416\n"
"v_mfma_f32_16x16x32_bf16 v[164:167], a[252:255], a[212:215], v[164:167]\n"
"ds_read_b64_tr_b16 a[246:247], v28 offset:13952\n"
"v_mfma_f32_16x16x32_bf16 v[168:171], a[248:251], a[200:203], v[168:171]\n"
"s_add_u32 m0, s58, 0\n"
"v_mfma_f32_16x16x32_bf16 v[168:171], a[252:255], a[232:235], v[168:171]\n"
"buffer_load_dwordx4 v33, s[16:19], s51 offen offset:1024 lds\n"
"v_mfma_f32_16x16x32_bf16 v[172:175], a[248:251], a[204:207], v[172:175]\n"
"s_add_u32 m0, s38, 40960\n"
"v_mfma_f32_16x16x32_bf16 v[172:175], a[252:255], a[236:239], v[172:175]\n"
"s_waitcnt lgkmcnt(8)\n"
"buffer_load_dwordx4 v37, s[20:23], s50 offen offset:1024 lds\n"
"v_mfma_f32_16x16x32_bf16 v[80:83], a[216:219], a[192:195], v[80:83]\n"
"ds_read_b64_tr_b16 a[248:249], v29 offset:128\n"
"v_mfma_f32_16x16x32_bf16 v[80:83], a[220:223], a[208:211], v[80:83]\n"
"ds_read_b64_tr_b16 a[250:251], v29 offset:1664\n"
"v_mfma_f32_16x16x32_bf16 v[84:87], a[216:219], a[196:199], v[84:87]\n"
"ds_read_b64_tr_b16 a[252:253], v29 offset:12416\n"
"v_mfma_f32_16x16x32_bf16 v[84:87], a[220:223], a[212:215], v[84:87]\n"
"ds_read_b64_tr_b16 a[254:255], v29 offset:13952\n"
"v_mfma_f32_16x16x32_bf16 v[88:91], a[216:219], a[200:203], v[88:91]\n"
"s_add_u32 m0, s58, 2048\n"
"s_add_u32 s51, s69, 1152\n"
"v_mfma_f32_16x16x32_bf16 v[88:91], a[220:223], a[232:235], v[88:91]\n"
"buffer_load_dwordx4 v32, s[16:19], s51 offen offset:0 lds\n"
"v_mfma_f32_16x16x32_bf16 v[92:95], a[216:219], a[204:207], v[92:95]\n"
"s_add_u32 m0, s38, 40960\n"
"v_mfma_f32_16x16x32_bf16 v[92:95], a[220:223], a[236:239], v[92:95]\n"
"s_waitcnt lgkmcnt(8)\n"
"buffer_load_dwordx4 v38, s[20:23], s50 offen offset:2048 lds\n"
"v_mfma_f32_16x16x32_bf16 v[176:179], a[224:227], a[192:195], v[176:179]\n"
"ds_read_b64_tr_b16 a[216:217], v30 offset:128\n"
"v_mfma_f32_16x16x32_bf16 v[176:179], a[228:231], a[208:211], v[176:179]\n"
"ds_read_b64_tr_b16 a[218:219], v30 offset:1664\n"
"v_mfma_f32_16x16x32_bf16 v[180:183], a[224:227], a[196:199], v[180:183]\n"
"ds_read_b64_tr_b16 a[220:221], v30 offset:12416\n"
"v_mfma_f32_16x16x32_bf16 v[180:183], a[228:231], a[212:215], v[180:183]\n"
"ds_read_b64_tr_b16 a[222:223], v30 offset:13952\n"
"v_mfma_f32_16x16x32_bf16 v[184:187], a[224:227], a[200:203], v[184:187]\n"
"s_add_u32 m0, s58, 2048\n"
"v_mfma_f32_16x16x32_bf16 v[184:187], a[228:231], a[232:235], v[184:187]\n"
"buffer_load_dwordx4 v33, s[16:19], s51 offen offset:1024 lds\n"
"v_mfma_f32_16x16x32_bf16 v[188:191], a[224:227], a[204:207], v[188:191]\n"
"s_add_u32 m0, s38, 53248\n"
"s_mov_b32 s50, 6684672\n"
"v_mfma_f32_16x16x32_bf16 v[188:191], a[228:231], a[236:239], v[188:191]\n"
"s_waitcnt lgkmcnt(8)\n"
"buffer_load_dwordx4 v36, s[20:23], s50 offen offset:0 lds\n"
"v_mfma_f32_16x16x32_bf16 v[96:99], a[240:243], a[192:195], v[96:99]\n"
"ds_read_b64_tr_b16 a[224:225], v31 offset:128\n"
"v_mfma_f32_16x16x32_bf16 v[96:99], a[244:247], a[208:211], v[96:99]\n"
"ds_read_b64_tr_b16 a[226:227], v31 offset:1664\n"
"v_mfma_f32_16x16x32_bf16 v[100:103], a[240:243], a[196:199], v[100:103]\n"
"ds_read_b64_tr_b16 a[228:229], v31 offset:12416\n"
"v_mfma_f32_16x16x32_bf16 v[100:103], a[244:247], a[212:215], v[100:103]\n"
"ds_read_b64_tr_b16 a[230:231], v31 offset:13952\n"
"v_mfma_f32_16x16x32_bf16 v[104:107], a[240:243], a[200:203], v[104:107]\n"
"s_add_u32 m0, s58, 4096\n"
"s_add_u32 s51, s70, 1152\n"
"v_mfma_f32_16x16x32_bf16 v[104:107], a[244:247], a[232:235], v[104:107]\n"
"buffer_load_dwordx4 v32, s[16:19], s51 offen offset:0 lds\n"
"v_mfma_f32_16x16x32_bf16 v[108:111], a[240:243], a[204:207], v[108:111]\n"
"s_add_u32 m0, s38, 53248\n"
"v_mfma_f32_16x16x32_bf16 v[108:111], a[244:247], a[236:239], v[108:111]\n"
"s_waitcnt lgkmcnt(8)\n"
"buffer_load_dwordx4 v37, s[20:23], s50 offen offset:1024 lds\n"
"v_mfma_f32_16x16x32_bf16 v[192:195], a[248:251], a[192:195], v[192:195]\n"
"ds_read_b64_tr_b16 a[240:241], v28 offset:256\n"
"v_mfma_f32_16x16x32_bf16 v[192:195], a[252:255], a[208:211], v[192:195]\n"
"ds_read_b64_tr_b16 a[242:243], v28 offset:1792\n"
"v_mfma_f32_16x16x32_bf16 v[196:199], a[248:251], a[196:199], v[196:199]\n"
"ds_read_b64_tr_b16 a[244:245], v28 offset:12544\n"
"v_mfma_f32_16x16x32_bf16 v[196:199], a[252:255], a[212:215], v[196:199]\n"
"ds_read_b64_tr_b16 a[246:247], v28 offset:14080\n"
"v_mfma_f32_16x16x32_bf16 v[200:203], a[248:251], a[200:203], v[200:203]\n"
"s_add_u32 m0, s58, 4096\n"
"v_add_u32 v28, 0x12000, v28\n"
"v_mfma_f32_16x16x32_bf16 v[200:203], a[252:255], a[232:235], v[200:203]\n"
"buffer_load_dwordx4 v33, s[16:19], s51 offen offset:1024 lds\n"
"v_mfma_f32_16x16x32_bf16 v[204:207], a[248:251], a[204:207], v[204:207]\n"
"s_add_u32 m0, s38, 53248\n"
"v_mfma_f32_16x16x32_bf16 v[204:207], a[252:255], a[236:239], v[204:207]\n"
"s_waitcnt lgkmcnt(8)\n"
"buffer_load_dwordx4 v38, s[20:23], s50 offen offset:2048 lds\n"
"v_mfma_f32_16x16x32_bf16 v[112:115], a[216:219], a[192:195], v[112:115]\n"
"ds_read_b64_tr_b16 a[248:249], v29 offset:256\n"
"v_mfma_f32_16x16x32_bf16 v[112:115], a[220:223], a[208:211], v[112:115]\n"
"ds_read_b64_tr_b16 a[250:251], v29 offset:1792\n"
"v_mfma_f32_16x16x32_bf16 v[116:119], a[216:219], a[196:199], v[116:119]\n"
"ds_read_b64_tr_b16 a[252:253], v29 offset:12544\n"
"v_mfma_f32_16x16x32_bf16 v[116:119], a[220:223], a[212:215], v[116:119]\n"
"ds_read_b64_tr_b16 a[254:255], v29 offset:14080\n"
"s_mov_b64 exec, 0\n"
"s_cmp_eq_u32 s38, 0\n"
"s_cbranch_scc1 L_pg4\n"
"s_cmp_eq_u32 s38, 3072\n"
"s_cbranch_scc1 L_pg5\n"
"s_cmp_eq_u32 s38, 6144\n"
"s_cbranch_scc1 L_pg6\n"
"s_branch L_pg7\n"
"L_bk1:\n"
"s_mov_b64 exec, -1\n"
"v_mfma_f32_16x16x32_bf16 v[120:123], a[216:219], a[200:203], v[120:123]\n"
"s_add_u32 m0, s58, 6144\n"
"s_add_u32 s51, s71, 1152\n"
"v_add_u32 v29, 0x12000, v29\n"
"v_mfma_f32_16x16x32_bf16 v[120:123], a[220:223], a[232:235], v[120:123]\n"
"buffer_load_dwordx4 v32, s[16:19], s51 offen offset:0 lds\n"
"v_mfma_f32_16x16x32_bf16 v[124:127], a[216:219], a[204:207], v[124:127]\n"
"v_mfma_f32_16x16x32_bf16 v[124:127], a[220:223], a[236:239], v[124:127]\n"
"s_waitcnt lgkmcnt(8)\n"
"v_mfma_f32_16x16x32_bf16 v[208:211], a[224:227], a[192:195], v[208:211]\n"
"ds_read_b64_tr_b16 a[216:217], v30 offset:256\n"
"v_mfma_f32_16x16x32_bf16 v[208:211], a[228:231], a[208:211], v[208:211]\n"
"ds_read_b64_tr_b16 a[218:219], v30 offset:1792\n"
"v_mfma_f32_16x16x32_bf16 v[212:215], a[224:227], a[196:199], v[212:215]\n"
"ds_read_b64_tr_b16 a[220:221], v30 offset:12544\n"
"v_mfma_f32_16x16x32_bf16 v[212:215], a[228:231], a[212:215], v[212:215]\n"
"ds_read_b64_tr_b16 a[222:223], v30 offset:14080\n"
"v_mfma_f32_16x16x32_bf16 v[216:219], a[224:227], a[200:203], v[216:219]\n"
"s_add_u32 m0, s58, 6144\n"
"v_add_u32 v30, 0x12000, v30\n"
"v_mfma_f32_16x16x32_bf16 v[216:219], a[228:231], a[232:235], v[216:219]\n"
"buffer_load_dwordx4 v33, s[16:19], s51 offen offset:1024 lds\n"
"v_mfma_f32_16x16x32_bf16 v[220:223], a[224:227], a[204:207], v[220:223]\n"
"s_add_u32 m0, s38, 16384\n"
"v_mfma_f32_16x16x32_bf16 v[220:223], a[228:231], a[236:239], v[220:223]\n"
"s_waitcnt lgkmcnt(8)\n"
"buffer_load_dwordx4 v9, s[12:15], s43 offen offset:0 lds\n"
"v_mfma_f32_16x16x32_bf16 v[128:131], a[240:243], a[192:195], v[128:131]\n"
"ds_read_b64_tr_b16 a[224:225], v31 offset:256\n"
"v_mfma_f32_16x16x32_bf16 v[128:131], a[244:247], a[208:211], v[128:131]\n"
"ds_read_b64_tr_b16 a[226:227], v31 offset:1792\n"
"v_mfma_f32_16x16x32_bf16 v[132:135], a[240:243], a[196:199], v[132:135]\n"
"ds_read_b64_tr_b16 a[228:229], v31 offset:12544\n"
"v_mfma_f32_16x16x32_bf16 v[132:135], a[244:247], a[212:215], v[132:135]\n"
"ds_read_b64_tr_b16 a[230:231], v31 offset:14080\n"
"v_mfma_f32_16x16x32_bf16 v[136:139], a[240:243], a[200:203], v[136:139]\n"
"s_add_u32 m0, s38, 16384\n"
"v_add_u32 v31, 0x12000, v31\n"
"v_mfma_f32_16x16x32_bf16 v[136:139], a[244:247], a[232:235], v[136:139]\n"
"buffer_load_dwordx4 v10, s[12:15], s43 offen offset:1024 lds\n"
"v_mfma_f32_16x16x32_bf16 v[140:143], a[240:243], a[204:207], v[140:143]\n"
"s_add_u32 m0, s38, 16384\n"
"v_mfma_f32_16x16x32_bf16 v[140:143], a[244:247], a[236:239], v[140:143]\n"
"s_waitcnt lgkmcnt(8)\n"
"buffer_load_dwordx4 v11, s[12:15], s43 offen offset:2048 lds\n"
"v_mfma_f32_16x16x32_bf16 v[224:227], a[248:251], a[192:195], v[224:227]\n"
"s_add_u32 s43, s43, 27648\n"
"s_or_b32 s43, s43, 0x18000\n"
"s_sub_u32 s62, s43, 0x60000\n"
"s_min_u32 s43, s43, s62\n"
"v_mfma_f32_16x16x32_bf16 v[224:227], a[252:255], a[208:211], v[224:227]\n"
"s_waitcnt vmcnt(19)\n"
"s_waitcnt vmcnt(35)\n"
"v_mfma_f32_16x16x32_bf16 v[228:231], a[248:251], a[196:199], v[228:231]\n"
"s_waitcnt lgkmcnt(0)\n"
"v_mfma_f32_16x16x32_bf16 v[228:231], a[252:255], a[212:215], v[228:231]\n"
"s_barrier\n"
"v_mfma_f32_16x16x32_bf16 v[232:235], a[248:251], a[200:203], v[232:235]\n"
"ds_read_b64_tr_b16 a[240:241], v28 offset:0\n"
"v_mfma_f32_16x16x32_bf16 v[232:235], a[252:255], a[232:235], v[232:235]\n"
"s_add_u32 s47, s38, s49\n"
"s_cmp_lg_u32 s47, 1\n"
"s_cbranch_scc1 L_pubq\n"
"v_mov_b32 v46, s76\n"
"v_mov_b32 v47, s77\n"
"s_mov_b64 exec, 1\n"
"buffer_store_dwordx2 v[46:47], off, s[24:27], s89 sc1\n"
"s_mov_b64 exec, -1\n"
"s_nop 1\n"
"L_pubq:\n"
"ds_read_b64_tr_b16 a[242:243], v28 offset:1536\n"
"v_mfma_f32_16x16x32_bf16 v[236:239], a[248:251], a[204:207], v[236:239]\n"
"ds_read_b64_tr_b16 a[244:245], v28 offset:12288\n"
"v_mfma_f32_16x16x32_bf16 v[236:239], a[252:255], a[236:239], v[236:239]\n"
"s_waitcnt lgkmcnt(3)\n"
"ds_read_b64_tr_b16 a[246:247], v28 offset:13824\n"
"v_mfma_f32_16x16x32_bf16 v[144:147], a[216:219], a[192:195], v[144:147]\n"
"ds_read_b64_tr_b16 a[248:249], v29 offset:0\n"
"v_mfma_f32_16x16x32_bf16 v[144:147], a[220:223], a[208:211], v[144:147]\n"
"ds_read_b64_tr_b16 a[250:251], v29 offset:1536\n"
"v_mfma_f32_16x16x32_bf16 v[148:151], a[216:219], a[196:199], v[148:151]\n"
"ds_read_b64_tr_b16 a[252:253], v29 offset:12288\n"
"v_mfma_f32_16x16x32_bf16 v[148:151], a[220:223], a[212:215], v[148:151]\n"
"s_waitcnt vmcnt(17)\n"
"ds_read_b64_tr_b16 a[254:255], v29 offset:13824\n"
"v_mfma_f32_16x16x32_bf16 v[152:155], a[216:219], a[200:203], v[152:155]\n"
"ds_read_b128 a[32:35], v34 offset:0\n"
"v_mfma_f32_16x16x32_bf16 v[152:155], a[220:223], a[232:235], v[152:155]\n"
"ds_read_b128 a[36:39], v34 offset:2048\n"
"v_mfma_f32_16x16x32_bf16 v[156:159], a[216:219], a[204:207], v[156:159]\n"
"ds_read_b128 a[40:43], v34 offset:4096\n"
"v_mfma_f32_16x16x32_bf16 v[156:159], a[220:223], a[236:239], v[156:159]\n"
"s_waitcnt lgkmcnt(8)\n"
"ds_read_b128 a[44:47], v34 offset:6144\n"
"v_add_u32 v34, v34, s59\n"
"v_mfma_f32_16x16x32_bf16 v[240:243], a[224:227], a[192:195], v[240:243]\n"
"ds_read_b64_tr_b16 a[216:217], v30 offset:0\n"
"v_mfma_f32_16x16x32_bf16 v[240:243], a[228:231], a[208:211], v[240:243]\n"
"ds_read_b64_tr_b16 a[218:219], v30 offset:1536\n"
"v_mfma_f32_16x16x32_bf16 v[244:247], a[224:227], a[196:199], v[244:247]\n"
"ds_read_b64_tr_b16 a[220:221], v30 offset:12288\n"
"v_mfma_f32_16x16x32_bf16 v[244:247], a[228:231], a[212:215], v[244:247]\n"
"s_waitcnt lgkmcnt(8)\n"
"ds_read_b64_tr_b16 a[222:223], v30 offset:13824\n"
"v_mfma_f32_16x16x32_bf16 v[248:251], a[224:227], a[200:203], v[248:251]\n"
"ds_read_b128 a[48:51], v35 offset:0\n"
"v_mfma_f32_16x16x32_bf16 v[248:251], a[228:231], a[232:235], v[248:251]\n"
"ds_read_b128 a[52:55], v35 offset:2048\n"
"v_mfma_f32_16x16x32_bf16 v[252:255], a[224:227], a[204:207], v[252:255]\n"
"ds_read_b128 a[56:59], v35 offset:4096\n"
"v_mfma_f32_16x16x32_bf16 v[252:255], a[228:231], a[236:239], v[252:255]\n"
"s_waitcnt lgkmcnt(8)\n"
"ds_read_b128 a[60:63], v35 offset:6144\n"
"v_add_u32 v35, v35, s59\n"
"v_mfma_f32_16x16x32_bf16 v[64:67], a[240:243], a[0:3], v[64:67]\n"
"ds_read_b64_tr_b16 a[224:225], v31 offset:0\n"
"v_mfma_f32_16x16x32_bf16 v[64:67], a[244:247], a[16:19], v[64:67]\n"
"ds_read_b64_tr_b16 a[226:227], v31 offset:1536\n"
"v_mfma_f32_16x16x32_bf16 v[68:71], a[240:243], a[4:7], v[68:71]\n"
"ds_read_b64_tr_b16 a[228:229], v31 offset:12288\n"
"v_mfma_f32_16x16x32_bf16 v[68:71], a[244:247], a[20:23], v[68:71]\n"
"ds_read_b64_tr_b16 a[230:231], v31 offset:13824\n"
"v_mfma_f32_16x16x32_bf16 v[72:75], a[240:243], a[8:11], v[72:75]\n"
"s_add_u32 m0, s38, 65536\n"
"s_mov_b32 s50, 7077888\n"
"v_mfma_f32_16x16x32_bf16 v[72:75], a[244:247], a[24:27], v[72:75]\n"
"buffer_load_dwordx4 v36, s[20:23], s50 offen offset:0 lds\n"
"v_mfma_f32_16x16x32_bf16 v[76:79], a[240:243], a[12:15], v[76:79]\n"
"s_add_u32 m0, s38, 65536\n"
"v_mfma_f32_16x16x32_bf16 v[76:79], a[244:247], a[28:31], v[76:79]\n"
"s_waitcnt lgkmcnt(8)\n"
"buffer_load_dwordx4 v37, s[20:23], s50 offen offset:1024 lds\n"
"v_mfma_f32_16x16x32_bf16 v[160:163], a[248:251], a[0:3], v[160:163]\n"
"ds_read_b64_tr_b16 a[240:241], v28 offset:128\n"
"v_mfma_f32_16x16x32_bf16 v[160:163], a[252:255], a[16:19], v[160:163]\n"
"ds_read_b64_tr_b16 a[242:243], v28 offset:1664\n"
"v_mfma_f32_16x16x32_bf16 v[164:167], a[248:251], a[4:7], v[164:167]\n"
"ds_read_b64_tr_b16 a[244:245], v28 offset:12416\n"
"v_mfma_f32_16x16x32_bf16 v[164:167], a[252:255], a[20:23], v[164:167]\n"
"ds_read_b64_tr_b16 a[246:247], v28 offset:13952\n"
"v_mfma_f32_16x16x32_bf16 v[168:171], a[248:251], a[8:11], v[168:171]\n"
"s_add_u32 m0, s38, 65536\n"
"v_mfma_f32_16x16x32_bf16 v[168:171], a[252:255], a[24:27], v[168:171]\n"
"buffer_load_dwordx4 v38, s[20:23], s50 offen offset:2048 lds\n"
"v_mfma_f32_16x16x32_bf16 v[172:175], a[248:251], a[12:15], v[172:175]\n"
"s_add_u32 m0, s38, 77824\n"
"s_mov_b32 s50, 7471104\n"
"v_mfma_f32_16x16x32_bf16 v[172:175], a[252:255], a[28:31], v[172:175]\n"
"s_waitcnt lgkmcnt(8)\n"
"buffer_load_dwordx4 v36, s[20:23], s50 offen offset:0 lds\n"
"v_mfma_f32_16x16x32_bf16 v[80:83], a[216:219], a[0:3], v[80:83]\n"
"ds_read_b64_tr_b16 a[248:249], v29 offset:128\n"
"v_mfma_f32_16x16x32_bf16 v[80:83], a[220:223], a[16:19], v[80:83]\n"
"ds_read_b64_tr_b16 a[250:251], v29 offset:1664\n"
"v_mfma_f32_16x16x32_bf16 v[84:87], a[216:219], a[4:7], v[84:87]\n"
"ds_read_b64_tr_b16 a[252:253], v29 offset:12416\n"
"v_mfma_f32_16x16x32_bf16 v[84:87], a[220:223], a[20:23], v[84:87]\n"
"ds_read_b64_tr_b16 a[254:255], v29 offset:13952\n"
"v_mfma_f32_16x16x32_bf16 v[88:91], a[216:219], a[8:11], v[88:91]\n"
"s_add_u32 m0, s38, 77824\n"
"v_mfma_f32_16x16x32_bf16 v[88:91], a[220:223], a[24:27], v[88:91]\n"
"buffer_load_dwordx4 v37, s[20:23], s50 offen offset:1024 lds\n"
"v_mfma_f32_16x16x32_bf16 v[92:95], a[216:219], a[12:15], v[92:95]\n"
"s_add_u32 m0, s38, 77824\n"
"v_mfma_f32_16x16x32_bf16 v[92:95], a[220:223], a[28:31], v[92:95]\n"
"s_waitcnt lgkmcnt(8)\n"
"buffer_load_dwordx4 v38, s[20:23], s50 offen offset:2048 lds\n"
"v_mfma_f32_16x16x32_bf16 v[176:179], a[224:227], a[0:3], v[176:179]\n"
"ds_read_b64_tr_b16 a[216:217], v30 offset:128\n"
"v_mfma_f32_16x16x32_bf16 v[176:179], a[228:231], a[16:19], v[176:179]\n"
"ds_read_b64_tr_b16 a[218:219], v30 offset:1664\n"
"v_mfma_f32_16x16x32_bf16 v[180:183], a[224:227], a[4:7], v[180:183]\n"
"ds_read_b64_tr_b16 a[220:221], v30 offset:12416\n"
"v_mfma_f32_16x16x32_bf16 v[180:183], a[228:231], a[20:23], v[180:183]\n"
"ds_read_b64_tr_b16 a[222:223], v30 offset:13952\n"
"v_mfma_f32_16x16x32_bf16 v[184:187], a[224:227], a[8:11], v[184:187]\n"
"v_mfma_f32_16x16x32_bf16 v[184:187], a[228:231], a[24:27], v[184:187]\n"
"v_mfma_f32_16x16x32_bf16 v[188:191], a[224:227], a[12:15], v[188:191]\n"
"v_mfma_f32_16x16x32_bf16 v[188:191], a[228:231], a[28:31], v[188:191]\n"
"s_waitcnt lgkmcnt(8)\n"
"v_mfma_f32_16x16x32_bf16 v[96:99], a[240:243], a[0:3], v[96:99]\n"
"ds_read_b64_tr_b16 a[224:225], v31 offset:128\n"
"v_mfma_f32_16x16x32_bf16 v[96:99], a[244:247], a[16:19], v[96:99]\n"
"ds_read_b64_tr_b16 a[226:227], v31 offset:1664\n"
"v_mfma_f32_16x16x32_bf16 v[100:103], a[240:243], a[4:7], v[100:103]\n"
"ds_read_b64_tr_b16 a[228:229], v31 offset:12416\n"
"v_mfma_f32_16x16x32_bf16 v[100:103], a[244:247], a[20:23], v[100:103]\n"
"ds_read_b64_tr_b16 a[230:231], v31 offset:13952\n"
"v_mfma_f32_16x16x32_bf16 v[104:107], a[240:243], a[8:11], v[104:107]\n"
"v_mfma_f32_16x16x32_bf16 v[104:107], a[244:247], a[24:27], v[104:107]\n"
"v_mfma_f32_16x16x32_bf16 v[108:111], a[240:243], a[12:15], v[108:111]\n"
"v_mfma_f32_16x16x32_bf16 v[108:111], a[244:247], a[28:31], v[108:111]\n"
"s_waitcnt lgkmcnt(8)\n"
"v_mfma_f32_16x16x32_bf16 v[192:195], a[248:251], a[0:3], v[192:195]\n"
"ds_read_b64_tr_b16 a[240:241], v28 offset:256\n"
"v_mfma_f32_16x16x32_bf16 v[192:195], a[252:255], a[16:19], v[192:195]\n"
"ds_read_b64_tr_b16 a[242:243], v28 offset:1792\n"
"v_mfma_f32_16x16x32_bf16 v[196:199], a[248:251], a[4:7], v[196:199]\n"
"ds_read_b64_tr_b16 a[244:245], v28 offset:12544\n"
"v_mfma_f32_16x16x32_bf16 v[196:199], a[252:255], a[20:23], v[196:199]\n"
"ds_read_b64_tr_b16 a[246:247], v28 offset:14080\n"
"v_mfma_f32_16x16x32_bf16 v[200:203], a[248:251], a[8:11], v[200:203]\n"
"v_add_u32 v28, 0xfffe8000, v28\n"
"v_mfma_f32_16x16x32_bf16 v[200:203], a[252:255], a[24:27], v[200:203]\n"
"v_mfma_f32_16x16x32_bf16 v[204:207], a[248:251], a[12:15], v[204:207]\n"
"v_mfma_f32_16x16x32_bf16 v[204:207], a[252:255], a[28:31], v[204:207]\n"
"s_waitcnt lgkmcnt(8)\n"
"v_mfma_f32_16x16x32_bf16 v[112:115], a[216:219], a[0:3], v[112:115]\n"
"ds_read_b64_tr_b16 a[248:249], v29 offset:256\n"
"v_mfma_f32_16x16x32_bf16 v[112:115], a[220:223], a[16:19], v[112:115]\n"
"ds_read_b64_tr_b16 a[250:251], v29 offset:1792\n"
"v_mfma_f32_16x16x32_bf16 v[116:119], a[216:219], a[4:7], v[116:119]\n"
"ds_read_b64_tr_b16 a[252:253], v29 offset:12544\n"
"v_mfma_f32_16x16x32_bf16 v[116:119], a[220:223], a[20:23], v[116:119]\n"
"ds_read_b64_tr_b16 a[254:255], v29 offset:14080\n"
"v_mfma_f32_16x16x32_bf16 v[120:123], a[216:219], a[8:11], v[120:123]\n"
"v_add_u32 v29, 0xfffe8000, v29\n"
"v_mfma_f32_16x16x32_bf16 v[120:123], a[220:223], a[24:27], v[120:123]\n"
"v_mfma_f32_16x16x32_bf16 v[124:127], a[216:219], a[12:15], v[124:127]\n"
"s_add_u32 m0, s38, 28672\n"
"v_mfma_f32_16x16x32_bf16 v[124:127], a[220:223], a[28:31], v[124:127]\n"
"s_waitcnt lgkmcnt(8)\n"
"buffer_load_dwordx4 v9, s[12:15], s43 offen offset:0 lds\n"
"v_mfma_f32_16x16x32_bf16 v[208:211], a[224:227], a[0:3], v[208:211]\n"
"ds_read_b64_tr_b16 a[216:217], v30 offset:256\n"
"v_mfma_f32_16x16x32_bf16 v[208:211], a[228:231], a[16:19], v[208:211]\n"
"ds_read_b64_tr_b16 a[218:219], v30 offset:1792\n"
"v_mfma_f32_16x16x32_bf16 v[212:215], a[224:227], a[4:7], v[212:215]\n"
"ds_read_b64_tr_b16 a[220:221], v30 offset:12544\n"
"v_mfma_f32_16x16x32_bf16 v[212:215], a[228:231], a[20:23], v[212:215]\n"
"ds_read_b64_tr_b16 a[222:223], v30 offset:14080\n"
"v_mfma_f32_16x16x32_bf16 v[216:219], a[224:227], a[8:11], v[216:219]\n"
"v_add_u32 v30, 0xfffe8000, v30\n"
"v_mfma_f32_16x16x32_bf16 v[216:219], a[228:231], a[24:27], v[216:219]\n"
"v_mfma_f32_16x16x32_bf16 v[220:223], a[224:227], a[12:15], v[220:223]\n"
"s_add_u32 m0, s38, 28672\n"
"v_mfma_f32_16x16x32_bf16 v[220:223], a[228:231], a[28:31], v[220:223]\n"
"s_waitcnt lgkmcnt(8)\n"
"buffer_load_dwordx4 v10, s[12:15], s43 offen offset:1024 nt lds\n"
"v_mfma_f32_16x16x32_bf16 v[128:131], a[240:243], a[0:3], v[128:131]\n"
"ds_read_b64_tr_b16 a[224:225], v31 offset:256\n"
"v_mfma_f32_16x16x32_bf16 v[128:131], a[244:247], a[16:19], v[128:131]\n"
"ds_read_b64_tr_b16 a[226:227], v31 offset:1792\n"
"v_mfma_f32_16x16x32_bf16 v[132:135], a[240:243], a[4:7], v[132:135]\n"
"ds_read_b64_tr_b16 a[228:229], v31 offset:12544\n"
"v_mfma_f32_16x16x32_bf16 v[132:135], a[244:247], a[20:23], v[132:135]\n"
"ds_read_b64_tr_b16 a[230:231], v31 offset:14080\n"
"v_mfma_f32_16x16x32_bf16 v[136:139], a[240:243], a[8:11], v[136:139]\n"
"v_add_u32 v31, 0xfffe8000, v31\n"
"v_mfma_f32_16x16x32_bf16 v[136:139], a[244:247], a[24:27], v[136:139]\n"
"v_mfma_f32_16x16x32_bf16 v[140:143], a[240:243], a[12:15], v[140:143]\n"
"s_add_u32 m0, s38, 28672\n"
"v_mfma_f32_16x16x32_bf16 v[140:143], a[244:247], a[28:31], v[140:143]\n"
"s_waitcnt lgkmcnt(8)\n"
"buffer_load_dwordx4 v11, s[12:15], s43 offen offset:2048 nt lds\n"
"v_mfma_f32_16x16x32_bf16 v[224:227], a[248:251], a[0:3], v[224:227]\n"
"s_add_u32 s43, s43, 27648\n"
"s_or_b32 s43, s43, 0x18000\n"
"s_sub_u32 s62, s43, 0x60000\n"
"s_min_u32 s43, s43, s62\n"
"v_mfma_f32_16x16x32_bf16 v[224:227], a[252:255], a[16:19], v[224:227]\n"
"s_waitcnt vmcnt(14)\n"
"v_mfma_f32_16x16x32_bf16 v[228:231], a[248:251], a[4:7], v[228:231]\n"
"s_waitcnt lgkmcnt(0)\n"
"v_mfma_f32_16x16x32_bf16 v[228:231], a[252:255], a[20:23], v[228:231]\n"
"s_barrier\n"
"v_mfma_f32_16x16x32_bf16 v[232:235], a[248:251], a[8:11], v[232:235]\n"
"ds_read_b64_tr_b16 a[240:241], v28 offset:0\n"
"v_mfma_f32_16x16x32_bf16 v[232:235], a[252:255], a[24:27], v[232:235]\n"
"ds_read_b64_tr_b16 a[242:243], v28 offset:1536\n"
"v_mfma_f32_16x16x32_bf16 v[236:239], a[248:251], a[12:15], v[236:239]\n"
"ds_read_b64_tr_b16 a[244:245], v28 offset:12288\n"
"v_mfma_f32_16x16x32_bf16 v[236:239], a[252:255], a[28:31], v[236:239]\n"
"s_waitcnt lgkmcnt(3)\n"
"ds_read_b64_tr_b16 a[246:247], v28 offset:13824\n"
"v_mfma_f32_16x16x32_bf16 v[144:147], a[216:219], a[0:3], v[144:147]\n"
"ds_read_b64_tr_b16 a[248:249], v29 offset:0\n"
"v_mfma_f32_16x16x32_bf16 v[144:147], a[220:223], a[16:19], v[144:147]\n"
"ds_read_b64_tr_b16 a[250:251], v29 offset:1536\n"
"v_mfma_f32_16x16x32_bf16 v[148:151], a[216:219], a[4:7], v[148:151]\n"
"ds_read_b64_tr_b16 a[252:253], v29 offset:12288\n"
"v_mfma_f32_16x16x32_bf16 v[148:151], a[220:223], a[20:23], v[148:151]\n"
"s_waitcnt vmcnt(12)\n"
"ds_read_b64_tr_b16 a[254:255], v29 offset:13824\n"
"v_mfma_f32_16x16x32_bf16 v[152:155], a[216:219], a[8:11], v[152:155]\n"
"ds_read_b128 a[64:67], v34 offset:0\n"
"v_mfma_f32_16x16x32_bf16 v[152:155], a[220:223], a[24:27], v[152:155]\n"
"ds_read_b128 a[68:71], v34 offset:2048\n"
"v_mfma_f32_16x16x32_bf16 v[156:159], a[216:219], a[12:15], v[156:159]\n"
"ds_read_b128 a[72:75], v34 offset:4096\n"
"v_mfma_f32_16x16x32_bf16 v[156:159], a[220:223], a[28:31], v[156:159]\n"
"s_waitcnt lgkmcnt(8)\n"
"ds_read_b128 a[76:79], v34 offset:6144\n"
"v_sub_u32 v34, v34, s59\n"
"v_mfma_f32_16x16x32_bf16 v[240:243], a[224:227], a[0:3], v[240:243]\n"
"ds_read_b64_tr_b16 a[216:217], v30 offset:0\n"
"v_mfma_f32_16x16x32_bf16 v[240:243], a[228:231], a[16:19], v[240:243]\n"
"ds_read_b64_tr_b16 a[218:219], v30 offset:1536\n"
"v_mfma_f32_16x16x32_bf16 v[244:247], a[224:227], a[4:7], v[244:247]\n"
"ds_read_b64_tr_b16 a[220:221], v30 offset:12288\n"
"v_mfma_f32_16x16x32_bf16 v[244:247], a[228:231], a[20:23], v[244:247]\n"
"s_waitcnt lgkmcnt(8)\n"
"ds_read_b64_tr_b16 a[222:223], v30 offset:13824\n"
"v_mfma_f32_16x16x32_bf16 v[248:251], a[224:227], a[8:11], v[248:251]\n"
"ds_read_b128 a[80:83], v35 offset:0\n"
"v_mfma_f32_16x16x32_bf16 v[248:251], a[228:231], a[24:27], v[248:251]\n"
"ds_read_b128 a[84:87], v35 offset:2048\n"
"v_mfma_f32_16x16x32_bf16 v[252:255], a[224:227], a[12:15], v[252:255]\n"
"ds_read_b128 a[88:91], v35 offset:4096\n"
"v_mfma_f32_16x16x32_bf16 v[252:255], a[228:231], a[28:31], v[252:255]\n"
"s_waitcnt lgkmcnt(8)\n"
"ds_read_b128 a[92:95], v35 offset:6144\n"
"v_sub_u32 v35, v35, s59\n"
"v_mfma_f32_16x16x32_bf16 v[64:67], a[240:243], a[32:35], v[64:67]\n"
"ds_read_b64_tr_b16 a[224:225], v31 offset:0\n"
"v_mfma_f32_16x16x32_bf16 v[64:67], a[244:247], a[48:51], v[64:67]\n"
"ds_read_b64_tr_b16 a[226:227], v31 offset:1536\n"
"v_mfma_f32_16x16x32_bf16 v[68:71], a[240:243], a[36:39], v[68:71]\n"
"ds_read_b64_tr_b16 a[228:229], v31 offset:12288\n"
"v_mfma_f32_16x16x32_bf16 v[68:71], a[244:247], a[52:55], v[68:71]\n"
"ds_read_b64_tr_b16 a[230:231], v31 offset:13824\n"
"v_mfma_f32_16x16x32_bf16 v[72:75], a[240:243], a[40:43], v[72:75]\n"
"s_add_u32 m0, s38, 90112\n"
"v_mfma_f32_16x16x32_bf16 v[72:75], a[244:247], a[56:59], v[72:75]\n"
"s_waitcnt vmcnt(3)\n"
"buffer_load_dwordx4 v6, s[8:11], s42 offen offset:0 nt lds\n"
"v_mfma_f32_16x16x32_bf16 v[76:79], a[240:243], a[44:47], v[76:79]\n"
"s_waitcnt lgkmcnt(4)\n"
"v_mfma_f32_16x16x32_bf16 v[76:79], a[244:247], a[60:63], v[76:79]\n"
"s_barrier\n"
"v_mfma_f32_16x16x32_bf16 v[160:163], a[248:251], a[32:35], v[160:163]\n"
"ds_read_b64_tr_b16 a[240:241], v28 offset:24576\n"
"v_mfma_f32_16x16x32_bf16 v[160:163], a[252:255], a[48:51], v[160:163]\n"
"ds_read_b64_tr_b16 a[242:243], v28 offset:26112\n"
"v_mfma_f32_16x16x32_bf16 v[164:167], a[248:251], a[36:39], v[164:167]\n"
"L_pg4:\n"
"s_cbranch_execz L_bk1\n"
"ds_read_b64_tr_b16 a[244:245], v28 offset:36864\n"
"v_mul_u32_u24 v18, 11, v12\n"
"v_mfma_f32_16x16x32_bf16 v[164:167], a[252:255], a[52:55], v[164:167]\n"
"ds_read_b64_tr_b16 a[246:247], v28 offset:38400\n"
"v_mfma_f32_16x16x32_bf16 v[168:171], a[248:251], a[40:43], v[168:171]\n"
"s_add_u32 m0, s38, 90112\n"
"ds_read_b128 v[18:21], v18 offset:16384\n"
"v_mfma_f32_16x16x32_bf16 v[168:171], a[252:255], a[56:59], v[168:171]\n"
"buffer_load_dwordx4 v7, s[8:11], s42 offen offset:1024 nt lds\n"
"v_mfma_f32_16x16x32_bf16 v[172:175], a[248:251], a[44:47], v[172:175]\n"
"s_add_u32 m0, s38, 90112\n"
"v_mfma_f32_16x16x32_bf16 v[172:175], a[252:255], a[60:63], v[172:175]\n"
"s_waitcnt lgkmcnt(8)\n"
"buffer_load_dwordx4 v8, s[8:11], s42 offen offset:2048 nt lds\n"
"v_mfma_f32_16x16x32_bf16 v[80:83], a[216:219], a[32:35], v[80:83]\n"
"s_add_u32 s42, s42, 27648\n"
"s_or_b32 s42, s42, 0x18000\n"
"s_sub_u32 s62, s42, 0x60000\n"
"s_min_u32 s42, s42, s62\n"
"ds_read_b64_tr_b16 a[248:249], v28 offset:128\n"
"v_mfma_f32_16x16x32_bf16 v[80:83], a[220:223], a[48:51], v[80:83]\n"
"ds_read_b64_tr_b16 a[250:251], v28 offset:1664\n"
"v_mfma_f32_16x16x32_bf16 v[84:87], a[216:219], a[36:39], v[84:87]\n"
"ds_read_b64_tr_b16 a[252:253], v28 offset:12416\n"
"v_mfma_f32_16x16x32_bf16 v[84:87], a[220:223], a[52:55], v[84:87]\n"
"ds_read_b64_tr_b16 a[254:255], v28 offset:13952\n"
"v_mfma_f32_16x16x32_bf16 v[88:91], a[216:219], a[40:43], v[88:91]\n"
"s_add_u32 m0, s38, 102400\n"
"s_waitcnt lgkmcnt(4)\n"
"v_max3_f32 v18, |v18|, |v19|, |v20|\n"
"v_max_f32 v18, v18, |v21|\n"
"v_mfma_f32_16x16x32_bf16 v[88:91], a[220:223], a[56:59], v[88:91]\n"
"buffer_load_dwordx4 v6, s[8:11], s42 offen offset:0 nt lds\n"
"v_mfma_f32_16x16x32_bf16 v[92:95], a[216:219], a[44:47], v[92:95]\n"
"s_add_u32 m0, s38, 102400\n"
"v_min_f32_dpp v18, v18, v18 row_shr:1 row_mask:0xf bank_mask:0xf\n"
"v_mfma_f32_16x16x32_bf16 v[92:95], a[220:223], a[60:63], v[92:95]\n"
"s_waitcnt lgkmcnt(4)\n"
"buffer_load_dwordx4 v7, s[8:11], s42 offen offset:1024 nt lds\n"
"v_mfma_f32_16x16x32_bf16 v[176:179], a[224:227], a[32:35], v[176:179]\n"
"ds_read_b64_tr_b16 a[216:217], v29 offset:24576\n"
"v_min_f32_dpp v18, v18, v18 row_shr:2 row_mask:0xf bank_mask:0xf\n"
"v_mfma_f32_16x16x32_bf16 v[176:179], a[228:231], a[48:51], v[176:179]\n"
"ds_read_b64_tr_b16 a[218:219], v29 offset:26112\n"
"v_min_f32_dpp v18, v18, v18 row_shr:4 row_mask:0xf bank_mask:0xf\n"
"v_mfma_f32_16x16x32_bf16 v[180:183], a[224:227], a[36:39], v[180:183]\n"
"ds_read_b64_tr_b16 a[220:221], v29 offset:36864\n"
"v_min_f32_dpp v18, v18, v18 row_shr:8 row_mask:0xf bank_mask:0xf\n"
"v_mfma_f32_16x16x32_bf16 v[180:183], a[228:231], a[52:55], v[180:183]\n"
"ds_read_b64_tr_b16 a[222:223], v29 offset:38400\n"
"v_min_f32_dpp v18, v18, v18 row_bcast:15 row_mask:0xa bank_mask:0xf\n"
"v_mfma_f32_16x16x32_bf16 v[184:187], a[224:227], a[40:43], v[184:187]\n"
"s_add_u32 m0, s38, 102400\n"
"v_mfma_f32_16x16x32_bf16 v[184:187], a[228:231], a[56:59], v[184:187]\n"
"buffer_load_dwordx4 v8, s[8:11], s42 offen offset:2048 nt lds\n"
"v_mfma_f32_16x16x32_bf16 v[188:191], a[224:227], a[44:47], v[188:191]\n"
"s_add_u32 s42, s42, 27648\n"
"s_or_b32 s42, s42, 0x18000\n"
"s_sub_u32 s62, s42, 0x60000\n"
"s_min_u32 s42, s42, s62\n"
"v_min_f32_dpp v18, v18, v18 row_bcast:31 row_mask:0xc bank_mask:0xf\n"
"v_mfma_f32_16x16x32_bf16 v[188:191], a[228:231], a[60:63], v[188:191]\n"
"s_waitcnt lgkmcnt(8)\n"
"v_readlane_b32 s47, v18, 63\n"
"v_mfma_f32_16x16x32_bf16 v[64:67], a[240:243], a[64:67], v[64:67]\n"
"ds_read_b64_tr_b16 a[224:225], v29 offset:128\n"
"v_mfma_f32_16x16x32_bf16 v[64:67], a[244:247], a[80:83], v[64:67]\n"
"s_lshr_b32 s47, s47, 23\n"
"ds_read_b64_tr_b16 a[226:227], v29 offset:1664\n"
"v_mfma_f32_16x16x32_bf16 v[68:71], a[240:243], a[68:71], v[68:71]\n"
"s_add_u32 s47, s47, 1\n"
"ds_read_b64_tr_b16 a[228:229], v29 offset:12416\n"
"v_mfma_f32_16x16x32_bf16 v[68:71], a[244:247], a[84:87], v[68:71]\n"
"s_and_b32 s47, s47, 0x1fc\n"
"ds_read_b64_tr_b16 a[230:231], v29 offset:13952\n"
"v_mfma_f32_16x16x32_bf16 v[72:75], a[240:243], a[72:75], v[72:75]\n"
"s_max_u32 s47, s47, 56\n"
"v_mfma_f32_16x16x32_bf16 v[72:75], a[244:247], a[88:91], v[72:75]\n"
"s_min_u32 s47, s47, 176\n"
"v_mul_f32 v64, s56, v64\n"
"v_mul_f32 v65, s56, v65\n"
"v_mfma_f32_16x16x32_bf16 v[76:79], a[240:243], a[76:79], v[76:79]\n"
"s_sub_u32 s47, 115, s47\n"
"v_mul_f32 v66, s56, v66\n"
"v_mul_f32 v67, s56, v67\n"
"v_mfma_f32_16x16x32_bf16 v[76:79], a[244:247], a[92:95], v[76:79]\n"
"s_waitcnt lgkmcnt(8)\n"
"v_mul_f32 v68, s56, v68\n"
"v_mul_f32 v69, s56, v69\n"
"v_mfma_f32_16x16x32_bf16 v[96:99], a[248:251], a[32:35], v[96:99]\n"
"s_add_u32 s54, s47, 157\n"
"ds_read_b64_tr_b16 a[240:241], v30 offset:24576\n"
"v_cvt_f32_i32 v48, s47\n"
"v_mfma_f32_16x16x32_bf16 v[96:99], a[252:255], a[48:51], v[96:99]\n"
"s_lshl_b32 s54, s54, 23\n"
"ds_read_b64_tr_b16 a[242:243], v30 offset:26112\n"
"v_cvt_f32_i32 v49, s47\n"
"v_mfma_f32_16x16x32_bf16 v[100:103], a[248:251], a[36:39], v[100:103]\n"
"ds_read_b64_tr_b16 a[244:245], v30 offset:36864\n"
"v_cvt_f32_i32 v50, s47\n"
"v_mfma_f32_16x16x32_bf16 v[100:103], a[252:255], a[52:55], v[100:103]\n"
"ds_read_b64_tr_b16 a[246:247], v30 offset:38400\n"
"v_cvt_f32_i32 v51, s47\n"
"v_mfma_f32_16x16x32_bf16 v[104:107], a[248:251], a[40:43], v[104:107]\n"
"v_mov_b32 v52, v48\n"
"v_mov_b32 v53, v48\n"
"v_mfma_f32_16x16x32_bf16 v[104:107], a[252:255], a[56:59], v[104:107]\n"
"v_mov_b32 v54, v48\n"
"v_mov_b32 v55, v48\n"
"v_mfma_f32_16x16x32_bf16 v[108:111], a[248:251], a[44:47], v[108:111]\n"
"v_mov_b32 v56, v48\n"
"v_mov_b32 v57, v48\n"
"v_mfma_f32_16x16x32_bf16 v[108:111], a[252:255], a[60:63], v[108:111]\n"
"s_waitcnt lgkmcnt(8)\n"
"v_mov_b32 v58, v48\n"
"v_mov_b32 v59, v48\n"
"v_mfma_f32_16x16x32_bf16 v[160:163], a[216:219], a[64:67], v[160:163]\n"
"ds_read_b64_tr_b16 a[248:249], v30 offset:128\n"
"v_mov_b32 v60, v48\n"
"v_mfma_f32_16x16x32_bf16 v[160:163], a[220:223], a[80:83], v[160:163]\n"
"ds_read_b64_tr_b16 a[250:251], v30 offset:1664\n"
"v_mov_b32 v61, v48\n"
"v_mfma_f32_16x16x32_bf16 v[164:167], a[216:219], a[68:71], v[164:167]\n"
"ds_read_b64_tr_b16 a[252:253], v30 offset:12416\n"
"v_mov_b32 v62, v48\n"
"v_mfma_f32_16x16x32_bf16 v[164:167], a[220:223], a[84:87], v[164:167]\n"
"ds_read_b64_tr_b16 a[254:255], v30 offset:13952\n"
"v_mov_b32 v63, v48\n"
"v_mfma_f32_16x16x32_bf16 v[168:171], a[216:219], a[72:75], v[168:171]\n"
"v_mul_f32 v70, s56, v70\n"
"v_mul_f32 v71, s56, v71\n"
"v_mfma_f32_16x16x32_bf16 v[168:171], a[220:223], a[88:91], v[168:171]\n"
"v_mul_f32 v72, s56, v72\n"
"v_mul_f32 v73, s56, v73\n"
"v_mfma_f32_16x16x32_bf16 v[172:175], a[216:219], a[76:79], v[172:175]\n"
"v_mul_f32 v74, s56, v74\n"
"v_mul_f32 v75, s56, v75\n"
"v_mfma_f32_16x16x32_bf16 v[172:175], a[220:223], a[92:95], v[172:175]\n"
"s_waitcnt lgkmcnt(8)\n"
"v_mul_f32 v76, s56, v76\n"
"v_mul_f32 v77, s56, v77\n"
"v_mfma_f32_16x16x32_bf16 v[192:195], a[224:227], a[32:35], v[192:195]\n"
"ds_read_b64_tr_b16 a[216:217], v31 offset:24576\n"
"v_mul_f32 v78, s56, v78\n"
"v_mfma_f32_16x16x32_bf16 v[192:195], a[228:231], a[48:51], v[192:195]\n"
"ds_read_b64_tr_b16 a[218:219], v31 offset:26112\n"
"v_mul_f32 v79, s56, v79\n"
"v_mfma_f32_16x16x32_bf16 v[196:199], a[224:227], a[36:39], v[196:199]\n"
"ds_read_b64_tr_b16 a[220:221], v31 offset:36864\n"
"v_mul_f32 v160, s56, v160\n"
"v_mfma_f32_16x16x32_bf16 v[196:199], a[228:231], a[52:55], v[196:199]\n"
"ds_read_b64_tr_b16 a[222:223], v31 offset:38400\n"
"v_lshlrev_b32 v26, 3, v12\n"
"v_mfma_f32_16x16x32_bf16 v[200:203], a[224:227], a[40:43], v[200:203]\n"
"v_mul_f32 v161, s56, v161\n"
"v_mul_f32 v162, s56, v162\n"
"v_mfma_f32_16x16x32_bf16 v[200:203], a[228:231], a[56:59], v[200:203]\n"
"v_and_b32 v26, 0x780, v26\n"
"v_mul_f32 v163, s56, v163\n"
"v_mfma_f32_16x16x32_bf16 v[204:207], a[224:227], a[44:47], v[204:207]\n"
"v_cvt_pk_bf16_f32 v163, v162, v163\n"
"v_cvt_pk_bf16_f32 v162, v160, v161\n"
"v_mfma_f32_16x16x32_bf16 v[204:207], a[228:231], a[60:63], v[204:207]\n"
"s_waitcnt lgkmcnt(8)\n"
"global_load_dword v27, v26, s[60:61]\n"
"v_mfma_f32_16x16x32_bf16 v[80:83], a[240:243], a[64:67], v[80:83]\n"
"ds_read_b64_tr_b16 a[224:225], v31 offset:128\n"
"v_cvt_pk_bf16_f32 v160, v64, v65\n"
"v_mfma_f32_16x16x32_bf16 v[80:83], a[244:247], a[80:83], v[80:83]\n"
"ds_read_b64_tr_b16 a[226:227], v31 offset:1664\n"
"v_cvt_pk_bf16_f32 v161, v66, v67\n"
"v_mfma_f32_16x16x32_bf16 v[84:87], a[240:243], a[68:71], v[84:87]\n"
"ds_read_b64_tr_b16 a[228:229], v31 offset:12416\n"
"v_mul_f32 v164, s56, v164\n"
"v_mfma_f32_16x16x32_bf16 v[84:87], a[244:247], a[84:87], v[84:87]\n"
"ds_read_b64_tr_b16 a[230:231], v31 offset:13952\n"
"v_mul_f32 v165, s56, v165\n"
"v_mfma_f32_16x16x32_bf16 v[88:91], a[240:243], a[72:75], v[88:91]\n"
"v_mul_f32 v166, s56, v166\n"
"v_mul_f32 v167, s56, v167\n"
"v_mfma_f32_16x16x32_bf16 v[88:91], a[244:247], a[88:91], v[88:91]\n"
"v_cvt_pk_bf16_f32 v167, v166, v167\n"
"v_cvt_pk_bf16_f32 v166, v164, v165\n"
"v_mfma_f32_16x16x32_bf16 v[92:95], a[240:243], a[76:79], v[92:95]\n"
"v_cvt_pk_bf16_f32 v164, v68, v69\n"
"v_cvt_pk_bf16_f32 v165, v70, v71\n"
"v_mfma_f32_16x16x32_bf16 v[92:95], a[244:247], a[92:95], v[92:95]\n"
"s_waitcnt lgkmcnt(8)\n"
"v_mul_f32 v168, s56, v168\n"
"v_mul_f32 v169, s56, v169\n"
"v_mfma_f32_16x16x32_bf16 v[112:115], a[248:251], a[32:35], v[112:115]\n"
"ds_read_b64_tr_b16 a[240:241], v28 offset:24704\n"
"v_mul_f32 v170, s56, v170\n"
"v_mfma_f32_16x16x32_bf16 v[112:115], a[252:255], a[48:51], v[112:115]\n"
"ds_read_b64_tr_b16 a[242:243], v28 offset:26240\n"
"v_mul_f32 v171, s56, v171\n"
"v_mfma_f32_16x16x32_bf16 v[116:119], a[248:251], a[36:39], v[116:119]\n"
"ds_read_b64_tr_b16 a[244:245], v28 offset:36992\n"
"v_cvt_pk_bf16_f32 v171, v170, v171\n"
"v_mfma_f32_16x16x32_bf16 v[116:119], a[252:255], a[52:55], v[116:119]\n"
"ds_read_b64_tr_b16 a[246:247], v28 offset:38528\n"
"v_cvt_pk_bf16_f32 v170, v168, v169\n"
"v_mfma_f32_16x16x32_bf16 v[120:123], a[248:251], a[40:43], v[120:123]\n"
"v_cvt_pk_bf16_f32 v168, v72, v73\n"
"v_cvt_pk_bf16_f32 v169, v74, v75\n"
"v_mfma_f32_16x16x32_bf16 v[120:123], a[252:255], a[56:59], v[120:123]\n"
"v_mul_f32 v172, s56, v172\n"
"v_mul_f32 v173, s56, v173\n"
"v_mfma_f32_16x16x32_bf16 v[124:127], a[248:251], a[44:47], v[124:127]\n"
"v_mul_f32 v174, s56, v174\n"
"v_mul_f32 v175, s56, v175\n"
"v_mfma_f32_16x16x32_bf16 v[124:127], a[252:255], a[60:63], v[124:127]\n"
"s_waitcnt lgkmcnt(8)\n"
"v_cvt_pk_bf16_f32 v175, v174, v175\n"
"v_cvt_pk_bf16_f32 v174, v172, v173\n"
"v_mfma_f32_16x16x32_bf16 v[176:179], a[216:219], a[64:67], v[176:179]\n"
"ds_read_b64_tr_b16 a[248:249], v28 offset:256\n"
"v_cvt_pk_bf16_f32 v172, v76, v77\n"
"v_mfma_f32_16x16x32_bf16 v[176:179], a[220:223], a[80:83], v[176:179]\n"
"ds_read_b64_tr_b16 a[250:251], v28 offset:1792\n"
"v_cvt_pk_bf16_f32 v173, v78, v79\n"
"v_mfma_f32_16x16x32_bf16 v[180:183], a[216:219], a[68:71], v[180:183]\n"
"ds_read_b64_tr_b16 a[252:253], v28 offset:12544\n"
"v_mul_f32 v80, s56, v80\n"
"v_mfma_f32_16x16x32_bf16 v[180:183], a[220:223], a[84:87], v[180:183]\n"
"ds_read_b64_tr_b16 a[254:255], v28 offset:14080\n"
"v_mul_f32 v81, s56, v81\n"
"v_mfma_f32_16x16x32_bf16 v[184:187], a[216:219], a[72:75], v[184:187]\n"
"s_add_u32 m0, s38, 114688\n"
"v_mul_f32 v82, s56, v82\n"
"v_mul_f32 v83, s56, v83\n"
"v_mfma_f32_16x16x32_bf16 v[184:187], a[220:223], a[88:91], v[184:187]\n"
"buffer_load_dwordx4 v6, s[8:11], s42 offen offset:0 nt lds\n"
"v_mfma_f32_16x16x32_bf16 v[188:191], a[216:219], a[76:79], v[188:191]\n"
"v_mul_f32 v84, s56, v84\n"
"v_mul_f32 v85, s56, v85\n"
"v_mfma_f32_16x16x32_bf16 v[188:191], a[220:223], a[92:95], v[188:191]\n"
"s_waitcnt lgkmcnt(8)\n"
"v_mul_f32 v86, s56, v86\n"
"v_mul_f32 v87, s56, v87\n"
"v_mfma_f32_16x16x32_bf16 v[208:211], a[224:227], a[32:35], v[208:211]\n"
"ds_read_b64_tr_b16 a[216:217], v29 offset:24704\n"
"v_mul_f32 v88, s56, v88\n"
"v_mfma_f32_16x16x32_bf16 v[208:211], a[228:231], a[48:51], v[208:211]\n"
"ds_read_b64_tr_b16 a[218:219], v29 offset:26240\n"
"v_mul_f32 v89, s56, v89\n"
"v_mfma_f32_16x16x32_bf16 v[212:215], a[224:227], a[36:39], v[212:215]\n"
"ds_read_b64_tr_b16 a[220:221], v29 offset:36992\n"
"v_mul_f32 v90, s56, v90\n"
"v_mfma_f32_16x16x32_bf16 v[212:215], a[228:231], a[52:55], v[212:215]\n"
"ds_read_b64_tr_b16 a[222:223], v29 offset:38528\n"
"v_mul_f32 v91, s56, v91\n"
"v_mfma_f32_16x16x32_bf16 v[216:219], a[224:227], a[40:43], v[216:219]\n"
"s_add_u32 m0, s38, 114688\n"
"v_mul_f32 v92, s56, v92\n"
"v_mul_f32 v93, s56, v93\n"
"v_mfma_f32_16x16x32_bf16 v[216:219], a[228:231], a[56:59], v[216:219]\n"
"buffer_load_dwordx4 v7, s[8:11], s42 offen offset:1024 nt lds\n"
"v_mfma_f32_16x16x32_bf16 v[220:223], a[224:227], a[44:47], v[220:223]\n"
"v_mul_f32 v94, s56, v94\n"
"v_mul_f32 v95, s56, v95\n"
"v_mfma_f32_16x16x32_bf16 v[220:223], a[228:231], a[60:63], v[220:223]\n"
"s_waitcnt lgkmcnt(8)\n"
"v_fma_f32 v176, s56, v176, v22\n"
"v_fma_f32 v177, s56, v177, v22\n"
"v_mfma_f32_16x16x32_bf16 v[96:99], a[240:243], a[64:67], v[96:99]\n"
"ds_read_b64_tr_b16 a[224:225], v29 offset:256\n"
"v_fma_f32 v178, s56, v178, v22\n"
"v_mfma_f32_16x16x32_bf16 v[96:99], a[244:247], a[80:83], v[96:99]\n"
"ds_read_b64_tr_b16 a[226:227], v29 offset:1792\n"
"v_fma_f32 v179, s56, v179, v22\n"
"v_mfma_f32_16x16x32_bf16 v[100:103], a[240:243], a[68:71], v[100:103]\n"
"ds_read_b64_tr_b16 a[228:229], v29 offset:12544\n"
"v_cvt_pk_bf16_f32 v179, v178, v179\n"
"v_mfma_f32_16x16x32_bf16 v[100:103], a[244:247], a[84:87], v[100:103]\n"
"ds_read_b64_tr_b16 a[230:231], v29 offset:14080\n"
"v_cvt_pk_bf16_f32 v178, v176, v177\n"
"v_mfma_f32_16x16x32_bf16 v[104:107], a[240:243], a[72:75], v[104:107]\n"
"s_add_u32 m0, s38, 114688\n"
"v_cvt_pk_bf16_f32 v176, v80, v81\n"
"v_cvt_pk_bf16_f32 v177, v82, v83\n"
"v_mfma_f32_16x16x32_bf16 v[104:107], a[244:247], a[88:91], v[104:107]\n"
"buffer_load_dwordx4 v8, s[8:11], s42 offen offset:2048 nt lds\n"
"v_mfma_f32_16x16x32_bf16 v[108:111], a[240:243], a[76:79], v[108:111]\n"
"s_add_u32 s42, s42, 27648\n"
"s_or_b32 s42, s42, 0x18000\n"
"s_sub_u32 s62, s42, 0x60000\n"
"s_min_u32 s42, s42, s62\n"
"v_fma_f32 v180, s56, v180, v22\n"
"v_fma_f32 v181, s56, v181, v22\n"
"v_mfma_f32_16x16x32_bf16 v[108:111], a[244:247], a[92:95], v[108:111]\n"
"s_waitcnt lgkmcnt(8)\n"
"v_fma_f32 v182, s56, v182, v22\n"
"v_fma_f32 v183, s56, v183, v22\n"
"v_mfma_f32_16x16x32_bf16 v[128:131], a[248:251], a[32:35], v[128:131]\n"
"ds_read_b64_tr_b16 a[240:241], v30 offset:24704\n"
"v_cvt_pk_bf16_f32 v183, v182, v183\n"
"v_mfma_f32_16x16x32_bf16 v[128:131], a[252:255], a[48:51], v[128:131]\n"
"ds_read_b64_tr_b16 a[242:243], v30 offset:26240\n"
"v_cvt_pk_bf16_f32 v182, v180, v181\n"
"v_mfma_f32_16x16x32_bf16 v[132:135], a[248:251], a[36:39], v[132:135]\n"
"ds_read_b64_tr_b16 a[244:245], v30 offset:36992\n"
"v_cvt_pk_bf16_f32 v180, v84, v85\n"
"v_mfma_f32_16x16x32_bf16 v[132:135], a[252:255], a[52:55], v[132:135]\n"
"ds_read_b64_tr_b16 a[246:247], v30 offset:38528\n"
"v_cvt_pk_bf16_f32 v181, v86, v87\n"
"v_mfma_f32_16x16x32_bf16 v[136:139], a[248:251], a[40:43], v[136:139]\n"
"s_add_u32 m0, s38, 126976\n"
"v_fma_f32 v184, s56, v184, v22\n"
"v_fma_f32 v185, s56, v185, v22\n"
"v_mfma_f32_16x16x32_bf16 v[136:139], a[252:255], a[56:59], v[136:139]\n"
"buffer_load_dwordx4 v6, s[8:11], s42 offen offset:0 nt lds\n"
"v_mfma_f32_16x16x32_bf16 v[140:143], a[248:251], a[44:47], v[140:143]\n"
"v_fma_f32 v186, s56, v186, v22\n"
"v_fma_f32 v187, s56, v187, v22\n"
"v_mfma_f32_16x16x32_bf16 v[140:143], a[252:255], a[60:63], v[140:143]\n"
"s_waitcnt lgkmcnt(8)\n"
"v_cvt_pk_bf16_f32 v187, v186, v187\n"
"v_cvt_pk_bf16_f32 v186, v184, v185\n"
"v_mfma_f32_16x16x32_bf16 v[192:195], a[216:219], a[64:67], v[192:195]\n"
"ds_read_b64_tr_b16 a[248:249], v30 offset:256\n"
"v_cvt_pk_bf16_f32 v184, v88, v89\n"
"v_mfma_f32_16x16x32_bf16 v[192:195], a[220:223], a[80:83], v[192:195]\n"
"ds_read_b64_tr_b16 a[250:251], v30 offset:1792\n"
"v_cvt_pk_bf16_f32 v185, v90, v91\n"
"v_mfma_f32_16x16x32_bf16 v[196:199], a[216:219], a[68:71], v[196:199]\n"
"ds_read_b64_tr_b16 a[252:253], v30 offset:12544\n"
"v_fma_f32 v188, s56, v188, v22\n"
"v_mfma_f32_16x16x32_bf16 v[196:199], a[220:223], a[84:87], v[196:199]\n"
"ds_read_b64_tr_b16 a[254:255], v30 offset:14080\n"
"v_fma_f32 v189, s56, v189, v22\n"
"v_mfma_f32_16x16x32_bf16 v[200:203], a[216:219], a[72:75], v[200:203]\n"
"s_add_u32 m0, s38, 126976\n"
"v_fma_f32 v190, s56, v190, v22\n"
"v_fma_f32 v191, s56, v191, v22\n"
"v_mfma_f32_16x16x32_bf16 v[200:203], a[220:223], a[88:91], v[200:203]\n"
"buffer_load_dwordx4 v7, s[8:11], s42 offen offset:1024 nt lds\n"
"v_mfma_f32_16x16x32_bf16 v[204:207], a[216:219], a[76:79], v[204:207]\n"
"v_cvt_pk_bf16_f32 v191, v190, v191\n"
"v_cvt_pk_bf16_f32 v190, v188, v189\n"
"v_mfma_f32_16x16x32_bf16 v[204:207], a[220:223], a[92:95], v[204:207]\n"
"s_waitcnt lgkmcnt(8)\n"
"v_cvt_pk_bf16_f32 v188, v92, v93\n"
"v_cvt_pk_bf16_f32 v189, v94, v95\n"
"v_mfma_f32_16x16x32_bf16 v[224:227], a[224:227], a[32:35], v[224:227]\n"
"ds_read_b64_tr_b16 a[216:217], v31 offset:24704\n"
"v_fma_f32 v96, s56, v96, v22\n"
"v_mfma_f32_16x16x32_bf16 v[224:227], a[228:231], a[48:51], v[224:227]\n"
"ds_read_b64_tr_b16 a[218:219], v31 offset:26240\n"
"v_fma_f32 v97, s56, v97, v22\n"
"v_mfma_f32_16x16x32_bf16 v[228:231], a[224:227], a[36:39], v[228:231]\n"
"ds_read_b64_tr_b16 a[220:221], v31 offset:36992\n"
"v_fma_f32 v98, s56, v98, v22\n"
"v_mfma_f32_16x16x32_bf16 v[228:231], a[228:231], a[52:55], v[228:231]\n"
"ds_read_b64_tr_b16 a[222:223], v31 offset:38528\n"
"v_fma_f32 v99, s56, v99, v22\n"
"v_mfma_f32_16x16x32_bf16 v[232:235], a[224:227], a[40:43], v[232:235]\n"
"s_add_u32 m0, s38, 126976\n"
"v_fma_f32 v100, s56, v100, v22\n"
"v_fma_f32 v101, s56, v101, v22\n"
"v_mfma_f32_16x16x32_bf16 v[232:235], a[228:231], a[56:59], v[232:235]\n"
"buffer_load_dwordx4 v8, s[8:11], s42 offen offset:2048 nt lds\n"
"v_mfma_f32_16x16x32_bf16 v[236:239], a[224:227], a[44:47], v[236:239]\n"
"s_add_u32 s42, s42, 27648\n"
"s_or_b32 s42, s42, 0x18000\n"
"s_sub_u32 s62, s42, 0x60000\n"
"s_min_u32 s42, s42, s62\n"
"v_fma_f32 v102, s56, v102, v22\n"
"v_fma_f32 v103, s56, v103, v22\n"
"v_mfma_f32_16x16x32_bf16 v[236:239], a[228:231], a[60:63], v[236:239]\n"
"s_waitcnt lgkmcnt(8)\n"
"v_fma_f32 v104, s56, v104, v22\n"
"v_fma_f32 v105, s56, v105, v22\n"
"v_mfma_f32_16x16x32_bf16 v[112:115], a[240:243], a[64:67], v[112:115]\n"
"ds_read_b64_tr_b16 a[224:225], v31 offset:256\n"
"v_fma_f32 v106, s56, v106, v22\n"
"v_mfma_f32_16x16x32_bf16 v[112:115], a[244:247], a[80:83], v[112:115]\n"
"ds_read_b64_tr_b16 a[226:227], v31 offset:1792\n"
"v_fma_f32 v107, s56, v107, v22\n"
"v_mfma_f32_16x16x32_bf16 v[116:119], a[240:243], a[68:71], v[116:119]\n"
"ds_read_b64_tr_b16 a[228:229], v31 offset:12544\n"
"v_fma_f32 v108, s56, v108, v22\n"
"v_mfma_f32_16x16x32_bf16 v[116:119], a[244:247], a[84:87], v[116:119]\n"
"ds_read_b64_tr_b16 a[230:231], v31 offset:14080\n"
"v_fma_f32 v109, s56, v109, v22\n"
"v_mfma_f32_16x16x32_bf16 v[120:123], a[240:243], a[72:75], v[120:123]\n"
"v_fma_f32 v110, s56, v110, v22\n"
"v_fma_f32 v111, s56, v111, v22\n"
"v_mfma_f32_16x16x32_bf16 v[120:123], a[244:247], a[88:91], v[120:123]\n"
"v_fma_f32 v192, s56, v192, v22\n"
"v_fma_f32 v193, s56, v193, v22\n"
"v_mfma_f32_16x16x32_bf16 v[124:127], a[240:243], a[76:79], v[124:127]\n"
"v_fma_f32 v194, s56, v194, v22\n"
"v_fma_f32 v195, s56, v195, v22\n"
"v_mfma_f32_16x16x32_bf16 v[124:127], a[244:247], a[92:95], v[124:127]\n"
"s_waitcnt lgkmcnt(8)\n"
"v_cvt_pk_bf16_f32 v195, v194, v195\n"
"v_cvt_pk_bf16_f32 v194, v192, v193\n"
"v_mfma_f32_16x16x32_bf16 v[144:147], a[248:251], a[32:35], v[144:147]\n"
"ds_read_b64_tr_b16 a[240:241], v28 offset:24832\n"
"v_cvt_pk_bf16_f32 v192, v96, v97\n"
"v_mfma_f32_16x16x32_bf16 v[144:147], a[252:255], a[48:51], v[144:147]\n"
"ds_read_b64_tr_b16 a[242:243], v28 offset:26368\n"
"v_cvt_pk_bf16_f32 v193, v98, v99\n"
"v_mfma_f32_16x16x32_bf16 v[148:151], a[248:251], a[36:39], v[148:151]\n"
"ds_read_b64_tr_b16 a[244:245], v28 offset:37120\n"
"v_fma_f32 v196, s56, v196, v22\n"
"v_mfma_f32_16x16x32_bf16 v[148:151], a[252:255], a[52:55], v[148:151]\n"
"ds_read_b64_tr_b16 a[246:247], v28 offset:38656\n"
"v_fma_f32 v197, s56, v197, v22\n"
"v_mfma_f32_16x16x32_bf16 v[152:155], a[248:251], a[40:43], v[152:155]\n"
"v_fma_f32 v198, s56, v198, v22\n"
"v_fma_f32 v199, s56, v199, v22\n"
"v_mfma_f32_16x16x32_bf16 v[152:155], a[252:255], a[56:59], v[152:155]\n"
"v_cvt_pk_bf16_f32 v199, v198, v199\n"
"v_cvt_pk_bf16_f32 v198, v196, v197\n"
"v_mfma_f32_16x16x32_bf16 v[156:159], a[248:251], a[44:47], v[156:159]\n"
"v_cvt_pk_bf16_f32 v196, v100, v101\n"
"v_cvt_pk_bf16_f32 v197, v102, v103\n"
"v_mfma_f32_16x16x32_bf16 v[156:159], a[252:255], a[60:63], v[156:159]\n"
"s_waitcnt lgkmcnt(8)\n"
"v_fma_f32 v200, s56, v200, v22\n"
"v_fma_f32 v201, s56, v201, v22\n"
"v_mfma_f32_16x16x32_bf16 v[208:211], a[216:219], a[64:67], v[208:211]\n"
"ds_read_b64_tr_b16 a[248:249], v29 offset:24832\n"
"v_fma_f32 v202, s56, v202, v22\n"
"v_mfma_f32_16x16x32_bf16 v[208:211], a[220:223], a[80:83], v[208:211]\n"
"ds_read_b64_tr_b16 a[250:251], v29 offset:26368\n"
"v_fma_f32 v203, s56, v203, v22\n"
"v_mfma_f32_16x16x32_bf16 v[212:215], a[216:219], a[68:71], v[212:215]\n"
"ds_read_b64_tr_b16 a[252:253], v29 offset:37120\n"
"v_cvt_pk_bf16_f32 v203, v202, v203\n"
"v_mfma_f32_16x16x32_bf16 v[212:215], a[220:223], a[84:87], v[212:215]\n"
"ds_read_b64_tr_b16 a[254:255], v29 offset:38656\n"
"v_cvt_pk_bf16_f32 v202, v200, v201\n"
"v_mfma_f32_16x16x32_bf16 v[216:219], a[216:219], a[72:75], v[216:219]\n"
"v_cvt_pk_bf16_f32 v200, v104, v105\n"
"v_cvt_pk_bf16_f32 v201, v106, v107\n"
"v_mfma_f32_16x16x32_bf16 v[216:219], a[220:223], a[88:91], v[216:219]\n"
"v_fma_f32 v204, s56, v204, v22\n"
"v_fma_f32 v205, s56, v205, v22\n"
"v_mfma_f32_16x16x32_bf16 v[220:223], a[216:219], a[76:79], v[220:223]\n"
"v_fma_f32 v206, s56, v206, v22\n"
"v_fma_f32 v207, s56, v207, v22\n"
"v_mfma_f32_16x16x32_bf16 v[220:223], a[220:223], a[92:95], v[220:223]\n"
"s_waitcnt lgkmcnt(8)\n"
"v_cvt_pk_bf16_f32 v207, v206, v207\n"
"v_cvt_pk_bf16_f32 v206, v204, v205\n"
"v_mfma_f32_16x16x32_bf16 v[240:243], a[224:227], a[32:35], v[240:243]\n"
"ds_read_b64_tr_b16 a[216:217], v30 offset:24832\n"
"v_cvt_pk_bf16_f32 v204, v108, v109\n"
"v_mfma_f32_16x16x32_bf16 v[240:243], a[228:231], a[48:51], v[240:243]\n"
"ds_read_b64_tr_b16 a[218:219], v30 offset:26368\n"
"v_cvt_pk_bf16_f32 v205, v110, v111\n"
"v_mfma_f32_16x16x32_bf16 v[244:247], a[224:227], a[36:39], v[244:247]\n"
"ds_read_b64_tr_b16 a[220:221], v30 offset:37120\n"
"v_fma_f32 v112, s56, v112, v22\n"
"v_mfma_f32_16x16x32_bf16 v[244:247], a[228:231], a[52:55], v[244:247]\n"
"ds_read_b64_tr_b16 a[222:223], v30 offset:38656\n"
"v_fma_f32 v113, s56, v113, v22\n"
"v_mfma_f32_16x16x32_bf16 v[248:251], a[224:227], a[40:43], v[248:251]\n"
"v_fma_f32 v114, s56, v114, v22\n"
"v_fma_f32 v115, s56, v115, v22\n"
"v_mfma_f32_16x16x32_bf16 v[248:251], a[228:231], a[56:59], v[248:251]\n"
"v_fma_f32 v116, s56, v116, v22\n"
"v_fma_f32 v117, s56, v117, v22\n"
"v_mfma_f32_16x16x32_bf16 v[252:255], a[224:227], a[44:47], v[252:255]\n"
"v_fma_f32 v118, s56, v118, v22\n"
"v_fma_f32 v119, s56, v119, v22\n"
"v_mfma_f32_16x16x32_bf16 v[252:255], a[228:231], a[60:63], v[252:255]\n"
"s_waitcnt lgkmcnt(8)\n"
"v_fma_f32 v120, s56, v120, v22\n"
"v_fma_f32 v121, s56, v121, v22\n"
"v_mfma_f32_16x16x32_bf16 v[128:131], a[240:243], a[64:67], v[128:131]\n"
"ds_read_b64_tr_b16 a[224:225], v31 offset:24832\n"
"v_fma_f32 v122, s56, v122, v22\n"
"v_mfma_f32_16x16x32_bf16 v[128:131], a[244:247], a[80:83], v[128:131]\n"
"ds_read_b64_tr_b16 a[226:227], v31 offset:26368\n"
"v_fma_f32 v123, s56, v123, v22\n"
"v_mfma_f32_16x16x32_bf16 v[132:135], a[240:243], a[68:71], v[132:135]\n"
"ds_read_b64_tr_b16 a[228:229], v31 offset:37120\n"
"v_fma_f32 v124, s56, v124, v22\n"
"v_mfma_f32_16x16x32_bf16 v[132:135], a[244:247], a[84:87], v[132:135]\n"
"ds_read_b64_tr_b16 a[230:231], v31 offset:38656\n"
"v_fma_f32 v125, s56, v125, v22\n"
"v_mfma_f32_16x16x32_bf16 v[136:139], a[240:243], a[72:75], v[136:139]\n"
"v_fma_f32 v126, s56, v126, v22\n"
"v_fma_f32 v127, s56, v127, v22\n"
"v_mfma_f32_16x16x32_bf16 v[136:139], a[244:247], a[88:91], v[136:139]\n"
"v_fma_f32 v208, s56, v208, v22\n"
"v_fma_f32 v209, s56, v209, v22\n"
"v_mfma_f32_16x16x32_bf16 v[140:143], a[240:243], a[76:79], v[140:143]\n"
"v_fma_f32 v210, s56, v210, v22\n"
"v_fma_f32 v211, s56, v211, v22\n"
"v_mfma_f32_16x16x32_bf16 v[140:143], a[244:247], a[92:95], v[140:143]\n"
"s_waitcnt lgkmcnt(8)\n"
"v_cvt_pk_bf16_f32 v211, v210, v211\n"
"v_cvt_pk_bf16_f32 v210, v208, v209\n"
"v_mfma_f32_16x16x32_bf16 v[224:227], a[248:251], a[64:67], v[224:227]\n"
"v_cvt_pk_bf16_f32 v208, v112, v113\n"
"v_cvt_pk_bf16_f32 v209, v114, v115\n"
"v_mfma_f32_16x16x32_bf16 v[224:227], a[252:255], a[80:83], v[224:227]\n"
"s_waitcnt vmcnt(7)\n"
"v_fma_f32 v212, s56, v212, v22\n"
"v_fma_f32 v213, s56, v213, v22\n"
"v_mfma_f32_16x16x32_bf16 v[228:231], a[248:251], a[68:71], v[228:231]\n"
"v_fma_f32 v214, s56, v214, v22\n"
"v_fma_f32 v215, s56, v215, v22\n"
"v_mfma_f32_16x16x32_bf16 v[228:231], a[252:255], a[84:87], v[228:231]\n"
"s_barrier\n"
"v_cvt_pk_bf16_f32 v215, v214, v215\n"
"v_cvt_pk_bf16_f32 v214, v212, v213\n"
"v_mfma_f32_16x16x32_bf16 v[232:235], a[248:251], a[72:75], v[232:235]\n"
"ds_read_b128 a[192:195], v0 offset:0\n"
"v_cvt_pk_bf16_f32 v212, v116, v117\n"
"v_mfma_f32_16x16x32_bf16 v[232:235], a[252:255], a[88:91], v[232:235]\n"
"v_cvt_pk_bf16_f32 v213, v118, v119\n"
"v_fma_f32 v216, s56, v216, v22\n"
"v_mfma_f32_16x16x32_bf16 v[236:239], a[248:251], a[76:79], v[236:239]\n"
"ds_read_b128 a[196:199], v1 offset:0\n"
"v_fma_f32 v217, s56, v217, v22\n"
"v_mfma_f32_16x16x32_bf16 v[236:239], a[252:255], a[92:95], v[236:239]\n"
"s_waitcnt lgkmcnt(6)\n"
"v_fma_f32 v218, s56, v218, v22\n"
"v_fma_f32 v219, s56, v219, v22\n"
"v_mfma_f32_16x16x32_bf16 v[144:147], a[216:219], a[64:67], v[144:147]\n"
"ds_read_b128 a[200:203], v0 offset:128\n"
"v_cvt_pk_bf16_f32 v219, v218, v219\n"
"v_mfma_f32_16x16x32_bf16 v[144:147], a[220:223], a[80:83], v[144:147]\n"
"v_cvt_pk_bf16_f32 v218, v216, v217\n"
"v_cvt_pk_bf16_f32 v216, v120, v121\n"
"v_mfma_f32_16x16x32_bf16 v[148:151], a[216:219], a[68:71], v[148:151]\n"
"ds_read_b128 a[204:207], v1 offset:128\n"
"v_cvt_pk_bf16_f32 v217, v122, v123\n"
"v_mfma_f32_16x16x32_bf16 v[148:151], a[220:223], a[84:87], v[148:151]\n"
"v_fma_f32 v220, s56, v220, v22\n"
"v_fma_f32 v221, s56, v221, v22\n"
"v_mfma_f32_16x16x32_bf16 v[152:155], a[216:219], a[72:75], v[152:155]\n"
"ds_read_b128 a[208:211], v0 offset:256\n"
"v_fma_f32 v222, s56, v222, v22\n"
"v_mfma_f32_16x16x32_bf16 v[152:155], a[220:223], a[88:91], v[152:155]\n"
"v_fma_f32 v223, s56, v223, v22\n"
"v_cvt_pk_bf16_f32 v223, v222, v223\n"
"v_mfma_f32_16x16x32_bf16 v[156:159], a[216:219], a[76:79], v[156:159]\n"
"ds_read_b128 a[212:215], v1 offset:256\n"
"v_cvt_pk_bf16_f32 v222, v220, v221\n"
"v_mfma_f32_16x16x32_bf16 v[156:159], a[220:223], a[92:95], v[156:159]\n"
"s_waitcnt lgkmcnt(6)\n"
"v_cvt_pk_bf16_f32 v220, v124, v125\n"
"v_cvt_pk_bf16_f32 v221, v126, v127\n"
"v_mfma_f32_16x16x32_bf16 v[240:243], a[224:227], a[64:67], v[240:243]\n"
"v_fma_f32 v128, s56, v128, v22\n"
"v_fma_f32 v129, s56, v129, v22\n"
"v_mfma_f32_16x16x32_bf16 v[240:243], a[228:231], a[80:83], v[240:243]\n"
"v_fma_f32 v130, s56, v130, v22\n"
"v_fma_f32 v131, s56, v131, v22\n"
"v_mfma_f32_16x16x32_bf16 v[244:247], a[224:227], a[68:71], v[244:247]\n"
"v_fma_f32 v132, s56, v132, v22\n"
"v_fma_f32 v133, s56, v133, v22\n"
"v_mfma_f32_16x16x32_bf16 v[244:247], a[228:231], a[84:87], v[244:247]\n"
"v_fma_f32 v134, s56, v134, v22\n"
"L_pg5:\n"
"s_cbranch_execz L_bk1\n"
"v_fma_f32 v135, s56, v135, v22\n"
"v_mfma_f32_16x16x32_bf16 v[248:251], a[224:227], a[72:75], v[248:251]\n"
"v_fma_f32 v136, s56, v136, v22\n"
"v_fma_f32 v137, s56, v137, v22\n"
"v_mfma_f32_16x16x32_bf16 v[248:251], a[228:231], a[88:91], v[248:251]\n"
"v_fma_f32 v138, s56, v138, v22\n"
"v_fma_f32 v139, s56, v139, v22\n"
"v_mfma_f32_16x16x32_bf16 v[252:255], a[224:227], a[76:79], v[252:255]\n"
"v_fma_f32 v140, s56, v140, v22\n"
"v_fma_f32 v141, s56, v141, v22\n"
"v_mfma_f32_16x16x32_bf16 v[252:255], a[228:231], a[92:95], v[252:255]\n"
"v_fma_f32 v142, s56, v142, v22\n"
"v_fma_f32 v143, s56, v143, v22\n"
"v_pk_fma_f32 v[224:225], v[224:225], s[56:57], v[22:23]\n"
"v_pk_fma_f32 v[226:227], v[226:227], s[56:57], v[22:23]\n"
"s_add_u32 m0, s38, 139264\n"
"v_cvt_pk_bf16_f32 v227, v226, v227\n"
"v_cvt_pk_bf16_f32 v226, v224, v225\n"
"buffer_load_dwordx4 v6, s[8:11], s42 offen offset:0 nt lds\n"
"v_cvt_pk_bf16_f32 v224, v128, v129\n"
"v_cvt_pk_bf16_f32 v225, v130, v131\n"
"v_pk_fma_f32 v[228:229], v[228:229], s[56:57], v[22:23]\n"
"v_pk_fma_f32 v[230:231], v[230:231], s[56:57], v[22:23]\n"
"v_cvt_pk_bf16_f32 v231, v230, v231\n"
"v_cvt_pk_bf16_f32 v230, v228, v229\n"
"v_cvt_pk_bf16_f32 v228, v132, v133\n"
"v_cvt_pk_bf16_f32 v229, v134, v135\n"
"v_pk_fma_f32 v[232:233], v[232:233], s[56:57], v[22:23]\n"
"v_pk_fma_f32 v[234:235], v[234:235], s[56:57], v[22:23]\n"
"buffer_load_dwordx4 v7, s[8:11], s42 offen offset:1024 nt lds\n"
"v_cvt_pk_bf16_f32 v235, v234, v235\n"
"v_cvt_pk_bf16_f32 v234, v232, v233\n"
"v_cvt_pk_bf16_f32 v232, v136, v137\n"
"v_cvt_pk_bf16_f32 v233, v138, v139\n"
"v_pk_fma_f32 v[236:237], v[236:237], s[56:57], v[22:23]\n"
"v_pk_fma_f32 v[238:239], v[238:239], s[56:57], v[22:23]\n"
"v_cvt_pk_bf16_f32 v239, v238, v239\n"
"v_cvt_pk_bf16_f32 v238, v236, v237\n"
"v_cvt_pk_bf16_f32 v236, v140, v141\n"
"v_cvt_pk_bf16_f32 v237, v142, v143\n"
"buffer_load_dwordx4 v8, s[8:11], s42 offen offset:2048 nt lds\n"
"s_add_u32 s42, s42, 27648\n"
"s_or_b32 s42, s42, 0x18000\n"
"s_sub_u32 s62, s42, 0x60000\n"
"s_min_u32 s42, s42, s62\n"
"v_pk_fma_f32 v[144:145], v[144:145], s[56:57], v[22:23]\n"
"v_pk_fma_f32 v[146:147], v[146:147], s[56:57], v[22:23]\n"
"v_pk_fma_f32 v[240:241], v[240:241], s[56:57], v[22:23]\n"
"v_pk_fma_f32 v[242:243], v[242:243], s[56:57], v[22:23]\n"
"v_cvt_pk_bf16_f32 v243, v242, v243\n"
"v_cvt_pk_bf16_f32 v242, v240, v241\n"
"v_cvt_pk_bf16_f32 v240, v144, v145\n"
"v_cvt_pk_bf16_f32 v241, v146, v147\n"
"s_add_u32 m0, s38, 151552\n"
"v_pk_fma_f32 v[148:149], v[148:149], s[56:57], v[22:23]\n"
"v_pk_fma_f32 v[150:151], v[150:151], s[56:57], v[22:23]\n"
"buffer_load_dwordx4 v6, s[8:11], s42 offen offset:0 nt lds\n"
"v_pk_fma_f32 v[244:245], v[244:245], s[56:57], v[22:23]\n"
"v_pk_fma_f32 v[246:247], v[246:247], s[56:57], v[22:23]\n"
"v_cvt_pk_bf16_f32 v247, v246, v247\n"
"v_cvt_pk_bf16_f32 v246, v244, v245\n"
"v_cvt_pk_bf16_f32 v244, v148, v149\n"
"v_cvt_pk_bf16_f32 v245, v150, v151\n"
"v_pk_fma_f32 v[152:153], v[152:153], s[56:57], v[22:23]\n"
"v_pk_fma_f32 v[154:155], v[154:155], s[56:57], v[22:23]\n"
"v_pk_fma_f32 v[248:249], v[248:249], s[56:57], v[22:23]\n"
"v_pk_fma_f32 v[250:251], v[250:251], s[56:57], v[22:23]\n"
"buffer_load_dwordx4 v7, s[8:11], s42 offen offset:1024 nt lds\n"
"v_cvt_pk_bf16_f32 v251, v250, v251\n"
"v_cvt_pk_bf16_f32 v250, v248, v249\n"
"v_cvt_pk_bf16_f32 v248, v152, v153\n"
"v_cvt_pk_bf16_f32 v249, v154, v155\n"
"v_pk_fma_f32 v[156:157], v[156:157], s[56:57], v[22:23]\n"
"v_pk_fma_f32 v[158:159], v[158:159], s[56:57], v[22:23]\n"
"v_pk_fma_f32 v[252:253], v[252:253], s[56:57], v[22:23]\n"
"v_pk_fma_f32 v[254:255], v[254:255], s[56:57], v[22:23]\n"
"v_cvt_pk_bf16_f32 v255, v254, v255\n"
"v_cvt_pk_bf16_f32 v254, v252, v253\n"
"buffer_load_dwordx4 v8, s[8:11], s42 offen offset:2048 nt lds\n"
"s_add_u32 s42, s42, 27648\n"
"s_or_b32 s42, s42, 0x18000\n"
"s_sub_u32 s62, s42, 0x60000\n"
"s_min_u32 s42, s42, s62\n"
"v_cvt_pk_bf16_f32 v252, v156, v157\n"
"v_cvt_pk_bf16_f32 v253, v158, v159\n"
"s_waitcnt lgkmcnt(3)\n"
"v_mfma_f32_16x16x32_bf16 v[96:99], a[192:195], v[160:163], v[48:51]\n"
"v_mfma_f32_16x16x32_bf16 v[96:99], a[196:199], v[176:179], v[96:99]\n"
"v_mfma_f32_16x16x32_bf16 v[96:99], a[200:203], v[192:195], v[96:99]\n"
"s_waitcnt lgkmcnt(0)\n"
"v_mfma_f32_16x16x32_bf16 v[96:99], a[204:207], v[208:211], v[96:99]\n"
"ds_read_b128 a[216:219], v0 offset:6144\n"
"v_mfma_f32_16x16x32_bf16 v[96:99], a[208:211], v[224:227], v[96:99]\n"
"v_mfma_f32_16x16x32_bf16 v[96:99], a[212:215], v[240:243], v[96:99]\n"
"v_mfma_f32_16x16x32_bf16 v[100:103], a[212:215], v[244:247], v[48:51]\n"
"ds_read_b128 a[220:223], v1 offset:6144\n"
"v_mfma_f32_16x16x32_bf16 v[100:103], a[208:211], v[228:231], v[100:103]\n"
"v_mfma_f32_16x16x32_bf16 v[100:103], a[204:207], v[212:215], v[100:103]\n"
"v_mfma_f32_16x16x32_bf16 v[100:103], a[200:203], v[196:199], v[100:103]\n"
"ds_read_b128 a[224:227], v0 offset:6272\n"
"v_mfma_f32_16x16x32_bf16 v[100:103], a[196:199], v[180:183], v[100:103]\n"
"s_nop 0\n"
"v_exp_f32 v96, v96\n"
"v_mfma_f32_16x16x32_bf16 v[100:103], a[192:195], v[164:167], v[100:103]\n"
"v_exp_f32 v97, v97\n"
"v_mfma_f32_16x16x32_bf16 v[104:107], a[192:195], v[168:171], v[48:51]\n"
"ds_read_b128 a[228:231], v1 offset:6272\n"
"v_add_f32 v18, v96, v97\n"
"v_mfma_f32_16x16x32_bf16 v[104:107], a[196:199], v[184:187], v[104:107]\n"
"v_exp_f32 v98, v98\n"
"v_mfma_f32_16x16x32_bf16 v[104:107], a[200:203], v[200:203], v[104:107]\n"
"v_exp_f32 v99, v99\n"
"v_mfma_f32_16x16x32_bf16 v[104:107], a[204:207], v[216:219], v[104:107]\n"
"ds_read_b128 a[232:235], v0 offset:6400\n"
"v_cvt_pk_bf16_f32 v96, v96, v97\n"
"v_mfma_f32_16x16x32_bf16 v[104:107], a[208:211], v[232:235], v[104:107]\n"
"v_exp_f32 v100, v100\n"
"v_mfma_f32_16x16x32_bf16 v[104:107], a[212:215], v[248:251], v[104:107]\n"
"v_exp_f32 v101, v101\n"
"v_mfma_f32_16x16x32_bf16 v[108:111], a[212:215], v[252:255], v[48:51]\n"
"ds_read_b128 a[236:239], v1 offset:6400\n"
"v_add_f32 v18, v18, v98\n"
"v_mfma_f32_16x16x32_bf16 v[108:111], a[208:211], v[236:239], v[108:111]\n"
"v_exp_f32 v102, v102\n"
"v_mfma_f32_16x16x32_bf16 v[108:111], a[204:207], v[220:223], v[108:111]\n"
"v_add_f32 v18, v18, v99\n"
"v_cvt_pk_bf16_f32 v97, v98, v99\n"
"v_mfma_f32_16x16x32_bf16 v[108:111], a[200:203], v[204:207], v[108:111]\n"
"v_exp_f32 v103, v103\n"
"v_mfma_f32_16x16x32_bf16 v[108:111], a[196:199], v[188:191], v[108:111]\n"
"v_add_f32 v19, v100, v101\n"
"v_cvt_pk_bf16_f32 v100, v100, v101\n"
"v_mfma_f32_16x16x32_bf16 v[108:111], a[192:195], v[172:175], v[108:111]\n"
"s_waitcnt lgkmcnt(0)\n"
"v_exp_f32 v104, v104\n"
"v_mfma_f32_16x16x32_bf16 v[64:67], a[216:219], v[160:163], v[48:51]\n"
"v_exp_f32 v105, v105\n"
"v_mfma_f32_16x16x32_bf16 v[64:67], a[220:223], v[176:179], v[64:67]\n"
"v_add_f32 v19, v19, v102\n"
"v_add_f32 v19, v19, v103\n"
"v_mfma_f32_16x16x32_bf16 v[64:67], a[224:227], v[192:195], v[64:67]\n"
"v_exp_f32 v106, v106\n"
"v_mfma_f32_16x16x32_bf16 v[64:67], a[228:231], v[208:211], v[64:67]\n"
"ds_read_b128 a[192:195], v0 offset:12288\n"
"v_cvt_pk_bf16_f32 v101, v102, v103\n"
"v_mfma_f32_16x16x32_bf16 v[64:67], a[232:235], v[224:227], v[64:67]\n"
"v_exp_f32 v107, v107\n"
"v_mfma_f32_16x16x32_bf16 v[64:67], a[236:239], v[240:243], v[64:67]\n"
"v_add_f32 v20, v104, v105\n"
"v_cvt_pk_bf16_f32 v104, v104, v105\n"
"v_mfma_f32_16x16x32_bf16 v[68:71], a[236:239], v[244:247], v[48:51]\n"
"ds_read_b128 a[196:199], v1 offset:12288\n"
"v_add_f32 v20, v20, v106\n"
"v_mfma_f32_16x16x32_bf16 v[68:71], a[232:235], v[228:231], v[68:71]\n"
"v_exp_f32 v108, v108\n"
"v_mfma_f32_16x16x32_bf16 v[68:71], a[228:231], v[212:215], v[68:71]\n"
"v_exp_f32 v109, v109\n"
"v_mfma_f32_16x16x32_bf16 v[68:71], a[224:227], v[196:199], v[68:71]\n"
"ds_read_b128 a[200:203], v0 offset:12416\n"
"v_add_f32 v20, v20, v107\n"
"v_mfma_f32_16x16x32_bf16 v[68:71], a[220:223], v[180:183], v[68:71]\n"
"v_exp_f32 v110, v110\n"
"v_mfma_f32_16x16x32_bf16 v[68:71], a[216:219], v[164:167], v[68:71]\n"
"v_exp_f32 v111, v111\n"
"v_mfma_f32_16x16x32_bf16 v[72:75], a[216:219], v[168:171], v[48:51]\n"
"ds_read_b128 a[204:207], v1 offset:12416\n"
"v_cvt_pk_bf16_f32 v105, v106, v107\n"
"v_mfma_f32_16x16x32_bf16 v[72:75], a[220:223], v[184:187], v[72:75]\n"
"v_add_f32 v21, v108, v109\n"
"v_cvt_pk_bf16_f32 v108, v108, v109\n"
"v_mfma_f32_16x16x32_bf16 v[72:75], a[224:227], v[200:203], v[72:75]\n"
"v_exp_f32 v64, v64\n"
"v_mfma_f32_16x16x32_bf16 v[72:75], a[228:231], v[216:219], v[72:75]\n"
"ds_read_b128 a[208:211], v0 offset:12544\n"
"v_add_f32 v21, v21, v110\n"
"v_mfma_f32_16x16x32_bf16 v[72:75], a[232:235], v[232:235], v[72:75]\n"
"v_exp_f32 v65, v65\n"
"v_mfma_f32_16x16x32_bf16 v[72:75], a[236:239], v[248:251], v[72:75]\n"
"v_add_f32 v21, v21, v111\n"
"v_cvt_pk_bf16_f32 v109, v110, v111\n"
"v_mfma_f32_16x16x32_bf16 v[76:79], a[236:239], v[252:255], v[48:51]\n"
"ds_read_b128 a[212:215], v1 offset:12544\n"
"v_add_f32 v18, v18, v64\n"
"v_mfma_f32_16x16x32_bf16 v[76:79], a[232:235], v[236:239], v[76:79]\n"
"v_exp_f32 v66, v66\n"
"v_mfma_f32_16x16x32_bf16 v[76:79], a[228:231], v[220:223], v[76:79]\n"
"v_exp_f32 v67, v67\n"
"v_mfma_f32_16x16x32_bf16 v[76:79], a[224:227], v[204:207], v[76:79]\n"
"v_add_f32 v18, v18, v65\n"
"v_cvt_pk_bf16_f32 v98, v64, v65\n"
"v_mfma_f32_16x16x32_bf16 v[76:79], a[220:223], v[188:191], v[76:79]\n"
"v_exp_f32 v68, v68\n"
"v_mfma_f32_16x16x32_bf16 v[76:79], a[216:219], v[172:175], v[76:79]\n"
"s_waitcnt lgkmcnt(0)\n"
"v_exp_f32 v69, v69\n"
"v_mfma_f32_16x16x32_bf16 v[112:115], a[192:195], v[160:163], v[48:51]\n"
"v_add_f32 v18, v18, v66\n"
"v_add_f32 v18, v18, v67\n"
"v_mfma_f32_16x16x32_bf16 v[112:115], a[196:199], v[176:179], v[112:115]\n"
"v_exp_f32 v70, v70\n"
"v_mfma_f32_16x16x32_bf16 v[112:115], a[200:203], v[192:195], v[112:115]\n"
"v_cvt_pk_bf16_f32 v99, v66, v67\n"
"v_add_f32 v19, v19, v68\n"
"v_mfma_f32_16x16x32_bf16 v[112:115], a[204:207], v[208:211], v[112:115]\n"
"ds_read_b128 a[216:219], v0 offset:18432\n"
"v_add_f32 v19, v19, v69\n"
"v_mfma_f32_16x16x32_bf16 v[112:115], a[208:211], v[224:227], v[112:115]\n"
"v_exp_f32 v71, v71\n"
"v_mfma_f32_16x16x32_bf16 v[112:115], a[212:215], v[240:243], v[112:115]\n"
"v_exp_f32 v72, v72\n"
"v_mfma_f32_16x16x32_bf16 v[116:119], a[212:215], v[244:247], v[48:51]\n"
"ds_read_b128 a[220:223], v1 offset:18432\n"
"v_cvt_pk_bf16_f32 v102, v68, v69\n"
"v_mfma_f32_16x16x32_bf16 v[116:119], a[208:211], v[228:231], v[116:119]\n"
"v_exp_f32 v73, v73\n"
"v_mfma_f32_16x16x32_bf16 v[116:119], a[204:207], v[212:215], v[116:119]\n"
"v_add_f32 v19, v19, v70\n"
"v_add_f32 v19, v19, v71\n"
"v_mfma_f32_16x16x32_bf16 v[116:119], a[200:203], v[196:199], v[116:119]\n"
"ds_read_b128 a[224:227], v0 offset:18560\n"
"v_cvt_pk_bf16_f32 v103, v70, v71\n"
"v_mfma_f32_16x16x32_bf16 v[116:119], a[196:199], v[180:183], v[116:119]\n"
"v_exp_f32 v74, v74\n"
"v_mfma_f32_16x16x32_bf16 v[116:119], a[192:195], v[164:167], v[116:119]\n"
"v_exp_f32 v75, v75\n"
"v_mfma_f32_16x16x32_bf16 v[120:123], a[192:195], v[168:171], v[48:51]\n"
"ds_read_b128 a[228:231], v1 offset:18560\n"
"v_add_f32 v20, v20, v72\n"
"v_mfma_f32_16x16x32_bf16 v[120:123], a[196:199], v[184:187], v[120:123]\n"
"v_add_f32 v20, v20, v73\n"
"v_cvt_pk_bf16_f32 v106, v72, v73\n"
"v_mfma_f32_16x16x32_bf16 v[120:123], a[200:203], v[200:203], v[120:123]\n"
"v_exp_f32 v76, v76\n"
"v_mfma_f32_16x16x32_bf16 v[120:123], a[204:207], v[216:219], v[120:123]\n"
"ds_read_b128 a[232:235], v0 offset:18688\n"
"v_add_f32 v20, v20, v74\n"
"v_mfma_f32_16x16x32_bf16 v[120:123], a[208:211], v[232:235], v[120:123]\n"
"v_exp_f32 v77, v77\n"
"v_mfma_f32_16x16x32_bf16 v[120:123], a[212:215], v[248:251], v[120:123]\n"
"v_add_f32 v20, v20, v75\n"
"v_cvt_pk_bf16_f32 v107, v74, v75\n"
"v_mfma_f32_16x16x32_bf16 v[124:127], a[212:215], v[252:255], v[48:51]\n"
"ds_read_b128 a[236:239], v1 offset:18688\n"
"v_add_f32 v21, v21, v76\n"
"v_mfma_f32_16x16x32_bf16 v[124:127], a[208:211], v[236:239], v[124:127]\n"
"v_exp_f32 v78, v78\n"
"v_mfma_f32_16x16x32_bf16 v[124:127], a[204:207], v[220:223], v[124:127]\n"
"v_exp_f32 v79, v79\n"
"v_mfma_f32_16x16x32_bf16 v[124:127], a[200:203], v[204:207], v[124:127]\n"
"v_add_f32 v21, v21, v77\n"
"v_cvt_pk_bf16_f32 v110, v76, v77\n"
"v_mfma_f32_16x16x32_bf16 v[124:127], a[196:199], v[188:191], v[124:127]\n"
"v_exp_f32 v112, v112\n"
"v_mfma_f32_16x16x32_bf16 v[124:127], a[192:195], v[172:175], v[124:127]\n"
"s_waitcnt lgkmcnt(0)\n"
"v_exp_f32 v113, v113\n"
"v_mfma_f32_16x16x32_bf16 v[80:83], a[216:219], v[160:163], v[48:51]\n"
"v_add_f32 v21, v21, v78\n"
"v_add_f32 v21, v21, v79\n"
"v_mfma_f32_16x16x32_bf16 v[80:83], a[220:223], v[176:179], v[80:83]\n"
"v_exp_f32 v114, v114\n"
"v_mfma_f32_16x16x32_bf16 v[80:83], a[224:227], v[192:195], v[80:83]\n"
"v_cvt_pk_bf16_f32 v111, v78, v79\n"
"v_add_f32 v18, v18, v112\n"
"v_mfma_f32_16x16x32_bf16 v[80:83], a[228:231], v[208:211], v[80:83]\n"
"v_exp_f32 v115, v115\n"
"v_mfma_f32_16x16x32_bf16 v[80:83], a[232:235], v[224:227], v[80:83]\n"
"v_add_f32 v18, v18, v113\n"
"v_cvt_pk_bf16_f32 v112, v112, v113\n"
"v_mfma_f32_16x16x32_bf16 v[80:83], a[236:239], v[240:243], v[80:83]\n"
"v_exp_f32 v116, v116\n"
"v_mfma_f32_16x16x32_bf16 v[84:87], a[236:239], v[244:247], v[48:51]\n"
"v_exp_f32 v117, v117\n"
"v_mfma_f32_16x16x32_bf16 v[84:87], a[232:235], v[228:231], v[84:87]\n"
"v_add_f32 v18, v18, v114\n"
"v_add_f32 v18, v18, v115\n"
"v_mfma_f32_16x16x32_bf16 v[84:87], a[228:231], v[212:215], v[84:87]\n"
"s_sub_u32 s50, s54, 0xf000000\n"
"v_exp_f32 v118, v118\n"
"v_mfma_f32_16x16x32_bf16 v[84:87], a[224:227], v[196:199], v[84:87]\n"
"v_cvt_pk_bf16_f32 v113, v114, v115\n"
"v_add_f32 v19, v19, v116\n"
"v_mfma_f32_16x16x32_bf16 v[84:87], a[220:223], v[180:183], v[84:87]\n"
"v_exp_f32 v119, v119\n"
"v_mfma_f32_16x16x32_bf16 v[84:87], a[216:219], v[164:167], v[84:87]\n"
"v_add_f32 v19, v19, v117\n"
"v_cvt_pk_bf16_f32 v116, v116, v117\n"
"v_mfma_f32_16x16x32_bf16 v[88:91], a[216:219], v[168:171], v[48:51]\n"
"v_exp_f32 v120, v120\n"
"v_mfma_f32_16x16x32_bf16 v[88:91], a[220:223], v[184:187], v[88:91]\n"
"v_exp_f32 v121, v121\n"
"v_mfma_f32_16x16x32_bf16 v[88:91], a[224:227], v[200:203], v[88:91]\n"
"v_add_f32 v19, v19, v118\n"
"v_add_f32 v19, v19, v119\n"
"v_mfma_f32_16x16x32_bf16 v[88:91], a[228:231], v[216:219], v[88:91]\n"
"v_exp_f32 v122, v122\n"
"v_mfma_f32_16x16x32_bf16 v[88:91], a[232:235], v[232:235], v[88:91]\n"
"v_cvt_pk_bf16_f32 v117, v118, v119\n"
"v_add_f32 v20, v20, v120\n"
"v_mfma_f32_16x16x32_bf16 v[88:91], a[236:239], v[248:251], v[88:91]\n"
"v_exp_f32 v123, v123\n"
"v_mfma_f32_16x16x32_bf16 v[92:95], a[236:239], v[252:255], v[48:51]\n"
"v_add_f32 v20, v20, v121\n"
"v_cvt_pk_bf16_f32 v120, v120, v121\n"
"v_mfma_f32_16x16x32_bf16 v[92:95], a[232:235], v[236:239], v[92:95]\n"
"v_exp_f32 v124, v124\n"
"v_mfma_f32_16x16x32_bf16 v[92:95], a[228:231], v[220:223], v[92:95]\n"
"v_exp_f32 v125, v125\n"
"v_mfma_f32_16x16x32_bf16 v[92:95], a[224:227], v[204:207], v[92:95]\n"
"v_add_f32 v20, v20, v122\n"
"v_add_f32 v20, v20, v123\n"
"v_mfma_f32_16x16x32_bf16 v[92:95], a[220:223], v[188:191], v[92:95]\n"
"v_exp_f32 v126, v126\n"
"v_mfma_f32_16x16x32_bf16 v[92:95], a[216:219], v[172:175], v[92:95]\n"
"v_cvt_pk_bf16_f32 v121, v122, v123\n"
"v_add_f32 v21, v21, v124\n"
"v_add_u32 v0, 0x6000, v0\n"
"v_add_u32 v1, 0x6000, v1\n"
"s_waitcnt vmcnt(6)\n"
"s_barrier\n"
"ds_read_b128 a[192:195], v0 offset:0\n"
"ds_read_b128 a[196:199], v1 offset:0\n"
"ds_read_b128 a[200:203], v0 offset:128\n"
"ds_read_b128 a[204:207], v1 offset:128\n"
"ds_read_b128 a[208:211], v0 offset:256\n"
"ds_read_b128 a[212:215], v1 offset:256\n"
"v_exp_f32 v127, v127\n"
"v_add_f32 v21, v21, v125\n"
"s_add_u32 m0, s38, 40960\n"
"v_cvt_pk_bf16_f32 v124, v124, v125\n"
"v_add_f32 v21, v21, v126\n"
"buffer_load_dwordx4 v9, s[12:15], s43 offen offset:0 lds\n"
"v_add_f32 v21, v21, v127\n"
"v_cvt_pk_bf16_f32 v125, v126, v127\n"
"v_exp_f32 v80, v80\n"
"v_exp_f32 v81, v81\n"
"v_exp_f32 v82, v82\n"
"v_exp_f32 v83, v83\n"
"v_add_f32 v18, v18, v80\n"
"v_add_f32 v18, v18, v81\n"
"buffer_load_dwordx4 v10, s[12:15], s43 offen offset:1024 lds\n"
"v_cvt_pk_bf16_f32 v114, v80, v81\n"
"v_add_f32 v18, v18, v82\n"
"v_add_f32 v18, v18, v83\n"
"v_cvt_pk_bf16_f32 v115, v82, v83\n"
"v_exp_f32 v84, v84\n"
"v_exp_f32 v85, v85\n"
"v_exp_f32 v86, v86\n"
"v_exp_f32 v87, v87\n"
"buffer_load_dwordx4 v11, s[12:15], s43 offen offset:2048 lds\n"
"s_add_u32 s43, s43, 27648\n"
"s_or_b32 s43, s43, 0x18000\n"
"s_sub_u32 s62, s43, 0x60000\n"
"s_min_u32 s43, s43, s62\n"
"v_add_f32 v19, v19, v84\n"
"v_add_f32 v19, v19, v85\n"
"v_cvt_pk_bf16_f32 v118, v84, v85\n"
"v_add_f32 v19, v19, v86\n"
"v_add_f32 v19, v19, v87\n"
"v_cvt_pk_bf16_f32 v119, v86, v87\n"
"s_add_u32 m0, s38, 53248\n"
"v_exp_f32 v88, v88\n"
"v_exp_f32 v89, v89\n"
"buffer_load_dwordx4 v9, s[12:15], s43 offen offset:0 lds\n"
"v_exp_f32 v90, v90\n"
"v_exp_f32 v91, v91\n"
"v_add_f32 v20, v20, v88\n"
"v_add_f32 v20, v20, v89\n"
"v_cvt_pk_bf16_f32 v122, v88, v89\n"
"v_add_f32 v20, v20, v90\n"
"v_add_f32 v20, v20, v91\n"
"v_cvt_pk_bf16_f32 v123, v90, v91\n"
"buffer_load_dwordx4 v10, s[12:15], s43 offen offset:1024 nt lds\n"
"v_exp_f32 v92, v92\n"
"v_exp_f32 v93, v93\n"
"v_exp_f32 v94, v94\n"
"v_exp_f32 v95, v95\n"
"v_add_f32 v21, v21, v92\n"
"v_add_f32 v21, v21, v93\n"
"v_cvt_pk_bf16_f32 v126, v92, v93\n"
"v_add_f32 v21, v21, v94\n"
"buffer_load_dwordx4 v11, s[12:15], s43 offen offset:2048 nt lds\n"
"s_add_u32 s43, s43, 27648\n"
"s_or_b32 s43, s43, 0x18000\n"
"s_sub_u32 s62, s43, 0x60000\n"
"s_min_u32 s43, s43, s62\n"
"v_add_f32 v21, v21, v95\n"
"v_cvt_pk_bf16_f32 v127, v94, v95\n"
"v_max3_f32 v22, v18, v19, v20\n"
"v_max_f32 v22, v22, v21\n"
"v_min3_f32 v23, v18, v19, v20\n"
"v_min_f32 v23, v23, v21\n"
"v_cmp_lt_f32 vcc, s54, v22\n"
"v_cmp_nle_f32 s[50:51], s50, v23\n"
"s_waitcnt lgkmcnt(3)\n"
"s_nop 1\n"
"s_or_b64 vcc, vcc, s[50:51]\n"
"s_cbranch_scc1 L_rc0\n"
"L_rr0:\n"
"v_mfma_f32_16x16x32_bf16 v[128:131], a[192:195], v[160:163], v[48:51]\n"
"v_mov_b32 v14, v18\n"
"v_mov_b32 v15, v19\n"
"v_mfma_f32_16x16x32_bf16 v[128:131], a[196:199], v[176:179], v[128:131]\n"
"s_add_u32 m0, s40, 0\n"
"v_mov_b32 v16, v20\n"
"v_mov_b32 v17, v21\n"
"v_mfma_f32_16x16x32_bf16 v[128:131], a[200:203], v[192:195], v[128:131]\n"
"s_waitcnt lgkmcnt(0)\n"
"buffer_load_dwordx4 v6, s[8:11], s42 offen offset:0 nt lds\n"
"v_mfma_f32_16x16x32_bf16 v[128:131], a[204:207], v[208:211], v[128:131]\n"
"ds_read_b128 a[216:219], v0 offset:6144\n"
"v_mfma_f32_16x16x32_bf16 v[128:131], a[208:211], v[224:227], v[128:131]\n"
"s_add_u32 s55, s55, 18\n"
"v_mfma_f32_16x16x32_bf16 v[128:131], a[212:215], v[240:243], v[128:131]\n"
"v_mfma_f32_16x16x32_bf16 v[132:135], a[212:215], v[244:247], v[52:55]\n"
"ds_read_b128 a[220:223], v1 offset:6144\n"
"v_mfma_f32_16x16x32_bf16 v[132:135], a[208:211], v[228:231], v[132:135]\n"
"v_mfma_f32_16x16x32_bf16 v[132:135], a[204:207], v[212:215], v[132:135]\n"
"v_mfma_f32_16x16x32_bf16 v[132:135], a[200:203], v[196:199], v[132:135]\n"
"ds_read_b128 a[224:227], v0 offset:6272\n"
"v_mfma_f32_16x16x32_bf16 v[132:135], a[196:199], v[180:183], v[132:135]\n"
"s_nop 0\n"
"v_exp_f32 v128, v128\n"
"v_mfma_f32_16x16x32_bf16 v[132:135], a[192:195], v[164:167], v[132:135]\n"
"v_exp_f32 v129, v129\n"
"v_mfma_f32_16x16x32_bf16 v[136:139], a[192:195], v[168:171], v[56:59]\n"
"ds_read_b128 a[228:231], v1 offset:6272\n"
"v_add_f32 v18, v128, v129\n"
"v_mfma_f32_16x16x32_bf16 v[136:139], a[196:199], v[184:187], v[136:139]\n"
"v_exp_f32 v130, v130\n"
"v_mfma_f32_16x16x32_bf16 v[136:139], a[200:203], v[200:203], v[136:139]\n"
"buffer_load_dwordx4 v7, s[8:11], s42 offen offset:1024 nt lds\n"
"v_mfma_f32_16x16x32_bf16 v[136:139], a[204:207], v[216:219], v[136:139]\n"
"ds_read_b128 a[232:235], v0 offset:6400\n"
"v_cvt_pk_bf16_f32 v128, v128, v129\n"
"v_mfma_f32_16x16x32_bf16 v[136:139], a[208:211], v[232:235], v[136:139]\n"
"v_exp_f32 v131, v131\n"
"v_mfma_f32_16x16x32_bf16 v[136:139], a[212:215], v[248:251], v[136:139]\n"
"v_exp_f32 v132, v132\n"
"v_mfma_f32_16x16x32_bf16 v[140:143], a[212:215], v[252:255], v[60:63]\n"
"ds_read_b128 a[236:239], v1 offset:6400\n"
"v_add_f32 v18, v18, v130\n"
"v_mfma_f32_16x16x32_bf16 v[140:143], a[208:211], v[236:239], v[140:143]\n"
"v_exp_f32 v133, v133\n"
"v_mfma_f32_16x16x32_bf16 v[140:143], a[204:207], v[220:223], v[140:143]\n"
"v_exp_f32 v134, v134\n"
"v_mfma_f32_16x16x32_bf16 v[140:143], a[200:203], v[204:207], v[140:143]\n"
"v_add_f32 v18, v18, v131\n"
"v_cvt_pk_bf16_f32 v129, v130, v131\n"
"v_mfma_f32_16x16x32_bf16 v[140:143], a[196:199], v[188:191], v[140:143]\n"
"v_exp_f32 v135, v135\n"
"v_mfma_f32_16x16x32_bf16 v[140:143], a[192:195], v[172:175], v[140:143]\n"
"s_waitcnt lgkmcnt(0)\n"
"v_add_f32 v19, v132, v133\n"
"v_cvt_pk_bf16_f32 v132, v132, v133\n"
"v_mfma_f32_16x16x32_bf16 v[64:67], a[216:219], v[160:163], v[48:51]\n"
"v_exp_f32 v136, v136\n"
"v_mfma_f32_16x16x32_bf16 v[64:67], a[220:223], v[176:179], v[64:67]\n"
"v_exp_f32 v137, v137\n"
"v_mfma_f32_16x16x32_bf16 v[64:67], a[224:227], v[192:195], v[64:67]\n"
"buffer_load_dwordx4 v8, s[8:11], s42 offen offset:2048 nt lds\n"
"v_mfma_f32_16x16x32_bf16 v[64:67], a[228:231], v[208:211], v[64:67]\n"
"ds_read_b128 a[192:195], v0 offset:12288\n"
"v_add_f32 v19, v19, v134\n"
"v_mfma_f32_16x16x32_bf16 v[64:67], a[232:235], v[224:227], v[64:67]\n"
"s_add_u32 s42, s42, 27648\n"
"v_exp_f32 v138, v138\n"
"v_mfma_f32_16x16x32_bf16 v[64:67], a[236:239], v[240:243], v[64:67]\n"
"s_or_b32 s42, s42, 0x18000\n"
"v_add_f32 v19, v19, v135\n"
"v_cvt_pk_bf16_f32 v133, v134, v135\n"
"v_mfma_f32_16x16x32_bf16 v[68:71], a[236:239], v[244:247], v[52:55]\n"
"ds_read_b128 a[196:199], v1 offset:12288\n"
"v_add_f32 v20, v136, v137\n"
"v_mfma_f32_16x16x32_bf16 v[68:71], a[232:235], v[228:231], v[68:71]\n"
"s_memtime s[52:53]\n"
"s_sub_u32 s62, s42, 0x60000\n"
"v_exp_f32 v139, v139\n"
"v_mfma_f32_16x16x32_bf16 v[68:71], a[228:231], v[212:215], v[68:71]\n"
"s_memrealtime s[0:1]\n"
"v_exp_f32 v140, v140\n"
"v_mfma_f32_16x16x32_bf16 v[68:71], a[224:227], v[196:199], v[68:71]\n"
"ds_read_b128 a[200:203], v0 offset:12416\n"
"v_cvt_pk_bf16_f32 v136, v136, v137\n"
"v_mfma_f32_16x16x32_bf16 v[68:71], a[220:223], v[180:183], v[68:71]\n"
"s_min_u32 s42, s42, s62\n"
"v_exp_f32 v141, v141\n"
"v_mfma_f32_16x16x32_bf16 v[68:71], a[216:219], v[164:167], v[68:71]\n"
"v_add_f32 v20, v20, v138\n"
"v_add_f32 v20, v20, v139\n"
"v_mfma_f32_16x16x32_bf16 v[72:75], a[216:219], v[168:171], v[56:59]\n"
"ds_read_b128 a[204:207], v1 offset:12416\n"
"v_cvt_pk_bf16_f32 v137, v138, v139\n"
"v_mfma_f32_16x16x32_bf16 v[72:75], a[220:223], v[184:187], v[72:75]\n"
"s_add_u32 m0, s40, 12288\n"
"v_exp_f32 v142, v142\n"
"v_mfma_f32_16x16x32_bf16 v[72:75], a[224:227], v[200:203], v[72:75]\n"
"buffer_load_dwordx4 v6, s[8:11], s42 offen offset:0 nt lds\n"
"v_mfma_f32_16x16x32_bf16 v[72:75], a[228:231], v[216:219], v[72:75]\n"
"ds_read_b128 a[208:211], v0 offset:12544\n"
"v_add_f32 v21, v140, v141\n"
"v_mfma_f32_16x16x32_bf16 v[72:75], a[232:235], v[232:235], v[72:75]\n"
"v_exp_f32 v143, v143\n"
"v_mfma_f32_16x16x32_bf16 v[72:75], a[236:239], v[248:251], v[72:75]\n"
"v_exp_f32 v64, v64\n"
"v_mfma_f32_16x16x32_bf16 v[76:79], a[236:239], v[252:255], v[60:63]\n"
"ds_read_b128 a[212:215], v1 offset:12544\n"
"v_cvt_pk_bf16_f32 v140, v140, v141\n"
"v_mfma_f32_16x16x32_bf16 v[76:79], a[232:235], v[236:239], v[76:79]\n"
"v_exp_f32 v65, v65\n"
"v_mfma_f32_16x16x32_bf16 v[76:79], a[228:231], v[220:223], v[76:79]\n"
"v_add_f32 v21, v21, v142\n"
"v_add_f32 v21, v21, v143\n"
"v_mfma_f32_16x16x32_bf16 v[76:79], a[224:227], v[204:207], v[76:79]\n"
"v_exp_f32 v66, v66\n"
"v_mfma_f32_16x16x32_bf16 v[76:79], a[220:223], v[188:191], v[76:79]\n"
"v_cvt_pk_bf16_f32 v141, v142, v143\n"
"v_add_f32 v18, v18, v64\n"
"v_mfma_f32_16x16x32_bf16 v[76:79], a[216:219], v[172:175], v[76:79]\n"
"s_waitcnt lgkmcnt(0)\n"
"s_cmp_lg_u32 s65, 0\n"
"s_cbranch_scc1 L_cls\n"
"L_clr:\n"
"v_exp_f32 v67, v67\n"
"v_mfma_f32_16x16x32_bf16 v[144:147], a[192:195], v[160:163], v[48:51]\n"
"s_sub_u32 s47, s97, s0\n"
"v_add_f32 v18, v18, v65\n"
"v_cvt_pk_bf16_f32 v130, v64, v65\n"
"v_mfma_f32_16x16x32_bf16 v[144:147], a[196:199], v[176:179], v[144:147]\n"
"s_add_u32 s47, s47, s33\n"
"v_exp_f32 v68, v68\n"
"v_mfma_f32_16x16x32_bf16 v[144:147], a[200:203], v[192:195], v[144:147]\n"
"buffer_load_dwordx4 v7, s[8:11], s42 offen offset:1024 nt lds\n"
"v_mfma_f32_16x16x32_bf16 v[144:147], a[204:207], v[208:211], v[144:147]\n"
"s_min_i32 s33, s47, 0\n"
"ds_read_b128 a[216:219], v0 offset:18432\n"
"v_add_f32 v18, v18, v66\n"
"v_mfma_f32_16x16x32_bf16 v[144:147], a[208:211], v[224:227], v[144:147]\n"
"s_max_i32 s33, s33, s32\n"
"v_exp_f32 v69, v69\n"
"v_mfma_f32_16x16x32_bf16 v[144:147], a[212:215], v[240:243], v[144:147]\n"
"s_max_i32 s47, s47, 0\n"
"v_add_f32 v18, v18, v67\n"
"v_cvt_pk_bf16_f32 v131, v66, v67\n"
"v_mfma_f32_16x16x32_bf16 v[148:151], a[212:215], v[244:247], v[52:55]\n"
"s_min_i32 s47, s47, 150\n"
"ds_read_b128 a[220:223], v1 offset:18432\n"
"v_add_f32 v19, v19, v68\n"
"v_mfma_f32_16x16x32_bf16 v[148:151], a[208:211], v[228:231], v[148:151]\n"
"s_add_u32 s97, s0, s47\n"
"v_exp_f32 v70, v70\n"
"v_mfma_f32_16x16x32_bf16 v[148:151], a[204:207], v[212:215], v[148:151]\n"
"s_add_u32 s97, s97, s99\n"
"v_exp_f32 v71, v71\n"
"v_mfma_f32_16x16x32_bf16 v[148:151], a[200:203], v[196:199], v[148:151]\n"
"s_add_u32 s97, s97, s79\n"
"ds_read_b128 a[224:227], v0 offset:18560\n"
"v_add_f32 v19, v19, v69\n"
"v_mfma_f32_16x16x32_bf16 v[148:151], a[196:199], v[180:183], v[148:151]\n"
"v_exp_f32 v72, v72\n"
"v_mfma_f32_16x16x32_bf16 v[148:151], a[192:195], v[164:167], v[148:151]\n"
"v_cvt_pk_bf16_f32 v134, v68, v69\n"
"v_add_f32 v19, v19, v70\n"
"v_mfma_f32_16x16x32_bf16 v[152:155], a[192:195], v[168:171], v[56:59]\n"
"ds_read_b128 a[228:231], v1 offset:18560\n"
"v_add_f32 v19, v19, v71\n"
"v_mfma_f32_16x16x32_bf16 v[152:155], a[196:199], v[184:187], v[152:155]\n"
"v_exp_f32 v73, v73\n"
"v_mfma_f32_16x16x32_bf16 v[152:155], a[200:203], v[200:203], v[152:155]\n"
"buffer_load_dwordx4 v8, s[8:11], s42 offen offset:2048 nt lds\n"
"v_mfma_f32_16x16x32_bf16 v[152:155], a[204:207], v[216:219], v[152:155]\n"
"ds_read_b128 a[232:235], v0 offset:18688\n"
"v_cvt_pk_bf16_f32 v135, v70, v71\n"
"v_mfma_f32_16x16x32_bf16 v[152:155], a[208:211], v[232:235], v[152:155]\n"
"v_exp_f32 v74, v74\n"
"v_mfma_f32_16x16x32_bf16 v[152:155], a[212:215], v[248:251], v[152:155]\n"
"v_exp_f32 v75, v75\n"
"v_mfma_f32_16x16x32_bf16 v[156:159], a[212:215], v[252:255], v[60:63]\n"
"s_add_u32 s40, s40, s44\n"
"ds_read_b128 a[236:239], v1 offset:18688\n"
"v_add_f32 v20, v20, v72\n"
"v_mfma_f32_16x16x32_bf16 v[156:159], a[208:211], v[236:239], v[156:159]\n"
"v_add_f32 v20, v20, v73\n"
"v_cvt_pk_bf16_f32 v138, v72, v73\n"
"v_mfma_f32_16x16x32_bf16 v[156:159], a[204:207], v[220:223], v[156:159]\n"
"v_exp_f32 v76, v76\n"
"v_mfma_f32_16x16x32_bf16 v[156:159], a[200:203], v[204:207], v[156:159]\n"
"v_add_u32 v0, s45, v0\n"
"v_add_f32 v20, v20, v74\n"
"v_mfma_f32_16x16x32_bf16 v[156:159], a[196:199], v[188:191], v[156:159]\n"
"v_exp_f32 v77, v77\n"
"v_mfma_f32_16x16x32_bf16 v[156:159], a[192:195], v[172:175], v[156:159]\n"
"s_waitcnt lgkmcnt(0)\n"
"v_add_f32 v20, v20, v75\n"
"v_cvt_pk_bf16_f32 v139, v74, v75\n"
"v_mfma_f32_16x16x32_bf16 v[80:83], a[216:219], v[160:163], v[48:51]\n"
"v_add_u32 v1, s45, v1\n"
"v_add_f32 v21, v21, v76\n"
"v_mfma_f32_16x16x32_bf16 v[80:83], a[220:223], v[176:179], v[80:83]\n"
"v_exp_f32 v78, v78\n"
"v_mfma_f32_16x16x32_bf16 v[80:83], a[224:227], v[192:195], v[80:83]\n"
"v_exp_f32 v79, v79\n"
"v_mfma_f32_16x16x32_bf16 v[80:83], a[228:231], v[208:211], v[80:83]\n"
"v_add_f32 v21, v21, v77\n"
"v_cvt_pk_bf16_f32 v142, v76, v77\n"
"v_mfma_f32_16x16x32_bf16 v[80:83], a[232:235], v[224:227], v[80:83]\n"
"v_exp_f32 v144, v144\n"
"v_mfma_f32_16x16x32_bf16 v[80:83], a[236:239], v[240:243], v[80:83]\n"
"v_exp_f32 v145, v145\n"
"v_mfma_f32_16x16x32_bf16 v[84:87], a[236:239], v[244:247], v[52:55]\n"
"s_waitcnt vmcnt(31)\n"
"v_add_f32 v21, v21, v78\n"
"v_add_f32 v21, v21, v79\n"
"v_mfma_f32_16x16x32_bf16 v[84:87], a[232:235], v[228:231], v[84:87]\n"
"s_barrier\n"
"v_exp_f32 v146, v146\n"
"v_mfma_f32_16x16x32_bf16 v[84:87], a[228:231], v[212:215], v[84:87]\n"
"v_cvt_pk_bf16_f32 v143, v78, v79\n"
"v_add_f32 v18, v18, v144\n"
"v_mfma_f32_16x16x32_bf16 v[84:87], a[224:227], v[196:199], v[84:87]\n"
"ds_read_b64_tr_b16 a[240:241], v2 offset:0\n"
"v_add_f32 v18, v18, v145\n"
"v_mfma_f32_16x16x32_bf16 v[84:87], a[220:223], v[180:183], v[84:87]\n"
"v_exp_f32 v147, v147\n"
"v_mfma_f32_16x16x32_bf16 v[84:87], a[216:219], v[164:167], v[84:87]\n"
"ds_read_b64_tr_b16 a[242:243], v2 offset:6144\n"
"v_cvt_pk_bf16_f32 v144, v144, v145\n"
"v_mfma_f32_16x16x32_bf16 v[88:91], a[216:219], v[168:171], v[56:59]\n"
"v_exp_f32 v148, v148\n"
"v_mfma_f32_16x16x32_bf16 v[88:91], a[220:223], v[184:187], v[88:91]\n"
"ds_read_b64_tr_b16 a[244:245], v2 offset:12288\n"
"v_add_f32 v18, v18, v146\n"
"v_mfma_f32_16x16x32_bf16 v[88:91], a[224:227], v[200:203], v[88:91]\n"
"L_pg6:\n"
"s_cbranch_execz L_bk1\n"
"v_exp_f32 v149, v149\n"
"v_mfma_f32_16x16x32_bf16 v[88:91], a[228:231], v[216:219], v[88:91]\n"
"ds_read_b64_tr_b16 a[246:247], v2 offset:18432\n"
"v_add_f32 v18, v18, v147\n"
"v_mfma_f32_16x16x32_bf16 v[88:91], a[232:235], v[232:235], v[88:91]\n"
"v_exp_f32 v150, v150\n"
"v_mfma_f32_16x16x32_bf16 v[88:91], a[236:239], v[248:251], v[88:91]\n"
"ds_read_b64_tr_b16 a[248:249], v3 offset:0\n"
"v_cvt_pk_bf16_f32 v145, v146, v147\n"
"v_mfma_f32_16x16x32_bf16 v[92:95], a[236:239], v[252:255], v[60:63]\n"
"v_exp_f32 v151, v151\n"
"v_mfma_f32_16x16x32_bf16 v[92:95], a[232:235], v[236:239], v[92:95]\n"
"ds_read_b64_tr_b16 a[250:251], v3 offset:6144\n"
"v_add_f32 v19, v19, v148\n"
"v_mfma_f32_16x16x32_bf16 v[92:95], a[228:231], v[220:223], v[92:95]\n"
"v_add_f32 v19, v19, v149\n"
"v_cvt_pk_bf16_f32 v148, v148, v149\n"
"v_mfma_f32_16x16x32_bf16 v[92:95], a[224:227], v[204:207], v[92:95]\n"
"ds_read_b64_tr_b16 a[252:253], v3 offset:12288\n"
"v_add_f32 v19, v19, v150\n"
"v_mfma_f32_16x16x32_bf16 v[92:95], a[220:223], v[188:191], v[92:95]\n"
"v_exp_f32 v152, v152\n"
"v_mfma_f32_16x16x32_bf16 v[92:95], a[216:219], v[172:175], v[92:95]\n"
"s_waitcnt lgkmcnt(3)\n"
"ds_read_b64_tr_b16 a[254:255], v3 offset:18432\n"
"v_add_f32 v19, v19, v151\n"
"v_mfma_f32_16x16x32_bf16 a[0:3], a[240:243], v[96:99], 1.0\n"
"v_exp_f32 v153, v153\n"
"v_mfma_f32_16x16x32_bf16 a[0:3], a[244:247], v[112:115], a[0:3]\n"
"s_add_u32 m0, s41, 0\n"
"ds_read_b64_tr_b16 a[216:217], v4 offset:0\n"
"v_cvt_pk_bf16_f32 v149, v150, v151\n"
"v_mfma_f32_16x16x32_bf16 a[4:7], a[240:243], v[100:103], 1.0\n"
"buffer_load_dwordx4 v9, s[12:15], s43 offen offset:0 lds\n"
"v_mfma_f32_16x16x32_bf16 a[4:7], a[244:247], v[116:119], a[4:7]\n"
"ds_read_b64_tr_b16 a[218:219], v4 offset:6144\n"
"v_add_f32 v20, v20, v152\n"
"v_mfma_f32_16x16x32_bf16 a[8:11], a[240:243], v[104:107], 1.0\n"
"v_exp_f32 v154, v154\n"
"v_mfma_f32_16x16x32_bf16 a[8:11], a[244:247], v[120:123], a[8:11]\n"
"ds_read_b64_tr_b16 a[220:221], v4 offset:12288\n"
"v_add_f32 v20, v20, v153\n"
"v_mfma_f32_16x16x32_bf16 a[12:15], a[240:243], v[108:111], 1.0\n"
"v_exp_f32 v155, v155\n"
"v_mfma_f32_16x16x32_bf16 a[12:15], a[244:247], v[124:127], a[12:15]\n"
"s_waitcnt lgkmcnt(3)\n"
"ds_read_b64_tr_b16 a[222:223], v4 offset:18432\n"
"v_cvt_pk_bf16_f32 v152, v152, v153\n"
"v_mfma_f32_16x16x32_bf16 a[16:19], a[248:251], v[96:99], 1.0\n"
"v_exp_f32 v156, v156\n"
"v_mfma_f32_16x16x32_bf16 a[16:19], a[252:255], v[112:115], a[16:19]\n"
"ds_read_b64_tr_b16 a[224:225], v5 offset:0\n"
"v_add_f32 v20, v20, v154\n"
"v_mfma_f32_16x16x32_bf16 a[20:23], a[248:251], v[100:103], 1.0\n"
"v_exp_f32 v157, v157\n"
"v_mfma_f32_16x16x32_bf16 a[20:23], a[252:255], v[116:119], a[20:23]\n"
"ds_read_b64_tr_b16 a[226:227], v5 offset:6144\n"
"v_add_f32 v20, v20, v155\n"
"v_mfma_f32_16x16x32_bf16 a[24:27], a[248:251], v[104:107], 1.0\n"
"v_exp_f32 v158, v158\n"
"v_mfma_f32_16x16x32_bf16 a[24:27], a[252:255], v[120:123], a[24:27]\n"
"ds_read_b64_tr_b16 a[228:229], v5 offset:12288\n"
"v_cvt_pk_bf16_f32 v153, v154, v155\n"
"v_mfma_f32_16x16x32_bf16 a[28:31], a[248:251], v[108:111], 1.0\n"
"buffer_load_dwordx4 v10, s[12:15], s43 offen offset:1024 lds\n"
"v_mfma_f32_16x16x32_bf16 a[28:31], a[252:255], v[124:127], a[28:31]\n"
"s_waitcnt lgkmcnt(3)\n"
"ds_read_b64_tr_b16 a[230:231], v5 offset:18432\n"
"v_add_f32 v21, v21, v156\n"
"v_mfma_f32_16x16x32_bf16 a[32:35], a[216:219], v[96:99], 1.0\n"
"v_exp_f32 v159, v159\n"
"v_mfma_f32_16x16x32_bf16 a[32:35], a[220:223], v[112:115], a[32:35]\n"
"ds_read_b64_tr_b16 a[240:241], v2 offset:128\n"
"v_add_f32 v21, v21, v157\n"
"v_mfma_f32_16x16x32_bf16 a[36:39], a[216:219], v[100:103], 1.0\n"
"v_exp_f32 v80, v80\n"
"v_mfma_f32_16x16x32_bf16 a[36:39], a[220:223], v[116:119], a[36:39]\n"
"ds_read_b64_tr_b16 a[242:243], v2 offset:6272\n"
"v_cvt_pk_bf16_f32 v156, v156, v157\n"
"v_mfma_f32_16x16x32_bf16 a[40:43], a[216:219], v[104:107], 1.0\n"
"v_exp_f32 v81, v81\n"
"v_mfma_f32_16x16x32_bf16 a[40:43], a[220:223], v[120:123], a[40:43]\n"
"ds_read_b64_tr_b16 a[244:245], v2 offset:12416\n"
"v_add_f32 v21, v21, v158\n"
"v_mfma_f32_16x16x32_bf16 a[44:47], a[216:219], v[108:111], 1.0\n"
"v_add_f32 v21, v21, v159\n"
"v_cvt_pk_bf16_f32 v157, v158, v159\n"
"v_mfma_f32_16x16x32_bf16 a[44:47], a[220:223], v[124:127], a[44:47]\n"
"s_waitcnt lgkmcnt(3)\n"
"ds_read_b64_tr_b16 a[246:247], v2 offset:18560\n"
"v_add_f32 v18, v18, v80\n"
"v_mfma_f32_16x16x32_bf16 a[48:51], a[224:227], v[96:99], 1.0\n"
"v_exp_f32 v82, v82\n"
"v_mfma_f32_16x16x32_bf16 a[48:51], a[228:231], v[112:115], a[48:51]\n"
"ds_read_b64_tr_b16 a[248:249], v3 offset:128\n"
"v_add_f32 v18, v18, v81\n"
"v_mfma_f32_16x16x32_bf16 a[52:55], a[224:227], v[100:103], 1.0\n"
"buffer_load_dwordx4 v11, s[12:15], s43 offen offset:2048 lds\n"
"v_mfma_f32_16x16x32_bf16 a[52:55], a[228:231], v[116:119], a[52:55]\n"
"ds_read_b64_tr_b16 a[250:251], v3 offset:6272\n"
"v_cvt_pk_bf16_f32 v146, v80, v81\n"
"v_mfma_f32_16x16x32_bf16 a[56:59], a[224:227], v[104:107], 1.0\n"
"s_add_u32 s43, s43, 27648\n"
"v_exp_f32 v83, v83\n"
"v_mfma_f32_16x16x32_bf16 a[56:59], a[228:231], v[120:123], a[56:59]\n"
"ds_read_b64_tr_b16 a[252:253], v3 offset:12416\n"
"v_add_f32 v18, v18, v82\n"
"v_mfma_f32_16x16x32_bf16 a[60:63], a[224:227], v[108:111], 1.0\n"
"s_or_b32 s43, s43, 0x18000\n"
"v_exp_f32 v84, v84\n"
"v_mfma_f32_16x16x32_bf16 a[60:63], a[228:231], v[124:127], a[60:63]\n"
"s_waitcnt lgkmcnt(3)\n"
"ds_read_b64_tr_b16 a[254:255], v3 offset:18560\n"
"v_add_f32 v18, v18, v83\n"
"v_mfma_f32_16x16x32_bf16 a[64:67], a[240:243], v[96:99], 1.0\n"
"s_sub_u32 s62, s43, 0x60000\n"
"v_exp_f32 v85, v85\n"
"v_mfma_f32_16x16x32_bf16 a[64:67], a[244:247], v[112:115], a[64:67]\n"
"ds_read_b64_tr_b16 a[216:217], v4 offset:128\n"
"v_cvt_pk_bf16_f32 v147, v82, v83\n"
"v_mfma_f32_16x16x32_bf16 a[68:71], a[240:243], v[100:103], 1.0\n"
"s_min_u32 s43, s43, s62\n"
"v_exp_f32 v86, v86\n"
"v_mfma_f32_16x16x32_bf16 a[68:71], a[244:247], v[116:119], a[68:71]\n"
"ds_read_b64_tr_b16 a[218:219], v4 offset:6272\n"
"v_add_f32 v19, v19, v84\n"
"v_mfma_f32_16x16x32_bf16 a[72:75], a[240:243], v[104:107], 1.0\n"
"v_exp_f32 v87, v87\n"
"v_mfma_f32_16x16x32_bf16 a[72:75], a[244:247], v[120:123], a[72:75]\n"
"s_add_u32 m0, s41, 12288\n"
"ds_read_b64_tr_b16 a[220:221], v4 offset:12416\n"
"v_add_f32 v19, v19, v85\n"
"v_mfma_f32_16x16x32_bf16 a[76:79], a[240:243], v[108:111], 1.0\n"
"buffer_load_dwordx4 v9, s[12:15], s43 offen offset:0 lds\n"
"v_mfma_f32_16x16x32_bf16 a[76:79], a[244:247], v[124:127], a[76:79]\n"
"s_waitcnt lgkmcnt(3)\n"
"ds_read_b64_tr_b16 a[222:223], v4 offset:18560\n"
"v_cvt_pk_bf16_f32 v150, v84, v85\n"
"v_mfma_f32_16x16x32_bf16 a[80:83], a[248:251], v[96:99], 1.0\n"
"v_exp_f32 v88, v88\n"
"v_mfma_f32_16x16x32_bf16 a[80:83], a[252:255], v[112:115], a[80:83]\n"
"ds_read_b64_tr_b16 a[224:225], v5 offset:128\n"
"v_add_f32 v19, v19, v86\n"
"v_mfma_f32_16x16x32_bf16 a[84:87], a[248:251], v[100:103], 1.0\n"
"v_exp_f32 v89, v89\n"
"v_mfma_f32_16x16x32_bf16 a[84:87], a[252:255], v[116:119], a[84:87]\n"
"ds_read_b64_tr_b16 a[226:227], v5 offset:6272\n"
"v_add_f32 v19, v19, v87\n"
"v_mfma_f32_16x16x32_bf16 a[88:91], a[248:251], v[104:107], 1.0\n"
"v_exp_f32 v90, v90\n"
"v_mfma_f32_16x16x32_bf16 a[88:91], a[252:255], v[120:123], a[88:91]\n"
"ds_read_b64_tr_b16 a[228:229], v5 offset:12416\n"
"v_cvt_pk_bf16_f32 v151, v86, v87\n"
"v_mfma_f32_16x16x32_bf16 a[92:95], a[248:251], v[108:111], 1.0\n"
"v_exp_f32 v91, v91\n"
"v_mfma_f32_16x16x32_bf16 a[92:95], a[252:255], v[124:127], a[92:95]\n"
"s_waitcnt lgkmcnt(3)\n"
"ds_read_b64_tr_b16 a[230:231], v5 offset:18560\n"
"v_add_f32 v20, v20, v88\n"
"v_mfma_f32_16x16x32_bf16 a[96:99], a[216:219], v[96:99], 1.0\n"
"v_add_f32 v20, v20, v89\n"
"v_cvt_pk_bf16_f32 v154, v88, v89\n"
"v_mfma_f32_16x16x32_bf16 a[96:99], a[220:223], v[112:115], a[96:99]\n"
"ds_read_b64_tr_b16 a[240:241], v2 offset:256\n"
"v_add_f32 v20, v20, v90\n"
"v_mfma_f32_16x16x32_bf16 a[100:103], a[216:219], v[100:103], 1.0\n"
"buffer_load_dwordx4 v10, s[12:15], s43 offen offset:1024 nt lds\n"
"v_mfma_f32_16x16x32_bf16 a[100:103], a[220:223], v[116:119], a[100:103]\n"
"ds_read_b64_tr_b16 a[242:243], v2 offset:6400\n"
"v_add_f32 v20, v20, v91\n"
"v_mfma_f32_16x16x32_bf16 a[104:107], a[216:219], v[104:107], 1.0\n"
"v_exp_f32 v92, v92\n"
"v_mfma_f32_16x16x32_bf16 a[104:107], a[220:223], v[120:123], a[104:107]\n"
"ds_read_b64_tr_b16 a[244:245], v2 offset:12544\n"
"v_cvt_pk_bf16_f32 v155, v90, v91\n"
"v_mfma_f32_16x16x32_bf16 a[108:111], a[216:219], v[108:111], 1.0\n"
"v_exp_f32 v93, v93\n"
"v_mfma_f32_16x16x32_bf16 a[108:111], a[220:223], v[124:127], a[108:111]\n"
"s_waitcnt lgkmcnt(3)\n"
"ds_read_b64_tr_b16 a[246:247], v2 offset:18688\n"
"v_add_f32 v21, v21, v92\n"
"v_mfma_f32_16x16x32_bf16 a[112:115], a[224:227], v[96:99], 1.0\n"
"v_exp_f32 v94, v94\n"
"v_mfma_f32_16x16x32_bf16 a[112:115], a[228:231], v[112:115], a[112:115]\n"
"ds_read_b64_tr_b16 a[248:249], v3 offset:256\n"
"v_add_f32 v21, v21, v93\n"
"v_mfma_f32_16x16x32_bf16 a[116:119], a[224:227], v[100:103], 1.0\n"
"v_exp_f32 v95, v95\n"
"v_mfma_f32_16x16x32_bf16 a[116:119], a[228:231], v[116:119], a[116:119]\n"
"ds_read_b64_tr_b16 a[250:251], v3 offset:6400\n"
"v_cvt_pk_bf16_f32 v158, v92, v93\n"
"v_mfma_f32_16x16x32_bf16 a[120:123], a[224:227], v[104:107], 1.0\n"
"v_add_f32 v21, v21, v94\n"
"v_add_f32 v21, v21, v95\n"
"v_mfma_f32_16x16x32_bf16 a[120:123], a[228:231], v[120:123], a[120:123]\n"
"ds_read_b64_tr_b16 a[252:253], v3 offset:12544\n"
"v_cvt_pk_bf16_f32 v159, v94, v95\n"
"v_mfma_f32_16x16x32_bf16 a[124:127], a[224:227], v[108:111], 1.0\n"
"buffer_load_dwordx4 v11, s[12:15], s43 offen offset:2048 nt lds\n"
"v_mfma_f32_16x16x32_bf16 a[124:127], a[228:231], v[124:127], a[124:127]\n"
"s_waitcnt lgkmcnt(3)\n"
"ds_read_b64_tr_b16 a[254:255], v3 offset:18688\n"
"v_max3_f32 v22, v18, v19, v20\n"
"v_mfma_f32_16x16x32_bf16 a[128:131], a[240:243], v[96:99], 1.0\n"
"v_max_f32 v22, v22, v21\n"
"v_cmp_lt_f32 vcc, s54, v22\n"
"v_mfma_f32_16x16x32_bf16 a[128:131], a[244:247], v[112:115], a[128:131]\n"
"ds_read_b64_tr_b16 a[216:217], v4 offset:256\n"
"v_mfma_f32_16x16x32_bf16 a[132:135], a[240:243], v[100:103], 1.0\n"
"s_add_u32 s41, s41, s46\n"
"v_mfma_f32_16x16x32_bf16 a[132:135], a[244:247], v[116:119], a[132:135]\n"
"ds_read_b64_tr_b16 a[218:219], v4 offset:6400\n"
"v_mfma_f32_16x16x32_bf16 a[136:139], a[240:243], v[104:107], 1.0\n"
"s_add_u32 s42, s42, 27648\n"
"v_mfma_f32_16x16x32_bf16 a[136:139], a[244:247], v[120:123], a[136:139]\n"
"ds_read_b64_tr_b16 a[220:221], v4 offset:12544\n"
"v_mfma_f32_16x16x32_bf16 a[140:143], a[240:243], v[108:111], 1.0\n"
"s_or_b32 s42, s42, 0x18000\n"
"v_mfma_f32_16x16x32_bf16 a[140:143], a[244:247], v[124:127], a[140:143]\n"
"s_waitcnt lgkmcnt(3)\n"
"ds_read_b64_tr_b16 a[222:223], v4 offset:18688\n"
"v_mfma_f32_16x16x32_bf16 a[144:147], a[248:251], v[96:99], 1.0\n"
"s_sub_u32 s62, s42, 0x60000\n"
"v_mfma_f32_16x16x32_bf16 a[144:147], a[252:255], v[112:115], a[144:147]\n"
"ds_read_b64_tr_b16 a[224:225], v5 offset:256\n"
"v_mfma_f32_16x16x32_bf16 a[148:151], a[248:251], v[100:103], 1.0\n"
"s_min_u32 s42, s42, s62\n"
"s_cmp_eq_u32 s49, 0\n"
"s_cbranch_scc1 L_nost\n"
"buffer_load_dwordx4 v[28:31], v12, s[24:27], s94 offen offset:0\n"
"buffer_load_dwordx4 v[32:35], v12, s[24:27], s94 offen offset:1024\n"
"L_nost:\n"
"v_mfma_f32_16x16x32_bf16 a[148:151], a[252:255], v[116:119], a[148:151]\n"
"ds_read_b64_tr_b16 a[226:227], v5 offset:6400\n"
"s_mov_b64 exec, 0\n"
"s_cmp_eq_u32 s38, 0\n"
"s_cbranch_scc1 L_pg8\n"
"s_cmp_eq_u32 s38, 3072\n"
"s_cbranch_scc1 L_pg9\n"
"s_cmp_eq_u32 s38, 6144\n"
"s_cbranch_scc1 L_pg10\n"
"s_branch L_pg11\n"
"L_bk2:\n"
"s_mov_b64 exec, -1\n"
"v_mfma_f32_16x16x32_bf16 a[152:155], a[248:251], v[104:107], 1.0\n"
"s_add_u32 s43, s43, 27648\n"
"v_mfma_f32_16x16x32_bf16 a[152:155], a[252:255], v[120:123], a[152:155]\n"
"s_waitcnt vmcnt(18)\n"
"ds_read_b64_tr_b16 a[228:229], v5 offset:12544\n"
"v_mfma_f32_16x16x32_bf16 a[156:159], a[248:251], v[108:111], 1.0\n"
"s_barrier\n"
"s_or_b32 s43, s43, 0x18000\n"
"v_mfma_f32_16x16x32_bf16 a[156:159], a[252:255], v[124:127], a[156:159]\n"
"s_waitcnt lgkmcnt(3)\n"
"ds_read_b64_tr_b16 a[230:231], v5 offset:18688\n"
"v_mfma_f32_16x16x32_bf16 a[160:163], a[216:219], v[96:99], 1.0\n"
"ds_read_b128 a[192:195], v0 offset:0\n"
"v_mfma_f32_16x16x32_bf16 a[160:163], a[220:223], v[112:115], a[160:163]\n"
"s_sub_u32 s62, s43, 0x60000\n"
"v_mfma_f32_16x16x32_bf16 a[164:167], a[216:219], v[100:103], 1.0\n"
"ds_read_b128 a[196:199], v1 offset:0\n"
"v_mfma_f32_16x16x32_bf16 a[164:167], a[220:223], v[116:119], a[164:167]\n"
"s_min_u32 s43, s43, s62\n"
"v_mfma_f32_16x16x32_bf16 a[168:171], a[216:219], v[104:107], 1.0\n"
"s_mov_b32 s47, s44\n"
"ds_read_b128 a[200:203], v0 offset:128\n"
"v_mfma_f32_16x16x32_bf16 a[168:171], a[220:223], v[120:123], a[168:171]\n"
"s_mov_b32 s44, s45\n"
"v_mfma_f32_16x16x32_bf16 a[172:175], a[216:219], v[108:111], 1.0\n"
"s_mov_b32 s45, s46\n"
"ds_read_b128 a[204:207], v1 offset:128\n"
"v_mfma_f32_16x16x32_bf16 a[172:175], a[220:223], v[124:127], a[172:175]\n"
"s_waitcnt lgkmcnt(4)\n"
"s_mov_b32 s46, s47\n"
"v_mfma_f32_16x16x32_bf16 a[176:179], a[224:227], v[96:99], 1.0\n"
"ds_read_b128 a[208:211], v0 offset:256\n"
"v_mfma_f32_16x16x32_bf16 a[176:179], a[228:231], v[112:115], a[176:179]\n"
"v_mfma_f32_16x16x32_bf16 a[180:183], a[224:227], v[100:103], 1.0\n"
"ds_read_b128 a[212:215], v1 offset:256\n"
"v_mfma_f32_16x16x32_bf16 a[180:183], a[228:231], v[116:119], a[180:183]\n"
"v_mfma_f32_16x16x32_bf16 a[184:187], a[224:227], v[104:107], 1.0\n"
"v_mfma_f32_16x16x32_bf16 a[184:187], a[228:231], v[120:123], a[184:187]\n"
"s_cmp_lg_u64 vcc, 0\n"
"v_mfma_f32_16x16x32_bf16 a[188:191], a[224:227], v[108:111], 1.0\n"
"s_waitcnt lgkmcnt(3)\n"
"v_mfma_f32_16x16x32_bf16 a[188:191], a[228:231], v[124:127], a[188:191]\n"
"s_cbranch_scc1 L_rc1\n"
"L_rr1:\n"
"s_mov_b32 s48, 6\n"
"s_cmp_eq_u32 s99, 0\n"
"s_cbranch_scc1 L_loopw\n"
"s_branch L_loop\n"
"L_pwa:\n"
"s_sleep 1\n"
"s_sub_u32 s47, s47, 4\n"
"s_cmp_gt_i32 s47, 0\n"
"s_cbranch_scc1 L_pwa\n"
"s_branch L_pra\n"
"L_pwb:\n"
"s_sleep 1\n"
"s_sub_u32 s47, s47, 4\n"
"s_cmp_gt_i32 s47, 0\n"
"s_cbranch_scc1 L_pwb\n"
"s_branch L_prb\n"
"L_cls:\n"
"s_sub_u32 s52, s52, s64\n"
"s_sub_u32 s53, s0, s66\n"
"s_mul_i32 s52, s52, 10\n"
"s_mul_i32 s53, s53, s65\n"
"s_mov_b32 s65, 0\n"
"s_cmp_gt_u32 s52, s53\n"
"s_cselect_b32 s99, 195, s99\n"
"s_cselect_b32 s67, 1630, s67\n"
"s_cselect_b32 s52, 0, 0\n"
"s_add_u32 s97, s97, s52\n"
"s_branch L_clr\n"
".p2align 6\n"
"L_loop:\n"
"v_mfma_f32_16x16x32_bf16 v[96:99], a[192:195], v[160:163], v[48:51]\n"
"v_add_f32 v14, v14, v18\n"
"v_add_f32 v15, v15, v19\n"
"v_mfma_f32_16x16x32_bf16 v[96:99], a[196:199], v[176:179], v[96:99]\n"
"s_add_u32 m0, s40, 0\n"
"v_add_f32 v16, v16, v20\n"
"v_add_f32 v17, v17, v21\n"
"v_mfma_f32_16x16x32_bf16 v[96:99], a[200:203], v[192:195], v[96:99]\n"
"s_waitcnt lgkmcnt(0)\n"
"buffer_load_dwordx4 v6, s[8:11], s42 offen offset:0 nt lds\n"
"v_mfma_f32_16x16x32_bf16 v[96:99], a[204:207], v[208:211], v[96:99]\n"
"ds_read_b128 a[216:219], v0 offset:6144\n"
"v_add_u32 v2, s46, v2\n"
"v_mfma_f32_16x16x32_bf16 v[96:99], a[208:211], v[224:227], v[96:99]\n"
"s_add_u32 s55, s55, 18\n"
"v_add_u32 v3, s46, v3\n"
"v_add_u32 v4, s46, v4\n"
"v_mfma_f32_16x16x32_bf16 v[96:99], a[212:215], v[240:243], v[96:99]\n"
"v_add_u32 v5, s46, v5\n"
"v_mfma_f32_16x16x32_bf16 v[100:103], a[212:215], v[244:247], v[52:55]\n"
"ds_read_b128 a[220:223], v1 offset:6144\n"
"v_mfma_f32_16x16x32_bf16 v[100:103], a[208:211], v[228:231], v[100:103]\n"
"v_mfma_f32_16x16x32_bf16 v[100:103], a[204:207], v[212:215], v[100:103]\n"
"v_mfma_f32_16x16x32_bf16 v[100:103], a[200:203], v[196:199], v[100:103]\n"
"ds_read_b128 a[224:227], v0 offset:6272\n"
"v_mfma_f32_16x16x32_bf16 v[100:103], a[196:199], v[180:183], v[100:103]\n"
"v_exp_f32 v96, v96\n"
"v_mfma_f32_16x16x32_bf16 v[100:103], a[192:195], v[164:167], v[100:103]\n"
"v_exp_f32 v97, v97\n"
"v_mfma_f32_16x16x32_bf16 v[104:107], a[192:195], v[168:171], v[56:59]\n"
"ds_read_b128 a[228:231], v1 offset:6272\n"
"v_add_f32 v18, v96, v97\n"
"v_mfma_f32_16x16x32_bf16 v[104:107], a[196:199], v[184:187], v[104:107]\n"
"v_exp_f32 v98, v98\n"
"v_mfma_f32_16x16x32_bf16 v[104:107], a[200:203], v[200:203], v[104:107]\n"
"buffer_load_dwordx4 v7, s[8:11], s42 offen offset:1024 nt lds\n"
"v_mfma_f32_16x16x32_bf16 v[104:107], a[204:207], v[216:219], v[104:107]\n"
"ds_read_b128 a[232:235], v0 offset:6400\n"
"v_cvt_pk_bf16_f32 v96, v96, v97\n"
"v_mfma_f32_16x16x32_bf16 v[104:107], a[208:211], v[232:235], v[104:107]\n"
"v_exp_f32 v99, v99\n"
"v_mfma_f32_16x16x32_bf16 v[104:107], a[212:215], v[248:251], v[104:107]\n"
"v_exp_f32 v100, v100\n"
"v_mfma_f32_16x16x32_bf16 v[108:111], a[212:215], v[252:255], v[60:63]\n"
"ds_read_b128 a[236:239], v1 offset:6400\n"
"v_add_f32 v18, v18, v98\n"
"v_mfma_f32_16x16x32_bf16 v[108:111], a[208:211], v[236:239], v[108:111]\n"
"v_exp_f32 v101, v101\n"
"v_mfma_f32_16x16x32_bf16 v[108:111], a[204:207], v[220:223], v[108:111]\n"
"v_exp_f32 v102, v102\n"
"v_mfma_f32_16x16x32_bf16 v[108:111], a[200:203], v[204:207], v[108:111]\n"
"v_add_f32 v18, v18, v99\n"
"v_cvt_pk_bf16_f32 v97, v98, v99\n"
"v_mfma_f32_16x16x32_bf16 v[108:111], a[196:199], v[188:191], v[108:111]\n"
"v_exp_f32 v103, v103\n"
"v_mfma_f32_16x16x32_bf16 v[108:111], a[192:195], v[172:175], v[108:111]\n"
"s_waitcnt lgkmcnt(0)\n"
"v_add_f32 v19, v100, v101\n"
"v_cvt_pk_bf16_f32 v100, v100, v101\n"
"v_mfma_f32_16x16x32_bf16 v[64:67], a[216:219], v[160:163], v[48:51]\n"
"v_exp_f32 v104, v104\n"
"v_mfma_f32_16x16x32_bf16 v[64:67], a[220:223], v[176:179], v[64:67]\n"
"v_exp_f32 v105, v105\n"
"v_mfma_f32_16x16x32_bf16 v[64:67], a[224:227], v[192:195], v[64:67]\n"
"buffer_load_dwordx4 v8, s[8:11], s42 offen offset:2048 nt lds\n"
"v_mfma_f32_16x16x32_bf16 v[64:67], a[228:231], v[208:211], v[64:67]\n"
"ds_read_b128 a[192:195], v0 offset:12288\n"
"v_add_f32 v19, v19, v102\n"
"v_mfma_f32_16x16x32_bf16 v[64:67], a[232:235], v[224:227], v[64:67]\n"
"s_add_u32 s42, s42, 27648\n"
"v_exp_f32 v106, v106\n"
"v_mfma_f32_16x16x32_bf16 v[64:67], a[236:239], v[240:243], v[64:67]\n"
"s_or_b32 s42, s42, 0x18000\n"
"v_add_f32 v19, v19, v103\n"
"v_cvt_pk_bf16_f32 v101, v102, v103\n"
"v_mfma_f32_16x16x32_bf16 v[68:71], a[236:239], v[244:247], v[52:55]\n"
"ds_read_b128 a[196:199], v1 offset:12288\n"
"v_add_f32 v20, v104, v105\n"
"v_mfma_f32_16x16x32_bf16 v[68:71], a[232:235], v[228:231], v[68:71]\n"
"s_sub_u32 s62, s42, 0x60000\n"
"v_exp_f32 v107, v107\n"
"v_mfma_f32_16x16x32_bf16 v[68:71], a[228:231], v[212:215], v[68:71]\n"
"s_memrealtime s[0:1]\n"
"v_exp_f32 v108, v108\n"
"v_mfma_f32_16x16x32_bf16 v[68:71], a[224:227], v[196:199], v[68:71]\n"
"ds_read_b128 a[200:203], v0 offset:12416\n"
"v_cvt_pk_bf16_f32 v104, v104, v105\n"
"v_mfma_f32_16x16x32_bf16 v[68:71], a[220:223], v[180:183], v[68:71]\n"
"s_min_u32 s42, s42, s62\n"
"v_exp_f32 v109, v109\n"
"v_mfma_f32_16x16x32_bf16 v[68:71], a[216:219], v[164:167], v[68:71]\n"
"v_add_f32 v20, v20, v106\n"
"v_add_f32 v20, v20, v107\n"
"v_mfma_f32_16x16x32_bf16 v[72:75], a[216:219], v[168:171], v[56:59]\n"
"ds_read_b128 a[204:207], v1 offset:12416\n"
"v_cvt_pk_bf16_f32 v105, v106, v107\n"
"v_mfma_f32_16x16x32_bf16 v[72:75], a[220:223], v[184:187], v[72:75]\n"
"s_add_u32 m0, s40, 12288\n"
"v_exp_f32 v110, v110\n"
"v_mfma_f32_16x16x32_bf16 v[72:75], a[224:227], v[200:203], v[72:75]\n"
"buffer_load_dwordx4 v6, s[8:11], s42 offen offset:0 nt lds\n"
"v_mfma_f32_16x16x32_bf16 v[72:75], a[228:231], v[216:219], v[72:75]\n"
"ds_read_b128 a[208:211], v0 offset:12544\n"
"v_add_f32 v21, v108, v109\n"
"v_mfma_f32_16x16x32_bf16 v[72:75], a[232:235], v[232:235], v[72:75]\n"
"s_sub_u32 s79, s79, 4\n"
"v_exp_f32 v111, v111\n"
"v_mfma_f32_16x16x32_bf16 v[72:75], a[236:239], v[248:251], v[72:75]\n"
"s_max_i32 s79, s79, 0\n"
"v_exp_f32 v64, v64\n"
"v_mfma_f32_16x16x32_bf16 v[76:79], a[236:239], v[252:255], v[60:63]\n"
"ds_read_b128 a[212:215], v1 offset:12544\n"
"v_cvt_pk_bf16_f32 v108, v108, v109\n"
"v_mfma_f32_16x16x32_bf16 v[76:79], a[232:235], v[236:239], v[76:79]\n"
"v_exp_f32 v65, v65\n"
"v_mfma_f32_16x16x32_bf16 v[76:79], a[228:231], v[220:223], v[76:79]\n"
"v_add_f32 v21, v21, v110\n"
"v_add_f32 v21, v21, v111\n"
"v_mfma_f32_16x16x32_bf16 v[76:79], a[224:227], v[204:207], v[76:79]\n"
"v_exp_f32 v66, v66\n"
"v_mfma_f32_16x16x32_bf16 v[76:79], a[220:223], v[188:191], v[76:79]\n"
"v_cvt_pk_bf16_f32 v109, v110, v111\n"
"v_add_f32 v18, v18, v64\n"
"v_mfma_f32_16x16x32_bf16 v[76:79], a[216:219], v[172:175], v[76:79]\n"
"s_waitcnt lgkmcnt(0)\n"
"v_exp_f32 v67, v67\n"
"v_mfma_f32_16x16x32_bf16 v[112:115], a[192:195], v[160:163], v[48:51]\n"
"v_add_f32 v18, v18, v65\n"
"v_cvt_pk_bf16_f32 v98, v64, v65\n"
"v_mfma_f32_16x16x32_bf16 v[112:115], a[196:199], v[176:179], v[112:115]\n"
"v_exp_f32 v68, v68\n"
"v_mfma_f32_16x16x32_bf16 v[112:115], a[200:203], v[192:195], v[112:115]\n"
"buffer_load_dwordx4 v7, s[8:11], s42 offen offset:1024 nt lds\n"
"v_mfma_f32_16x16x32_bf16 v[112:115], a[204:207], v[208:211], v[112:115]\n"
"ds_read_b128 a[216:219], v0 offset:18432\n"
"v_add_f32 v18, v18, v66\n"
"v_mfma_f32_16x16x32_bf16 v[112:115], a[208:211], v[224:227], v[112:115]\n"
"v_exp_f32 v69, v69\n"
"v_mfma_f32_16x16x32_bf16 v[112:115], a[212:215], v[240:243], v[112:115]\n"
"v_add_f32 v18, v18, v67\n"
"v_cvt_pk_bf16_f32 v99, v66, v67\n"
"v_mfma_f32_16x16x32_bf16 v[116:119], a[212:215], v[244:247], v[52:55]\n"
"ds_read_b128 a[220:223], v1 offset:18432\n"
"v_add_f32 v19, v19, v68\n"
"v_mfma_f32_16x16x32_bf16 v[116:119], a[208:211], v[228:231], v[116:119]\n"
"v_exp_f32 v70, v70\n"
"v_mfma_f32_16x16x32_bf16 v[116:119], a[204:207], v[212:215], v[116:119]\n"
"s_sub_u32 s47, s97, s0\n"
"v_exp_f32 v71, v71\n"
"v_mfma_f32_16x16x32_bf16 v[116:119], a[200:203], v[196:199], v[116:119]\n"
"s_add_u32 s47, s47, s33\n"
"ds_read_b128 a[224:227], v0 offset:18560\n"
"v_add_f32 v19, v19, v69\n"
"v_mfma_f32_16x16x32_bf16 v[116:119], a[196:199], v[180:183], v[116:119]\n"
"s_min_i32 s33, s47, 0\n"
"v_exp_f32 v72, v72\n"
"v_mfma_f32_16x16x32_bf16 v[116:119], a[192:195], v[164:167], v[116:119]\n"
"s_max_i32 s33, s33, s32\n"
"v_cvt_pk_bf16_f32 v102, v68, v69\n"
"v_add_f32 v19, v19, v70\n"
"v_mfma_f32_16x16x32_bf16 v[120:123], a[192:195], v[168:171], v[56:59]\n"
"s_max_i32 s47, s47, 0\n"
"ds_read_b128 a[228:231], v1 offset:18560\n"
"v_add_f32 v19, v19, v71\n"
"v_mfma_f32_16x16x32_bf16 v[120:123], a[196:199], v[184:187], v[120:123]\n"
"s_add_u32 s97, s0, s47\n"
"v_exp_f32 v73, v73\n"
"v_mfma_f32_16x16x32_bf16 v[120:123], a[200:203], v[200:203], v[120:123]\n"
"buffer_load_dwordx4 v8, s[8:11], s42 offen offset:2048 nt lds\n"
"v_mfma_f32_16x16x32_bf16 v[120:123], a[204:207], v[216:219], v[120:123]\n"
"ds_read_b128 a[232:235], v0 offset:18688\n"
"v_cvt_pk_bf16_f32 v103, v70, v71\n"
"v_mfma_f32_16x16x32_bf16 v[120:123], a[208:211], v[232:235], v[120:123]\n"
"s_add_u32 s97, s97, s99\n"
"v_exp_f32 v74, v74\n"
"v_mfma_f32_16x16x32_bf16 v[120:123], a[212:215], v[248:251], v[120:123]\n"
"s_add_u32 s97, s97, s79\n"
"v_exp_f32 v75, v75\n"
"v_mfma_f32_16x16x32_bf16 v[124:127], a[212:215], v[252:255], v[60:63]\n"
"s_add_u32 s40, s40, s44\n"
"ds_read_b128 a[236:239], v1 offset:18688\n"
"v_add_f32 v20, v20, v72\n"
"v_mfma_f32_16x16x32_bf16 v[124:127], a[208:211], v[236:239], v[124:127]\n"
"s_cmp_gt_i32 s47, 0\n"
"v_add_f32 v20, v20, v73\n"
"v_cvt_pk_bf16_f32 v106, v72, v73\n"
"v_mfma_f32_16x16x32_bf16 v[124:127], a[204:207], v[220:223], v[124:127]\n"
"s_cbranch_scc1 L_pwa\n"
"L_pra:\n"
"v_exp_f32 v76, v76\n"
"v_mfma_f32_16x16x32_bf16 v[124:127], a[200:203], v[204:207], v[124:127]\n"
"v_add_u32 v0, s45, v0\n"
"v_add_f32 v20, v20, v74\n"
"v_mfma_f32_16x16x32_bf16 v[124:127], a[196:199], v[188:191], v[124:127]\n"
"v_exp_f32 v77, v77\n"
"v_mfma_f32_16x16x32_bf16 v[124:127], a[192:195], v[172:175], v[124:127]\n"
"s_waitcnt lgkmcnt(0)\n"
"v_add_f32 v20, v20, v75\n"
"v_cvt_pk_bf16_f32 v107, v74, v75\n"
"v_mfma_f32_16x16x32_bf16 v[80:83], a[216:219], v[160:163], v[48:51]\n"
"v_add_u32 v1, s45, v1\n"
"v_add_f32 v21, v21, v76\n"
"v_mfma_f32_16x16x32_bf16 v[80:83], a[220:223], v[176:179], v[80:83]\n"
"v_exp_f32 v78, v78\n"
"v_mfma_f32_16x16x32_bf16 v[80:83], a[224:227], v[192:195], v[80:83]\n"
"v_exp_f32 v79, v79\n"
"v_mfma_f32_16x16x32_bf16 v[80:83], a[228:231], v[208:211], v[80:83]\n"
"v_add_f32 v21, v21, v77\n"
"v_cvt_pk_bf16_f32 v110, v76, v77\n"
"v_mfma_f32_16x16x32_bf16 v[80:83], a[232:235], v[224:227], v[80:83]\n"
"v_exp_f32 v112, v112\n"
"v_mfma_f32_16x16x32_bf16 v[80:83], a[236:239], v[240:243], v[80:83]\n"
"v_exp_f32 v113, v113\n"
"v_mfma_f32_16x16x32_bf16 v[84:87], a[236:239], v[244:247], v[52:55]\n"
"s_waitcnt vmcnt(12)\n"
"v_add_f32 v21, v21, v78\n"
"v_add_f32 v21, v21, v79\n"
"v_mfma_f32_16x16x32_bf16 v[84:87], a[232:235], v[228:231], v[84:87]\n"
"s_barrier\n"
"v_exp_f32 v114, v114\n"
"v_mfma_f32_16x16x32_bf16 v[84:87], a[228:231], v[212:215], v[84:87]\n"
"v_cvt_pk_bf16_f32 v111, v78, v79\n"
"v_add_f32 v18, v18, v112\n"
"v_mfma_f32_16x16x32_bf16 v[84:87], a[224:227], v[196:199], v[84:87]\n"
"ds_read_b64_tr_b16 a[240:241], v2 offset:0\n"
"v_add_f32 v18, v18, v113\n"
"v_mfma_f32_16x16x32_bf16 v[84:87], a[220:223], v[180:183], v[84:87]\n"
"v_exp_f32 v115, v115\n"
"v_mfma_f32_16x16x32_bf16 v[84:87], a[216:219], v[164:167], v[84:87]\n"
"ds_read_b64_tr_b16 a[242:243], v2 offset:6144\n"
"v_cvt_pk_bf16_f32 v112, v112, v113\n"
"v_mfma_f32_16x16x32_bf16 v[88:91], a[216:219], v[168:171], v[56:59]\n"
"v_exp_f32 v116, v116\n"
"v_mfma_f32_16x16x32_bf16 v[88:91], a[220:223], v[184:187], v[88:91]\n"
"ds_read_b64_tr_b16 a[244:245], v2 offset:12288\n"
"v_add_f32 v18, v18, v114\n"
"v_mfma_f32_16x16x32_bf16 v[88:91], a[224:227], v[200:203], v[88:91]\n"
"v_exp_f32 v117, v117\n"
"v_mfma_f32_16x16x32_bf16 v[88:91], a[228:231], v[216:219], v[88:91]\n"
"ds_read_b64_tr_b16 a[246:247], v2 offset:18432\n"
"v_add_f32 v18, v18, v115\n"
"v_mfma_f32_16x16x32_bf16 v[88:91], a[232:235], v[232:235], v[88:91]\n"
"v_exp_f32 v118, v118\n"
"v_mfma_f32_16x16x32_bf16 v[88:91], a[236:239], v[248:251], v[88:91]\n"
"ds_read_b64_tr_b16 a[248:249], v3 offset:0\n"
"v_cvt_pk_bf16_f32 v113, v114, v115\n"
"v_mfma_f32_16x16x32_bf16 v[92:95], a[236:239], v[252:255], v[60:63]\n"
"v_exp_f32 v119, v119\n"
"v_mfma_f32_16x16x32_bf16 v[92:95], a[232:235], v[236:239], v[92:95]\n"
"ds_read_b64_tr_b16 a[250:251], v3 offset:6144\n"
"v_add_f32 v19, v19, v116\n"
"v_mfma_f32_16x16x32_bf16 v[92:95], a[228:231], v[220:223], v[92:95]\n"
"v_add_f32 v19, v19, v117\n"
"v_cvt_pk_bf16_f32 v116, v116, v117\n"
"v_mfma_f32_16x16x32_bf16 v[92:95], a[224:227], v[204:207], v[92:95]\n"
"ds_read_b64_tr_b16 a[252:253], v3 offset:12288\n"
"v_add_f32 v19, v19, v118\n"
"v_mfma_f32_16x16x32_bf16 v[92:95], a[220:223], v[188:191], v[92:95]\n"
"v_exp_f32 v120, v120\n"
"v_mfma_f32_16x16x32_bf16 v[92:95], a[216:219], v[172:175], v[92:95]\n"
"s_waitcnt lgkmcnt(3)\n"
"ds_read_b64_tr_b16 a[254:255], v3 offset:18432\n"
"v_add_f32 v19, v19, v119\n"
"v_mfma_f32_16x16x32_bf16 a[0:3], a[240:243], v[128:131], a[0:3]\n"
"v_exp_f32 v121, v121\n"
"v_mfma_f32_16x16x32_bf16 a[0:3], a[244:247], v[144:147], a[0:3]\n"
"s_add_u32 m0, s41, 0\n"
"ds_read_b64_tr_b16 a[216:217], v4 offset:0\n"
"v_cvt_pk_bf16_f32 v117, v118, v119\n"
"v_mfma_f32_16x16x32_bf16 a[4:7], a[240:243], v[132:135], a[4:7]\n"
"buffer_load_dwordx4 v9, s[12:15], s43 offen offset:0 nt lds\n"
"v_mfma_f32_16x16x32_bf16 a[4:7], a[244:247], v[148:151], a[4:7]\n"
"ds_read_b64_tr_b16 a[218:219], v4 offset:6144\n"
"v_add_f32 v20, v20, v120\n"
"v_mfma_f32_16x16x32_bf16 a[8:11], a[240:243], v[136:139], a[8:11]\n"
"v_exp_f32 v122, v122\n"
"v_mfma_f32_16x16x32_bf16 a[8:11], a[244:247], v[152:155], a[8:11]\n"
"ds_read_b64_tr_b16 a[220:221], v4 offset:12288\n"
"v_add_f32 v20, v20, v121\n"
"v_mfma_f32_16x16x32_bf16 a[12:15], a[240:243], v[140:143], a[12:15]\n"
"v_exp_f32 v123, v123\n"
"v_mfma_f32_16x16x32_bf16 a[12:15], a[244:247], v[156:159], a[12:15]\n"
"s_waitcnt lgkmcnt(3)\n"
"ds_read_b64_tr_b16 a[222:223], v4 offset:18432\n"
"v_cvt_pk_bf16_f32 v120, v120, v121\n"
"v_mfma_f32_16x16x32_bf16 a[16:19], a[248:251], v[128:131], a[16:19]\n"
"v_exp_f32 v124, v124\n"
"v_mfma_f32_16x16x32_bf16 a[16:19], a[252:255], v[144:147], a[16:19]\n"
"ds_read_b64_tr_b16 a[224:225], v5 offset:0\n"
"v_add_f32 v20, v20, v122\n"
"v_mfma_f32_16x16x32_bf16 a[20:23], a[248:251], v[132:135], a[20:23]\n"
"v_exp_f32 v125, v125\n"
"v_mfma_f32_16x16x32_bf16 a[20:23], a[252:255], v[148:151], a[20:23]\n"
"ds_read_b64_tr_b16 a[226:227], v5 offset:6144\n"
"v_add_f32 v20, v20, v123\n"
"v_mfma_f32_16x16x32_bf16 a[24:27], a[248:251], v[136:139], a[24:27]\n"
"v_exp_f32 v126, v126\n"
"v_mfma_f32_16x16x32_bf16 a[24:27], a[252:255], v[152:155], a[24:27]\n"
"ds_read_b64_tr_b16 a[228:229], v5 offset:12288\n"
"v_cvt_pk_bf16_f32 v121, v122, v123\n"
"v_mfma_f32_16x16x32_bf16 a[28:31], a[248:251], v[140:143], a[28:31]\n"
"buffer_load_dwordx4 v10, s[12:15], s43 offen offset:1024 nt lds\n"
"v_mfma_f32_16x16x32_bf16 a[28:31], a[252:255], v[156:159], a[28:31]\n"
"s_waitcnt lgkmcnt(3)\n"
"ds_read_b64_tr_b16 a[230:231], v5 offset:18432\n"
"v_add_f32 v21, v21, v124\n"
"v_mfma_f32_16x16x32_bf16 a[32:35], a[216:219], v[128:131], a[32:35]\n"
"v_exp_f32 v127, v127\n"
"v_mfma_f32_16x16x32_bf16 a[32:35], a[220:223], v[144:147], a[32:35]\n"
"ds_read_b64_tr_b16 a[240:241], v2 offset:128\n"
"v_add_f32 v21, v21, v125\n"
"v_mfma_f32_16x16x32_bf16 a[36:39], a[216:219], v[132:135], a[36:39]\n"
"v_exp_f32 v80, v80\n"
"v_mfma_f32_16x16x32_bf16 a[36:39], a[220:223], v[148:151], a[36:39]\n"
"ds_read_b64_tr_b16 a[242:243], v2 offset:6272\n"
"v_cvt_pk_bf16_f32 v124, v124, v125\n"
"v_mfma_f32_16x16x32_bf16 a[40:43], a[216:219], v[136:139], a[40:43]\n"
"v_exp_f32 v81, v81\n"
"v_mfma_f32_16x16x32_bf16 a[40:43], a[220:223], v[152:155], a[40:43]\n"
"ds_read_b64_tr_b16 a[244:245], v2 offset:12416\n"
"v_add_f32 v21, v21, v126\n"
"v_mfma_f32_16x16x32_bf16 a[44:47], a[216:219], v[140:143], a[44:47]\n"
"L_pg7:\n"
"s_cbranch_execz L_bk1\n"
"v_add_f32 v21, v21, v127\n"
"v_cvt_pk_bf16_f32 v125, v126, v127\n"
"v_mfma_f32_16x16x32_bf16 a[44:47], a[220:223], v[156:159], a[44:47]\n"
"s_waitcnt lgkmcnt(3)\n"
"ds_read_b64_tr_b16 a[246:247], v2 offset:18560\n"
"v_add_f32 v18, v18, v80\n"
"v_mfma_f32_16x16x32_bf16 a[48:51], a[224:227], v[128:131], a[48:51]\n"
"v_exp_f32 v82, v82\n"
"v_mfma_f32_16x16x32_bf16 a[48:51], a[228:231], v[144:147], a[48:51]\n"
"ds_read_b64_tr_b16 a[248:249], v3 offset:128\n"
"v_add_f32 v18, v18, v81\n"
"v_mfma_f32_16x16x32_bf16 a[52:55], a[224:227], v[132:135], a[52:55]\n"
"buffer_load_dwordx4 v11, s[12:15], s43 offen offset:2048 nt lds\n"
"v_mfma_f32_16x16x32_bf16 a[52:55], a[228:231], v[148:151], a[52:55]\n"
"ds_read_b64_tr_b16 a[250:251], v3 offset:6272\n"
"v_cvt_pk_bf16_f32 v114, v80, v81\n"
"v_mfma_f32_16x16x32_bf16 a[56:59], a[224:227], v[136:139], a[56:59]\n"
"s_add_u32 s43, s43, 27648\n"
"v_exp_f32 v83, v83\n"
"v_mfma_f32_16x16x32_bf16 a[56:59], a[228:231], v[152:155], a[56:59]\n"
"ds_read_b64_tr_b16 a[252:253], v3 offset:12416\n"
"v_add_f32 v18, v18, v82\n"
"v_mfma_f32_16x16x32_bf16 a[60:63], a[224:227], v[140:143], a[60:63]\n"
"s_or_b32 s43, s43, 0x18000\n"
"v_exp_f32 v84, v84\n"
"v_mfma_f32_16x16x32_bf16 a[60:63], a[228:231], v[156:159], a[60:63]\n"
"s_waitcnt lgkmcnt(3)\n"
"ds_read_b64_tr_b16 a[254:255], v3 offset:18560\n"
"v_add_f32 v18, v18, v83\n"
"v_mfma_f32_16x16x32_bf16 a[64:67], a[240:243], v[128:131], a[64:67]\n"
"s_sub_u32 s62, s43, 0x60000\n"
"v_exp_f32 v85, v85\n"
"v_mfma_f32_16x16x32_bf16 a[64:67], a[244:247], v[144:147], a[64:67]\n"
"ds_read_b64_tr_b16 a[216:217], v4 offset:128\n"
"v_cvt_pk_bf16_f32 v115, v82, v83\n"
"v_mfma_f32_16x16x32_bf16 a[68:71], a[240:243], v[132:135], a[68:71]\n"
"s_min_u32 s43, s43, s62\n"
"v_exp_f32 v86, v86\n"
"v_mfma_f32_16x16x32_bf16 a[68:71], a[244:247], v[148:151], a[68:71]\n"
"ds_read_b64_tr_b16 a[218:219], v4 offset:6272\n"
"v_add_f32 v19, v19, v84\n"
"v_mfma_f32_16x16x32_bf16 a[72:75], a[240:243], v[136:139], a[72:75]\n"
"v_exp_f32 v87, v87\n"
"v_mfma_f32_16x16x32_bf16 a[72:75], a[244:247], v[152:155], a[72:75]\n"
"s_add_u32 m0, s41, 12288\n"
"ds_read_b64_tr_b16 a[220:221], v4 offset:12416\n"
"v_add_f32 v19, v19, v85\n"
"v_mfma_f32_16x16x32_bf16 a[76:79], a[240:243], v[140:143], a[76:79]\n"
"buffer_load_dwordx4 v9, s[12:15], s43 offen offset:0 nt lds\n"
"v_mfma_f32_16x16x32_bf16 a[76:79], a[244:247], v[156:159], a[76:79]\n"
"s_waitcnt lgkmcnt(3)\n"
"ds_read_b64_tr_b16 a[222:223], v4 offset:18560\n"
"v_cvt_pk_bf16_f32 v118, v84, v85\n"
"v_mfma_f32_16x16x32_bf16 a[80:83], a[248:251], v[128:131], a[80:83]\n"
"v_exp_f32 v88, v88\n"
"v_mfma_f32_16x16x32_bf16 a[80:83], a[252:255], v[144:147], a[80:83]\n"
"ds_read_b64_tr_b16 a[224:225], v5 offset:128\n"
"v_add_f32 v19, v19, v86\n"
"v_mfma_f32_16x16x32_bf16 a[84:87], a[248:251], v[132:135], a[84:87]\n"
"v_exp_f32 v89, v89\n"
"v_mfma_f32_16x16x32_bf16 a[84:87], a[252:255], v[148:151], a[84:87]\n"
"ds_read_b64_tr_b16 a[226:227], v5 offset:6272\n"
"v_add_f32 v19, v19, v87\n"
"v_mfma_f32_16x16x32_bf16 a[88:91], a[248:251], v[136:139], a[88:91]\n"
"v_exp_f32 v90, v90\n"
"v_mfma_f32_16x16x32_bf16 a[88:91], a[252:255], v[152:155], a[88:91]\n"
"ds_read_b64_tr_b16 a[228:229], v5 offset:12416\n"
"v_cvt_pk_bf16_f32 v119, v86, v87\n"
"v_mfma_f32_16x16x32_bf16 a[92:95], a[248:251], v[140:143], a[92:95]\n"
"v_exp_f32 v91, v91\n"
"v_mfma_f32_16x16x32_bf16 a[92:95], a[252:255], v[156:159], a[92:95]\n"
"s_waitcnt lgkmcnt(3)\n"
"ds_read_b64_tr_b16 a[230:231], v5 offset:18560\n"
"v_add_f32 v20, v20, v88\n"
"v_mfma_f32_16x16x32_bf16 a[96:99], a[216:219], v[128:131], a[96:99]\n"
"v_add_f32 v20, v20, v89\n"
"v_cvt_pk_bf16_f32 v122, v88, v89\n"
"v_mfma_f32_16x16x32_bf16 a[96:99], a[220:223], v[144:147], a[96:99]\n"
"ds_read_b64_tr_b16 a[240:241], v2 offset:256\n"
"v_add_f32 v20, v20, v90\n"
"v_mfma_f32_16x16x32_bf16 a[100:103], a[216:219], v[132:135], a[100:103]\n"
"buffer_load_dwordx4 v10, s[12:15], s43 offen offset:1024 nt lds\n"
"v_mfma_f32_16x16x32_bf16 a[100:103], a[220:223], v[148:151], a[100:103]\n"
"ds_read_b64_tr_b16 a[242:243], v2 offset:6400\n"
"v_add_f32 v20, v20, v91\n"
"v_mfma_f32_16x16x32_bf16 a[104:107], a[216:219], v[136:139], a[104:107]\n"
"v_exp_f32 v92, v92\n"
"v_mfma_f32_16x16x32_bf16 a[104:107], a[220:223], v[152:155], a[104:107]\n"
"ds_read_b64_tr_b16 a[244:245], v2 offset:12544\n"
"v_cvt_pk_bf16_f32 v123, v90, v91\n"
"v_mfma_f32_16x16x32_bf16 a[108:111], a[216:219], v[140:143], a[108:111]\n"
"v_exp_f32 v93, v93\n"
"v_mfma_f32_16x16x32_bf16 a[108:111], a[220:223], v[156:159], a[108:111]\n"
"s_waitcnt lgkmcnt(3)\n"
"ds_read_b64_tr_b16 a[246:247], v2 offset:18688\n"
"v_add_f32 v21, v21, v92\n"
"v_mfma_f32_16x16x32_bf16 a[112:115], a[224:227], v[128:131], a[112:115]\n"
"v_exp_f32 v94, v94\n"
"v_mfma_f32_16x16x32_bf16 a[112:115], a[228:231], v[144:147], a[112:115]\n"
"ds_read_b64_tr_b16 a[248:249], v3 offset:256\n"
"v_add_f32 v21, v21, v93\n"
"v_mfma_f32_16x16x32_bf16 a[116:119], a[224:227], v[132:135], a[116:119]\n"
"v_exp_f32 v95, v95\n"
"v_mfma_f32_16x16x32_bf16 a[116:119], a[228:231], v[148:151], a[116:119]\n"
"ds_read_b64_tr_b16 a[250:251], v3 offset:6400\n"
"v_cvt_pk_bf16_f32 v126, v92, v93\n"
"v_mfma_f32_16x16x32_bf16 a[120:123], a[224:227], v[136:139], a[120:123]\n"
"v_add_f32 v21, v21, v94\n"
"v_add_f32 v21, v21, v95\n"
"v_mfma_f32_16x16x32_bf16 a[120:123], a[228:231], v[152:155], a[120:123]\n"
"ds_read_b64_tr_b16 a[252:253], v3 offset:12544\n"
"v_cvt_pk_bf16_f32 v127, v94, v95\n"
"v_mfma_f32_16x16x32_bf16 a[124:127], a[224:227], v[140:143], a[124:127]\n"
"buffer_load_dwordx4 v11, s[12:15], s43 offen offset:2048 nt lds\n"
"v_mfma_f32_16x16x32_bf16 a[124:127], a[228:231], v[156:159], a[124:127]\n"
"s_waitcnt lgkmcnt(3)\n"
"ds_read_b64_tr_b16 a[254:255], v3 offset:18688\n"
"v_max3_f32 v22, v18, v19, v20\n"
"v_mfma_f32_16x16x32_bf16 a[128:131], a[240:243], v[128:131], a[128:131]\n"
"v_max_f32 v22, v22, v21\n"
"v_cmp_lt_f32 vcc, s54, v22\n"
"v_mfma_f32_16x16x32_bf16 a[128:131], a[244:247], v[144:147], a[128:131]\n"
"ds_read_b64_tr_b16 a[216:217], v4 offset:256\n"
"v_mfma_f32_16x16x32_bf16 a[132:135], a[240:243], v[132:135], a[132:135]\n"
"s_add_u32 s41, s41, s46\n"
"v_mfma_f32_16x16x32_bf16 a[132:135], a[244:247], v[148:151], a[132:135]\n"
"ds_read_b64_tr_b16 a[218:219], v4 offset:6400\n"
"v_mfma_f32_16x16x32_bf16 a[136:139], a[240:243], v[136:139], a[136:139]\n"
"s_add_u32 s42, s42, 27648\n"
"v_mfma_f32_16x16x32_bf16 a[136:139], a[244:247], v[152:155], a[136:139]\n"
"ds_read_b64_tr_b16 a[220:221], v4 offset:12544\n"
"v_mfma_f32_16x16x32_bf16 a[140:143], a[240:243], v[140:143], a[140:143]\n"
"s_or_b32 s42, s42, 0x18000\n"
"v_mfma_f32_16x16x32_bf16 a[140:143], a[244:247], v[156:159], a[140:143]\n"
"s_waitcnt lgkmcnt(3)\n"
"ds_read_b64_tr_b16 a[222:223], v4 offset:18688\n"
"v_mfma_f32_16x16x32_bf16 a[144:147], a[248:251], v[128:131], a[144:147]\n"
"s_sub_u32 s62, s42, 0x60000\n"
"v_mfma_f32_16x16x32_bf16 a[144:147], a[252:255], v[144:147], a[144:147]\n"
"ds_read_b64_tr_b16 a[224:225], v5 offset:256\n"
"v_mfma_f32_16x16x32_bf16 a[148:151], a[248:251], v[132:135], a[148:151]\n"
"s_min_u32 s42, s42, s62\n"
"v_mfma_f32_16x16x32_bf16 a[148:151], a[252:255], v[148:151], a[148:151]\n"
"ds_read_b64_tr_b16 a[226:227], v5 offset:6400\n"
"v_mfma_f32_16x16x32_bf16 a[152:155], a[248:251], v[136:139], a[152:155]\n"
"s_add_u32 s43, s43, 27648\n"
"v_mfma_f32_16x16x32_bf16 a[152:155], a[252:255], v[152:155], a[152:155]\n"
"ds_read_b64_tr_b16 a[228:229], v5 offset:12544\n"
"v_mfma_f32_16x16x32_bf16 a[156:159], a[248:251], v[140:143], a[156:159]\n"
"s_or_b32 s43, s43, 0x18000\n"
"v_mfma_f32_16x16x32_bf16 a[156:159], a[252:255], v[156:159], a[156:159]\n"
"s_waitcnt lgkmcnt(3)\n"
"ds_read_b64_tr_b16 a[230:231], v5 offset:18688\n"
"v_mfma_f32_16x16x32_bf16 a[160:163], a[216:219], v[128:131], a[160:163]\n"
"ds_read_b128 a[192:195], v0 offset:0\n"
"v_mfma_f32_16x16x32_bf16 a[160:163], a[220:223], v[144:147], a[160:163]\n"
"s_sub_u32 s62, s43, 0x60000\n"
"v_mfma_f32_16x16x32_bf16 a[164:167], a[216:219], v[132:135], a[164:167]\n"
"ds_read_b128 a[196:199], v1 offset:0\n"
"v_mfma_f32_16x16x32_bf16 a[164:167], a[220:223], v[148:151], a[164:167]\n"
"s_min_u32 s43, s43, s62\n"
"v_mfma_f32_16x16x32_bf16 a[168:171], a[216:219], v[136:139], a[168:171]\n"
"s_mov_b32 s47, s44\n"
"ds_read_b128 a[200:203], v0 offset:128\n"
"v_mfma_f32_16x16x32_bf16 a[168:171], a[220:223], v[152:155], a[168:171]\n"
"s_mov_b32 s44, s45\n"
"v_mfma_f32_16x16x32_bf16 a[172:175], a[216:219], v[140:143], a[172:175]\n"
"s_mov_b32 s45, s46\n"
"ds_read_b128 a[204:207], v1 offset:128\n"
"v_mfma_f32_16x16x32_bf16 a[172:175], a[220:223], v[156:159], a[172:175]\n"
"s_waitcnt lgkmcnt(4)\n"
"s_mov_b32 s46, s47\n"
"v_mfma_f32_16x16x32_bf16 a[176:179], a[224:227], v[128:131], a[176:179]\n"
"ds_read_b128 a[208:211], v0 offset:256\n"
"v_mfma_f32_16x16x32_bf16 a[176:179], a[228:231], v[144:147], a[176:179]\n"
"v_mfma_f32_16x16x32_bf16 a[180:183], a[224:227], v[132:135], a[180:183]\n"
"ds_read_b128 a[212:215], v1 offset:256\n"
"v_mfma_f32_16x16x32_bf16 a[180:183], a[228:231], v[148:151], a[180:183]\n"
"v_mfma_f32_16x16x32_bf16 a[184:187], a[224:227], v[136:139], a[184:187]\n"
"v_mfma_f32_16x16x32_bf16 a[184:187], a[228:231], v[152:155], a[184:187]\n"
"s_cmp_lg_u64 vcc, 0\n"
"v_mfma_f32_16x16x32_bf16 a[188:191], a[224:227], v[140:143], a[188:191]\n"
"s_waitcnt lgkmcnt(3)\n"
"v_mfma_f32_16x16x32_bf16 a[188:191], a[228:231], v[156:159], a[188:191]\n"
"s_cbranch_scc1 L_rc2\n"
"L_rr2:\n"
"v_mfma_f32_16x16x32_bf16 v[128:131], a[192:195], v[160:163], v[48:51]\n"
"v_add_f32 v14, v14, v18\n"
"v_add_f32 v15, v15, v19\n"
"v_mfma_f32_16x16x32_bf16 v[128:131], a[196:199], v[176:179], v[128:131]\n"
"s_add_u32 m0, s40, 0\n"
"v_add_f32 v16, v16, v20\n"
"v_add_f32 v17, v17, v21\n"
"v_mfma_f32_16x16x32_bf16 v[128:131], a[200:203], v[192:195], v[128:131]\n"
"s_waitcnt lgkmcnt(0)\n"
"buffer_load_dwordx4 v6, s[8:11], s42 offen offset:0 nt lds\n"
"v_mfma_f32_16x16x32_bf16 v[128:131], a[204:207], v[208:211], v[128:131]\n"
"ds_read_b128 a[216:219], v0 offset:6144\n"
"v_add_u32 v2, s46, v2\n"
"v_mfma_f32_16x16x32_bf16 v[128:131], a[208:211], v[224:227], v[128:131]\n"
"s_add_u32 s55, s55, 18\n"
"v_add_u32 v3, s46, v3\n"
"v_add_u32 v4, s46, v4\n"
"v_mfma_f32_16x16x32_bf16 v[128:131], a[212:215], v[240:243], v[128:131]\n"
"v_add_u32 v5, s46, v5\n"
"v_mfma_f32_16x16x32_bf16 v[132:135], a[212:215], v[244:247], v[52:55]\n"
"ds_read_b128 a[220:223], v1 offset:6144\n"
"v_mfma_f32_16x16x32_bf16 v[132:135], a[208:211], v[228:231], v[132:135]\n"
"v_mfma_f32_16x16x32_bf16 v[132:135], a[204:207], v[212:215], v[132:135]\n"
"v_mfma_f32_16x16x32_bf16 v[132:135], a[200:203], v[196:199], v[132:135]\n"
"ds_read_b128 a[224:227], v0 offset:6272\n"
"v_mfma_f32_16x16x32_bf16 v[132:135], a[196:199], v[180:183], v[132:135]\n"
"v_exp_f32 v128, v128\n"
"v_mfma_f32_16x16x32_bf16 v[132:135], a[192:195], v[164:167], v[132:135]\n"
"v_exp_f32 v129, v129\n"
"v_mfma_f32_16x16x32_bf16 v[136:139], a[192:195], v[168:171], v[56:59]\n"
"ds_read_b128 a[228:231], v1 offset:6272\n"
"v_add_f32 v18, v128, v129\n"
"v_mfma_f32_16x16x32_bf16 v[136:139], a[196:199], v[184:187], v[136:139]\n"
"v_exp_f32 v130, v130\n"
"v_mfma_f32_16x16x32_bf16 v[136:139], a[200:203], v[200:203], v[136:139]\n"
"buffer_load_dwordx4 v7, s[8:11], s42 offen offset:1024 nt lds\n"
"v_mfma_f32_16x16x32_bf16 v[136:139], a[204:207], v[216:219], v[136:139]\n"
"ds_read_b128 a[232:235], v0 offset:6400\n"
"v_cvt_pk_bf16_f32 v128, v128, v129\n"
"v_mfma_f32_16x16x32_bf16 v[136:139], a[208:211], v[232:235], v[136:139]\n"
"v_exp_f32 v131, v131\n"
"v_mfma_f32_16x16x32_bf16 v[136:139], a[212:215], v[248:251], v[136:139]\n"
"v_exp_f32 v132, v132\n"
"v_mfma_f32_16x16x32_bf16 v[140:143], a[212:215], v[252:255], v[60:63]\n"
"ds_read_b128 a[236:239], v1 offset:6400\n"
"v_add_f32 v18, v18, v130\n"
"v_mfma_f32_16x16x32_bf16 v[140:143], a[208:211], v[236:239], v[140:143]\n"
"v_exp_f32 v133, v133\n"
"v_mfma_f32_16x16x32_bf16 v[140:143], a[204:207], v[220:223], v[140:143]\n"
"v_exp_f32 v134, v134\n"
"v_mfma_f32_16x16x32_bf16 v[140:143], a[200:203], v[204:207], v[140:143]\n"
"v_add_f32 v18, v18, v131\n"
"v_cvt_pk_bf16_f32 v129, v130, v131\n"
"v_mfma_f32_16x16x32_bf16 v[140:143], a[196:199], v[188:191], v[140:143]\n"
"v_exp_f32 v135, v135\n"
"v_mfma_f32_16x16x32_bf16 v[140:143], a[192:195], v[172:175], v[140:143]\n"
"s_waitcnt lgkmcnt(0)\n"
"v_add_f32 v19, v132, v133\n"
"v_cvt_pk_bf16_f32 v132, v132, v133\n"
"v_mfma_f32_16x16x32_bf16 v[64:67], a[216:219], v[160:163], v[48:51]\n"
"v_exp_f32 v136, v136\n"
"v_mfma_f32_16x16x32_bf16 v[64:67], a[220:223], v[176:179], v[64:67]\n"
"v_exp_f32 v137, v137\n"
"v_mfma_f32_16x16x32_bf16 v[64:67], a[224:227], v[192:195], v[64:67]\n"
"buffer_load_dwordx4 v8, s[8:11], s42 offen offset:2048 nt lds\n"
"v_mfma_f32_16x16x32_bf16 v[64:67], a[228:231], v[208:211], v[64:67]\n"
"ds_read_b128 a[192:195], v0 offset:12288\n"
"v_add_f32 v19, v19, v134\n"
"v_mfma_f32_16x16x32_bf16 v[64:67], a[232:235], v[224:227], v[64:67]\n"
"s_add_u32 s42, s42, 27648\n"
"v_exp_f32 v138, v138\n"
"v_mfma_f32_16x16x32_bf16 v[64:67], a[236:239], v[240:243], v[64:67]\n"
"s_or_b32 s42, s42, 0x18000\n"
"v_add_f32 v19, v19, v135\n"
"v_cvt_pk_bf16_f32 v133, v134, v135\n"
"v_mfma_f32_16x16x32_bf16 v[68:71], a[236:239], v[244:247], v[52:55]\n"
"ds_read_b128 a[196:199], v1 offset:12288\n"
"v_add_f32 v20, v136, v137\n"
"v_mfma_f32_16x16x32_bf16 v[68:71], a[232:235], v[228:231], v[68:71]\n"
"s_sub_u32 s62, s42, 0x60000\n"
"v_exp_f32 v139, v139\n"
"v_mfma_f32_16x16x32_bf16 v[68:71], a[228:231], v[212:215], v[68:71]\n"
"s_memrealtime s[0:1]\n"
"v_exp_f32 v140, v140\n"
"v_mfma_f32_16x16x32_bf16 v[68:71], a[224:227], v[196:199], v[68:71]\n"
"ds_read_b128 a[200:203], v0 offset:12416\n"
"v_cvt_pk_bf16_f32 v136, v136, v137\n"
"v_mfma_f32_16x16x32_bf16 v[68:71], a[220:223], v[180:183], v[68:71]\n"
"s_min_u32 s42, s42, s62\n"
"v_exp_f32 v141, v141\n"
"v_mfma_f32_16x16x32_bf16 v[68:71], a[216:219], v[164:167], v[68:71]\n"
"v_add_f32 v20, v20, v138\n"
"v_add_f32 v20, v20, v139\n"
"v_mfma_f32_16x16x32_bf16 v[72:75], a[216:219], v[168:171], v[56:59]\n"
"ds_read_b128 a[204:207], v1 offset:12416\n"
"v_cvt_pk_bf16_f32 v137, v138, v139\n"
"v_mfma_f32_16x16x32_bf16 v[72:75], a[220:223], v[184:187], v[72:75]\n"
"s_add_u32 m0, s40, 12288\n"
"v_exp_f32 v142, v142\n"
"v_mfma_f32_16x16x32_bf16 v[72:75], a[224:227], v[200:203], v[72:75]\n"
"buffer_load_dwordx4 v6, s[8:11], s42 offen offset:0 nt lds\n"
"v_mfma_f32_16x16x32_bf16 v[72:75], a[228:231], v[216:219], v[72:75]\n"
"ds_read_b128 a[208:211], v0 offset:12544\n"
"v_add_f32 v21, v140, v141\n"
"v_mfma_f32_16x16x32_bf16 v[72:75], a[232:235], v[232:235], v[72:75]\n"
"s_sub_u32 s79, s79, 4\n"
"v_exp_f32 v143, v143\n"
"v_mfma_f32_16x16x32_bf16 v[72:75], a[236:239], v[248:251], v[72:75]\n"
"s_max_i32 s79, s79, 0\n"
"v_exp_f32 v64, v64\n"
"v_mfma_f32_16x16x32_bf16 v[76:79], a[236:239], v[252:255], v[60:63]\n"
"ds_read_b128 a[212:215], v1 offset:12544\n"
"v_cvt_pk_bf16_f32 v140, v140, v141\n"
"v_mfma_f32_16x16x32_bf16 v[76:79], a[232:235], v[236:239], v[76:79]\n"
"v_exp_f32 v65, v65\n"
"v_mfma_f32_16x16x32_bf16 v[76:79], a[228:231], v[220:223], v[76:79]\n"
"v_add_f32 v21, v21, v142\n"
"v_add_f32 v21, v21, v143\n"
"v_mfma_f32_16x16x32_bf16 v[76:79], a[224:227], v[204:207], v[76:79]\n"
"v_exp_f32 v66, v66\n"
"v_mfma_f32_16x16x32_bf16 v[76:79], a[220:223], v[188:191], v[76:79]\n"
"v_cvt_pk_bf16_f32 v141, v142, v143\n"
"v_add_f32 v18, v18, v64\n"
"v_mfma_f32_16x16x32_bf16 v[76:79], a[216:219], v[172:175], v[76:79]\n"
"s_waitcnt lgkmcnt(0)\n"
"v_exp_f32 v67, v67\n"
"v_mfma_f32_16x16x32_bf16 v[144:147], a[192:195], v[160:163], v[48:51]\n"
"v_add_f32 v18, v18, v65\n"
"v_cvt_pk_bf16_f32 v130, v64, v65\n"
"v_mfma_f32_16x16x32_bf16 v[144:147], a[196:199], v[176:179], v[144:147]\n"
"v_exp_f32 v68, v68\n"
"v_mfma_f32_16x16x32_bf16 v[144:147], a[200:203], v[192:195], v[144:147]\n"
"buffer_load_dwordx4 v7, s[8:11], s42 offen offset:1024 nt lds\n"
"v_mfma_f32_16x16x32_bf16 v[144:147], a[204:207], v[208:211], v[144:147]\n"
"ds_read_b128 a[216:219], v0 offset:18432\n"
"v_add_f32 v18, v18, v66\n"
"v_mfma_f32_16x16x32_bf16 v[144:147], a[208:211], v[224:227], v[144:147]\n"
"v_exp_f32 v69, v69\n"
"v_mfma_f32_16x16x32_bf16 v[144:147], a[212:215], v[240:243], v[144:147]\n"
"v_add_f32 v18, v18, v67\n"
"v_cvt_pk_bf16_f32 v131, v66, v67\n"
"v_mfma_f32_16x16x32_bf16 v[148:151], a[212:215], v[244:247], v[52:55]\n"
"ds_read_b128 a[220:223], v1 offset:18432\n"
"v_add_f32 v19, v19, v68\n"
"v_mfma_f32_16x16x32_bf16 v[148:151], a[208:211], v[228:231], v[148:151]\n"
"v_exp_f32 v70, v70\n"
"v_mfma_f32_16x16x32_bf16 v[148:151], a[204:207], v[212:215], v[148:151]\n"
"s_sub_u32 s47, s97, s0\n"
"v_exp_f32 v71, v71\n"
"v_mfma_f32_16x16x32_bf16 v[148:151], a[200:203], v[196:199], v[148:151]\n"
"s_add_u32 s47, s47, s33\n"
"ds_read_b128 a[224:227], v0 offset:18560\n"
"v_add_f32 v19, v19, v69\n"
"v_mfma_f32_16x16x32_bf16 v[148:151], a[196:199], v[180:183], v[148:151]\n"
"s_min_i32 s33, s47, 0\n"
"v_exp_f32 v72, v72\n"
"v_mfma_f32_16x16x32_bf16 v[148:151], a[192:195], v[164:167], v[148:151]\n"
"s_max_i32 s33, s33, s32\n"
"v_cvt_pk_bf16_f32 v134, v68, v69\n"
"v_add_f32 v19, v19, v70\n"
"v_mfma_f32_16x16x32_bf16 v[152:155], a[192:195], v[168:171], v[56:59]\n"
"s_max_i32 s47, s47, 0\n"
"ds_read_b128 a[228:231], v1 offset:18560\n"
"v_add_f32 v19, v19, v71\n"
"v_mfma_f32_16x16x32_bf16 v[152:155], a[196:199], v[184:187], v[152:155]\n"
"s_add_u32 s97, s0, s47\n"
"v_exp_f32 v73, v73\n"
"v_mfma_f32_16x16x32_bf16 v[152:155], a[200:203], v[200:203], v[152:155]\n"
"buffer_load_dwordx4 v8, s[8:11], s42 offen offset:2048 nt lds\n"
"v_mfma_f32_16x16x32_bf16 v[152:155], a[204:207], v[216:219], v[152:155]\n"
"ds_read_b128 a[232:235], v0 offset:18688\n"
"v_cvt_pk_bf16_f32 v135, v70, v71\n"
"v_mfma_f32_16x16x32_bf16 v[152:155], a[208:211], v[232:235], v[152:155]\n"
"s_add_u32 s97, s97, s99\n"
"v_exp_f32 v74, v74\n"
"v_mfma_f32_16x16x32_bf16 v[152:155], a[212:215], v[248:251], v[152:155]\n"
"s_add_u32 s97, s97, s79\n"
"v_exp_f32 v75, v75\n"
"v_mfma_f32_16x16x32_bf16 v[156:159], a[212:215], v[252:255], v[60:63]\n"
"s_add_u32 s40, s40, s44\n"
"ds_read_b128 a[236:239], v1 offset:18688\n"
"v_add_f32 v20, v20, v72\n"
"v_mfma_f32_16x16x32_bf16 v[156:159], a[208:211], v[236:239], v[156:159]\n"
"s_cmp_gt_i32 s47, 0\n"
"v_add_f32 v20, v20, v73\n"
"v_cvt_pk_bf16_f32 v138, v72, v73\n"
"v_mfma_f32_16x16x32_bf16 v[156:159], a[204:207], v[220:223], v[156:159]\n"
"s_cbranch_scc1 L_pwb\n"
"L_prb:\n"
"v_exp_f32 v76, v76\n"
"v_mfma_f32_16x16x32_bf16 v[156:159], a[200:203], v[204:207], v[156:159]\n"
"v_add_u32 v0, s45, v0\n"
"v_add_f32 v20, v20, v74\n"
"v_mfma_f32_16x16x32_bf16 v[156:159], a[196:199], v[188:191], v[156:159]\n"
"v_exp_f32 v77, v77\n"
"v_mfma_f32_16x16x32_bf16 v[156:159], a[192:195], v[172:175], v[156:159]\n"
"s_waitcnt lgkmcnt(0)\n"
"v_add_f32 v20, v20, v75\n"
"v_cvt_pk_bf16_f32 v139, v74, v75\n"
"v_mfma_f32_16x16x32_bf16 v[80:83], a[216:219], v[160:163], v[48:51]\n"
"v_add_u32 v1, s45, v1\n"
"v_add_f32 v21, v21, v76\n"
"v_mfma_f32_16x16x32_bf16 v[80:83], a[220:223], v[176:179], v[80:83]\n"
"v_exp_f32 v78, v78\n"
"v_mfma_f32_16x16x32_bf16 v[80:83], a[224:227], v[192:195], v[80:83]\n"
"v_exp_f32 v79, v79\n"
"v_mfma_f32_16x16x32_bf16 v[80:83], a[228:231], v[208:211], v[80:83]\n"
"v_add_f32 v21, v21, v77\n"
"v_cvt_pk_bf16_f32 v142, v76, v77\n"
"v_mfma_f32_16x16x32_bf16 v[80:83], a[232:235], v[224:227], v[80:83]\n"
"v_exp_f32 v144, v144\n"
"v_mfma_f32_16x16x32_bf16 v[80:83], a[236:239], v[240:243], v[80:83]\n"
"v_exp_f32 v145, v145\n"
"v_mfma_f32_16x16x32_bf16 v[84:87], a[236:239], v[244:247], v[52:55]\n"
"s_waitcnt vmcnt(12)\n"
"v_add_f32 v21, v21, v78\n"
"v_add_f32 v21, v21, v79\n"
"v_mfma_f32_16x16x32_bf16 v[84:87], a[232:235], v[228:231], v[84:87]\n"
"s_barrier\n"
"v_exp_f32 v146, v146\n"
"v_mfma_f32_16x16x32_bf16 v[84:87], a[228:231], v[212:215], v[84:87]\n"
"v_cvt_pk_bf16_f32 v143, v78, v79\n"
"v_add_f32 v18, v18, v144\n"
"v_mfma_f32_16x16x32_bf16 v[84:87], a[224:227], v[196:199], v[84:87]\n"
"ds_read_b64_tr_b16 a[240:241], v2 offset:0\n"
"v_add_f32 v18, v18, v145\n"
"v_mfma_f32_16x16x32_bf16 v[84:87], a[220:223], v[180:183], v[84:87]\n"
"v_exp_f32 v147, v147\n"
"v_mfma_f32_16x16x32_bf16 v[84:87], a[216:219], v[164:167], v[84:87]\n"
"ds_read_b64_tr_b16 a[242:243], v2 offset:6144\n"
"v_cvt_pk_bf16_f32 v144, v144, v145\n"
"v_mfma_f32_16x16x32_bf16 v[88:91], a[216:219], v[168:171], v[56:59]\n"
"v_exp_f32 v148, v148\n"
"v_mfma_f32_16x16x32_bf16 v[88:91], a[220:223], v[184:187], v[88:91]\n"
"ds_read_b64_tr_b16 a[244:245], v2 offset:12288\n"
"v_add_f32 v18, v18, v146\n"
"v_mfma_f32_16x16x32_bf16 v[88:91], a[224:227], v[200:203], v[88:91]\n"
"v_exp_f32 v149, v149\n"
"v_mfma_f32_16x16x32_bf16 v[88:91], a[228:231], v[216:219], v[88:91]\n"
"ds_read_b64_tr_b16 a[246:247], v2 offset:18432\n"
"v_add_f32 v18, v18, v147\n"
"v_mfma_f32_16x16x32_bf16 v[88:91], a[232:235], v[232:235], v[88:91]\n"
"v_exp_f32 v150, v150\n"
"v_mfma_f32_16x16x32_bf16 v[88:91], a[236:239], v[248:251], v[88:91]\n"
"ds_read_b64_tr_b16 a[248:249], v3 offset:0\n"
"v_cvt_pk_bf16_f32 v145, v146, v147\n"
"v_mfma_f32_16x16x32_bf16 v[92:95], a[236:239], v[252:255], v[60:63]\n"
"v_exp_f32 v151, v151\n"
"v_mfma_f32_16x16x32_bf16 v[92:95], a[232:235], v[236:239], v[92:95]\n"
"ds_read_b64_tr_b16 a[250:251], v3 offset:6144\n"
"v_add_f32 v19, v19, v148\n"
"v_mfma_f32_16x16x32_bf16 v[92:95], a[228:231], v[220:223], v[92:95]\n"
"v_add_f32 v19, v19, v149\n"
"v_cvt_pk_bf16_f32 v148, v148, v149\n"
"v_mfma_f32_16x16x32_bf16 v[92:95], a[224:227], v[204:207], v[92:95]\n"
"ds_read_b64_tr_b16 a[252:253], v3 offset:12288\n"
"v_add_f32 v19, v19, v150\n"
"v_mfma_f32_16x16x32_bf16 v[92:95], a[220:223], v[188:191], v[92:95]\n"
"v_exp_f32 v152, v152\n"
"v_mfma_f32_16x16x32_bf16 v[92:95], a[216:219], v[172:175], v[92:95]\n"
"s_waitcnt lgkmcnt(3)\n"
"ds_read_b64_tr_b16 a[254:255], v3 offset:18432\n"
"v_add_f32 v19, v19, v151\n"
"v_mfma_f32_16x16x32_bf16 a[0:3], a[240:243], v[96:99], a[0:3]\n"
"v_exp_f32 v153, v153\n"
"v_mfma_f32_16x16x32_bf16 a[0:3], a[244:247], v[112:115], a[0:3]\n"
"s_add_u32 m0, s41, 0\n"
"ds_read_b64_tr_b16 a[216:217], v4 offset:0\n"
"v_cvt_pk_bf16_f32 v149, v150, v151\n"
"v_mfma_f32_16x16x32_bf16 a[4:7], a[240:243], v[100:103], a[4:7]\n"
"buffer_load_dwordx4 v9, s[12:15], s43 offen offset:0 nt lds\n"
"v_mfma_f32_16x16x32_bf16 a[4:7], a[244:247], v[116:119], a[4:7]\n"
"ds_read_b64_tr_b16 a[218:219], v4 offset:6144\n"
"v_add_f32 v20, v20, v152\n"
"v_mfma_f32_16x16x32_bf16 a[8:11], a[240:243], v[104:107], a[8:11]\n"
"v_exp_f32 v154, v154\n"
"v_mfma_f32_16x16x32_bf16 a[8:11], a[244:247], v[120:123], a[8:11]\n"
"ds_read_b64_tr_b16 a[220:221], v4 offset:12288\n"
"v_add_f32 v20, v20, v153\n"
"v_mfma_f32_16x16x32_bf16 a[12:15], a[240:243], v[108:111], a[12:15]\n"
"v_exp_f32 v155, v155\n"
"v_mfma_f32_16x16x32_bf16 a[12:15], a[244:247], v[124:127], a[12:15]\n"
"s_waitcnt lgkmcnt(3)\n"
"ds_read_b64_tr_b16 a[222:223], v4 offset:18432\n"
"v_cvt_pk_bf16_f32 v152, v152, v153\n"
"v_mfma_f32_16x16x32_bf16 a[16:19], a[248:251], v[96:99], a[16:19]\n"
"v_exp_f32 v156, v156\n"
"v_mfma_f32_16x16x32_bf16 a[16:19], a[252:255], v[112:115], a[16:19]\n"
"ds_read_b64_tr_b16 a[224:225], v5 offset:0\n"
"v_add_f32 v20, v20, v154\n"
"v_mfma_f32_16x16x32_bf16 a[20:23], a[248:251], v[100:103], a[20:23]\n"
"v_exp_f32 v157, v157\n"
"v_mfma_f32_16x16x32_bf16 a[20:23], a[252:255], v[116:119], a[20:23]\n"
"ds_read_b64_tr_b16 a[226:227], v5 offset:6144\n"
"v_add_f32 v20, v20, v155\n"
"v_mfma_f32_16x16x32_bf16 a[24:27], a[248:251], v[104:107], a[24:27]\n"
"v_exp_f32 v158, v158\n"
"v_mfma_f32_16x16x32_bf16 a[24:27], a[252:255], v[120:123], a[24:27]\n"
"ds_read_b64_tr_b16 a[228:229], v5 offset:12288\n"
"v_cvt_pk_bf16_f32 v153, v154, v155\n"
"v_mfma_f32_16x16x32_bf16 a[28:31], a[248:251], v[108:111], a[28:31]\n"
"buffer_load_dwordx4 v10, s[12:15], s43 offen offset:1024 nt lds\n"
"v_mfma_f32_16x16x32_bf16 a[28:31], a[252:255], v[124:127], a[28:31]\n"
"s_waitcnt lgkmcnt(3)\n"
"ds_read_b64_tr_b16 a[230:231], v5 offset:18432\n"
"v_add_f32 v21, v21, v156\n"
"v_mfma_f32_16x16x32_bf16 a[32:35], a[216:219], v[96:99], a[32:35]\n"
"v_exp_f32 v159, v159\n"
"v_mfma_f32_16x16x32_bf16 a[32:35], a[220:223], v[112:115], a[32:35]\n"
"ds_read_b64_tr_b16 a[240:241], v2 offset:128\n"
"v_add_f32 v21, v21, v157\n"
"v_mfma_f32_16x16x32_bf16 a[36:39], a[216:219], v[100:103], a[36:39]\n"
"v_exp_f32 v80, v80\n"
"v_mfma_f32_16x16x32_bf16 a[36:39], a[220:223], v[116:119], a[36:39]\n"
"ds_read_b64_tr_b16 a[242:243], v2 offset:6272\n"
"v_cvt_pk_bf16_f32 v156, v156, v157\n"
"v_mfma_f32_16x16x32_bf16 a[40:43], a[216:219], v[104:107], a[40:43]\n"
"v_exp_f32 v81, v81\n"
"v_mfma_f32_16x16x32_bf16 a[40:43], a[220:223], v[120:123], a[40:43]\n"
"ds_read_b64_tr_b16 a[244:245], v2 offset:12416\n"
"v_add_f32 v21, v21, v158\n"
"v_mfma_f32_16x16x32_bf16 a[44:47], a[216:219], v[108:111], a[44:47]\n"
"v_add_f32 v21, v21, v159\n"
"v_cvt_pk_bf16_f32 v157, v158, v159\n"
"v_mfma_f32_16x16x32_bf16 a[44:47], a[220:223], v[124:127], a[44:47]\n"
"s_waitcnt lgkmcnt(3)\n"
"ds_read_b64_tr_b16 a[246:247], v2 offset:18560\n"
"v_add_f32 v18, v18, v80\n"
"v_mfma_f32_16x16x32_bf16 a[48:51], a[224:227], v[96:99], a[48:51]\n"
"v_exp_f32 v82, v82\n"
"v_mfma_f32_16x16x32_bf16 a[48:51], a[228:231], v[112:115], a[48:51]\n"
"ds_read_b64_tr_b16 a[248:249], v3 offset:128\n"
"v_add_f32 v18, v18, v81\n"
"v_mfma_f32_16x16x32_bf16 a[52:55], a[224:227], v[100:103], a[52:55]\n"
"buffer_load_dwordx4 v11, s[12:15], s43 offen offset:2048 nt lds\n"
"v_mfma_f32_16x16x32_bf16 a[52:55], a[228:231], v[116:119], a[52:55]\n"
"ds_read_b64_tr_b16 a[250:251], v3 offset:6272\n"
"v_cvt_pk_bf16_f32 v146, v80, v81\n"
"v_mfma_f32_16x16x32_bf16 a[56:59], a[224:227], v[104:107], a[56:59]\n"
"s_add_u32 s43, s43, 27648\n"
"v_exp_f32 v83, v83\n"
"v_mfma_f32_16x16x32_bf16 a[56:59], a[228:231], v[120:123], a[56:59]\n"
"ds_read_b64_tr_b16 a[252:253], v3 offset:12416\n"
"v_add_f32 v18, v18, v82\n"
"v_mfma_f32_16x16x32_bf16 a[60:63], a[224:227], v[108:111], a[60:63]\n"
"s_or_b32 s43, s43, 0x18000\n"
"v_exp_f32 v84, v84\n"
"v_mfma_f32_16x16x32_bf16 a[60:63], a[228:231], v[124:127], a[60:63]\n"
"s_waitcnt lgkmcnt(3)\n"
"ds_read_b64_tr_b16 a[254:255], v3 offset:18560\n"
"v_add_f32 v18, v18, v83\n"
"v_mfma_f32_16x16x32_bf16 a[64:67], a[240:243], v[96:99], a[64:67]\n"
"s_sub_u32 s62, s43, 0x60000\n"
"v_exp_f32 v85, v85\n"
"v_mfma_f32_16x16x32_bf16 a[64:67], a[244:247], v[112:115], a[64:67]\n"
"ds_read_b64_tr_b16 a[216:217], v4 offset:128\n"
"v_cvt_pk_bf16_f32 v147, v82, v83\n"
"v_mfma_f32_16x16x32_bf16 a[68:71], a[240:243], v[100:103], a[68:71]\n"
"s_min_u32 s43, s43, s62\n"
"v_exp_f32 v86, v86\n"
"v_mfma_f32_16x16x32_bf16 a[68:71], a[244:247], v[116:119], a[68:71]\n"
"ds_read_b64_tr_b16 a[218:219], v4 offset:6272\n"
"v_add_f32 v19, v19, v84\n"
"v_mfma_f32_16x16x32_bf16 a[72:75], a[240:243], v[104:107], a[72:75]\n"
"v_exp_f32 v87, v87\n"
"v_mfma_f32_16x16x32_bf16 a[72:75], a[244:247], v[120:123], a[72:75]\n"
"s_add_u32 m0, s41, 12288\n"
"ds_read_b64_tr_b16 a[220:221], v4 offset:12416\n"
"v_add_f32 v19, v19, v85\n"
"v_mfma_f32_16x16x32_bf16 a[76:79], a[240:243], v[108:111], a[76:79]\n"
"buffer_load_dwordx4 v9, s[12:15], s43 offen offset:0 nt lds\n"
"v_mfma_f32_16x16x32_bf16 a[76:79], a[244:247], v[124:127], a[76:79]\n"
"s_waitcnt lgkmcnt(3)\n"
"ds_read_b64_tr_b16 a[222:223], v4 offset:18560\n"
"v_cvt_pk_bf16_f32 v150, v84, v85\n"
"v_mfma_f32_16x16x32_bf16 a[80:83], a[248:251], v[96:99], a[80:83]\n"
"v_exp_f32 v88, v88\n"
"v_mfma_f32_16x16x32_bf16 a[80:83], a[252:255], v[112:115], a[80:83]\n"
"ds_read_b64_tr_b16 a[224:225], v5 offset:128\n"
"v_add_f32 v19, v19, v86\n"
"v_mfma_f32_16x16x32_bf16 a[84:87], a[248:251], v[100:103], a[84:87]\n"
"v_exp_f32 v89, v89\n"
"v_mfma_f32_16x16x32_bf16 a[84:87], a[252:255], v[116:119], a[84:87]\n"
"ds_read_b64_tr_b16 a[226:227], v5 offset:6272\n"
"v_add_f32 v19, v19, v87\n"
"v_mfma_f32_16x16x32_bf16 a[88:91], a[248:251], v[104:107], a[88:91]\n"
"v_exp_f32 v90, v90\n"
"v_mfma_f32_16x16x32_bf16 a[88:91], a[252:255], v[120:123], a[88:91]\n"
"ds_read_b64_tr_b16 a[228:229], v5 offset:12416\n"
"v_cvt_pk_bf16_f32 v151, v86, v87\n"
"v_mfma_f32_16x16x32_bf16 a[92:95], a[248:251], v[108:111], a[92:95]\n"
"v_exp_f32 v91, v91\n"
"v_mfma_f32_16x16x32_bf16 a[92:95], a[252:255], v[124:127], a[92:95]\n"
"s_waitcnt lgkmcnt(3)\n"
"ds_read_b64_tr_b16 a[230:231], v5 offset:18560\n"
"v_add_f32 v20, v20, v88\n"
"v_mfma_f32_16x16x32_bf16 a[96:99], a[216:219], v[96:99], a[96:99]\n"
"v_add_f32 v20, v20, v89\n"
"v_cvt_pk_bf16_f32 v154, v88, v89\n"
"v_mfma_f32_16x16x32_bf16 a[96:99], a[220:223], v[112:115], a[96:99]\n"
"ds_read_b64_tr_b16 a[240:241], v2 offset:256\n"
"v_add_f32 v20, v20, v90\n"
"v_mfma_f32_16x16x32_bf16 a[100:103], a[216:219], v[100:103], a[100:103]\n"
"buffer_load_dwordx4 v10, s[12:15], s43 offen offset:1024 nt lds\n"
"v_mfma_f32_16x16x32_bf16 a[100:103], a[220:223], v[116:119], a[100:103]\n"
"ds_read_b64_tr_b16 a[242:243], v2 offset:6400\n"
"v_add_f32 v20, v20, v91\n"
"v_mfma_f32_16x16x32_bf16 a[104:107], a[216:219], v[104:107], a[104:107]\n"
"v_exp_f32 v92, v92\n"
"v_mfma_f32_16x16x32_bf16 a[104:107], a[220:223], v[120:123], a[104:107]\n"
"ds_read_b64_tr_b16 a[244:245], v2 offset:12544\n"
"v_cvt_pk_bf16_f32 v155, v90, v91\n"
"v_mfma_f32_16x16x32_bf16 a[108:111], a[216:219], v[108:111], a[108:111]\n"
"v_exp_f32 v93, v93\n"
"v_mfma_f32_16x16x32_bf16 a[108:111], a[220:223], v[124:127], a[108:111]\n"
"s_waitcnt lgkmcnt(3)\n"
"ds_read_b64_tr_b16 a[246:247], v2 offset:18688\n"
"v_add_f32 v21, v21, v92\n"
"v_mfma_f32_16x16x32_bf16 a[112:115], a[224:227], v[96:99], a[112:115]\n"
"v_exp_f32 v94, v94\n"
"v_mfma_f32_16x16x32_bf16 a[112:115], a[228:231], v[112:115], a[112:115]\n"
"ds_read_b64_tr_b16 a[248:249], v3 offset:256\n"
"v_add_f32 v21, v21, v93\n"
"v_mfma_f32_16x16x32_bf16 a[116:119], a[224:227], v[100:103], a[116:119]\n"
"v_exp_f32 v95, v95\n"
"v_mfma_f32_16x16x32_bf16 a[116:119], a[228:231], v[116:119], a[116:119]\n"
"ds_read_b64_tr_b16 a[250:251], v3 offset:6400\n"
"v_cvt_pk_bf16_f32 v158, v92, v93\n"
"v_mfma_f32_16x16x32_bf16 a[120:123], a[224:227], v[104:107], a[120:123]\n"
"v_add_f32 v21, v21, v94\n"
"v_add_f32 v21, v21, v95\n"
"v_mfma_f32_16x16x32_bf16 a[120:123], a[228:231], v[120:123], a[120:123]\n"
"ds_read_b64_tr_b16 a[252:253], v3 offset:12544\n"
"v_cvt_pk_bf16_f32 v159, v94, v95\n"
"v_mfma_f32_16x16x32_bf16 a[124:127], a[224:227], v[108:111], a[124:127]\n"
"buffer_load_dwordx4 v11, s[12:15], s43 offen offset:2048 nt lds\n"
"v_mfma_f32_16x16x32_bf16 a[124:127], a[228:231], v[124:127], a[124:127]\n"
"L_pg8:\n"
"s_cbranch_execz L_bk2\n"
"s_waitcnt lgkmcnt(3)\n"
"ds_read_b64_tr_b16 a[254:255], v3 offset:18688\n"
"v_max3_f32 v22, v18, v19, v20\n"
"v_mfma_f32_16x16x32_bf16 a[128:131], a[240:243], v[96:99], a[128:131]\n"
"v_max_f32 v22, v22, v21\n"
"v_cmp_lt_f32 vcc, s54, v22\n"
"v_mfma_f32_16x16x32_bf16 a[128:131], a[244:247], v[112:115], a[128:131]\n"
"ds_read_b64_tr_b16 a[216:217], v4 offset:256\n"
"v_mfma_f32_16x16x32_bf16 a[132:135], a[240:243], v[100:103], a[132:135]\n"
"s_add_u32 s41, s41, s46\n"
"v_mfma_f32_16x16x32_bf16 a[132:135], a[244:247], v[116:119], a[132:135]\n"
"ds_read_b64_tr_b16 a[218:219], v4 offset:6400\n"
"v_mfma_f32_16x16x32_bf16 a[136:139], a[240:243], v[104:107], a[136:139]\n"
"s_add_u32 s42, s42, 27648\n"
"v_mfma_f32_16x16x32_bf16 a[136:139], a[244:247], v[120:123], a[136:139]\n"
"ds_read_b64_tr_b16 a[220:221], v4 offset:12544\n"
"v_mfma_f32_16x16x32_bf16 a[140:143], a[240:243], v[108:111], a[140:143]\n"
"s_or_b32 s42, s42, 0x18000\n"
"v_mfma_f32_16x16x32_bf16 a[140:143], a[244:247], v[124:127], a[140:143]\n"
"s_waitcnt lgkmcnt(3)\n"
"ds_read_b64_tr_b16 a[222:223], v4 offset:18688\n"
"v_mfma_f32_16x16x32_bf16 a[144:147], a[248:251], v[96:99], a[144:147]\n"
"s_sub_u32 s62, s42, 0x60000\n"
"v_mfma_f32_16x16x32_bf16 a[144:147], a[252:255], v[112:115], a[144:147]\n"
"ds_read_b64_tr_b16 a[224:225], v5 offset:256\n"
"v_mfma_f32_16x16x32_bf16 a[148:151], a[248:251], v[100:103], a[148:151]\n"
"s_min_u32 s42, s42, s62\n"
"v_mfma_f32_16x16x32_bf16 a[148:151], a[252:255], v[116:119], a[148:151]\n"
"ds_read_b64_tr_b16 a[226:227], v5 offset:6400\n"
"v_mfma_f32_16x16x32_bf16 a[152:155], a[248:251], v[104:107], a[152:155]\n"
"s_add_u32 s43, s43, 27648\n"
"v_mfma_f32_16x16x32_bf16 a[152:155], a[252:255], v[120:123], a[152:155]\n"
"ds_read_b64_tr_b16 a[228:229], v5 offset:12544\n"
"v_mfma_f32_16x16x32_bf16 a[156:159], a[248:251], v[108:111], a[156:159]\n"
"s_or_b32 s43, s43, 0x18000\n"
"v_mfma_f32_16x16x32_bf16 a[156:159], a[252:255], v[124:127], a[156:159]\n"
"s_waitcnt lgkmcnt(3)\n"
"ds_read_b64_tr_b16 a[230:231], v5 offset:18688\n"
"v_mfma_f32_16x16x32_bf16 a[160:163], a[216:219], v[96:99], a[160:163]\n"
"ds_read_b128 a[192:195], v0 offset:0\n"
"v_mfma_f32_16x16x32_bf16 a[160:163], a[220:223], v[112:115], a[160:163]\n"
"s_sub_u32 s62, s43, 0x60000\n"
"v_mfma_f32_16x16x32_bf16 a[164:167], a[216:219], v[100:103], a[164:167]\n"
"ds_read_b128 a[196:199], v1 offset:0\n"
"v_mfma_f32_16x16x32_bf16 a[164:167], a[220:223], v[116:119], a[164:167]\n"
"s_min_u32 s43, s43, s62\n"
"v_mfma_f32_16x16x32_bf16 a[168:171], a[216:219], v[104:107], a[168:171]\n"
"s_mov_b32 s47, s44\n"
"s_sub_u32 s48, s48, 1\n"
"ds_read_b128 a[200:203], v0 offset:128\n"
"v_mfma_f32_16x16x32_bf16 a[168:171], a[220:223], v[120:123], a[168:171]\n"
"s_mov_b32 s44, s45\n"
"v_mfma_f32_16x16x32_bf16 a[172:175], a[216:219], v[108:111], a[172:175]\n"
"s_mov_b32 s45, s46\n"
"ds_read_b128 a[204:207], v1 offset:128\n"
"v_mfma_f32_16x16x32_bf16 a[172:175], a[220:223], v[124:127], a[172:175]\n"
"s_waitcnt lgkmcnt(4)\n"
"s_mov_b32 s46, s47\n"
"v_mfma_f32_16x16x32_bf16 a[176:179], a[224:227], v[96:99], a[176:179]\n"
"ds_read_b128 a[208:211], v0 offset:256\n"
"v_mfma_f32_16x16x32_bf16 a[176:179], a[228:231], v[112:115], a[176:179]\n"
"v_mfma_f32_16x16x32_bf16 a[180:183], a[224:227], v[100:103], a[180:183]\n"
"ds_read_b128 a[212:215], v1 offset:256\n"
"v_mfma_f32_16x16x32_bf16 a[180:183], a[228:231], v[116:119], a[180:183]\n"
"v_mfma_f32_16x16x32_bf16 a[184:187], a[224:227], v[104:107], a[184:187]\n"
"v_mfma_f32_16x16x32_bf16 a[184:187], a[228:231], v[120:123], a[184:187]\n"
"s_cmp_lg_u64 vcc, 0\n"
"v_mfma_f32_16x16x32_bf16 a[188:191], a[224:227], v[108:111], a[188:191]\n"
"s_waitcnt lgkmcnt(3)\n"
"v_mfma_f32_16x16x32_bf16 a[188:191], a[228:231], v[124:127], a[188:191]\n"
"s_cbranch_scc1 L_rc3\n"
"L_rr3:\n"
"s_cmp_lg_u32 s48, 0\n"
"s_cbranch_scc1 L_loop\n"
"s_branch L_sel\n"
".p2align 6\n"
"L_loopw:\n"
"v_mfma_f32_16x16x32_bf16 v[96:99], a[192:195], v[160:163], v[48:51]\n"
"v_add_f32 v14, v14, v18\n"
"v_add_f32 v15, v15, v19\n"
"v_mfma_f32_16x16x32_bf16 v[96:99], a[196:199], v[176:179], v[96:99]\n"
"s_add_u32 m0, s40, 0\n"
"v_add_f32 v16, v16, v20\n"
"v_add_f32 v17, v17, v21\n"
"v_mfma_f32_16x16x32_bf16 v[96:99], a[200:203], v[192:195], v[96:99]\n"
"s_waitcnt lgkmcnt(0)\n"
"buffer_load_dwordx4 v6, s[8:11], s42 offen offset:0 nt lds\n"
"v_mfma_f32_16x16x32_bf16 v[96:99], a[204:207], v[208:211], v[96:99]\n"
"ds_read_b128 a[216:219], v0 offset:6144\n"
"v_add_u32 v2, s46, v2\n"
"v_mfma_f32_16x16x32_bf16 v[96:99], a[208:211], v[224:227], v[96:99]\n"
"s_add_u32 s55, s55, 18\n"
"v_add_u32 v3, s46, v3\n"
"v_add_u32 v4, s46, v4\n"
"v_mfma_f32_16x16x32_bf16 v[96:99], a[212:215], v[240:243], v[96:99]\n"
"v_add_u32 v5, s46, v5\n"
"v_mfma_f32_16x16x32_bf16 v[100:103], a[212:215], v[244:247], v[52:55]\n"
"ds_read_b128 a[220:223], v1 offset:6144\n"
"v_mfma_f32_16x16x32_bf16 v[100:103], a[208:211], v[228:231], v[100:103]\n"
"v_mfma_f32_16x16x32_bf16 v[100:103], a[204:207], v[212:215], v[100:103]\n"
"v_mfma_f32_16x16x32_bf16 v[100:103], a[200:203], v[196:199], v[100:103]\n"
"ds_read_b128 a[224:227], v0 offset:6272\n"
"v_mfma_f32_16x16x32_bf16 v[100:103], a[196:199], v[180:183], v[100:103]\n"
"v_exp_f32 v96, v96\n"
"v_mfma_f32_16x16x32_bf16 v[100:103], a[192:195], v[164:167], v[100:103]\n"
"v_exp_f32 v97, v97\n"
"v_mfma_f32_16x16x32_bf16 v[104:107], a[192:195], v[168:171], v[56:59]\n"
"ds_read_b128 a[228:231], v1 offset:6272\n"
"v_add_f32 v18, v96, v97\n"
"v_mfma_f32_16x16x32_bf16 v[104:107], a[196:199], v[184:187], v[104:107]\n"
"v_exp_f32 v98, v98\n"
"v_mfma_f32_16x16x32_bf16 v[104:107], a[200:203], v[200:203], v[104:107]\n"
"buffer_load_dwordx4 v7, s[8:11], s42 offen offset:1024 nt lds\n"
"v_mfma_f32_16x16x32_bf16 v[104:107], a[204:207], v[216:219], v[104:107]\n"
"ds_read_b128 a[232:235], v0 offset:6400\n"
"v_cvt_pk_bf16_f32 v96, v96, v97\n"
"v_mfma_f32_16x16x32_bf16 v[104:107], a[208:211], v[232:235], v[104:107]\n"
"v_exp_f32 v99, v99\n"
"v_mfma_f32_16x16x32_bf16 v[104:107], a[212:215], v[248:251], v[104:107]\n"
"v_exp_f32 v100, v100\n"
"v_mfma_f32_16x16x32_bf16 v[108:111], a[212:215], v[252:255], v[60:63]\n"
"ds_read_b128 a[236:239], v1 offset:6400\n"
"v_add_f32 v18, v18, v98\n"
"v_mfma_f32_16x16x32_bf16 v[108:111], a[208:211], v[236:239], v[108:111]\n"
"v_exp_f32 v101, v101\n"
"v_mfma_f32_16x16x32_bf16 v[108:111], a[204:207], v[220:223], v[108:111]\n"
"v_exp_f32 v102, v102\n"
"v_mfma_f32_16x16x32_bf16 v[108:111], a[200:203], v[204:207], v[108:111]\n"
"v_add_f32 v18, v18, v99\n"
"v_cvt_pk_bf16_f32 v97, v98, v99\n"
"v_mfma_f32_16x16x32_bf16 v[108:111], a[196:199], v[188:191], v[108:111]\n"
"v_exp_f32 v103, v103\n"
"v_mfma_f32_16x16x32_bf16 v[108:111], a[192:195], v[172:175], v[108:111]\n"
"s_waitcnt lgkmcnt(0)\n"
"v_add_f32 v19, v100, v101\n"
"v_cvt_pk_bf16_f32 v100, v100, v101\n"
"v_mfma_f32_16x16x32_bf16 v[64:67], a[216:219], v[160:163], v[48:51]\n"
"v_exp_f32 v104, v104\n"
"v_mfma_f32_16x16x32_bf16 v[64:67], a[220:223], v[176:179], v[64:67]\n"
"v_exp_f32 v105, v105\n"
"v_mfma_f32_16x16x32_bf16 v[64:67], a[224:227], v[192:195], v[64:67]\n"
"buffer_load_dwordx4 v8, s[8:11], s42 offen offset:2048 nt lds\n"
"v_mfma_f32_16x16x32_bf16 v[64:67], a[228:231], v[208:211], v[64:67]\n"
"ds_read_b128 a[192:195], v0 offset:12288\n"
"v_add_f32 v19, v19, v102\n"
"v_mfma_f32_16x16x32_bf16 v[64:67], a[232:235], v[224:227], v[64:67]\n"
"s_add_u32 s42, s42, 27648\n"
"v_exp_f32 v106, v106\n"
"v_mfma_f32_16x16x32_bf16 v[64:67], a[236:239], v[240:243], v[64:67]\n"
"s_or_b32 s42, s42, 0x18000\n"
"v_add_f32 v19, v19, v103\n"
"v_cvt_pk_bf16_f32 v101, v102, v103\n"
"v_mfma_f32_16x16x32_bf16 v[68:71], a[236:239], v[244:247], v[52:55]\n"
"ds_read_b128 a[196:199], v1 offset:12288\n"
"v_add_f32 v20, v104, v105\n"
"v_mfma_f32_16x16x32_bf16 v[68:71], a[232:235], v[228:231], v[68:71]\n"
"s_sub_u32 s62, s42, 0x60000\n"
"v_exp_f32 v107, v107\n"
"v_mfma_f32_16x16x32_bf16 v[68:71], a[228:231], v[212:215], v[68:71]\n"
"v_exp_f32 v108, v108\n"
"v_mfma_f32_16x16x32_bf16 v[68:71], a[224:227], v[196:199], v[68:71]\n"
"ds_read_b128 a[200:203], v0 offset:12416\n"
"v_cvt_pk_bf16_f32 v104, v104, v105\n"
"v_mfma_f32_16x16x32_bf16 v[68:71], a[220:223], v[180:183], v[68:71]\n"
"s_min_u32 s42, s42, s62\n"
"v_exp_f32 v109, v109\n"
"v_mfma_f32_16x16x32_bf16 v[68:71], a[216:219], v[164:167], v[68:71]\n"
"v_add_f32 v20, v20, v106\n"
"v_add_f32 v20, v20, v107\n"
"v_mfma_f32_16x16x32_bf16 v[72:75], a[216:219], v[168:171], v[56:59]\n"
"ds_read_b128 a[204:207], v1 offset:12416\n"
"v_cvt_pk_bf16_f32 v105, v106, v107\n"
"v_mfma_f32_16x16x32_bf16 v[72:75], a[220:223], v[184:187], v[72:75]\n"
"s_add_u32 m0, s40, 12288\n"
"v_exp_f32 v110, v110\n"
"v_mfma_f32_16x16x32_bf16 v[72:75], a[224:227], v[200:203], v[72:75]\n"
"buffer_load_dwordx4 v6, s[8:11], s42 offen offset:0 nt lds\n"
"v_mfma_f32_16x16x32_bf16 v[72:75], a[228:231], v[216:219], v[72:75]\n"
"ds_read_b128 a[208:211], v0 offset:12544\n"
"v_add_f32 v21, v108, v109\n"
"v_mfma_f32_16x16x32_bf16 v[72:75], a[232:235], v[232:235], v[72:75]\n"
"v_exp_f32 v111, v111\n"
"v_mfma_f32_16x16x32_bf16 v[72:75], a[236:239], v[248:251], v[72:75]\n"
"v_exp_f32 v64, v64\n"
"v_mfma_f32_16x16x32_bf16 v[76:79], a[236:239], v[252:255], v[60:63]\n"
"ds_read_b128 a[212:215], v1 offset:12544\n"
"v_cvt_pk_bf16_f32 v108, v108, v109\n"
"v_mfma_f32_16x16x32_bf16 v[76:79], a[232:235], v[236:239], v[76:79]\n"
"v_exp_f32 v65, v65\n"
"v_mfma_f32_16x16x32_bf16 v[76:79], a[228:231], v[220:223], v[76:79]\n"
"v_add_f32 v21, v21, v110\n"
"v_add_f32 v21, v21, v111\n"
"v_mfma_f32_16x16x32_bf16 v[76:79], a[224:227], v[204:207], v[76:79]\n"
"v_exp_f32 v66, v66\n"
"v_mfma_f32_16x16x32_bf16 v[76:79], a[220:223], v[188:191], v[76:79]\n"
"v_cvt_pk_bf16_f32 v109, v110, v111\n"
"v_add_f32 v18, v18, v64\n"
"v_mfma_f32_16x16x32_bf16 v[76:79], a[216:219], v[172:175], v[76:79]\n"
"s_waitcnt lgkmcnt(0)\n"
"v_exp_f32 v67, v67\n"
"v_mfma_f32_16x16x32_bf16 v[112:115], a[192:195], v[160:163], v[48:51]\n"
"v_add_f32 v18, v18, v65\n"
"v_cvt_pk_bf16_f32 v98, v64, v65\n"
"v_mfma_f32_16x16x32_bf16 v[112:115], a[196:199], v[176:179], v[112:115]\n"
"v_exp_f32 v68, v68\n"
"v_mfma_f32_16x16x32_bf16 v[112:115], a[200:203], v[192:195], v[112:115]\n"
"buffer_load_dwordx4 v7, s[8:11], s42 offen offset:1024 nt lds\n"
"v_mfma_f32_16x16x32_bf16 v[112:115], a[204:207], v[208:211], v[112:115]\n"
"ds_read_b128 a[216:219], v0 offset:18432\n"
"v_add_f32 v18, v18, v66\n"
"v_mfma_f32_16x16x32_bf16 v[112:115], a[208:211], v[224:227], v[112:115]\n"
"v_exp_f32 v69, v69\n"
"v_mfma_f32_16x16x32_bf16 v[112:115], a[212:215], v[240:243], v[112:115]\n"
"v_add_f32 v18, v18, v67\n"
"v_cvt_pk_bf16_f32 v99, v66, v67\n"
"v_mfma_f32_16x16x32_bf16 v[116:119], a[212:215], v[244:247], v[52:55]\n"
"ds_read_b128 a[220:223], v1 offset:18432\n"
"v_add_f32 v19, v19, v68\n"
"v_mfma_f32_16x16x32_bf16 v[116:119], a[208:211], v[228:231], v[116:119]\n"
"v_exp_f32 v70, v70\n"
"v_mfma_f32_16x16x32_bf16 v[116:119], a[204:207], v[212:215], v[116:119]\n"
"v_exp_f32 v71, v71\n"
"v_mfma_f32_16x16x32_bf16 v[116:119], a[200:203], v[196:199], v[116:119]\n"
"ds_read_b128 a[224:227], v0 offset:18560\n"
"v_add_f32 v19, v19, v69\n"
"v_mfma_f32_16x16x32_bf16 v[116:119], a[196:199], v[180:183], v[116:119]\n"
"v_exp_f32 v72, v72\n"
"v_mfma_f32_16x16x32_bf16 v[116:119], a[192:195], v[164:167], v[116:119]\n"
"v_cvt_pk_bf16_f32 v102, v68, v69\n"
"v_add_f32 v19, v19, v70\n"
"v_mfma_f32_16x16x32_bf16 v[120:123], a[192:195], v[168:171], v[56:59]\n"
"ds_read_b128 a[228:231], v1 offset:18560\n"
"v_add_f32 v19, v19, v71\n"
"v_mfma_f32_16x16x32_bf16 v[120:123], a[196:199], v[184:187], v[120:123]\n"
"v_exp_f32 v73, v73\n"
"v_mfma_f32_16x16x32_bf16 v[120:123], a[200:203], v[200:203], v[120:123]\n"
"buffer_load_dwordx4 v8, s[8:11], s42 offen offset:2048 nt lds\n"
"v_mfma_f32_16x16x32_bf16 v[120:123], a[204:207], v[216:219], v[120:123]\n"
"ds_read_b128 a[232:235], v0 offset:18688\n"
"v_cvt_pk_bf16_f32 v103, v70, v71\n"
"v_mfma_f32_16x16x32_bf16 v[120:123], a[208:211], v[232:235], v[120:123]\n"
"v_exp_f32 v74, v74\n"
"v_mfma_f32_16x16x32_bf16 v[120:123], a[212:215], v[248:251], v[120:123]\n"
"v_exp_f32 v75, v75\n"
"v_mfma_f32_16x16x32_bf16 v[124:127], a[212:215], v[252:255], v[60:63]\n"
"s_add_u32 s40, s40, s44\n"
"ds_read_b128 a[236:239], v1 offset:18688\n"
"v_add_f32 v20, v20, v72\n"
"v_mfma_f32_16x16x32_bf16 v[124:127], a[208:211], v[236:239], v[124:127]\n"
"v_add_f32 v20, v20, v73\n"
"v_cvt_pk_bf16_f32 v106, v72, v73\n"
"v_mfma_f32_16x16x32_bf16 v[124:127], a[204:207], v[220:223], v[124:127]\n"
"v_exp_f32 v76, v76\n"
"v_mfma_f32_16x16x32_bf16 v[124:127], a[200:203], v[204:207], v[124:127]\n"
"v_add_u32 v0, s45, v0\n"
"v_add_f32 v20, v20, v74\n"
"v_mfma_f32_16x16x32_bf16 v[124:127], a[196:199], v[188:191], v[124:127]\n"
"v_exp_f32 v77, v77\n"
"v_mfma_f32_16x16x32_bf16 v[124:127], a[192:195], v[172:175], v[124:127]\n"
"s_waitcnt lgkmcnt(0)\n"
"v_add_f32 v20, v20, v75\n"
"v_cvt_pk_bf16_f32 v107, v74, v75\n"
"v_mfma_f32_16x16x32_bf16 v[80:83], a[216:219], v[160:163], v[48:51]\n"
"v_add_u32 v1, s45, v1\n"
"v_add_f32 v21, v21, v76\n"
"v_mfma_f32_16x16x32_bf16 v[80:83], a[220:223], v[176:179], v[80:83]\n"
"v_exp_f32 v78, v78\n"
"v_mfma_f32_16x16x32_bf16 v[80:83], a[224:227], v[192:195], v[80:83]\n"
"v_exp_f32 v79, v79\n"
"v_mfma_f32_16x16x32_bf16 v[80:83], a[228:231], v[208:211], v[80:83]\n"
"v_add_f32 v21, v21, v77\n"
"v_cvt_pk_bf16_f32 v110, v76, v77\n"
"v_mfma_f32_16x16x32_bf16 v[80:83], a[232:235], v[224:227], v[80:83]\n"
"v_exp_f32 v112, v112\n"
"v_mfma_f32_16x16x32_bf16 v[80:83], a[236:239], v[240:243], v[80:83]\n"
"v_exp_f32 v113, v113\n"
"v_mfma_f32_16x16x32_bf16 v[84:87], a[236:239], v[244:247], v[52:55]\n"
"s_waitcnt vmcnt(12)\n"
"v_add_f32 v21, v21, v78\n"
"v_add_f32 v21, v21, v79\n"
"v_mfma_f32_16x16x32_bf16 v[84:87], a[232:235], v[228:231], v[84:87]\n"
"s_barrier\n"
"v_exp_f32 v114, v114\n"
"v_mfma_f32_16x16x32_bf16 v[84:87], a[228:231], v[212:215], v[84:87]\n"
"v_cvt_pk_bf16_f32 v111, v78, v79\n"
"v_add_f32 v18, v18, v112\n"
"v_mfma_f32_16x16x32_bf16 v[84:87], a[224:227], v[196:199], v[84:87]\n"
"ds_read_b64_tr_b16 a[240:241], v2 offset:0\n"
"v_add_f32 v18, v18, v113\n"
"v_mfma_f32_16x16x32_bf16 v[84:87], a[220:223], v[180:183], v[84:87]\n"
"v_exp_f32 v115, v115\n"
"v_mfma_f32_16x16x32_bf16 v[84:87], a[216:219], v[164:167], v[84:87]\n"
"ds_read_b64_tr_b16 a[242:243], v2 offset:6144\n"
"v_cvt_pk_bf16_f32 v112, v112, v113\n"
"v_mfma_f32_16x16x32_bf16 v[88:91], a[216:219], v[168:171], v[56:59]\n"
"v_exp_f32 v116, v116\n"
"v_mfma_f32_16x16x32_bf16 v[88:91], a[220:223], v[184:187], v[88:91]\n"
"ds_read_b64_tr_b16 a[244:245], v2 offset:12288\n"
"v_add_f32 v18, v18, v114\n"
"v_mfma_f32_16x16x32_bf16 v[88:91], a[224:227], v[200:203], v[88:91]\n"
"v_exp_f32 v117, v117\n"
"v_mfma_f32_16x16x32_bf16 v[88:91], a[228:231], v[216:219], v[88:91]\n"
"ds_read_b64_tr_b16 a[246:247], v2 offset:18432\n"
"v_add_f32 v18, v18, v115\n"
"v_mfma_f32_16x16x32_bf16 v[88:91], a[232:235], v[232:235], v[88:91]\n"
"v_exp_f32 v118, v118\n"
"v_mfma_f32_16x16x32_bf16 v[88:91], a[236:239], v[248:251], v[88:91]\n"
"ds_read_b64_tr_b16 a[248:249], v3 offset:0\n"
"v_cvt_pk_bf16_f32 v113, v114, v115\n"
"v_mfma_f32_16x16x32_bf16 v[92:95], a[236:239], v[252:255], v[60:63]\n"
"v_exp_f32 v119, v119\n"
"v_mfma_f32_16x16x32_bf16 v[92:95], a[232:235], v[236:239], v[92:95]\n"
"ds_read_b64_tr_b16 a[250:251], v3 offset:6144\n"
"v_add_f32 v19, v19, v116\n"
"v_mfma_f32_16x16x32_bf16 v[92:95], a[228:231], v[220:223], v[92:95]\n"
"v_add_f32 v19, v19, v117\n"
"v_cvt_pk_bf16_f32 v116, v116, v117\n"
"v_mfma_f32_16x16x32_bf16 v[92:95], a[224:227], v[204:207], v[92:95]\n"
"ds_read_b64_tr_b16 a[252:253], v3 offset:12288\n"
"v_add_f32 v19, v19, v118\n"
"v_mfma_f32_16x16x32_bf16 v[92:95], a[220:223], v[188:191], v[92:95]\n"
"v_exp_f32 v120, v120\n"
"v_mfma_f32_16x16x32_bf16 v[92:95], a[216:219], v[172:175], v[92:95]\n"
"s_waitcnt lgkmcnt(3)\n"
"ds_read_b64_tr_b16 a[254:255], v3 offset:18432\n"
"v_add_f32 v19, v19, v119\n"
"v_mfma_f32_16x16x32_bf16 a[0:3], a[240:243], v[128:131], a[0:3]\n"
"v_exp_f32 v121, v121\n"
"v_mfma_f32_16x16x32_bf16 a[0:3], a[244:247], v[144:147], a[0:3]\n"
"s_add_u32 m0, s41, 0\n"
"ds_read_b64_tr_b16 a[216:217], v4 offset:0\n"
"v_cvt_pk_bf16_f32 v117, v118, v119\n"
"v_mfma_f32_16x16x32_bf16 a[4:7], a[240:243], v[132:135], a[4:7]\n"
"buffer_load_dwordx4 v9, s[12:15], s43 offen offset:0 lds\n"
"v_mfma_f32_16x16x32_bf16 a[4:7], a[244:247], v[148:151], a[4:7]\n"
"ds_read_b64_tr_b16 a[218:219], v4 offset:6144\n"
"v_add_f32 v20, v20, v120\n"
"v_mfma_f32_16x16x32_bf16 a[8:11], a[240:243], v[136:139], a[8:11]\n"
"v_exp_f32 v122, v122\n"
"v_mfma_f32_16x16x32_bf16 a[8:11], a[244:247], v[152:155], a[8:11]\n"
"ds_read_b64_tr_b16 a[220:221], v4 offset:12288\n"
"v_add_f32 v20, v20, v121\n"
"v_mfma_f32_16x16x32_bf16 a[12:15], a[240:243], v[140:143], a[12:15]\n"
"v_exp_f32 v123, v123\n"
"v_mfma_f32_16x16x32_bf16 a[12:15], a[244:247], v[156:159], a[12:15]\n"
"s_waitcnt lgkmcnt(3)\n"
"ds_read_b64_tr_b16 a[222:223], v4 offset:18432\n"
"v_cvt_pk_bf16_f32 v120, v120, v121\n"
"v_mfma_f32_16x16x32_bf16 a[16:19], a[248:251], v[128:131], a[16:19]\n"
"v_exp_f32 v124, v124\n"
"v_mfma_f32_16x16x32_bf16 a[16:19], a[252:255], v[144:147], a[16:19]\n"
"ds_read_b64_tr_b16 a[224:225], v5 offset:0\n"
"v_add_f32 v20, v20, v122\n"
"v_mfma_f32_16x16x32_bf16 a[20:23], a[248:251], v[132:135], a[20:23]\n"
"v_exp_f32 v125, v125\n"
"v_mfma_f32_16x16x32_bf16 a[20:23], a[252:255], v[148:151], a[20:23]\n"
"ds_read_b64_tr_b16 a[226:227], v5 offset:6144\n"
"v_add_f32 v20, v20, v123\n"
"v_mfma_f32_16x16x32_bf16 a[24:27], a[248:251], v[136:139], a[24:27]\n"
"v_exp_f32 v126, v126\n"
"v_mfma_f32_16x16x32_bf16 a[24:27], a[252:255], v[152:155], a[24:27]\n"
"ds_read_b64_tr_b16 a[228:229], v5 offset:12288\n"
"v_cvt_pk_bf16_f32 v121, v122, v123\n"
"v_mfma_f32_16x16x32_bf16 a[28:31], a[248:251], v[140:143], a[28:31]\n"
"buffer_load_dwordx4 v10, s[12:15], s43 offen offset:1024 lds\n"
"v_mfma_f32_16x16x32_bf16 a[28:31], a[252:255], v[156:159], a[28:31]\n"
"s_waitcnt lgkmcnt(3)\n"
"ds_read_b64_tr_b16 a[230:231], v5 offset:18432\n"
"v_add_f32 v21, v21, v124\n"
"v_mfma_f32_16x16x32_bf16 a[32:35], a[216:219], v[128:131], a[32:35]\n"
"v_exp_f32 v127, v127\n"
"v_mfma_f32_16x16x32_bf16 a[32:35], a[220:223], v[144:147], a[32:35]\n"
"ds_read_b64_tr_b16 a[240:241], v2 offset:128\n"
"v_add_f32 v21, v21, v125\n"
"v_mfma_f32_16x16x32_bf16 a[36:39], a[216:219], v[132:135], a[36:39]\n"
"v_exp_f32 v80, v80\n"
"v_mfma_f32_16x16x32_bf16 a[36:39], a[220:223], v[148:151], a[36:39]\n"
"ds_read_b64_tr_b16 a[242:243], v2 offset:6272\n"
"v_cvt_pk_bf16_f32 v124, v124, v125\n"
"v_mfma_f32_16x16x32_bf16 a[40:43], a[216:219], v[136:139], a[40:43]\n"
"v_exp_f32 v81, v81\n"
"v_mfma_f32_16x16x32_bf16 a[40:43], a[220:223], v[152:155], a[40:43]\n"
"ds_read_b64_tr_b16 a[244:245], v2 offset:12416\n"
"v_add_f32 v21, v21, v126\n"
"v_mfma_f32_16x16x32_bf16 a[44:47], a[216:219], v[140:143], a[44:47]\n"
"v_add_f32 v21, v21, v127\n"
"v_cvt_pk_bf16_f32 v125, v126, v127\n"
"v_mfma_f32_16x16x32_bf16 a[44:47], a[220:223], v[156:159], a[44:47]\n"
"s_waitcnt lgkmcnt(3)\n"
"ds_read_b64_tr_b16 a[246:247], v2 offset:18560\n"
"v_add_f32 v18, v18, v80\n"
"v_mfma_f32_16x16x32_bf16 a[48:51], a[224:227], v[128:131], a[48:51]\n"
"v_exp_f32 v82, v82\n"
"v_mfma_f32_16x16x32_bf16 a[48:51], a[228:231], v[144:147], a[48:51]\n"
"ds_read_b64_tr_b16 a[248:249], v3 offset:128\n"
"v_add_f32 v18, v18, v81\n"
"v_mfma_f32_16x16x32_bf16 a[52:55], a[224:227], v[132:135], a[52:55]\n"
"buffer_load_dwordx4 v11, s[12:15], s43 offen offset:2048 lds\n"
"v_mfma_f32_16x16x32_bf16 a[52:55], a[228:231], v[148:151], a[52:55]\n"
"ds_read_b64_tr_b16 a[250:251], v3 offset:6272\n"
"v_cvt_pk_bf16_f32 v114, v80, v81\n"
"v_mfma_f32_16x16x32_bf16 a[56:59], a[224:227], v[136:139], a[56:59]\n"
"s_add_u32 s43, s43, 27648\n"
"v_exp_f32 v83, v83\n"
"v_mfma_f32_16x16x32_bf16 a[56:59], a[228:231], v[152:155], a[56:59]\n"
"ds_read_b64_tr_b16 a[252:253], v3 offset:12416\n"
"v_add_f32 v18, v18, v82\n"
"v_mfma_f32_16x16x32_bf16 a[60:63], a[224:227], v[140:143], a[60:63]\n"
"s_or_b32 s43, s43, 0x18000\n"
"v_exp_f32 v84, v84\n"
"v_mfma_f32_16x16x32_bf16 a[60:63], a[228:231], v[156:159], a[60:63]\n"
"s_waitcnt lgkmcnt(3)\n"
"ds_read_b64_tr_b16 a[254:255], v3 offset:18560\n"
"v_add_f32 v18, v18, v83\n"
"v_mfma_f32_16x16x32_bf16 a[64:67], a[240:243], v[128:131], a[64:67]\n"
"s_sub_u32 s62, s43, 0x60000\n"
"v_exp_f32 v85, v85\n"
"v_mfma_f32_16x16x32_bf16 a[64:67], a[244:247], v[144:147], a[64:67]\n"
"ds_read_b64_tr_b16 a[216:217], v4 offset:128\n"
"v_cvt_pk_bf16_f32 v115, v82, v83\n"
"v_mfma_f32_16x16x32_bf16 a[68:71], a[240:243], v[132:135], a[68:71]\n"
"s_min_u32 s43, s43, s62\n"
"v_exp_f32 v86, v86\n"
"v_mfma_f32_16x16x32_bf16 a[68:71], a[244:247], v[148:151], a[68:71]\n"
"ds_read_b64_tr_b16 a[218:219], v4 offset:6272\n"
"v_add_f32 v19, v19, v84\n"
"v_mfma_f32_16x16x32_bf16 a[72:75], a[240:243], v[136:139], a[72:75]\n"
"v_exp_f32 v87, v87\n"
"v_mfma_f32_16x16x32_bf16 a[72:75], a[244:247], v[152:155], a[72:75]\n"
"s_add_u32 m0, s41, 12288\n"
"ds_read_b64_tr_b16 a[220:221], v4 offset:12416\n"
"v_add_f32 v19, v19, v85\n"
"v_mfma_f32_16x16x32_bf16 a[76:79], a[240:243], v[140:143], a[76:79]\n"
"buffer_load_dwordx4 v9, s[12:15], s43 offen offset:0 lds\n"
"v_mfma_f32_16x16x32_bf16 a[76:79], a[244:247], v[156:159], a[76:79]\n"
"s_waitcnt lgkmcnt(3)\n"
"ds_read_b64_tr_b16 a[222:223], v4 offset:18560\n"
"v_cvt_pk_bf16_f32 v118, v84, v85\n"
"v_mfma_f32_16x16x32_bf16 a[80:83], a[248:251], v[128:131], a[80:83]\n"
"v_exp_f32 v88, v88\n"
"v_mfma_f32_16x16x32_bf16 a[80:83], a[252:255], v[144:147], a[80:83]\n"
"ds_read_b64_tr_b16 a[224:225], v5 offset:128\n"
"v_add_f32 v19, v19, v86\n"
"v_mfma_f32_16x16x32_bf16 a[84:87], a[248:251], v[132:135], a[84:87]\n"
"v_exp_f32 v89, v89\n"
"v_mfma_f32_16x16x32_bf16 a[84:87], a[252:255], v[148:151], a[84:87]\n"
"ds_read_b64_tr_b16 a[226:227], v5 offset:6272\n"
"v_add_f32 v19, v19, v87\n"
"v_mfma_f32_16x16x32_bf16 a[88:91], a[248:251], v[136:139], a[88:91]\n"
"v_exp_f32 v90, v90\n"
"v_mfma_f32_16x16x32_bf16 a[88:91], a[252:255], v[152:155], a[88:91]\n"
"ds_read_b64_tr_b16 a[228:229], v5 offset:12416\n"
"v_cvt_pk_bf16_f32 v119, v86, v87\n"
"v_mfma_f32_16x16x32_bf16 a[92:95], a[248:251], v[140:143], a[92:95]\n"
"v_exp_f32 v91, v91\n"
"v_mfma_f32_16x16x32_bf16 a[92:95], a[252:255], v[156:159], a[92:95]\n"
"s_waitcnt lgkmcnt(3)\n"
"ds_read_b64_tr_b16 a[230:231], v5 offset:18560\n"
"v_add_f32 v20, v20, v88\n"
"v_mfma_f32_16x16x32_bf16 a[96:99], a[216:219], v[128:131], a[96:99]\n"
"v_add_f32 v20, v20, v89\n"
"v_cvt_pk_bf16_f32 v122, v88, v89\n"
"v_mfma_f32_16x16x32_bf16 a[96:99], a[220:223], v[144:147], a[96:99]\n"
"ds_read_b64_tr_b16 a[240:241], v2 offset:256\n"
"v_add_f32 v20, v20, v90\n"
"v_mfma_f32_16x16x32_bf16 a[100:103], a[216:219], v[132:135], a[100:103]\n"
"buffer_load_dwordx4 v10, s[12:15], s43 offen offset:1024 nt lds\n"
"v_mfma_f32_16x16x32_bf16 a[100:103], a[220:223], v[148:151], a[100:103]\n"
"ds_read_b64_tr_b16 a[242:243], v2 offset:6400\n"
"v_add_f32 v20, v20, v91\n"
"v_mfma_f32_16x16x32_bf16 a[104:107], a[216:219], v[136:139], a[104:107]\n"
"v_exp_f32 v92, v92\n"
"v_mfma_f32_16x16x32_bf16 a[104:107], a[220:223], v[152:155], a[104:107]\n"
"ds_read_b64_tr_b16 a[244:245], v2 offset:12544\n"
"v_cvt_pk_bf16_f32 v123, v90, v91\n"
"v_mfma_f32_16x16x32_bf16 a[108:111], a[216:219], v[140:143], a[108:111]\n"
"v_exp_f32 v93, v93\n"
"v_mfma_f32_16x16x32_bf16 a[108:111], a[220:223], v[156:159], a[108:111]\n"
"s_waitcnt lgkmcnt(3)\n"
"ds_read_b64_tr_b16 a[246:247], v2 offset:18688\n"
"v_add_f32 v21, v21, v92\n"
"v_mfma_f32_16x16x32_bf16 a[112:115], a[224:227], v[128:131], a[112:115]\n"
"v_exp_f32 v94, v94\n"
"v_mfma_f32_16x16x32_bf16 a[112:115], a[228:231], v[144:147], a[112:115]\n"
"ds_read_b64_tr_b16 a[248:249], v3 offset:256\n"
"v_add_f32 v21, v21, v93\n"
"v_mfma_f32_16x16x32_bf16 a[116:119], a[224:227], v[132:135], a[116:119]\n"
"v_exp_f32 v95, v95\n"
"v_mfma_f32_16x16x32_bf16 a[116:119], a[228:231], v[148:151], a[116:119]\n"
"ds_read_b64_tr_b16 a[250:251], v3 offset:6400\n"
"v_cvt_pk_bf16_f32 v126, v92, v93\n"
"v_mfma_f32_16x16x32_bf16 a[120:123], a[224:227], v[136:139], a[120:123]\n"
"v_add_f32 v21, v21, v94\n"
"v_add_f32 v21, v21, v95\n"
"v_mfma_f32_16x16x32_bf16 a[120:123], a[228:231], v[152:155], a[120:123]\n"
"ds_read_b64_tr_b16 a[252:253], v3 offset:12544\n"
"v_cvt_pk_bf16_f32 v127, v94, v95\n"
"v_mfma_f32_16x16x32_bf16 a[124:127], a[224:227], v[140:143], a[124:127]\n"
"buffer_load_dwordx4 v11, s[12:15], s43 offen offset:2048 nt lds\n"
"v_mfma_f32_16x16x32_bf16 a[124:127], a[228:231], v[156:159], a[124:127]\n"
"s_waitcnt lgkmcnt(3)\n"
"ds_read_b64_tr_b16 a[254:255], v3 offset:18688\n"
"v_max3_f32 v22, v18, v19, v20\n"
"v_mfma_f32_16x16x32_bf16 a[128:131], a[240:243], v[128:131], a[128:131]\n"
"v_max_f32 v22, v22, v21\n"
"v_cmp_lt_f32 vcc, s54, v22\n"
"v_mfma_f32_16x16x32_bf16 a[128:131], a[244:247], v[144:147], a[128:131]\n"
"ds_read_b64_tr_b16 a[216:217], v4 offset:256\n"
"v_mfma_f32_16x16x32_bf16 a[132:135], a[240:243], v[132:135], a[132:135]\n"
"s_add_u32 s41, s41, s46\n"
"v_mfma_f32_16x16x32_bf16 a[132:135], a[244:247], v[148:151], a[132:135]\n"
"ds_read_b64_tr_b16 a[218:219], v4 offset:6400\n"
"v_mfma_f32_16x16x32_bf16 a[136:139], a[240:243], v[136:139], a[136:139]\n"
"s_add_u32 s42, s42, 27648\n"
"v_mfma_f32_16x16x32_bf16 a[136:139], a[244:247], v[152:155], a[136:139]\n"
"ds_read_b64_tr_b16 a[220:221], v4 offset:12544\n"
"v_mfma_f32_16x16x32_bf16 a[140:143], a[240:243], v[140:143], a[140:143]\n"
"s_or_b32 s42, s42, 0x18000\n"
"v_mfma_f32_16x16x32_bf16 a[140:143], a[244:247], v[156:159], a[140:143]\n"
"s_waitcnt lgkmcnt(3)\n"
"ds_read_b64_tr_b16 a[222:223], v4 offset:18688\n"
"v_mfma_f32_16x16x32_bf16 a[144:147], a[248:251], v[128:131], a[144:147]\n"
"s_sub_u32 s62, s42, 0x60000\n"
"v_mfma_f32_16x16x32_bf16 a[144:147], a[252:255], v[144:147], a[144:147]\n"
"ds_read_b64_tr_b16 a[224:225], v5 offset:256\n"
"v_mfma_f32_16x16x32_bf16 a[148:151], a[248:251], v[132:135], a[148:151]\n"
"s_min_u32 s42, s42, s62\n"
"v_mfma_f32_16x16x32_bf16 a[148:151], a[252:255], v[148:151], a[148:151]\n"
"ds_read_b64_tr_b16 a[226:227], v5 offset:6400\n"
"v_mfma_f32_16x16x32_bf16 a[152:155], a[248:251], v[136:139], a[152:155]\n"
"s_add_u32 s43, s43, 27648\n"
"v_mfma_f32_16x16x32_bf16 a[152:155], a[252:255], v[152:155], a[152:155]\n"
"ds_read_b64_tr_b16 a[228:229], v5 offset:12544\n"
"v_mfma_f32_16x16x32_bf16 a[156:159], a[248:251], v[140:143], a[156:159]\n"
"s_or_b32 s43, s43, 0x18000\n"
"v_mfma_f32_16x16x32_bf16 a[156:159], a[252:255], v[156:159], a[156:159]\n"
"s_waitcnt lgkmcnt(3)\n"
"ds_read_b64_tr_b16 a[230:231], v5 offset:18688\n"
"v_mfma_f32_16x16x32_bf16 a[160:163], a[216:219], v[128:131], a[160:163]\n"
"ds_read_b128 a[192:195], v0 offset:0\n"
"v_mfma_f32_16x16x32_bf16 a[160:163], a[220:223], v[144:147], a[160:163]\n"
"s_sub_u32 s62, s43, 0x60000\n"
"v_mfma_f32_16x16x32_bf16 a[164:167], a[216:219], v[132:135], a[164:167]\n"
"ds_read_b128 a[196:199], v1 offset:0\n"
"v_mfma_f32_16x16x32_bf16 a[164:167], a[220:223], v[148:151], a[164:167]\n"
"s_min_u32 s43, s43, s62\n"
"v_mfma_f32_16x16x32_bf16 a[168:171], a[216:219], v[136:139], a[168:171]\n"
"s_mov_b32 s47, s44\n"
"ds_read_b128 a[200:203], v0 offset:128\n"
"v_mfma_f32_16x16x32_bf16 a[168:171], a[220:223], v[152:155], a[168:171]\n"
"s_mov_b32 s44, s45\n"
"v_mfma_f32_16x16x32_bf16 a[172:175], a[216:219], v[140:143], a[172:175]\n"
"s_mov_b32 s45, s46\n"
"ds_read_b128 a[204:207], v1 offset:128\n"
"v_mfma_f32_16x16x32_bf16 a[172:175], a[220:223], v[156:159], a[172:175]\n"
"s_waitcnt lgkmcnt(4)\n"
"s_mov_b32 s46, s47\n"
"v_mfma_f32_16x16x32_bf16 a[176:179], a[224:227], v[128:131], a[176:179]\n"
"ds_read_b128 a[208:211], v0 offset:256\n"
"v_mfma_f32_16x16x32_bf16 a[176:179], a[228:231], v[144:147], a[176:179]\n"
"v_mfma_f32_16x16x32_bf16 a[180:183], a[224:227], v[132:135], a[180:183]\n"
"ds_read_b128 a[212:215], v1 offset:256\n"
"v_mfma_f32_16x16x32_bf16 a[180:183], a[228:231], v[148:151], a[180:183]\n"
"v_mfma_f32_16x16x32_bf16 a[184:187], a[224:227], v[136:139], a[184:187]\n"
"v_mfma_f32_16x16x32_bf16 a[184:187], a[228:231], v[152:155], a[184:187]\n"
"s_cmp_lg_u64 vcc, 0\n"
"v_mfma_f32_16x16x32_bf16 a[188:191], a[224:227], v[140:143], a[188:191]\n"
"s_waitcnt lgkmcnt(3)\n"
"v_mfma_f32_16x16x32_bf16 a[188:191], a[228:231], v[156:159], a[188:191]\n"
"s_cbranch_scc1 L_rc4\n"
"L_rr4:\n"
"v_mfma_f32_16x16x32_bf16 v[128:131], a[192:195], v[160:163], v[48:51]\n"
"v_add_f32 v14, v14, v18\n"
"v_add_f32 v15, v15, v19\n"
"v_mfma_f32_16x16x32_bf16 v[128:131], a[196:199], v[176:179], v[128:131]\n"
"s_add_u32 m0, s40, 0\n"
"v_add_f32 v16, v16, v20\n"
"v_add_f32 v17, v17, v21\n"
"v_mfma_f32_16x16x32_bf16 v[128:131], a[200:203], v[192:195], v[128:131]\n"
"s_waitcnt lgkmcnt(0)\n"
"buffer_load_dwordx4 v6, s[8:11], s42 offen offset:0 nt lds\n"
"v_mfma_f32_16x16x32_bf16 v[128:131], a[204:207], v[208:211], v[128:131]\n"
"ds_read_b128 a[216:219], v0 offset:6144\n"
"v_add_u32 v2, s46, v2\n"
"v_mfma_f32_16x16x32_bf16 v[128:131], a[208:211], v[224:227], v[128:131]\n"
"s_add_u32 s55, s55, 18\n"
"v_add_u32 v3, s46, v3\n"
"v_add_u32 v4, s46, v4\n"
"v_mfma_f32_16x16x32_bf16 v[128:131], a[212:215], v[240:243], v[128:131]\n"
"v_add_u32 v5, s46, v5\n"
"v_mfma_f32_16x16x32_bf16 v[132:135], a[212:215], v[244:247], v[52:55]\n"
"ds_read_b128 a[220:223], v1 offset:6144\n"
"v_mfma_f32_16x16x32_bf16 v[132:135], a[208:211], v[228:231], v[132:135]\n"
"v_mfma_f32_16x16x32_bf16 v[132:135], a[204:207], v[212:215], v[132:135]\n"
"v_mfma_f32_16x16x32_bf16 v[132:135], a[200:203], v[196:199], v[132:135]\n"
"ds_read_b128 a[224:227], v0 offset:6272\n"
"v_mfma_f32_16x16x32_bf16 v[132:135], a[196:199], v[180:183], v[132:135]\n"
"v_exp_f32 v128, v128\n"
"v_mfma_f32_16x16x32_bf16 v[132:135], a[192:195], v[164:167], v[132:135]\n"
"v_exp_f32 v129, v129\n"
"v_mfma_f32_16x16x32_bf16 v[136:139], a[192:195], v[168:171], v[56:59]\n"
"ds_read_b128 a[228:231], v1 offset:6272\n"
"v_add_f32 v18, v128, v129\n"
"v_mfma_f32_16x16x32_bf16 v[136:139], a[196:199], v[184:187], v[136:139]\n"
"v_exp_f32 v130, v130\n"
"v_mfma_f32_16x16x32_bf16 v[136:139], a[200:203], v[200:203], v[136:139]\n"
"buffer_load_dwordx4 v7, s[8:11], s42 offen offset:1024 nt lds\n"
"v_mfma_f32_16x16x32_bf16 v[136:139], a[204:207], v[216:219], v[136:139]\n"
"ds_read_b128 a[232:235], v0 offset:6400\n"
"v_cvt_pk_bf16_f32 v128, v128, v129\n"
"v_mfma_f32_16x16x32_bf16 v[136:139], a[208:211], v[232:235], v[136:139]\n"
"L_pg9:\n"
"s_cbranch_execz L_bk2\n"
"v_exp_f32 v131, v131\n"
"v_mfma_f32_16x16x32_bf16 v[136:139], a[212:215], v[248:251], v[136:139]\n"
"v_exp_f32 v132, v132\n"
"v_mfma_f32_16x16x32_bf16 v[140:143], a[212:215], v[252:255], v[60:63]\n"
"ds_read_b128 a[236:239], v1 offset:6400\n"
"v_add_f32 v18, v18, v130\n"
"v_mfma_f32_16x16x32_bf16 v[140:143], a[208:211], v[236:239], v[140:143]\n"
"v_exp_f32 v133, v133\n"
"v_mfma_f32_16x16x32_bf16 v[140:143], a[204:207], v[220:223], v[140:143]\n"
"v_exp_f32 v134, v134\n"
"v_mfma_f32_16x16x32_bf16 v[140:143], a[200:203], v[204:207], v[140:143]\n"
"v_add_f32 v18, v18, v131\n"
"v_cvt_pk_bf16_f32 v129, v130, v131\n"
"v_mfma_f32_16x16x32_bf16 v[140:143], a[196:199], v[188:191], v[140:143]\n"
"v_exp_f32 v135, v135\n"
"v_mfma_f32_16x16x32_bf16 v[140:143], a[192:195], v[172:175], v[140:143]\n"
"s_waitcnt lgkmcnt(0)\n"
"v_add_f32 v19, v132, v133\n"
"v_cvt_pk_bf16_f32 v132, v132, v133\n"
"v_mfma_f32_16x16x32_bf16 v[64:67], a[216:219], v[160:163], v[48:51]\n"
"v_exp_f32 v136, v136\n"
"v_mfma_f32_16x16x32_bf16 v[64:67], a[220:223], v[176:179], v[64:67]\n"
"v_exp_f32 v137, v137\n"
"v_mfma_f32_16x16x32_bf16 v[64:67], a[224:227], v[192:195], v[64:67]\n"
"buffer_load_dwordx4 v8, s[8:11], s42 offen offset:2048 nt lds\n"
"v_mfma_f32_16x16x32_bf16 v[64:67], a[228:231], v[208:211], v[64:67]\n"
"ds_read_b128 a[192:195], v0 offset:12288\n"
"v_add_f32 v19, v19, v134\n"
"v_mfma_f32_16x16x32_bf16 v[64:67], a[232:235], v[224:227], v[64:67]\n"
"s_add_u32 s42, s42, 27648\n"
"v_exp_f32 v138, v138\n"
"v_mfma_f32_16x16x32_bf16 v[64:67], a[236:239], v[240:243], v[64:67]\n"
"s_or_b32 s42, s42, 0x18000\n"
"v_add_f32 v19, v19, v135\n"
"v_cvt_pk_bf16_f32 v133, v134, v135\n"
"v_mfma_f32_16x16x32_bf16 v[68:71], a[236:239], v[244:247], v[52:55]\n"
"ds_read_b128 a[196:199], v1 offset:12288\n"
"v_add_f32 v20, v136, v137\n"
"v_mfma_f32_16x16x32_bf16 v[68:71], a[232:235], v[228:231], v[68:71]\n"
"s_sub_u32 s62, s42, 0x60000\n"
"v_exp_f32 v139, v139\n"
"v_mfma_f32_16x16x32_bf16 v[68:71], a[228:231], v[212:215], v[68:71]\n"
"v_exp_f32 v140, v140\n"
"v_mfma_f32_16x16x32_bf16 v[68:71], a[224:227], v[196:199], v[68:71]\n"
"ds_read_b128 a[200:203], v0 offset:12416\n"
"v_cvt_pk_bf16_f32 v136, v136, v137\n"
"v_mfma_f32_16x16x32_bf16 v[68:71], a[220:223], v[180:183], v[68:71]\n"
"s_min_u32 s42, s42, s62\n"
"v_exp_f32 v141, v141\n"
"v_mfma_f32_16x16x32_bf16 v[68:71], a[216:219], v[164:167], v[68:71]\n"
"v_add_f32 v20, v20, v138\n"
"v_add_f32 v20, v20, v139\n"
"v_mfma_f32_16x16x32_bf16 v[72:75], a[216:219], v[168:171], v[56:59]\n"
"ds_read_b128 a[204:207], v1 offset:12416\n"
"v_cvt_pk_bf16_f32 v137, v138, v139\n"
"v_mfma_f32_16x16x32_bf16 v[72:75], a[220:223], v[184:187], v[72:75]\n"
"s_add_u32 m0, s40, 12288\n"
"v_exp_f32 v142, v142\n"
"v_mfma_f32_16x16x32_bf16 v[72:75], a[224:227], v[200:203], v[72:75]\n"
"buffer_load_dwordx4 v6, s[8:11], s42 offen offset:0 nt lds\n"
"v_mfma_f32_16x16x32_bf16 v[72:75], a[228:231], v[216:219], v[72:75]\n"
"ds_read_b128 a[208:211], v0 offset:12544\n"
"v_add_f32 v21, v140, v141\n"
"v_mfma_f32_16x16x32_bf16 v[72:75], a[232:235], v[232:235], v[72:75]\n"
"v_exp_f32 v143, v143\n"
"v_mfma_f32_16x16x32_bf16 v[72:75], a[236:239], v[248:251], v[72:75]\n"
"v_exp_f32 v64, v64\n"
"v_mfma_f32_16x16x32_bf16 v[76:79], a[236:239], v[252:255], v[60:63]\n"
"ds_read_b128 a[212:215], v1 offset:12544\n"
"v_cvt_pk_bf16_f32 v140, v140, v141\n"
"v_mfma_f32_16x16x32_bf16 v[76:79], a[232:235], v[236:239], v[76:79]\n"
"v_exp_f32 v65, v65\n"
"v_mfma_f32_16x16x32_bf16 v[76:79], a[228:231], v[220:223], v[76:79]\n"
"v_add_f32 v21, v21, v142\n"
"v_add_f32 v21, v21, v143\n"
"v_mfma_f32_16x16x32_bf16 v[76:79], a[224:227], v[204:207], v[76:79]\n"
"v_exp_f32 v66, v66\n"
"v_mfma_f32_16x16x32_bf16 v[76:79], a[220:223], v[188:191], v[76:79]\n"
"v_cvt_pk_bf16_f32 v141, v142, v143\n"
"v_add_f32 v18, v18, v64\n"
"v_mfma_f32_16x16x32_bf16 v[76:79], a[216:219], v[172:175], v[76:79]\n"
"s_waitcnt lgkmcnt(0)\n"
"v_exp_f32 v67, v67\n"
"v_mfma_f32_16x16x32_bf16 v[144:147], a[192:195], v[160:163], v[48:51]\n"
"v_add_f32 v18, v18, v65\n"
"v_cvt_pk_bf16_f32 v130, v64, v65\n"
"v_mfma_f32_16x16x32_bf16 v[144:147], a[196:199], v[176:179], v[144:147]\n"
"v_exp_f32 v68, v68\n"
"v_mfma_f32_16x16x32_bf16 v[144:147], a[200:203], v[192:195], v[144:147]\n"
"buffer_load_dwordx4 v7, s[8:11], s42 offen offset:1024 nt lds\n"
"v_mfma_f32_16x16x32_bf16 v[144:147], a[204:207], v[208:211], v[144:147]\n"
"ds_read_b128 a[216:219], v0 offset:18432\n"
"v_add_f32 v18, v18, v66\n"
"v_mfma_f32_16x16x32_bf16 v[144:147], a[208:211], v[224:227], v[144:147]\n"
"v_exp_f32 v69, v69\n"
"v_mfma_f32_16x16x32_bf16 v[144:147], a[212:215], v[240:243], v[144:147]\n"
"v_add_f32 v18, v18, v67\n"
"v_cvt_pk_bf16_f32 v131, v66, v67\n"
"v_mfma_f32_16x16x32_bf16 v[148:151], a[212:215], v[244:247], v[52:55]\n"
"ds_read_b128 a[220:223], v1 offset:18432\n"
"v_add_f32 v19, v19, v68\n"
"v_mfma_f32_16x16x32_bf16 v[148:151], a[208:211], v[228:231], v[148:151]\n"
"v_exp_f32 v70, v70\n"
"v_mfma_f32_16x16x32_bf16 v[148:151], a[204:207], v[212:215], v[148:151]\n"
"v_exp_f32 v71, v71\n"
"v_mfma_f32_16x16x32_bf16 v[148:151], a[200:203], v[196:199], v[148:151]\n"
"ds_read_b128 a[224:227], v0 offset:18560\n"
"v_add_f32 v19, v19, v69\n"
"v_mfma_f32_16x16x32_bf16 v[148:151], a[196:199], v[180:183], v[148:151]\n"
"v_exp_f32 v72, v72\n"
"v_mfma_f32_16x16x32_bf16 v[148:151], a[192:195], v[164:167], v[148:151]\n"
"v_cvt_pk_bf16_f32 v134, v68, v69\n"
"v_add_f32 v19, v19, v70\n"
"v_mfma_f32_16x16x32_bf16 v[152:155], a[192:195], v[168:171], v[56:59]\n"
"ds_read_b128 a[228:231], v1 offset:18560\n"
"v_add_f32 v19, v19, v71\n"
"v_mfma_f32_16x16x32_bf16 v[152:155], a[196:199], v[184:187], v[152:155]\n"
"v_exp_f32 v73, v73\n"
"v_mfma_f32_16x16x32_bf16 v[152:155], a[200:203], v[200:203], v[152:155]\n"
"buffer_load_dwordx4 v8, s[8:11], s42 offen offset:2048 nt lds\n"
"v_mfma_f32_16x16x32_bf16 v[152:155], a[204:207], v[216:219], v[152:155]\n"
"ds_read_b128 a[232:235], v0 offset:18688\n"
"v_cvt_pk_bf16_f32 v135, v70, v71\n"
"v_mfma_f32_16x16x32_bf16 v[152:155], a[208:211], v[232:235], v[152:155]\n"
"v_exp_f32 v74, v74\n"
"v_mfma_f32_16x16x32_bf16 v[152:155], a[212:215], v[248:251], v[152:155]\n"
"v_exp_f32 v75, v75\n"
"v_mfma_f32_16x16x32_bf16 v[156:159], a[212:215], v[252:255], v[60:63]\n"
"s_add_u32 s40, s40, s44\n"
"ds_read_b128 a[236:239], v1 offset:18688\n"
"v_add_f32 v20, v20, v72\n"
"v_mfma_f32_16x16x32_bf16 v[156:159], a[208:211], v[236:239], v[156:159]\n"
"v_add_f32 v20, v20, v73\n"
"v_cvt_pk_bf16_f32 v138, v72, v73\n"
"v_mfma_f32_16x16x32_bf16 v[156:159], a[204:207], v[220:223], v[156:159]\n"
"v_exp_f32 v76, v76\n"
"v_mfma_f32_16x16x32_bf16 v[156:159], a[200:203], v[204:207], v[156:159]\n"
"v_add_u32 v0, s45, v0\n"
"v_add_f32 v20, v20, v74\n"
"v_mfma_f32_16x16x32_bf16 v[156:159], a[196:199], v[188:191], v[156:159]\n"
"v_exp_f32 v77, v77\n"
"v_mfma_f32_16x16x32_bf16 v[156:159], a[192:195], v[172:175], v[156:159]\n"
"s_waitcnt lgkmcnt(0)\n"
"v_add_f32 v20, v20, v75\n"
"v_cvt_pk_bf16_f32 v139, v74, v75\n"
"v_mfma_f32_16x16x32_bf16 v[80:83], a[216:219], v[160:163], v[48:51]\n"
"v_add_u32 v1, s45, v1\n"
"v_add_f32 v21, v21, v76\n"
"v_mfma_f32_16x16x32_bf16 v[80:83], a[220:223], v[176:179], v[80:83]\n"
"v_exp_f32 v78, v78\n"
"v_mfma_f32_16x16x32_bf16 v[80:83], a[224:227], v[192:195], v[80:83]\n"
"v_exp_f32 v79, v79\n"
"v_mfma_f32_16x16x32_bf16 v[80:83], a[228:231], v[208:211], v[80:83]\n"
"v_add_f32 v21, v21, v77\n"
"v_cvt_pk_bf16_f32 v142, v76, v77\n"
"v_mfma_f32_16x16x32_bf16 v[80:83], a[232:235], v[224:227], v[80:83]\n"
"v_exp_f32 v144, v144\n"
"v_mfma_f32_16x16x32_bf16 v[80:83], a[236:239], v[240:243], v[80:83]\n"
"v_exp_f32 v145, v145\n"
"v_mfma_f32_16x16x32_bf16 v[84:87], a[236:239], v[244:247], v[52:55]\n"
"s_waitcnt vmcnt(12)\n"
"v_add_f32 v21, v21, v78\n"
"v_add_f32 v21, v21, v79\n"
"v_mfma_f32_16x16x32_bf16 v[84:87], a[232:235], v[228:231], v[84:87]\n"
"s_barrier\n"
"v_exp_f32 v146, v146\n"
"v_mfma_f32_16x16x32_bf16 v[84:87], a[228:231], v[212:215], v[84:87]\n"
"v_cvt_pk_bf16_f32 v143, v78, v79\n"
"v_add_f32 v18, v18, v144\n"
"v_mfma_f32_16x16x32_bf16 v[84:87], a[224:227], v[196:199], v[84:87]\n"
"ds_read_b64_tr_b16 a[240:241], v2 offset:0\n"
"v_add_f32 v18, v18, v145\n"
"v_mfma_f32_16x16x32_bf16 v[84:87], a[220:223], v[180:183], v[84:87]\n"
"v_exp_f32 v147, v147\n"
"v_mfma_f32_16x16x32_bf16 v[84:87], a[216:219], v[164:167], v[84:87]\n"
"ds_read_b64_tr_b16 a[242:243], v2 offset:6144\n"
"v_cvt_pk_bf16_f32 v144, v144, v145\n"
"v_mfma_f32_16x16x32_bf16 v[88:91], a[216:219], v[168:171], v[56:59]\n"
"v_exp_f32 v148, v148\n"
"v_mfma_f32_16x16x32_bf16 v[88:91], a[220:223], v[184:187], v[88:91]\n"
"ds_read_b64_tr_b16 a[244:245], v2 offset:12288\n"
"v_add_f32 v18, v18, v146\n"
"v_mfma_f32_16x16x32_bf16 v[88:91], a[224:227], v[200:203], v[88:91]\n"
"v_exp_f32 v149, v149\n"
"v_mfma_f32_16x16x32_bf16 v[88:91], a[228:231], v[216:219], v[88:91]\n"
"ds_read_b64_tr_b16 a[246:247], v2 offset:18432\n"
"v_add_f32 v18, v18, v147\n"
"v_mfma_f32_16x16x32_bf16 v[88:91], a[232:235], v[232:235], v[88:91]\n"
"v_exp_f32 v150, v150\n"
"v_mfma_f32_16x16x32_bf16 v[88:91], a[236:239], v[248:251], v[88:91]\n"
"ds_read_b64_tr_b16 a[248:249], v3 offset:0\n"
"v_cvt_pk_bf16_f32 v145, v146, v147\n"
"v_mfma_f32_16x16x32_bf16 v[92:95], a[236:239], v[252:255], v[60:63]\n"
"v_exp_f32 v151, v151\n"
"v_mfma_f32_16x16x32_bf16 v[92:95], a[232:235], v[236:239], v[92:95]\n"
"ds_read_b64_tr_b16 a[250:251], v3 offset:6144\n"
"v_add_f32 v19, v19, v148\n"
"v_mfma_f32_16x16x32_bf16 v[92:95], a[228:231], v[220:223], v[92:95]\n"
"v_add_f32 v19, v19, v149\n"
"v_cvt_pk_bf16_f32 v148, v148, v149\n"
"v_mfma_f32_16x16x32_bf16 v[92:95], a[224:227], v[204:207], v[92:95]\n"
"ds_read_b64_tr_b16 a[252:253], v3 offset:12288\n"
"v_add_f32 v19, v19, v150\n"
"v_mfma_f32_16x16x32_bf16 v[92:95], a[220:223], v[188:191], v[92:95]\n"
"v_exp_f32 v152, v152\n"
"v_mfma_f32_16x16x32_bf16 v[92:95], a[216:219], v[172:175], v[92:95]\n"
"s_waitcnt lgkmcnt(3)\n"
"ds_read_b64_tr_b16 a[254:255], v3 offset:18432\n"
"v_add_f32 v19, v19, v151\n"
"v_mfma_f32_16x16x32_bf16 a[0:3], a[240:243], v[96:99], a[0:3]\n"
"v_exp_f32 v153, v153\n"
"v_mfma_f32_16x16x32_bf16 a[0:3], a[244:247], v[112:115], a[0:3]\n"
"s_add_u32 m0, s41, 0\n"
"ds_read_b64_tr_b16 a[216:217], v4 offset:0\n"
"v_cvt_pk_bf16_f32 v149, v150, v151\n"
"v_mfma_f32_16x16x32_bf16 a[4:7], a[240:243], v[100:103], a[4:7]\n"
"buffer_load_dwordx4 v9, s[12:15], s43 offen offset:0 lds\n"
"v_mfma_f32_16x16x32_bf16 a[4:7], a[244:247], v[116:119], a[4:7]\n"
"ds_read_b64_tr_b16 a[218:219], v4 offset:6144\n"
"v_add_f32 v20, v20, v152\n"
"v_mfma_f32_16x16x32_bf16 a[8:11], a[240:243], v[104:107], a[8:11]\n"
"v_exp_f32 v154, v154\n"
"v_mfma_f32_16x16x32_bf16 a[8:11], a[244:247], v[120:123], a[8:11]\n"
"ds_read_b64_tr_b16 a[220:221], v4 offset:12288\n"
"v_add_f32 v20, v20, v153\n"
"v_mfma_f32_16x16x32_bf16 a[12:15], a[240:243], v[108:111], a[12:15]\n"
"v_exp_f32 v155, v155\n"
"v_mfma_f32_16x16x32_bf16 a[12:15], a[244:247], v[124:127], a[12:15]\n"
"s_waitcnt lgkmcnt(3)\n"
"ds_read_b64_tr_b16 a[222:223], v4 offset:18432\n"
"v_cvt_pk_bf16_f32 v152, v152, v153\n"
"v_mfma_f32_16x16x32_bf16 a[16:19], a[248:251], v[96:99], a[16:19]\n"
"v_exp_f32 v156, v156\n"
"v_mfma_f32_16x16x32_bf16 a[16:19], a[252:255], v[112:115], a[16:19]\n"
"ds_read_b64_tr_b16 a[224:225], v5 offset:0\n"
"v_add_f32 v20, v20, v154\n"
"v_mfma_f32_16x16x32_bf16 a[20:23], a[248:251], v[100:103], a[20:23]\n"
"v_exp_f32 v157, v157\n"
"v_mfma_f32_16x16x32_bf16 a[20:23], a[252:255], v[116:119], a[20:23]\n"
"ds_read_b64_tr_b16 a[226:227], v5 offset:6144\n"
"v_add_f32 v20, v20, v155\n"
"v_mfma_f32_16x16x32_bf16 a[24:27], a[248:251], v[104:107], a[24:27]\n"
"v_exp_f32 v158, v158\n"
"v_mfma_f32_16x16x32_bf16 a[24:27], a[252:255], v[120:123], a[24:27]\n"
"ds_read_b64_tr_b16 a[228:229], v5 offset:12288\n"
"v_cvt_pk_bf16_f32 v153, v154, v155\n"
"v_mfma_f32_16x16x32_bf16 a[28:31], a[248:251], v[108:111], a[28:31]\n"
"buffer_load_dwordx4 v10, s[12:15], s43 offen offset:1024 lds\n"
"v_mfma_f32_16x16x32_bf16 a[28:31], a[252:255], v[124:127], a[28:31]\n"
"s_waitcnt lgkmcnt(3)\n"
"ds_read_b64_tr_b16 a[230:231], v5 offset:18432\n"
"v_add_f32 v21, v21, v156\n"
"v_mfma_f32_16x16x32_bf16 a[32:35], a[216:219], v[96:99], a[32:35]\n"
"v_exp_f32 v159, v159\n"
"v_mfma_f32_16x16x32_bf16 a[32:35], a[220:223], v[112:115], a[32:35]\n"
"ds_read_b64_tr_b16 a[240:241], v2 offset:128\n"
"v_add_f32 v21, v21, v157\n"
"v_mfma_f32_16x16x32_bf16 a[36:39], a[216:219], v[100:103], a[36:39]\n"
"v_exp_f32 v80, v80\n"
"v_mfma_f32_16x16x32_bf16 a[36:39], a[220:223], v[116:119], a[36:39]\n"
"ds_read_b64_tr_b16 a[242:243], v2 offset:6272\n"
"v_cvt_pk_bf16_f32 v156, v156, v157\n"
"v_mfma_f32_16x16x32_bf16 a[40:43], a[216:219], v[104:107], a[40:43]\n"
"v_exp_f32 v81, v81\n"
"v_mfma_f32_16x16x32_bf16 a[40:43], a[220:223], v[120:123], a[40:43]\n"
"ds_read_b64_tr_b16 a[244:245], v2 offset:12416\n"
"v_add_f32 v21, v21, v158\n"
"v_mfma_f32_16x16x32_bf16 a[44:47], a[216:219], v[108:111], a[44:47]\n"
"v_add_f32 v21, v21, v159\n"
"v_cvt_pk_bf16_f32 v157, v158, v159\n"
"v_mfma_f32_16x16x32_bf16 a[44:47], a[220:223], v[124:127], a[44:47]\n"
"s_waitcnt lgkmcnt(3)\n"
"ds_read_b64_tr_b16 a[246:247], v2 offset:18560\n"
"v_add_f32 v18, v18, v80\n"
"v_mfma_f32_16x16x32_bf16 a[48:51], a[224:227], v[96:99], a[48:51]\n"
"v_exp_f32 v82, v82\n"
"v_mfma_f32_16x16x32_bf16 a[48:51], a[228:231], v[112:115], a[48:51]\n"
"ds_read_b64_tr_b16 a[248:249], v3 offset:128\n"
"v_add_f32 v18, v18, v81\n"
"v_mfma_f32_16x16x32_bf16 a[52:55], a[224:227], v[100:103], a[52:55]\n"
"buffer_load_dwordx4 v11, s[12:15], s43 offen offset:2048 lds\n"
"v_mfma_f32_16x16x32_bf16 a[52:55], a[228:231], v[116:119], a[52:55]\n"
"ds_read_b64_tr_b16 a[250:251], v3 offset:6272\n"
"v_cvt_pk_bf16_f32 v146, v80, v81\n"
"v_mfma_f32_16x16x32_bf16 a[56:59], a[224:227], v[104:107], a[56:59]\n"
"s_add_u32 s43, s43, 27648\n"
"v_exp_f32 v83, v83\n"
"v_mfma_f32_16x16x32_bf16 a[56:59], a[228:231], v[120:123], a[56:59]\n"
"ds_read_b64_tr_b16 a[252:253], v3 offset:12416\n"
"v_add_f32 v18, v18, v82\n"
"v_mfma_f32_16x16x32_bf16 a[60:63], a[224:227], v[108:111], a[60:63]\n"
"s_or_b32 s43, s43, 0x18000\n"
"v_exp_f32 v84, v84\n"
"v_mfma_f32_16x16x32_bf16 a[60:63], a[228:231], v[124:127], a[60:63]\n"
"s_waitcnt lgkmcnt(3)\n"
"ds_read_b64_tr_b16 a[254:255], v3 offset:18560\n"
"v_add_f32 v18, v18, v83\n"
"v_mfma_f32_16x16x32_bf16 a[64:67], a[240:243], v[96:99], a[64:67]\n"
"s_sub_u32 s62, s43, 0x60000\n"
"v_exp_f32 v85, v85\n"
"v_mfma_f32_16x16x32_bf16 a[64:67], a[244:247], v[112:115], a[64:67]\n"
"ds_read_b64_tr_b16 a[216:217], v4 offset:128\n"
"v_cvt_pk_bf16_f32 v147, v82, v83\n"
"v_mfma_f32_16x16x32_bf16 a[68:71], a[240:243], v[100:103], a[68:71]\n"
"s_min_u32 s43, s43, s62\n"
"v_exp_f32 v86, v86\n"
"v_mfma_f32_16x16x32_bf16 a[68:71], a[244:247], v[116:119], a[68:71]\n"
"ds_read_b64_tr_b16 a[218:219], v4 offset:6272\n"
"v_add_f32 v19, v19, v84\n"
"v_mfma_f32_16x16x32_bf16 a[72:75], a[240:243], v[104:107], a[72:75]\n"
"v_exp_f32 v87, v87\n"
"v_mfma_f32_16x16x32_bf16 a[72:75], a[244:247], v[120:123], a[72:75]\n"
"s_add_u32 m0, s41, 12288\n"
"ds_read_b64_tr_b16 a[220:221], v4 offset:12416\n"
"v_add_f32 v19, v19, v85\n"
"v_mfma_f32_16x16x32_bf16 a[76:79], a[240:243], v[108:111], a[76:79]\n"
"buffer_load_dwordx4 v9, s[12:15], s43 offen offset:0 lds\n"
"v_mfma_f32_16x16x32_bf16 a[76:79], a[244:247], v[124:127], a[76:79]\n"
"s_waitcnt lgkmcnt(3)\n"
"ds_read_b64_tr_b16 a[222:223], v4 offset:18560\n"
"v_cvt_pk_bf16_f32 v150, v84, v85\n"
"v_mfma_f32_16x16x32_bf16 a[80:83], a[248:251], v[96:99], a[80:83]\n"
"v_exp_f32 v88, v88\n"
"v_mfma_f32_16x16x32_bf16 a[80:83], a[252:255], v[112:115], a[80:83]\n"
"ds_read_b64_tr_b16 a[224:225], v5 offset:128\n"
"v_add_f32 v19, v19, v86\n"
"v_mfma_f32_16x16x32_bf16 a[84:87], a[248:251], v[100:103], a[84:87]\n"
"v_exp_f32 v89, v89\n"
"v_mfma_f32_16x16x32_bf16 a[84:87], a[252:255], v[116:119], a[84:87]\n"
"ds_read_b64_tr_b16 a[226:227], v5 offset:6272\n"
"v_add_f32 v19, v19, v87\n"
"v_mfma_f32_16x16x32_bf16 a[88:91], a[248:251], v[104:107], a[88:91]\n"
"v_exp_f32 v90, v90\n"
"v_mfma_f32_16x16x32_bf16 a[88:91], a[252:255], v[120:123], a[88:91]\n"
"ds_read_b64_tr_b16 a[228:229], v5 offset:12416\n"
"v_cvt_pk_bf16_f32 v151, v86, v87\n"
"v_mfma_f32_16x16x32_bf16 a[92:95], a[248:251], v[108:111], a[92:95]\n"
"v_exp_f32 v91, v91\n"
"v_mfma_f32_16x16x32_bf16 a[92:95], a[252:255], v[124:127], a[92:95]\n"
"s_waitcnt lgkmcnt(3)\n"
"ds_read_b64_tr_b16 a[230:231], v5 offset:18560\n"
"v_add_f32 v20, v20, v88\n"
"v_mfma_f32_16x16x32_bf16 a[96:99], a[216:219], v[96:99], a[96:99]\n"
"v_add_f32 v20, v20, v89\n"
"v_cvt_pk_bf16_f32 v154, v88, v89\n"
"v_mfma_f32_16x16x32_bf16 a[96:99], a[220:223], v[112:115], a[96:99]\n"
"ds_read_b64_tr_b16 a[240:241], v2 offset:256\n"
"v_add_f32 v20, v20, v90\n"
"v_mfma_f32_16x16x32_bf16 a[100:103], a[216:219], v[100:103], a[100:103]\n"
"buffer_load_dwordx4 v10, s[12:15], s43 offen offset:1024 nt lds\n"
"v_mfma_f32_16x16x32_bf16 a[100:103], a[220:223], v[116:119], a[100:103]\n"
"ds_read_b64_tr_b16 a[242:243], v2 offset:6400\n"
"v_add_f32 v20, v20, v91\n"
"v_mfma_f32_16x16x32_bf16 a[104:107], a[216:219], v[104:107], a[104:107]\n"
"v_exp_f32 v92, v92\n"
"v_mfma_f32_16x16x32_bf16 a[104:107], a[220:223], v[120:123], a[104:107]\n"
"ds_read_b64_tr_b16 a[244:245], v2 offset:12544\n"
"v_cvt_pk_bf16_f32 v155, v90, v91\n"
"v_mfma_f32_16x16x32_bf16 a[108:111], a[216:219], v[108:111], a[108:111]\n"
"v_exp_f32 v93, v93\n"
"v_mfma_f32_16x16x32_bf16 a[108:111], a[220:223], v[124:127], a[108:111]\n"
"s_waitcnt lgkmcnt(3)\n"
"ds_read_b64_tr_b16 a[246:247], v2 offset:18688\n"
"v_add_f32 v21, v21, v92\n"
"v_mfma_f32_16x16x32_bf16 a[112:115], a[224:227], v[96:99], a[112:115]\n"
"v_exp_f32 v94, v94\n"
"v_mfma_f32_16x16x32_bf16 a[112:115], a[228:231], v[112:115], a[112:115]\n"
"ds_read_b64_tr_b16 a[248:249], v3 offset:256\n"
"v_add_f32 v21, v21, v93\n"
"v_mfma_f32_16x16x32_bf16 a[116:119], a[224:227], v[100:103], a[116:119]\n"
"v_exp_f32 v95, v95\n"
"v_mfma_f32_16x16x32_bf16 a[116:119], a[228:231], v[116:119], a[116:119]\n"
"ds_read_b64_tr_b16 a[250:251], v3 offset:6400\n"
"v_cvt_pk_bf16_f32 v158, v92, v93\n"
"v_mfma_f32_16x16x32_bf16 a[120:123], a[224:227], v[104:107], a[120:123]\n"
"v_add_f32 v21, v21, v94\n"
"v_add_f32 v21, v21, v95\n"
"v_mfma_f32_16x16x32_bf16 a[120:123], a[228:231], v[120:123], a[120:123]\n"
"ds_read_b64_tr_b16 a[252:253], v3 offset:12544\n"
"v_cvt_pk_bf16_f32 v159, v94, v95\n"
"v_mfma_f32_16x16x32_bf16 a[124:127], a[224:227], v[108:111], a[124:127]\n"
"buffer_load_dwordx4 v11, s[12:15], s43 offen offset:2048 nt lds\n"
"v_mfma_f32_16x16x32_bf16 a[124:127], a[228:231], v[124:127], a[124:127]\n"
"s_waitcnt lgkmcnt(3)\n"
"ds_read_b64_tr_b16 a[254:255], v3 offset:18688\n"
"v_max3_f32 v22, v18, v19, v20\n"
"v_mfma_f32_16x16x32_bf16 a[128:131], a[240:243], v[96:99], a[128:131]\n"
"v_max_f32 v22, v22, v21\n"
"v_cmp_lt_f32 vcc, s54, v22\n"
"v_mfma_f32_16x16x32_bf16 a[128:131], a[244:247], v[112:115], a[128:131]\n"
"ds_read_b64_tr_b16 a[216:217], v4 offset:256\n"
"v_mfma_f32_16x16x32_bf16 a[132:135], a[240:243], v[100:103], a[132:135]\n"
"s_add_u32 s41, s41, s46\n"
"v_mfma_f32_16x16x32_bf16 a[132:135], a[244:247], v[116:119], a[132:135]\n"
"ds_read_b64_tr_b16 a[218:219], v4 offset:6400\n"
"v_mfma_f32_16x16x32_bf16 a[136:139], a[240:243], v[104:107], a[136:139]\n"
"s_add_u32 s42, s42, 27648\n"
"v_mfma_f32_16x16x32_bf16 a[136:139], a[244:247], v[120:123], a[136:139]\n"
"ds_read_b64_tr_b16 a[220:221], v4 offset:12544\n"
"v_mfma_f32_16x16x32_bf16 a[140:143], a[240:243], v[108:111], a[140:143]\n"
"s_or_b32 s42, s42, 0x18000\n"
"v_mfma_f32_16x16x32_bf16 a[140:143], a[244:247], v[124:127], a[140:143]\n"
"s_waitcnt lgkmcnt(3)\n"
"ds_read_b64_tr_b16 a[222:223], v4 offset:18688\n"
"v_mfma_f32_16x16x32_bf16 a[144:147], a[248:251], v[96:99], a[144:147]\n"
"s_sub_u32 s62, s42, 0x60000\n"
"v_mfma_f32_16x16x32_bf16 a[144:147], a[252:255], v[112:115], a[144:147]\n"
"ds_read_b64_tr_b16 a[224:225], v5 offset:256\n"
"v_mfma_f32_16x16x32_bf16 a[148:151], a[248:251], v[100:103], a[148:151]\n"
"s_min_u32 s42, s42, s62\n"
"v_mfma_f32_16x16x32_bf16 a[148:151], a[252:255], v[116:119], a[148:151]\n"
"ds_read_b64_tr_b16 a[226:227], v5 offset:6400\n"
"v_mfma_f32_16x16x32_bf16 a[152:155], a[248:251], v[104:107], a[152:155]\n"
"s_add_u32 s43, s43, 27648\n"
"v_mfma_f32_16x16x32_bf16 a[152:155], a[252:255], v[120:123], a[152:155]\n"
"ds_read_b64_tr_b16 a[228:229], v5 offset:12544\n"
"v_mfma_f32_16x16x32_bf16 a[156:159], a[248:251], v[108:111], a[156:159]\n"
"s_or_b32 s43, s43, 0x18000\n"
"v_mfma_f32_16x16x32_bf16 a[156:159], a[252:255], v[124:127], a[156:159]\n"
"s_waitcnt lgkmcnt(3)\n"
"ds_read_b64_tr_b16 a[230:231], v5 offset:18688\n"
"v_mfma_f32_16x16x32_bf16 a[160:163], a[216:219], v[96:99], a[160:163]\n"
"ds_read_b128 a[192:195], v0 offset:0\n"
"v_mfma_f32_16x16x32_bf16 a[160:163], a[220:223], v[112:115], a[160:163]\n"
"s_sub_u32 s62, s43, 0x60000\n"
"v_mfma_f32_16x16x32_bf16 a[164:167], a[216:219], v[100:103], a[164:167]\n"
"ds_read_b128 a[196:199], v1 offset:0\n"
"v_mfma_f32_16x16x32_bf16 a[164:167], a[220:223], v[116:119], a[164:167]\n"
"s_min_u32 s43, s43, s62\n"
"v_mfma_f32_16x16x32_bf16 a[168:171], a[216:219], v[104:107], a[168:171]\n"
"s_mov_b32 s47, s44\n"
"s_sub_u32 s48, s48, 1\n"
"ds_read_b128 a[200:203], v0 offset:128\n"
"v_mfma_f32_16x16x32_bf16 a[168:171], a[220:223], v[120:123], a[168:171]\n"
"s_mov_b32 s44, s45\n"
"v_mfma_f32_16x16x32_bf16 a[172:175], a[216:219], v[108:111], a[172:175]\n"
"s_mov_b32 s45, s46\n"
"ds_read_b128 a[204:207], v1 offset:128\n"
"v_mfma_f32_16x16x32_bf16 a[172:175], a[220:223], v[124:127], a[172:175]\n"
"s_waitcnt lgkmcnt(4)\n"
"s_mov_b32 s46, s47\n"
"v_mfma_f32_16x16x32_bf16 a[176:179], a[224:227], v[96:99], a[176:179]\n"
"ds_read_b128 a[208:211], v0 offset:256\n"
"v_mfma_f32_16x16x32_bf16 a[176:179], a[228:231], v[112:115], a[176:179]\n"
"v_mfma_f32_16x16x32_bf16 a[180:183], a[224:227], v[100:103], a[180:183]\n"
"ds_read_b128 a[212:215], v1 offset:256\n"
"v_mfma_f32_16x16x32_bf16 a[180:183], a[228:231], v[116:119], a[180:183]\n"
"v_mfma_f32_16x16x32_bf16 a[184:187], a[224:227], v[104:107], a[184:187]\n"
"v_mfma_f32_16x16x32_bf16 a[184:187], a[228:231], v[120:123], a[184:187]\n"
"s_cmp_lg_u64 vcc, 0\n"
"v_mfma_f32_16x16x32_bf16 a[188:191], a[224:227], v[108:111], a[188:191]\n"
"s_waitcnt lgkmcnt(3)\n"
"v_mfma_f32_16x16x32_bf16 a[188:191], a[228:231], v[124:127], a[188:191]\n"
"s_cbranch_scc1 L_rc5\n"
"L_rr5:\n"
"s_cmp_lg_u32 s48, 0\n"
"s_cbranch_scc1 L_loopw\n"
"L_sel:\n"
"v_mfma_f32_16x16x32_bf16 v[96:99], a[192:195], v[160:163], v[48:51]\n"
"v_add_f32 v14, v14, v18\n"
"v_add_f32 v15, v15, v19\n"
"v_mfma_f32_16x16x32_bf16 v[96:99], a[196:199], v[176:179], v[96:99]\n"
"v_add_f32 v16, v16, v20\n"
"v_add_f32 v17, v17, v21\n"
"v_mfma_f32_16x16x32_bf16 v[96:99], a[200:203], v[192:195], v[96:99]\n"
"s_waitcnt lgkmcnt(0)\n"
"v_add_u32 v2, s46, v2\n"
"v_add_u32 v3, s46, v3\n"
"v_mfma_f32_16x16x32_bf16 v[96:99], a[204:207], v[208:211], v[96:99]\n"
"ds_read_b128 a[216:219], v0 offset:6144\n"
"v_add_u32 v4, s46, v4\n"
"v_mfma_f32_16x16x32_bf16 v[96:99], a[208:211], v[224:227], v[96:99]\n"
"s_add_u32 s55, s55, 18\n"
"v_add_u32 v5, s46, v5\n"
"v_mfma_f32_16x16x32_bf16 v[96:99], a[212:215], v[240:243], v[96:99]\n"
"v_mfma_f32_16x16x32_bf16 v[100:103], a[212:215], v[244:247], v[52:55]\n"
"ds_read_b128 a[220:223], v1 offset:6144\n"
"v_mfma_f32_16x16x32_bf16 v[100:103], a[208:211], v[228:231], v[100:103]\n"
"v_mfma_f32_16x16x32_bf16 v[100:103], a[204:207], v[212:215], v[100:103]\n"
"v_mfma_f32_16x16x32_bf16 v[100:103], a[200:203], v[196:199], v[100:103]\n"
"ds_read_b128 a[224:227], v0 offset:6272\n"
"v_mfma_f32_16x16x32_bf16 v[100:103], a[196:199], v[180:183], v[100:103]\n"
"s_nop 0\n"
"v_exp_f32 v96, v96\n"
"v_mfma_f32_16x16x32_bf16 v[100:103], a[192:195], v[164:167], v[100:103]\n"
"v_exp_f32 v97, v97\n"
"v_mfma_f32_16x16x32_bf16 v[104:107], a[192:195], v[168:171], v[56:59]\n"
"ds_read_b128 a[228:231], v1 offset:6272\n"
"v_add_f32 v18, v96, v97\n"
"v_mfma_f32_16x16x32_bf16 v[104:107], a[196:199], v[184:187], v[104:107]\n"
"v_exp_f32 v98, v98\n"
"v_mfma_f32_16x16x32_bf16 v[104:107], a[200:203], v[200:203], v[104:107]\n"
"v_exp_f32 v99, v99\n"
"v_mfma_f32_16x16x32_bf16 v[104:107], a[204:207], v[216:219], v[104:107]\n"
"ds_read_b128 a[232:235], v0 offset:6400\n"
"v_cvt_pk_bf16_f32 v96, v96, v97\n"
"v_mfma_f32_16x16x32_bf16 v[104:107], a[208:211], v[232:235], v[104:107]\n"
"v_exp_f32 v100, v100\n"
"v_mfma_f32_16x16x32_bf16 v[104:107], a[212:215], v[248:251], v[104:107]\n"
"v_exp_f32 v101, v101\n"
"v_mfma_f32_16x16x32_bf16 v[108:111], a[212:215], v[252:255], v[60:63]\n"
"ds_read_b128 a[236:239], v1 offset:6400\n"
"v_add_f32 v18, v18, v98\n"
"v_mfma_f32_16x16x32_bf16 v[108:111], a[208:211], v[236:239], v[108:111]\n"
"v_exp_f32 v102, v102\n"
"v_mfma_f32_16x16x32_bf16 v[108:111], a[204:207], v[220:223], v[108:111]\n"
"buffer_load_dword v27, v39, s[80:83], s96 offen\n"
"v_mfma_f32_16x16x32_bf16 v[108:111], a[200:203], v[204:207], v[108:111]\n"
"s_add_u32 s96, s96, s36\n"
"v_add_f32 v18, v18, v99\n"
"v_cvt_pk_bf16_f32 v97, v98, v99\n"
"v_mfma_f32_16x16x32_bf16 v[108:111], a[196:199], v[188:191], v[108:111]\n"
"v_exp_f32 v103, v103\n"
"v_mfma_f32_16x16x32_bf16 v[108:111], a[192:195], v[172:175], v[108:111]\n"
"s_waitcnt lgkmcnt(0)\n"
"v_add_f32 v19, v100, v101\n"
"v_cvt_pk_bf16_f32 v100, v100, v101\n"
"v_mfma_f32_16x16x32_bf16 v[64:67], a[216:219], v[160:163], v[48:51]\n"
"v_exp_f32 v104, v104\n"
"v_mfma_f32_16x16x32_bf16 v[64:67], a[220:223], v[176:179], v[64:67]\n"
"v_exp_f32 v105, v105\n"
"v_mfma_f32_16x16x32_bf16 v[64:67], a[224:227], v[192:195], v[64:67]\n"
"v_add_f32 v19, v19, v102\n"
"v_add_f32 v19, v19, v103\n"
"v_mfma_f32_16x16x32_bf16 v[64:67], a[228:231], v[208:211], v[64:67]\n"
"ds_read_b128 a[192:195], v0 offset:12288\n"
"v_cvt_pk_bf16_f32 v101, v102, v103\n"
"v_mfma_f32_16x16x32_bf16 v[64:67], a[232:235], v[224:227], v[64:67]\n"
"v_exp_f32 v106, v106\n"
"v_mfma_f32_16x16x32_bf16 v[64:67], a[236:239], v[240:243], v[64:67]\n"
"v_exp_f32 v107, v107\n"
"v_mfma_f32_16x16x32_bf16 v[68:71], a[236:239], v[244:247], v[52:55]\n"
"ds_read_b128 a[196:199], v1 offset:12288\n"
"v_add_f32 v20, v104, v105\n"
"v_mfma_f32_16x16x32_bf16 v[68:71], a[232:235], v[228:231], v[68:71]\n"
"v_exp_f32 v108, v108\n"
"v_mfma_f32_16x16x32_bf16 v[68:71], a[228:231], v[212:215], v[68:71]\n"
"v_exp_f32 v109, v109\n"
"v_mfma_f32_16x16x32_bf16 v[68:71], a[224:227], v[196:199], v[68:71]\n"
"ds_read_b128 a[200:203], v0 offset:12416\n"
"v_cvt_pk_bf16_f32 v104, v104, v105\n"
"v_mfma_f32_16x16x32_bf16 v[68:71], a[220:223], v[180:183], v[68:71]\n"
"v_add_f32 v20, v20, v106\n"
"v_add_f32 v20, v20, v107\n"
"v_mfma_f32_16x16x32_bf16 v[68:71], a[216:219], v[164:167], v[68:71]\n"
"v_exp_f32 v110, v110\n"
"v_mfma_f32_16x16x32_bf16 v[72:75], a[216:219], v[168:171], v[56:59]\n"
"ds_read_b128 a[204:207], v1 offset:12416\n"
"v_cvt_pk_bf16_f32 v105, v106, v107\n"
"v_mfma_f32_16x16x32_bf16 v[72:75], a[220:223], v[184:187], v[72:75]\n"
"v_exp_f32 v111, v111\n"
"v_mfma_f32_16x16x32_bf16 v[72:75], a[224:227], v[200:203], v[72:75]\n"
"v_add_f32 v21, v108, v109\n"
"v_cvt_pk_bf16_f32 v108, v108, v109\n"
"v_mfma_f32_16x16x32_bf16 v[72:75], a[228:231], v[216:219], v[72:75]\n"
"ds_read_b128 a[208:211], v0 offset:12544\n"
"v_add_f32 v21, v21, v110\n"
"v_mfma_f32_16x16x32_bf16 v[72:75], a[232:235], v[232:235], v[72:75]\n"
"v_exp_f32 v64, v64\n"
"v_mfma_f32_16x16x32_bf16 v[72:75], a[236:239], v[248:251], v[72:75]\n"
"v_exp_f32 v65, v65\n"
"v_mfma_f32_16x16x32_bf16 v[76:79], a[236:239], v[252:255], v[60:63]\n"
"ds_read_b128 a[212:215], v1 offset:12544\n"
"v_add_f32 v21, v21, v111\n"
"v_mfma_f32_16x16x32_bf16 v[76:79], a[232:235], v[236:239], v[76:79]\n"
"v_exp_f32 v66, v66\n"
"v_mfma_f32_16x16x32_bf16 v[76:79], a[228:231], v[220:223], v[76:79]\n"
"v_cvt_pk_bf16_f32 v109, v110, v111\n"
"v_add_f32 v18, v18, v64\n"
"v_mfma_f32_16x16x32_bf16 v[76:79], a[224:227], v[204:207], v[76:79]\n"
"v_exp_f32 v67, v67\n"
"v_mfma_f32_16x16x32_bf16 v[76:79], a[220:223], v[188:191], v[76:79]\n"
"v_add_f32 v18, v18, v65\n"
"v_cvt_pk_bf16_f32 v98, v64, v65\n"
"v_mfma_f32_16x16x32_bf16 v[76:79], a[216:219], v[172:175], v[76:79]\n"
"s_waitcnt lgkmcnt(0)\n"
"v_exp_f32 v68, v68\n"
"v_mfma_f32_16x16x32_bf16 v[112:115], a[192:195], v[160:163], v[48:51]\n"
"v_exp_f32 v69, v69\n"
"v_mfma_f32_16x16x32_bf16 v[112:115], a[196:199], v[176:179], v[112:115]\n"
"v_add_f32 v18, v18, v66\n"
"v_add_f32 v18, v18, v67\n"
"v_mfma_f32_16x16x32_bf16 v[112:115], a[200:203], v[192:195], v[112:115]\n"
"v_exp_f32 v70, v70\n"
"v_mfma_f32_16x16x32_bf16 v[112:115], a[204:207], v[208:211], v[112:115]\n"
"ds_read_b128 a[216:219], v0 offset:18432\n"
"v_cvt_pk_bf16_f32 v99, v66, v67\n"
"v_mfma_f32_16x16x32_bf16 v[112:115], a[208:211], v[224:227], v[112:115]\n"
"v_exp_f32 v71, v71\n"
"v_mfma_f32_16x16x32_bf16 v[112:115], a[212:215], v[240:243], v[112:115]\n"
"v_add_f32 v19, v19, v68\n"
"v_add_f32 v19, v19, v69\n"
"v_mfma_f32_16x16x32_bf16 v[116:119], a[212:215], v[244:247], v[52:55]\n"
"ds_read_b128 a[220:223], v1 offset:18432\n"
"v_cvt_pk_bf16_f32 v102, v68, v69\n"
"v_mfma_f32_16x16x32_bf16 v[116:119], a[208:211], v[228:231], v[116:119]\n"
"v_exp_f32 v72, v72\n"
"v_mfma_f32_16x16x32_bf16 v[116:119], a[204:207], v[212:215], v[116:119]\n"
"v_exp_f32 v73, v73\n"
"v_mfma_f32_16x16x32_bf16 v[116:119], a[200:203], v[196:199], v[116:119]\n"
"ds_read_b128 a[224:227], v0 offset:18560\n"
"v_add_f32 v19, v19, v70\n"
"v_mfma_f32_16x16x32_bf16 v[116:119], a[196:199], v[180:183], v[116:119]\n"
"v_add_f32 v19, v19, v71\n"
"v_cvt_pk_bf16_f32 v103, v70, v71\n"
"v_mfma_f32_16x16x32_bf16 v[116:119], a[192:195], v[164:167], v[116:119]\n"
"v_exp_f32 v74, v74\n"
"v_mfma_f32_16x16x32_bf16 v[120:123], a[192:195], v[168:171], v[56:59]\n"
"ds_read_b128 a[228:231], v1 offset:18560\n"
"v_add_f32 v20, v20, v72\n"
"v_mfma_f32_16x16x32_bf16 v[120:123], a[196:199], v[184:187], v[120:123]\n"
"v_exp_f32 v75, v75\n"
"v_mfma_f32_16x16x32_bf16 v[120:123], a[200:203], v[200:203], v[120:123]\n"
"v_add_f32 v20, v20, v73\n"
"v_cvt_pk_bf16_f32 v106, v72, v73\n"
"v_mfma_f32_16x16x32_bf16 v[120:123], a[204:207], v[216:219], v[120:123]\n"
"ds_read_b128 a[232:235], v0 offset:18688\n"
"v_add_f32 v20, v20, v74\n"
"v_mfma_f32_16x16x32_bf16 v[120:123], a[208:211], v[232:235], v[120:123]\n"
"v_exp_f32 v76, v76\n"
"v_mfma_f32_16x16x32_bf16 v[120:123], a[212:215], v[248:251], v[120:123]\n"
"v_exp_f32 v77, v77\n"
"v_mfma_f32_16x16x32_bf16 v[124:127], a[212:215], v[252:255], v[60:63]\n"
"ds_read_b128 a[236:239], v1 offset:18688\n"
"v_add_f32 v20, v20, v75\n"
"v_mfma_f32_16x16x32_bf16 v[124:127], a[208:211], v[236:239], v[124:127]\n"
"L_pg10:\n"
"s_cbranch_execz L_bk2\n"
"v_exp_f32 v78, v78\n"
"v_mfma_f32_16x16x32_bf16 v[124:127], a[204:207], v[220:223], v[124:127]\n"
"v_cvt_pk_bf16_f32 v107, v74, v75\n"
"v_add_f32 v21, v21, v76\n"
"v_mfma_f32_16x16x32_bf16 v[124:127], a[200:203], v[204:207], v[124:127]\n"
"v_add_u32 v0, s45, v0\n"
"v_add_f32 v21, v21, v77\n"
"v_mfma_f32_16x16x32_bf16 v[124:127], a[196:199], v[188:191], v[124:127]\n"
"v_exp_f32 v79, v79\n"
"v_mfma_f32_16x16x32_bf16 v[124:127], a[192:195], v[172:175], v[124:127]\n"
"s_waitcnt lgkmcnt(0)\n"
"v_exp_f32 v112, v112\n"
"v_mfma_f32_16x16x32_bf16 v[80:83], a[216:219], v[160:163], v[48:51]\n"
"v_add_u32 v1, s45, v1\n"
"v_cvt_pk_bf16_f32 v110, v76, v77\n"
"v_mfma_f32_16x16x32_bf16 v[80:83], a[220:223], v[176:179], v[80:83]\n"
"v_exp_f32 v113, v113\n"
"v_mfma_f32_16x16x32_bf16 v[80:83], a[224:227], v[192:195], v[80:83]\n"
"v_add_f32 v21, v21, v78\n"
"v_add_f32 v21, v21, v79\n"
"v_mfma_f32_16x16x32_bf16 v[80:83], a[228:231], v[208:211], v[80:83]\n"
"v_exp_f32 v114, v114\n"
"v_mfma_f32_16x16x32_bf16 v[80:83], a[232:235], v[224:227], v[80:83]\n"
"v_cvt_pk_bf16_f32 v111, v78, v79\n"
"v_add_f32 v18, v18, v112\n"
"v_mfma_f32_16x16x32_bf16 v[80:83], a[236:239], v[240:243], v[80:83]\n"
"v_exp_f32 v115, v115\n"
"v_mfma_f32_16x16x32_bf16 v[84:87], a[236:239], v[244:247], v[52:55]\n"
"s_waitcnt vmcnt(7)\n"
"v_add_f32 v18, v18, v113\n"
"v_cvt_pk_bf16_f32 v112, v112, v113\n"
"v_mfma_f32_16x16x32_bf16 v[84:87], a[232:235], v[228:231], v[84:87]\n"
"s_barrier\n"
"v_exp_f32 v116, v116\n"
"v_mfma_f32_16x16x32_bf16 v[84:87], a[228:231], v[212:215], v[84:87]\n"
"v_exp_f32 v117, v117\n"
"v_mfma_f32_16x16x32_bf16 v[84:87], a[224:227], v[196:199], v[84:87]\n"
"ds_read_b64_tr_b16 a[240:241], v2 offset:0\n"
"v_add_f32 v18, v18, v114\n"
"v_mfma_f32_16x16x32_bf16 v[84:87], a[220:223], v[180:183], v[84:87]\n"
"v_add_f32 v18, v18, v115\n"
"v_cvt_pk_bf16_f32 v113, v114, v115\n"
"v_mfma_f32_16x16x32_bf16 v[84:87], a[216:219], v[164:167], v[84:87]\n"
"ds_read_b64_tr_b16 a[242:243], v2 offset:6144\n"
"v_add_f32 v19, v19, v116\n"
"v_mfma_f32_16x16x32_bf16 v[88:91], a[216:219], v[168:171], v[56:59]\n"
"v_exp_f32 v118, v118\n"
"v_mfma_f32_16x16x32_bf16 v[88:91], a[220:223], v[184:187], v[88:91]\n"
"ds_read_b64_tr_b16 a[244:245], v2 offset:12288\n"
"v_add_f32 v19, v19, v117\n"
"v_mfma_f32_16x16x32_bf16 v[88:91], a[224:227], v[200:203], v[88:91]\n"
"v_exp_f32 v119, v119\n"
"v_mfma_f32_16x16x32_bf16 v[88:91], a[228:231], v[216:219], v[88:91]\n"
"ds_read_b64_tr_b16 a[246:247], v2 offset:18432\n"
"v_cvt_pk_bf16_f32 v116, v116, v117\n"
"v_mfma_f32_16x16x32_bf16 v[88:91], a[232:235], v[232:235], v[88:91]\n"
"v_exp_f32 v120, v120\n"
"v_mfma_f32_16x16x32_bf16 v[88:91], a[236:239], v[248:251], v[88:91]\n"
"ds_read_b64_tr_b16 a[248:249], v3 offset:0\n"
"v_add_f32 v19, v19, v118\n"
"v_mfma_f32_16x16x32_bf16 v[92:95], a[236:239], v[252:255], v[60:63]\n"
"v_exp_f32 v121, v121\n"
"v_mfma_f32_16x16x32_bf16 v[92:95], a[232:235], v[236:239], v[92:95]\n"
"ds_read_b64_tr_b16 a[250:251], v3 offset:6144\n"
"v_add_f32 v19, v19, v119\n"
"v_mfma_f32_16x16x32_bf16 v[92:95], a[228:231], v[220:223], v[92:95]\n"
"v_exp_f32 v122, v122\n"
"v_mfma_f32_16x16x32_bf16 v[92:95], a[224:227], v[204:207], v[92:95]\n"
"ds_read_b64_tr_b16 a[252:253], v3 offset:12288\n"
"v_cvt_pk_bf16_f32 v117, v118, v119\n"
"v_mfma_f32_16x16x32_bf16 v[92:95], a[220:223], v[188:191], v[92:95]\n"
"v_exp_f32 v123, v123\n"
"v_mfma_f32_16x16x32_bf16 v[92:95], a[216:219], v[172:175], v[92:95]\n"
"s_waitcnt lgkmcnt(3)\n"
"ds_read_b64_tr_b16 a[254:255], v3 offset:18432\n"
"v_add_f32 v20, v20, v120\n"
"v_mfma_f32_16x16x32_bf16 a[0:3], a[240:243], v[128:131], a[0:3]\n"
"v_add_f32 v20, v20, v121\n"
"v_cvt_pk_bf16_f32 v120, v120, v121\n"
"v_mfma_f32_16x16x32_bf16 a[0:3], a[244:247], v[144:147], a[0:3]\n"
"s_add_u32 m0, s41, 0\n"
"ds_read_b64_tr_b16 a[216:217], v4 offset:0\n"
"v_add_f32 v20, v20, v122\n"
"v_mfma_f32_16x16x32_bf16 a[4:7], a[240:243], v[132:135], a[4:7]\n"
"buffer_load_dwordx4 v9, s[12:15], s43 offen offset:0 lds\n"
"v_mfma_f32_16x16x32_bf16 a[4:7], a[244:247], v[148:151], a[4:7]\n"
"ds_read_b64_tr_b16 a[218:219], v4 offset:6144\n"
"v_add_f32 v20, v20, v123\n"
"v_mfma_f32_16x16x32_bf16 a[8:11], a[240:243], v[136:139], a[8:11]\n"
"v_exp_f32 v124, v124\n"
"v_mfma_f32_16x16x32_bf16 a[8:11], a[244:247], v[152:155], a[8:11]\n"
"ds_read_b64_tr_b16 a[220:221], v4 offset:12288\n"
"v_cvt_pk_bf16_f32 v121, v122, v123\n"
"v_mfma_f32_16x16x32_bf16 a[12:15], a[240:243], v[140:143], a[12:15]\n"
"v_exp_f32 v125, v125\n"
"v_mfma_f32_16x16x32_bf16 a[12:15], a[244:247], v[156:159], a[12:15]\n"
"s_waitcnt lgkmcnt(3)\n"
"ds_read_b64_tr_b16 a[222:223], v4 offset:18432\n"
"v_add_f32 v21, v21, v124\n"
"v_mfma_f32_16x16x32_bf16 a[16:19], a[248:251], v[128:131], a[16:19]\n"
"v_exp_f32 v126, v126\n"
"v_mfma_f32_16x16x32_bf16 a[16:19], a[252:255], v[144:147], a[16:19]\n"
"ds_read_b64_tr_b16 a[224:225], v5 offset:0\n"
"v_add_f32 v21, v21, v125\n"
"v_mfma_f32_16x16x32_bf16 a[20:23], a[248:251], v[132:135], a[20:23]\n"
"v_exp_f32 v127, v127\n"
"v_mfma_f32_16x16x32_bf16 a[20:23], a[252:255], v[148:151], a[20:23]\n"
"ds_read_b64_tr_b16 a[226:227], v5 offset:6144\n"
"v_cvt_pk_bf16_f32 v124, v124, v125\n"
"v_mfma_f32_16x16x32_bf16 a[24:27], a[248:251], v[136:139], a[24:27]\n"
"v_exp_f32 v80, v80\n"
"v_mfma_f32_16x16x32_bf16 a[24:27], a[252:255], v[152:155], a[24:27]\n"
"ds_read_b64_tr_b16 a[228:229], v5 offset:12288\n"
"v_add_f32 v21, v21, v126\n"
"v_mfma_f32_16x16x32_bf16 a[28:31], a[248:251], v[140:143], a[28:31]\n"
"buffer_load_dwordx4 v10, s[12:15], s43 offen offset:1024 lds\n"
"v_mfma_f32_16x16x32_bf16 a[28:31], a[252:255], v[156:159], a[28:31]\n"
"s_waitcnt lgkmcnt(3)\n"
"ds_read_b64_tr_b16 a[230:231], v5 offset:18432\n"
"v_add_f32 v21, v21, v127\n"
"v_mfma_f32_16x16x32_bf16 a[32:35], a[216:219], v[128:131], a[32:35]\n"
"v_exp_f32 v81, v81\n"
"v_mfma_f32_16x16x32_bf16 a[32:35], a[220:223], v[144:147], a[32:35]\n"
"ds_read_b64_tr_b16 a[240:241], v2 offset:128\n"
"v_cvt_pk_bf16_f32 v125, v126, v127\n"
"v_mfma_f32_16x16x32_bf16 a[36:39], a[216:219], v[132:135], a[36:39]\n"
"v_exp_f32 v82, v82\n"
"v_mfma_f32_16x16x32_bf16 a[36:39], a[220:223], v[148:151], a[36:39]\n"
"ds_read_b64_tr_b16 a[242:243], v2 offset:6272\n"
"v_add_f32 v18, v18, v80\n"
"v_mfma_f32_16x16x32_bf16 a[40:43], a[216:219], v[136:139], a[40:43]\n"
"v_exp_f32 v83, v83\n"
"v_mfma_f32_16x16x32_bf16 a[40:43], a[220:223], v[152:155], a[40:43]\n"
"ds_read_b64_tr_b16 a[244:245], v2 offset:12416\n"
"v_add_f32 v18, v18, v81\n"
"v_mfma_f32_16x16x32_bf16 a[44:47], a[216:219], v[140:143], a[44:47]\n"
"v_exp_f32 v84, v84\n"
"v_mfma_f32_16x16x32_bf16 a[44:47], a[220:223], v[156:159], a[44:47]\n"
"s_waitcnt lgkmcnt(3)\n"
"ds_read_b64_tr_b16 a[246:247], v2 offset:18560\n"
"v_cvt_pk_bf16_f32 v114, v80, v81\n"
"v_mfma_f32_16x16x32_bf16 a[48:51], a[224:227], v[128:131], a[48:51]\n"
"v_exp_f32 v85, v85\n"
"v_mfma_f32_16x16x32_bf16 a[48:51], a[228:231], v[144:147], a[48:51]\n"
"ds_read_b64_tr_b16 a[248:249], v3 offset:128\n"
"v_add_f32 v18, v18, v82\n"
"v_mfma_f32_16x16x32_bf16 a[52:55], a[224:227], v[132:135], a[52:55]\n"
"buffer_load_dwordx4 v11, s[12:15], s43 offen offset:2048 lds\n"
"v_mfma_f32_16x16x32_bf16 a[52:55], a[228:231], v[148:151], a[52:55]\n"
"ds_read_b64_tr_b16 a[250:251], v3 offset:6272\n"
"v_add_f32 v18, v18, v83\n"
"v_mfma_f32_16x16x32_bf16 a[56:59], a[224:227], v[136:139], a[56:59]\n"
"s_add_u32 s43, s43, 27648\n"
"v_exp_f32 v86, v86\n"
"v_mfma_f32_16x16x32_bf16 a[56:59], a[228:231], v[152:155], a[56:59]\n"
"ds_read_b64_tr_b16 a[252:253], v3 offset:12416\n"
"v_cvt_pk_bf16_f32 v115, v82, v83\n"
"v_mfma_f32_16x16x32_bf16 a[60:63], a[224:227], v[140:143], a[60:63]\n"
"s_or_b32 s43, s43, 0x18000\n"
"v_exp_f32 v87, v87\n"
"v_mfma_f32_16x16x32_bf16 a[60:63], a[228:231], v[156:159], a[60:63]\n"
"s_waitcnt lgkmcnt(3)\n"
"ds_read_b64_tr_b16 a[254:255], v3 offset:18560\n"
"v_add_f32 v19, v19, v84\n"
"v_mfma_f32_16x16x32_bf16 a[64:67], a[240:243], v[128:131], a[64:67]\n"
"s_sub_u32 s62, s43, 0x60000\n"
"v_add_f32 v19, v19, v85\n"
"v_cvt_pk_bf16_f32 v118, v84, v85\n"
"v_mfma_f32_16x16x32_bf16 a[64:67], a[244:247], v[144:147], a[64:67]\n"
"ds_read_b64_tr_b16 a[216:217], v4 offset:128\n"
"v_add_f32 v19, v19, v86\n"
"v_mfma_f32_16x16x32_bf16 a[68:71], a[240:243], v[132:135], a[68:71]\n"
"s_min_u32 s43, s43, s62\n"
"v_exp_f32 v88, v88\n"
"v_mfma_f32_16x16x32_bf16 a[68:71], a[244:247], v[148:151], a[68:71]\n"
"ds_read_b64_tr_b16 a[218:219], v4 offset:6272\n"
"v_add_f32 v19, v19, v87\n"
"v_mfma_f32_16x16x32_bf16 a[72:75], a[240:243], v[136:139], a[72:75]\n"
"v_exp_f32 v89, v89\n"
"v_mfma_f32_16x16x32_bf16 a[72:75], a[244:247], v[152:155], a[72:75]\n"
"s_add_u32 m0, s41, 12288\n"
"ds_read_b64_tr_b16 a[220:221], v4 offset:12416\n"
"v_cvt_pk_bf16_f32 v119, v86, v87\n"
"v_mfma_f32_16x16x32_bf16 a[76:79], a[240:243], v[140:143], a[76:79]\n"
"buffer_load_dwordx4 v9, s[12:15], s43 offen offset:0 lds\n"
"v_mfma_f32_16x16x32_bf16 a[76:79], a[244:247], v[156:159], a[76:79]\n"
"s_waitcnt lgkmcnt(3)\n"
"ds_read_b64_tr_b16 a[222:223], v4 offset:18560\n"
"v_add_f32 v20, v20, v88\n"
"v_mfma_f32_16x16x32_bf16 a[80:83], a[248:251], v[128:131], a[80:83]\n"
"v_exp_f32 v90, v90\n"
"v_mfma_f32_16x16x32_bf16 a[80:83], a[252:255], v[144:147], a[80:83]\n"
"ds_read_b64_tr_b16 a[224:225], v5 offset:128\n"
"v_add_f32 v20, v20, v89\n"
"v_mfma_f32_16x16x32_bf16 a[84:87], a[248:251], v[132:135], a[84:87]\n"
"v_exp_f32 v91, v91\n"
"v_mfma_f32_16x16x32_bf16 a[84:87], a[252:255], v[148:151], a[84:87]\n"
"ds_read_b64_tr_b16 a[226:227], v5 offset:6272\n"
"v_cvt_pk_bf16_f32 v122, v88, v89\n"
"v_mfma_f32_16x16x32_bf16 a[88:91], a[248:251], v[136:139], a[88:91]\n"
"v_exp_f32 v92, v92\n"
"v_mfma_f32_16x16x32_bf16 a[88:91], a[252:255], v[152:155], a[88:91]\n"
"ds_read_b64_tr_b16 a[228:229], v5 offset:12416\n"
"v_add_f32 v20, v20, v90\n"
"v_mfma_f32_16x16x32_bf16 a[92:95], a[248:251], v[140:143], a[92:95]\n"
"v_exp_f32 v93, v93\n"
"v_mfma_f32_16x16x32_bf16 a[92:95], a[252:255], v[156:159], a[92:95]\n"
"s_waitcnt lgkmcnt(3)\n"
"ds_read_b64_tr_b16 a[230:231], v5 offset:18560\n"
"v_add_f32 v20, v20, v91\n"
"v_mfma_f32_16x16x32_bf16 a[96:99], a[216:219], v[128:131], a[96:99]\n"
"v_exp_f32 v94, v94\n"
"v_mfma_f32_16x16x32_bf16 a[96:99], a[220:223], v[144:147], a[96:99]\n"
"ds_read_b64_tr_b16 a[240:241], v2 offset:256\n"
"v_cvt_pk_bf16_f32 v123, v90, v91\n"
"v_mfma_f32_16x16x32_bf16 a[100:103], a[216:219], v[132:135], a[100:103]\n"
"buffer_load_dwordx4 v10, s[12:15], s43 offen offset:1024 nt lds\n"
"v_mfma_f32_16x16x32_bf16 a[100:103], a[220:223], v[148:151], a[100:103]\n"
"ds_read_b64_tr_b16 a[242:243], v2 offset:6400\n"
"v_add_f32 v21, v21, v92\n"
"v_mfma_f32_16x16x32_bf16 a[104:107], a[216:219], v[136:139], a[104:107]\n"
"v_exp_f32 v95, v95\n"
"v_mfma_f32_16x16x32_bf16 a[104:107], a[220:223], v[152:155], a[104:107]\n"
"ds_read_b64_tr_b16 a[244:245], v2 offset:12544\n"
"v_add_f32 v21, v21, v93\n"
"v_mfma_f32_16x16x32_bf16 a[108:111], a[216:219], v[140:143], a[108:111]\n"
"v_cvt_pk_bf16_f32 v126, v92, v93\n"
"v_add_f32 v21, v21, v94\n"
"v_mfma_f32_16x16x32_bf16 a[108:111], a[220:223], v[156:159], a[108:111]\n"
"s_waitcnt lgkmcnt(3)\n"
"ds_read_b64_tr_b16 a[246:247], v2 offset:18688\n"
"v_add_f32 v21, v21, v95\n"
"v_mfma_f32_16x16x32_bf16 a[112:115], a[224:227], v[128:131], a[112:115]\n"
"v_cvt_pk_bf16_f32 v127, v94, v95\n"
"v_max3_f32 v22, v18, v19, v20\n"
"v_mfma_f32_16x16x32_bf16 a[112:115], a[228:231], v[144:147], a[112:115]\n"
"ds_read_b64_tr_b16 a[248:249], v3 offset:256\n"
"v_max_f32 v22, v22, v21\n"
"v_mfma_f32_16x16x32_bf16 a[116:119], a[224:227], v[132:135], a[116:119]\n"
"v_cmp_lt_f32 vcc, s54, v22\n"
"v_mfma_f32_16x16x32_bf16 a[116:119], a[228:231], v[148:151], a[116:119]\n"
"ds_read_b64_tr_b16 a[250:251], v3 offset:6400\n"
"v_mfma_f32_16x16x32_bf16 a[120:123], a[224:227], v[136:139], a[120:123]\n"
"v_mfma_f32_16x16x32_bf16 a[120:123], a[228:231], v[152:155], a[120:123]\n"
"ds_read_b64_tr_b16 a[252:253], v3 offset:12544\n"
"v_mfma_f32_16x16x32_bf16 a[124:127], a[224:227], v[140:143], a[124:127]\n"
"buffer_load_dwordx4 v11, s[12:15], s43 offen offset:2048 nt lds\n"
"v_mfma_f32_16x16x32_bf16 a[124:127], a[228:231], v[156:159], a[124:127]\n"
"s_waitcnt lgkmcnt(3)\n"
"ds_read_b64_tr_b16 a[254:255], v3 offset:18688\n"
"v_mfma_f32_16x16x32_bf16 a[128:131], a[240:243], v[128:131], a[128:131]\n"
"v_mfma_f32_16x16x32_bf16 a[128:131], a[244:247], v[144:147], a[128:131]\n"
"ds_read_b64_tr_b16 a[216:217], v4 offset:256\n"
"v_mfma_f32_16x16x32_bf16 a[132:135], a[240:243], v[132:135], a[132:135]\n"
"s_add_u32 s41, s41, s46\n"
"v_mfma_f32_16x16x32_bf16 a[132:135], a[244:247], v[148:151], a[132:135]\n"
"ds_read_b64_tr_b16 a[218:219], v4 offset:6400\n"
"v_mfma_f32_16x16x32_bf16 a[136:139], a[240:243], v[136:139], a[136:139]\n"
"v_lshlrev_b32 v26, 3, v12\n"
"v_mfma_f32_16x16x32_bf16 a[136:139], a[244:247], v[152:155], a[136:139]\n"
"ds_read_b64_tr_b16 a[220:221], v4 offset:12544\n"
"v_mfma_f32_16x16x32_bf16 a[140:143], a[240:243], v[140:143], a[140:143]\n"
"v_and_b32 v26, 0x780, v26\n"
"v_mfma_f32_16x16x32_bf16 a[140:143], a[244:247], v[156:159], a[140:143]\n"
"s_waitcnt lgkmcnt(3)\n"
"ds_read_b64_tr_b16 a[222:223], v4 offset:18688\n"
"v_mfma_f32_16x16x32_bf16 a[144:147], a[248:251], v[128:131], a[144:147]\n"
"global_load_dword v27, v26, s[60:61]\n"
"v_mfma_f32_16x16x32_bf16 a[144:147], a[252:255], v[144:147], a[144:147]\n"
"ds_read_b64_tr_b16 a[224:225], v5 offset:256\n"
"v_mfma_f32_16x16x32_bf16 a[148:151], a[248:251], v[132:135], a[148:151]\n"
"v_mfma_f32_16x16x32_bf16 a[148:151], a[252:255], v[148:151], a[148:151]\n"
"ds_read_b64_tr_b16 a[226:227], v5 offset:6400\n"
"v_mfma_f32_16x16x32_bf16 a[152:155], a[248:251], v[136:139], a[152:155]\n"
"s_add_u32 s43, s43, 27648\n"
"v_mfma_f32_16x16x32_bf16 a[152:155], a[252:255], v[152:155], a[152:155]\n"
"ds_read_b64_tr_b16 a[228:229], v5 offset:12544\n"
"v_mfma_f32_16x16x32_bf16 a[156:159], a[248:251], v[140:143], a[156:159]\n"
"s_or_b32 s43, s43, 0x18000\n"
"v_mfma_f32_16x16x32_bf16 a[156:159], a[252:255], v[156:159], a[156:159]\n"
"s_waitcnt lgkmcnt(3)\n"
"ds_read_b64_tr_b16 a[230:231], v5 offset:18688\n"
"v_and_b32 v46, s98, v12\n"
"v_mfma_f32_16x16x32_bf16 a[160:163], a[216:219], v[128:131], a[160:163]\n"
"ds_read_b128 a[192:195], v0 offset:0\n"
"v_mfma_f32_16x16x32_bf16 a[160:163], a[220:223], v[144:147], a[160:163]\n"
"s_sub_u32 s62, s43, 0x60000\n"
"buffer_load_dwordx2 v[46:47], v46, s[24:27], s90 offen sc1\n"
"v_mfma_f32_16x16x32_bf16 a[164:167], a[216:219], v[132:135], a[164:167]\n"
"ds_read_b128 a[196:199], v1 offset:0\n"
"v_mfma_f32_16x16x32_bf16 a[164:167], a[220:223], v[148:151], a[164:167]\n"
"s_min_u32 s43, s43, s62\n"
"v_mfma_f32_16x16x32_bf16 a[168:171], a[216:219], v[136:139], a[168:171]\n"
"s_mov_b32 s47, s44\n"
"ds_read_b128 a[200:203], v0 offset:128\n"
"v_mfma_f32_16x16x32_bf16 a[168:171], a[220:223], v[152:155], a[168:171]\n"
"s_mov_b32 s44, s45\n"
"v_mfma_f32_16x16x32_bf16 a[172:175], a[216:219], v[140:143], a[172:175]\n"
"s_mov_b32 s45, s46\n"
"ds_read_b128 a[204:207], v1 offset:128\n"
"v_mfma_f32_16x16x32_bf16 a[172:175], a[220:223], v[156:159], a[172:175]\n"
"s_waitcnt lgkmcnt(4)\n"
"s_mov_b32 s46, s47\n"
"v_mfma_f32_16x16x32_bf16 a[176:179], a[224:227], v[128:131], a[176:179]\n"
"ds_read_b128 a[208:211], v0 offset:256\n"
"v_mfma_f32_16x16x32_bf16 a[176:179], a[228:231], v[144:147], a[176:179]\n"
"v_mfma_f32_16x16x32_bf16 a[180:183], a[224:227], v[132:135], a[180:183]\n"
"ds_read_b128 a[212:215], v1 offset:256\n"
"v_mfma_f32_16x16x32_bf16 a[180:183], a[228:231], v[148:151], a[180:183]\n"
"v_mfma_f32_16x16x32_bf16 a[184:187], a[224:227], v[136:139], a[184:187]\n"
"v_mfma_f32_16x16x32_bf16 a[184:187], a[228:231], v[152:155], a[184:187]\n"
"s_cmp_lg_u64 vcc, 0\n"
"v_mfma_f32_16x16x32_bf16 a[188:191], a[224:227], v[140:143], a[188:191]\n"
"s_waitcnt lgkmcnt(3)\n"
"v_mfma_f32_16x16x32_bf16 a[188:191], a[228:231], v[156:159], a[188:191]\n"
"s_cbranch_scc1 L_rc6\n"
"L_rr6:\n"
"s_cmp_lg_u32 s49, 0\n"
"s_cbranch_scc1 L_lastB\n"
"v_mfma_f32_16x16x32_bf16 v[128:131], a[192:195], v[160:163], v[48:51]\n"
"s_mov_b64 s[20:21], s[34:35]\n"
"v_add_f32 v14, v14, v18\n"
"v_add_f32 v15, v15, v19\n"
"v_mfma_f32_16x16x32_bf16 v[128:131], a[196:199], v[176:179], v[128:131]\n"
"s_add_u32 m0, s58, 0\n"
"s_add_u32 s51, s68, 0\n"
"v_add_f32 v16, v16, v20\n"
"v_add_f32 v17, v17, v21\n"
"v_mfma_f32_16x16x32_bf16 v[128:131], a[200:203], v[192:195], v[128:131]\n"
"s_waitcnt lgkmcnt(0)\n"
"buffer_load_dwordx4 v32, s[16:19], s51 offen offset:0 lds\n"
"v_mfma_f32_16x16x32_bf16 v[128:131], a[204:207], v[208:211], v[128:131]\n"
"ds_read_b128 a[216:219], v0 offset:6144\n"
"v_add_u32 v2, s46, v2\n"
"v_mfma_f32_16x16x32_bf16 v[128:131], a[208:211], v[224:227], v[128:131]\n"
"s_add_u32 s55, s55, 18\n"
"v_add_u32 v3, s46, v3\n"
"v_add_u32 v4, s46, v4\n"
"v_mfma_f32_16x16x32_bf16 v[128:131], a[212:215], v[240:243], v[128:131]\n"
"buffer_load_dwordx4 v33, s[16:19], s51 offen offset:1024 lds\n"
"v_mfma_f32_16x16x32_bf16 v[132:135], a[212:215], v[244:247], v[52:55]\n"
"ds_read_b128 a[220:223], v1 offset:6144\n"
"v_add_u32 v5, s46, v5\n"
"v_mfma_f32_16x16x32_bf16 v[132:135], a[208:211], v[228:231], v[132:135]\n"
"s_add_u32 m0, s58, 2048\n"
"s_add_u32 s51, s69, 0\n"
"v_mfma_f32_16x16x32_bf16 v[132:135], a[204:207], v[212:215], v[132:135]\n"
"buffer_load_dwordx4 v32, s[16:19], s51 offen offset:0 lds\n"
"v_mfma_f32_16x16x32_bf16 v[132:135], a[200:203], v[196:199], v[132:135]\n"
"ds_read_b128 a[224:227], v0 offset:6272\n"
"v_mfma_f32_16x16x32_bf16 v[132:135], a[196:199], v[180:183], v[132:135]\n"
"v_exp_f32 v128, v128\n"
"v_mfma_f32_16x16x32_bf16 v[132:135], a[192:195], v[164:167], v[132:135]\n"
"buffer_load_dwordx4 v33, s[16:19], s51 offen offset:1024 lds\n"
"v_mfma_f32_16x16x32_bf16 v[136:139], a[192:195], v[168:171], v[56:59]\n"
"ds_read_b128 a[228:231], v1 offset:6272\n"
"v_mfma_f32_16x16x32_bf16 v[136:139], a[196:199], v[184:187], v[136:139]\n"
"s_add_u32 m0, s58, 4096\n"
"s_add_u32 s51, s70, 0\n"
"v_exp_f32 v129, v129\n"
"v_mfma_f32_16x16x32_bf16 v[136:139], a[200:203], v[200:203], v[136:139]\n"
"buffer_load_dwordx4 v32, s[16:19], s51 offen offset:0 lds\n"
"v_mfma_f32_16x16x32_bf16 v[136:139], a[204:207], v[216:219], v[136:139]\n"
"ds_read_b128 a[232:235], v0 offset:6400\n"
"v_add_f32 v18, v128, v129\n"
"v_mfma_f32_16x16x32_bf16 v[136:139], a[208:211], v[232:235], v[136:139]\n"
"v_exp_f32 v130, v130\n"
"v_mfma_f32_16x16x32_bf16 v[136:139], a[212:215], v[248:251], v[136:139]\n"
"buffer_load_dwordx4 v33, s[16:19], s51 offen offset:1024 lds\n"
"v_mfma_f32_16x16x32_bf16 v[140:143], a[212:215], v[252:255], v[60:63]\n"
"ds_read_b128 a[236:239], v1 offset:6400\n"
"v_cvt_pk_bf16_f32 v128, v128, v129\n"
"v_mfma_f32_16x16x32_bf16 v[140:143], a[208:211], v[236:239], v[140:143]\n"
"s_add_u32 m0, s58, 6144\n"
"s_add_u32 s51, s71, 0\n"
"v_exp_f32 v131, v131\n"
"v_mfma_f32_16x16x32_bf16 v[140:143], a[204:207], v[220:223], v[140:143]\n"
"buffer_load_dwordx4 v32, s[16:19], s51 offen offset:0 lds\n"
"v_mfma_f32_16x16x32_bf16 v[140:143], a[200:203], v[204:207], v[140:143]\n"
"v_exp_f32 v132, v132\n"
"v_mfma_f32_16x16x32_bf16 v[140:143], a[196:199], v[188:191], v[140:143]\n"
"v_exp_f32 v133, v133\n"
"v_mfma_f32_16x16x32_bf16 v[140:143], a[192:195], v[172:175], v[140:143]\n"
"s_waitcnt lgkmcnt(0)\n"
"buffer_load_dwordx4 v33, s[16:19], s51 offen offset:1024 lds\n"
"v_mfma_f32_16x16x32_bf16 v[64:67], a[216:219], v[160:163], v[48:51]\n"
"v_add_f32 v18, v18, v130\n"
"v_add_f32 v18, v18, v131\n"
"v_mfma_f32_16x16x32_bf16 v[64:67], a[220:223], v[176:179], v[64:67]\n"
"s_add_u32 m0, s38, 139264\n"
"s_mov_b32 s50, 0\n"
"v_exp_f32 v134, v134\n"
"v_mfma_f32_16x16x32_bf16 v[64:67], a[224:227], v[192:195], v[64:67]\n"
"buffer_load_dwordx4 v36, s[20:23], s50 offen offset:0 lds\n"
"v_mfma_f32_16x16x32_bf16 v[64:67], a[228:231], v[208:211], v[64:67]\n"
"ds_read_b128 a[192:195], v0 offset:12288\n"
"v_cvt_pk_bf16_f32 v129, v130, v131\n"
"v_mfma_f32_16x16x32_bf16 v[64:67], a[232:235], v[224:227], v[64:67]\n"
"v_exp_f32 v135, v135\n"
"v_mfma_f32_16x16x32_bf16 v[64:67], a[236:239], v[240:243], v[64:67]\n"
"buffer_load_dwordx4 v37, s[20:23], s50 offen offset:1024 lds\n"
"v_mfma_f32_16x16x32_bf16 v[68:71], a[236:239], v[244:247], v[52:55]\n"
"ds_read_b128 a[196:199], v1 offset:12288\n"
"v_add_f32 v19, v132, v133\n"
"v_mfma_f32_16x16x32_bf16 v[68:71], a[232:235], v[228:231], v[68:71]\n"
"v_exp_f32 v136, v136\n"
"v_mfma_f32_16x16x32_bf16 v[68:71], a[228:231], v[212:215], v[68:71]\n"
"buffer_load_dwordx4 v38, s[20:23], s50 offen offset:2048 lds\n"
"v_mfma_f32_16x16x32_bf16 v[68:71], a[224:227], v[196:199], v[68:71]\n"
"ds_read_b128 a[200:203], v0 offset:12416\n"
"v_cvt_pk_bf16_f32 v132, v132, v133\n"
"v_mfma_f32_16x16x32_bf16 v[68:71], a[220:223], v[180:183], v[68:71]\n"
"s_add_u32 m0, s38, 151552\n"
"s_mov_b32 s50, 393216\n"
"v_exp_f32 v137, v137\n"
"v_mfma_f32_16x16x32_bf16 v[68:71], a[216:219], v[164:167], v[68:71]\n"
"buffer_load_dwordx4 v36, s[20:23], s50 offen offset:0 lds\n"
"v_mfma_f32_16x16x32_bf16 v[72:75], a[216:219], v[168:171], v[56:59]\n"
"ds_read_b128 a[204:207], v1 offset:12416\n"
"v_add_f32 v19, v19, v134\n"
"v_mfma_f32_16x16x32_bf16 v[72:75], a[220:223], v[184:187], v[72:75]\n"
"v_exp_f32 v138, v138\n"
"v_mfma_f32_16x16x32_bf16 v[72:75], a[224:227], v[200:203], v[72:75]\n"
"buffer_load_dwordx4 v37, s[20:23], s50 offen offset:1024 lds\n"
"v_mfma_f32_16x16x32_bf16 v[72:75], a[228:231], v[216:219], v[72:75]\n"
"ds_read_b128 a[208:211], v0 offset:12544\n"
"v_add_f32 v19, v19, v135\n"
"v_mfma_f32_16x16x32_bf16 v[72:75], a[232:235], v[232:235], v[72:75]\n"
"v_exp_f32 v139, v139\n"
"v_mfma_f32_16x16x32_bf16 v[72:75], a[236:239], v[248:251], v[72:75]\n"
"buffer_load_dwordx4 v38, s[20:23], s50 offen offset:2048 lds\n"
"v_mfma_f32_16x16x32_bf16 v[76:79], a[236:239], v[252:255], v[60:63]\n"
"ds_read_b128 a[212:215], v1 offset:12544\n"
"v_cvt_pk_bf16_f32 v133, v134, v135\n"
"v_mfma_f32_16x16x32_bf16 v[76:79], a[232:235], v[236:239], v[76:79]\n"
"v_add_f32 v20, v136, v137\n"
"v_cvt_pk_bf16_f32 v136, v136, v137\n"
"v_mfma_f32_16x16x32_bf16 v[76:79], a[228:231], v[220:223], v[76:79]\n"
"v_exp_f32 v140, v140\n"
"v_mfma_f32_16x16x32_bf16 v[76:79], a[224:227], v[204:207], v[76:79]\n"
"v_add_u32 v34, v34, s59\n"
"v_add_f32 v20, v20, v138\n"
"v_mfma_f32_16x16x32_bf16 v[76:79], a[220:223], v[188:191], v[76:79]\n"
"v_exp_f32 v141, v141\n"
"v_mfma_f32_16x16x32_bf16 v[76:79], a[216:219], v[172:175], v[76:79]\n"
"s_waitcnt lgkmcnt(0)\n"
"buffer_load_dword v27, v39, s[80:83], s96 offen\n"
"v_mfma_f32_16x16x32_bf16 v[144:147], a[192:195], v[160:163], v[48:51]\n"
"s_add_u32 s96, s96, s36\n"
"v_add_u32 v35, v35, s59\n"
"v_add_f32 v20, v20, v139\n"
"v_mfma_f32_16x16x32_bf16 v[144:147], a[196:199], v[176:179], v[144:147]\n"
"v_exp_f32 v142, v142\n"
"v_mfma_f32_16x16x32_bf16 v[144:147], a[200:203], v[192:195], v[144:147]\n"
"v_exp_f32 v143, v143\n"
"v_mfma_f32_16x16x32_bf16 v[144:147], a[204:207], v[208:211], v[144:147]\n"
"ds_read_b128 a[216:219], v0 offset:18432\n"
"v_add_u32 v28, 0x18000, v28\n"
"v_mfma_f32_16x16x32_bf16 v[144:147], a[208:211], v[224:227], v[144:147]\n"
"v_cvt_pk_bf16_f32 v137, v138, v139\n"
"v_add_f32 v21, v140, v141\n"
"v_mfma_f32_16x16x32_bf16 v[144:147], a[212:215], v[240:243], v[144:147]\n"
"v_exp_f32 v64, v64\n"
"v_mfma_f32_16x16x32_bf16 v[148:151], a[212:215], v[244:247], v[52:55]\n"
"ds_read_b128 a[220:223], v1 offset:18432\n"
"v_add_u32 v29, 0x18000, v29\n"
"v_mfma_f32_16x16x32_bf16 v[148:151], a[208:211], v[228:231], v[148:151]\n"
"v_cvt_pk_bf16_f32 v140, v140, v141\n"
"v_add_f32 v21, v21, v142\n"
"v_mfma_f32_16x16x32_bf16 v[148:151], a[204:207], v[212:215], v[148:151]\n"
"v_exp_f32 v65, v65\n"
"v_mfma_f32_16x16x32_bf16 v[148:151], a[200:203], v[196:199], v[148:151]\n"
"ds_read_b128 a[224:227], v0 offset:18560\n"
"v_add_u32 v30, 0x18000, v30\n"
"v_mfma_f32_16x16x32_bf16 v[148:151], a[196:199], v[180:183], v[148:151]\n"
"v_add_f32 v21, v21, v143\n"
"v_cvt_pk_bf16_f32 v141, v142, v143\n"
"v_mfma_f32_16x16x32_bf16 v[148:151], a[192:195], v[164:167], v[148:151]\n"
"v_exp_f32 v66, v66\n"
"v_mfma_f32_16x16x32_bf16 v[152:155], a[192:195], v[168:171], v[56:59]\n"
"ds_read_b128 a[228:231], v1 offset:18560\n"
"v_add_u32 v31, 0x18000, v31\n"
"v_mfma_f32_16x16x32_bf16 v[152:155], a[196:199], v[184:187], v[152:155]\n"
"v_exp_f32 v67, v67\n"
"v_mfma_f32_16x16x32_bf16 v[152:155], a[200:203], v[200:203], v[152:155]\n"
"v_add_f32 v18, v18, v64\n"
"v_add_f32 v18, v18, v65\n"
"v_mfma_f32_16x16x32_bf16 v[152:155], a[204:207], v[216:219], v[152:155]\n"
"ds_read_b128 a[232:235], v0 offset:18688\n"
"v_cvt_pk_bf16_f32 v130, v64, v65\n"
"v_mfma_f32_16x16x32_bf16 v[152:155], a[208:211], v[232:235], v[152:155]\n"
"v_exp_f32 v68, v68\n"
"v_mfma_f32_16x16x32_bf16 v[152:155], a[212:215], v[248:251], v[152:155]\n"
"v_exp_f32 v69, v69\n"
"v_mfma_f32_16x16x32_bf16 v[156:159], a[212:215], v[252:255], v[60:63]\n"
"ds_read_b128 a[236:239], v1 offset:18688\n"
"v_add_f32 v18, v18, v66\n"
"v_mfma_f32_16x16x32_bf16 v[156:159], a[208:211], v[236:239], v[156:159]\n"
"v_add_f32 v18, v18, v67\n"
"v_cvt_pk_bf16_f32 v131, v66, v67\n"
"v_mfma_f32_16x16x32_bf16 v[156:159], a[204:207], v[220:223], v[156:159]\n"
"v_exp_f32 v70, v70\n"
"v_mfma_f32_16x16x32_bf16 v[156:159], a[200:203], v[204:207], v[156:159]\n"
"v_exp_f32 v71, v71\n"
"v_mfma_f32_16x16x32_bf16 v[156:159], a[196:199], v[188:191], v[156:159]\n"
"v_add_f32 v19, v19, v68\n"
"v_add_f32 v19, v19, v69\n"
"v_mfma_f32_16x16x32_bf16 v[156:159], a[192:195], v[172:175], v[156:159]\n"
"s_waitcnt lgkmcnt(0)\n"
"v_exp_f32 v72, v72\n"
"v_mfma_f32_16x16x32_bf16 v[80:83], a[216:219], v[160:163], v[48:51]\n"
"v_cvt_pk_bf16_f32 v134, v68, v69\n"
"v_add_f32 v19, v19, v70\n"
"v_mfma_f32_16x16x32_bf16 v[80:83], a[220:223], v[176:179], v[80:83]\n"
"v_exp_f32 v73, v73\n"
"v_mfma_f32_16x16x32_bf16 v[80:83], a[224:227], v[192:195], v[80:83]\n"
"v_add_f32 v19, v19, v71\n"
"v_cvt_pk_bf16_f32 v135, v70, v71\n"
"v_mfma_f32_16x16x32_bf16 v[80:83], a[228:231], v[208:211], v[80:83]\n"
"v_exp_f32 v74, v74\n"
"v_mfma_f32_16x16x32_bf16 v[80:83], a[232:235], v[224:227], v[80:83]\n"
"v_exp_f32 v75, v75\n"
"v_mfma_f32_16x16x32_bf16 v[80:83], a[236:239], v[240:243], v[80:83]\n"
"v_add_f32 v20, v20, v72\n"
"v_add_f32 v20, v20, v73\n"
"v_mfma_f32_16x16x32_bf16 v[84:87], a[236:239], v[244:247], v[52:55]\n"
"s_waitcnt vmcnt(24)\n"
"v_exp_f32 v76, v76\n"
"v_mfma_f32_16x16x32_bf16 v[84:87], a[232:235], v[228:231], v[84:87]\n"
"s_barrier\n"
"v_cvt_pk_bf16_f32 v138, v72, v73\n"
"v_add_f32 v20, v20, v74\n"
"v_mfma_f32_16x16x32_bf16 v[84:87], a[228:231], v[212:215], v[84:87]\n"
"v_exp_f32 v77, v77\n"
"v_mfma_f32_16x16x32_bf16 v[84:87], a[224:227], v[196:199], v[84:87]\n"
"s_add_u32 m0, s38, 40960\n"
"s_mov_b32 s50, 786432\n"
"ds_read_b64_tr_b16 a[240:241], v2 offset:0\n"
"v_add_f32 v20, v20, v75\n"
"v_mfma_f32_16x16x32_bf16 v[84:87], a[220:223], v[180:183], v[84:87]\n"
"buffer_load_dwordx4 v36, s[20:23], s50 offen offset:0 lds\n"
"v_mfma_f32_16x16x32_bf16 v[84:87], a[216:219], v[164:167], v[84:87]\n"
"ds_read_b64_tr_b16 a[242:243], v2 offset:6144\n"
"v_cvt_pk_bf16_f32 v139, v74, v75\n"
"v_mfma_f32_16x16x32_bf16 v[88:91], a[216:219], v[168:171], v[56:59]\n"
"v_exp_f32 v78, v78\n"
"v_mfma_f32_16x16x32_bf16 v[88:91], a[220:223], v[184:187], v[88:91]\n"
"ds_read_b64_tr_b16 a[244:245], v2 offset:12288\n"
"v_add_f32 v21, v21, v76\n"
"v_mfma_f32_16x16x32_bf16 v[88:91], a[224:227], v[200:203], v[88:91]\n"
"buffer_load_dwordx4 v37, s[20:23], s50 offen offset:1024 lds\n"
"v_mfma_f32_16x16x32_bf16 v[88:91], a[228:231], v[216:219], v[88:91]\n"
"ds_read_b64_tr_b16 a[246:247], v2 offset:18432\n"
"v_add_f32 v21, v21, v77\n"
"v_mfma_f32_16x16x32_bf16 v[88:91], a[232:235], v[232:235], v[88:91]\n"
"v_exp_f32 v79, v79\n"
"v_mfma_f32_16x16x32_bf16 v[88:91], a[236:239], v[248:251], v[88:91]\n"
"ds_read_b64_tr_b16 a[248:249], v3 offset:0\n"
"v_cvt_pk_bf16_f32 v142, v76, v77\n"
"v_mfma_f32_16x16x32_bf16 v[92:95], a[236:239], v[252:255], v[60:63]\n"
"buffer_load_dwordx4 v38, s[20:23], s50 offen offset:2048 lds\n"
"v_mfma_f32_16x16x32_bf16 v[92:95], a[232:235], v[236:239], v[92:95]\n"
"ds_read_b64_tr_b16 a[250:251], v3 offset:6144\n"
"v_add_f32 v21, v21, v78\n"
"v_mfma_f32_16x16x32_bf16 v[92:95], a[228:231], v[220:223], v[92:95]\n"
"v_exp_f32 v144, v144\n"
"v_mfma_f32_16x16x32_bf16 v[92:95], a[224:227], v[204:207], v[92:95]\n"
"s_add_u32 m0, s38, 53248\n"
"s_mov_b32 s50, 1179648\n"
"ds_read_b64_tr_b16 a[252:253], v3 offset:12288\n"
"v_add_f32 v21, v21, v79\n"
"v_mfma_f32_16x16x32_bf16 v[92:95], a[220:223], v[188:191], v[92:95]\n"
"buffer_load_dwordx4 v36, s[20:23], s50 offen offset:0 lds\n"
"v_mfma_f32_16x16x32_bf16 v[92:95], a[216:219], v[172:175], v[92:95]\n"
"s_waitcnt lgkmcnt(3)\n"
"ds_read_b64_tr_b16 a[254:255], v3 offset:18432\n"
"v_cvt_pk_bf16_f32 v143, v78, v79\n"
"v_mfma_f32_16x16x32_bf16 a[0:3], a[240:243], v[96:99], a[0:3]\n"
"v_exp_f32 v145, v145\n"
"v_mfma_f32_16x16x32_bf16 a[0:3], a[244:247], v[112:115], a[0:3]\n"
"ds_read_b64_tr_b16 a[216:217], v4 offset:0\n"
"v_add_f32 v18, v18, v144\n"
"v_mfma_f32_16x16x32_bf16 a[4:7], a[240:243], v[100:103], a[4:7]\n"
"buffer_load_dwordx4 v37, s[20:23], s50 offen offset:1024 lds\n"
"v_mfma_f32_16x16x32_bf16 a[4:7], a[244:247], v[116:119], a[4:7]\n"
"ds_read_b64_tr_b16 a[218:219], v4 offset:6144\n"
"v_add_f32 v18, v18, v145\n"
"v_mfma_f32_16x16x32_bf16 a[8:11], a[240:243], v[104:107], a[8:11]\n"
"v_exp_f32 v146, v146\n"
"v_mfma_f32_16x16x32_bf16 a[8:11], a[244:247], v[120:123], a[8:11]\n"
"ds_read_b64_tr_b16 a[220:221], v4 offset:12288\n"
"v_cvt_pk_bf16_f32 v144, v144, v145\n"
"v_mfma_f32_16x16x32_bf16 a[12:15], a[240:243], v[108:111], a[12:15]\n"
"buffer_load_dwordx4 v38, s[20:23], s50 offen offset:2048 lds\n"
"v_mfma_f32_16x16x32_bf16 a[12:15], a[244:247], v[124:127], a[12:15]\n"
"s_waitcnt lgkmcnt(3)\n"
"ds_read_b64_tr_b16 a[222:223], v4 offset:18432\n"
"v_add_f32 v18, v18, v146\n"
"v_mfma_f32_16x16x32_bf16 a[16:19], a[248:251], v[96:99], a[16:19]\n"
"v_exp_f32 v147, v147\n"
"v_mfma_f32_16x16x32_bf16 a[16:19], a[252:255], v[112:115], a[16:19]\n"
"ds_read_b64_tr_b16 a[224:225], v5 offset:0\n"
"v_add_f32 v18, v18, v147\n"
"v_mfma_f32_16x16x32_bf16 a[20:23], a[248:251], v[100:103], a[20:23]\n"
"v_exp_f32 v148, v148\n"
"v_mfma_f32_16x16x32_bf16 a[20:23], a[252:255], v[116:119], a[20:23]\n"
"ds_read_b64_tr_b16 a[226:227], v5 offset:6144\n"
"v_cvt_pk_bf16_f32 v145, v146, v147\n"
"v_mfma_f32_16x16x32_bf16 a[24:27], a[248:251], v[104:107], a[24:27]\n"
"v_exp_f32 v149, v149\n"
"v_mfma_f32_16x16x32_bf16 a[24:27], a[252:255], v[120:123], a[24:27]\n"
"ds_read_b64_tr_b16 a[228:229], v5 offset:12288\n"
"L_pg11:\n"
"s_cbranch_execz L_bk2\n"
"v_add_f32 v19, v19, v148\n"
"v_mfma_f32_16x16x32_bf16 a[28:31], a[248:251], v[108:111], a[28:31]\n"
"buffer_load_dword v27, v39, s[80:83], s96 offen\n"
"v_mfma_f32_16x16x32_bf16 a[28:31], a[252:255], v[124:127], a[28:31]\n"
"s_waitcnt lgkmcnt(3)\n"
"s_add_u32 s96, s96, s36\n"
"ds_read_b64_tr_b16 a[230:231], v5 offset:18432\n"
"v_add_f32 v19, v19, v149\n"
"v_mfma_f32_16x16x32_bf16 a[32:35], a[216:219], v[96:99], a[32:35]\n"
"v_exp_f32 v150, v150\n"
"v_mfma_f32_16x16x32_bf16 a[32:35], a[220:223], v[112:115], a[32:35]\n"
"ds_read_b64_tr_b16 a[240:241], v2 offset:128\n"
"v_cvt_pk_bf16_f32 v148, v148, v149\n"
"v_mfma_f32_16x16x32_bf16 a[36:39], a[216:219], v[100:103], a[36:39]\n"
"v_exp_f32 v151, v151\n"
"v_mfma_f32_16x16x32_bf16 a[36:39], a[220:223], v[116:119], a[36:39]\n"
"ds_read_b64_tr_b16 a[242:243], v2 offset:6272\n"
"v_add_f32 v19, v19, v150\n"
"v_mfma_f32_16x16x32_bf16 a[40:43], a[216:219], v[104:107], a[40:43]\n"
"v_exp_f32 v152, v152\n"
"v_mfma_f32_16x16x32_bf16 a[40:43], a[220:223], v[120:123], a[40:43]\n"
"ds_read_b64_tr_b16 a[244:245], v2 offset:12416\n"
"v_add_f32 v19, v19, v151\n"
"v_mfma_f32_16x16x32_bf16 a[44:47], a[216:219], v[108:111], a[44:47]\n"
"v_exp_f32 v153, v153\n"
"v_mfma_f32_16x16x32_bf16 a[44:47], a[220:223], v[124:127], a[44:47]\n"
"s_waitcnt lgkmcnt(3)\n"
"ds_read_b64_tr_b16 a[246:247], v2 offset:18560\n"
"v_cvt_pk_bf16_f32 v149, v150, v151\n"
"v_mfma_f32_16x16x32_bf16 a[48:51], a[224:227], v[96:99], a[48:51]\n"
"s_waitcnt vmcnt(14)\n"
"v_exp_f32 v154, v154\n"
"v_mfma_f32_16x16x32_bf16 a[48:51], a[228:231], v[112:115], a[48:51]\n"
"ds_read_b64_tr_b16 a[248:249], v3 offset:128\n"
"ds_read_b128 a[192:195], v34 offset:0\n"
"v_mfma_f32_16x16x32_bf16 a[52:55], a[224:227], v[100:103], a[52:55]\n"
"v_exp_f32 v155, v155\n"
"v_mfma_f32_16x16x32_bf16 a[52:55], a[228:231], v[116:119], a[52:55]\n"
"ds_read_b64_tr_b16 a[250:251], v3 offset:6272\n"
"v_add_f32 v20, v20, v152\n"
"v_mfma_f32_16x16x32_bf16 a[56:59], a[224:227], v[104:107], a[56:59]\n"
"v_add_f32 v20, v20, v153\n"
"v_cvt_pk_bf16_f32 v152, v152, v153\n"
"v_mfma_f32_16x16x32_bf16 a[56:59], a[228:231], v[120:123], a[56:59]\n"
"ds_read_b64_tr_b16 a[252:253], v3 offset:12416\n"
"ds_read_b128 a[196:199], v34 offset:2048\n"
"v_mfma_f32_16x16x32_bf16 a[60:63], a[224:227], v[108:111], a[60:63]\n"
"v_exp_f32 v156, v156\n"
"v_mfma_f32_16x16x32_bf16 a[60:63], a[228:231], v[124:127], a[60:63]\n"
"s_waitcnt lgkmcnt(5)\n"
"ds_read_b64_tr_b16 a[254:255], v3 offset:18560\n"
"v_add_f32 v20, v20, v154\n"
"v_mfma_f32_16x16x32_bf16 a[64:67], a[240:243], v[96:99], a[64:67]\n"
"v_exp_f32 v157, v157\n"
"v_mfma_f32_16x16x32_bf16 a[64:67], a[244:247], v[112:115], a[64:67]\n"
"ds_read_b64_tr_b16 a[216:217], v4 offset:128\n"
"ds_read_b128 a[200:203], v34 offset:4096\n"
"v_mfma_f32_16x16x32_bf16 a[68:71], a[240:243], v[100:103], a[68:71]\n"
"v_add_f32 v20, v20, v155\n"
"v_cvt_pk_bf16_f32 v153, v154, v155\n"
"v_mfma_f32_16x16x32_bf16 a[68:71], a[244:247], v[116:119], a[68:71]\n"
"ds_read_b64_tr_b16 a[218:219], v4 offset:6272\n"
"v_add_f32 v21, v21, v156\n"
"v_mfma_f32_16x16x32_bf16 a[72:75], a[240:243], v[104:107], a[72:75]\n"
"v_exp_f32 v158, v158\n"
"v_mfma_f32_16x16x32_bf16 a[72:75], a[244:247], v[120:123], a[72:75]\n"
"ds_read_b64_tr_b16 a[220:221], v4 offset:12416\n"
"ds_read_b128 a[204:207], v34 offset:6144\n"
"v_mfma_f32_16x16x32_bf16 a[76:79], a[240:243], v[108:111], a[76:79]\n"
"v_exp_f32 v159, v159\n"
"v_mfma_f32_16x16x32_bf16 a[76:79], a[244:247], v[124:127], a[76:79]\n"
"s_waitcnt lgkmcnt(5)\n"
"ds_read_b64_tr_b16 a[222:223], v4 offset:18560\n"
"v_add_f32 v21, v21, v157\n"
"v_mfma_f32_16x16x32_bf16 a[80:83], a[248:251], v[96:99], a[80:83]\n"
"v_exp_f32 v80, v80\n"
"v_mfma_f32_16x16x32_bf16 a[80:83], a[252:255], v[112:115], a[80:83]\n"
"ds_read_b64_tr_b16 a[224:225], v5 offset:128\n"
"ds_read_b128 a[208:211], v35 offset:0\n"
"v_mfma_f32_16x16x32_bf16 a[84:87], a[248:251], v[100:103], a[84:87]\n"
"v_cvt_pk_bf16_f32 v156, v156, v157\n"
"v_add_f32 v21, v21, v158\n"
"v_mfma_f32_16x16x32_bf16 a[84:87], a[252:255], v[116:119], a[84:87]\n"
"ds_read_b64_tr_b16 a[226:227], v5 offset:6272\n"
"v_add_f32 v21, v21, v159\n"
"v_mfma_f32_16x16x32_bf16 a[88:91], a[248:251], v[104:107], a[88:91]\n"
"v_exp_f32 v81, v81\n"
"v_mfma_f32_16x16x32_bf16 a[88:91], a[252:255], v[120:123], a[88:91]\n"
"ds_read_b64_tr_b16 a[228:229], v5 offset:12416\n"
"ds_read_b128 a[212:215], v35 offset:2048\n"
"v_mfma_f32_16x16x32_bf16 a[92:95], a[248:251], v[108:111], a[92:95]\n"
"v_exp_f32 v82, v82\n"
"v_mfma_f32_16x16x32_bf16 a[92:95], a[252:255], v[124:127], a[92:95]\n"
"s_waitcnt lgkmcnt(5)\n"
"ds_read_b64_tr_b16 a[230:231], v5 offset:18560\n"
"v_cvt_pk_bf16_f32 v157, v158, v159\n"
"v_mfma_f32_16x16x32_bf16 a[96:99], a[216:219], v[96:99], a[96:99]\n"
"v_exp_f32 v83, v83\n"
"v_mfma_f32_16x16x32_bf16 a[96:99], a[220:223], v[112:115], a[96:99]\n"
"ds_read_b64_tr_b16 a[240:241], v2 offset:256\n"
"ds_read_b128 a[232:235], v35 offset:4096\n"
"v_mfma_f32_16x16x32_bf16 a[100:103], a[216:219], v[100:103], a[100:103]\n"
"v_add_f32 v18, v18, v80\n"
"v_add_f32 v18, v18, v81\n"
"v_mfma_f32_16x16x32_bf16 a[100:103], a[220:223], v[116:119], a[100:103]\n"
"ds_read_b64_tr_b16 a[242:243], v2 offset:6400\n"
"v_cvt_pk_bf16_f32 v146, v80, v81\n"
"v_mfma_f32_16x16x32_bf16 a[104:107], a[216:219], v[104:107], a[104:107]\n"
"v_exp_f32 v84, v84\n"
"v_mfma_f32_16x16x32_bf16 a[104:107], a[220:223], v[120:123], a[104:107]\n"
"ds_read_b64_tr_b16 a[244:245], v2 offset:12544\n"
"ds_read_b128 a[236:239], v35 offset:6144\n"
"v_mfma_f32_16x16x32_bf16 a[108:111], a[216:219], v[108:111], a[108:111]\n"
"v_exp_f32 v85, v85\n"
"v_mfma_f32_16x16x32_bf16 a[108:111], a[220:223], v[124:127], a[108:111]\n"
"s_waitcnt lgkmcnt(5)\n"
"ds_read_b64_tr_b16 a[246:247], v2 offset:18688\n"
"v_add_f32 v18, v18, v82\n"
"v_mfma_f32_16x16x32_bf16 a[112:115], a[224:227], v[96:99], a[112:115]\n"
"v_add_f32 v18, v18, v83\n"
"v_cvt_pk_bf16_f32 v147, v82, v83\n"
"v_mfma_f32_16x16x32_bf16 a[112:115], a[228:231], v[112:115], a[112:115]\n"
"ds_read_b64_tr_b16 a[248:249], v3 offset:256\n"
"v_add_f32 v19, v19, v84\n"
"v_mfma_f32_16x16x32_bf16 a[116:119], a[224:227], v[100:103], a[116:119]\n"
"v_exp_f32 v86, v86\n"
"v_mfma_f32_16x16x32_bf16 a[116:119], a[228:231], v[116:119], a[116:119]\n"
"ds_read_b64_tr_b16 a[250:251], v3 offset:6400\n"
"v_add_f32 v19, v19, v85\n"
"v_mfma_f32_16x16x32_bf16 a[120:123], a[224:227], v[104:107], a[120:123]\n"
"v_exp_f32 v87, v87\n"
"v_mfma_f32_16x16x32_bf16 a[120:123], a[228:231], v[120:123], a[120:123]\n"
"ds_read_b64_tr_b16 a[252:253], v3 offset:12544\n"
"v_cvt_pk_bf16_f32 v150, v84, v85\n"
"v_mfma_f32_16x16x32_bf16 a[124:127], a[224:227], v[108:111], a[124:127]\n"
"v_exp_f32 v88, v88\n"
"v_mfma_f32_16x16x32_bf16 a[124:127], a[228:231], v[124:127], a[124:127]\n"
"s_waitcnt lgkmcnt(3)\n"
"ds_read_b64_tr_b16 a[254:255], v3 offset:18688\n"
"v_add_f32 v19, v19, v86\n"
"v_mfma_f32_16x16x32_bf16 a[128:131], a[240:243], v[96:99], a[128:131]\n"
"buffer_load_dword v27, v39, s[80:83], s96 offen\n"
"v_mfma_f32_16x16x32_bf16 a[128:131], a[244:247], v[112:115], a[128:131]\n"
"s_add_u32 s96, s96, s36\n"
"ds_read_b64_tr_b16 a[216:217], v4 offset:256\n"
"v_add_f32 v19, v19, v87\n"
"v_mfma_f32_16x16x32_bf16 a[132:135], a[240:243], v[100:103], a[132:135]\n"
"v_exp_f32 v89, v89\n"
"v_mfma_f32_16x16x32_bf16 a[132:135], a[244:247], v[116:119], a[132:135]\n"
"ds_read_b64_tr_b16 a[218:219], v4 offset:6400\n"
"v_cvt_pk_bf16_f32 v151, v86, v87\n"
"v_mfma_f32_16x16x32_bf16 a[136:139], a[240:243], v[104:107], a[136:139]\n"
"v_exp_f32 v90, v90\n"
"v_mfma_f32_16x16x32_bf16 a[136:139], a[244:247], v[120:123], a[136:139]\n"
"ds_read_b64_tr_b16 a[220:221], v4 offset:12544\n"
"v_add_f32 v20, v20, v88\n"
"v_mfma_f32_16x16x32_bf16 a[140:143], a[240:243], v[108:111], a[140:143]\n"
"v_exp_f32 v91, v91\n"
"v_mfma_f32_16x16x32_bf16 a[140:143], a[244:247], v[124:127], a[140:143]\n"
"s_waitcnt lgkmcnt(3)\n"
"ds_read_b64_tr_b16 a[222:223], v4 offset:18688\n"
"v_add_f32 v20, v20, v89\n"
"v_mfma_f32_16x16x32_bf16 a[144:147], a[248:251], v[96:99], a[144:147]\n"
"v_exp_f32 v92, v92\n"
"v_mfma_f32_16x16x32_bf16 a[144:147], a[252:255], v[112:115], a[144:147]\n"
"ds_read_b64_tr_b16 a[224:225], v5 offset:256\n"
"v_cvt_pk_bf16_f32 v154, v88, v89\n"
"v_mfma_f32_16x16x32_bf16 a[148:151], a[248:251], v[100:103], a[148:151]\n"
"v_exp_f32 v93, v93\n"
"v_mfma_f32_16x16x32_bf16 a[148:151], a[252:255], v[116:119], a[148:151]\n"
"ds_read_b64_tr_b16 a[226:227], v5 offset:6400\n"
"v_add_f32 v20, v20, v90\n"
"v_mfma_f32_16x16x32_bf16 a[152:155], a[248:251], v[104:107], a[152:155]\n"
"v_add_f32 v20, v20, v91\n"
"v_cvt_pk_bf16_f32 v155, v90, v91\n"
"v_mfma_f32_16x16x32_bf16 a[152:155], a[252:255], v[120:123], a[152:155]\n"
"ds_read_b64_tr_b16 a[228:229], v5 offset:12544\n"
"v_add_f32 v21, v21, v92\n"
"v_mfma_f32_16x16x32_bf16 a[156:159], a[248:251], v[108:111], a[156:159]\n"
"s_waitcnt lgkmcnt(7)\n"
"v_exp_f32 v94, v94\n"
"v_mfma_f32_16x16x32_bf16 a[156:159], a[252:255], v[124:127], a[156:159]\n"
"s_waitcnt lgkmcnt(3)\n"
"s_add_u32 m0, s58, 0\n"
"s_add_u32 s51, s68, 128\n"
"ds_read_b64_tr_b16 a[230:231], v5 offset:18688\n"
"v_add_f32 v21, v21, v93\n"
"v_mfma_f32_16x16x32_bf16 a[160:163], a[216:219], v[96:99], a[160:163]\n"
"buffer_load_dwordx4 v32, s[16:19], s51 offen offset:0 lds\n"
"v_mfma_f32_16x16x32_bf16 a[160:163], a[220:223], v[112:115], a[160:163]\n"
"v_exp_f32 v95, v95\n"
"v_mfma_f32_16x16x32_bf16 a[164:167], a[216:219], v[100:103], a[164:167]\n"
"buffer_load_dwordx4 v33, s[16:19], s51 offen offset:1024 lds\n"
"v_mfma_f32_16x16x32_bf16 a[164:167], a[220:223], v[116:119], a[164:167]\n"
"s_add_u32 m0, s58, 2048\n"
"s_add_u32 s51, s69, 128\n"
"v_cvt_pk_bf16_f32 v158, v92, v93\n"
"v_add_f32 v21, v21, v94\n"
"v_mfma_f32_16x16x32_bf16 a[168:171], a[216:219], v[104:107], a[168:171]\n"
"s_mov_b32 s47, s44\n"
"buffer_load_dwordx4 v32, s[16:19], s51 offen offset:0 lds\n"
"v_mfma_f32_16x16x32_bf16 a[168:171], a[220:223], v[120:123], a[168:171]\n"
"s_mov_b32 s44, s45\n"
"v_add_f32 v21, v21, v95\n"
"v_cvt_pk_bf16_f32 v159, v94, v95\n"
"v_mfma_f32_16x16x32_bf16 a[172:175], a[216:219], v[108:111], a[172:175]\n"
"s_mov_b32 s45, s46\n"
"buffer_load_dwordx4 v33, s[16:19], s51 offen offset:1024 lds\n"
"v_mfma_f32_16x16x32_bf16 a[172:175], a[220:223], v[124:127], a[172:175]\n"
"s_waitcnt lgkmcnt(0)\n"
"s_add_u32 m0, s58, 4096\n"
"s_add_u32 s51, s70, 128\n"
"s_mov_b32 s46, s47\n"
"v_max3_f32 v22, v18, v19, v20\n"
"v_max_f32 v22, v22, v21\n"
"v_mfma_f32_16x16x32_bf16 a[176:179], a[224:227], v[96:99], a[176:179]\n"
"buffer_load_dwordx4 v32, s[16:19], s51 offen offset:0 lds\n"
"v_mfma_f32_16x16x32_bf16 a[176:179], a[228:231], v[112:115], a[176:179]\n"
"v_cmp_lt_f32 vcc, s54, v22\n"
"v_mfma_f32_16x16x32_bf16 a[180:183], a[224:227], v[100:103], a[180:183]\n"
"buffer_load_dwordx4 v33, s[16:19], s51 offen offset:1024 lds\n"
"v_mfma_f32_16x16x32_bf16 a[180:183], a[228:231], v[116:119], a[180:183]\n"
"s_add_u32 m0, s58, 6144\n"
"s_add_u32 s51, s71, 128\n"
"v_mfma_f32_16x16x32_bf16 a[184:187], a[224:227], v[104:107], a[184:187]\n"
"buffer_load_dwordx4 v32, s[16:19], s51 offen offset:0 lds\n"
"v_mfma_f32_16x16x32_bf16 a[184:187], a[228:231], v[120:123], a[184:187]\n"
"s_cmp_lg_u64 vcc, 0\n"
"v_mfma_f32_16x16x32_bf16 a[188:191], a[224:227], v[108:111], a[188:191]\n"
"buffer_load_dwordx4 v33, s[16:19], s51 offen offset:1024 lds\n"
"v_mfma_f32_16x16x32_bf16 a[188:191], a[228:231], v[124:127], a[188:191]\n"
"s_cbranch_scc1 L_rc7\n"
"L_rr7:\n"
"v_add_f32 v14, v14, v18\n"
"v_add_f32 v15, v15, v19\n"
"v_add_f32 v16, v16, v20\n"
"v_add_f32 v17, v17, v21\n"
"v_add_u32 v2, s46, v2\n"
"v_add_u32 v3, s46, v3\n"
"v_add_u32 v4, s46, v4\n"
"v_add_u32 v5, s46, v5\n"
"s_waitcnt vmcnt(33)\n"
"s_waitcnt vmcnt(17)\n"
"s_barrier\n"
"ds_read_b64_tr_b16 a[240:241], v2 offset:0\n"
"ds_read_b64_tr_b16 a[242:243], v2 offset:6144\n"
"ds_read_b64_tr_b16 a[244:245], v2 offset:12288\n"
"ds_read_b64_tr_b16 a[246:247], v2 offset:18432\n"
"ds_read_b64_tr_b16 a[248:249], v3 offset:0\n"
"ds_read_b64_tr_b16 a[250:251], v3 offset:6144\n"
"ds_read_b64_tr_b16 a[252:253], v3 offset:12288\n"
"ds_read_b64_tr_b16 a[254:255], v3 offset:18432\n"
"s_mov_b64 s[8:9], s[28:29]\n"
"s_mov_b64 s[12:13], s[30:31]\n"
"v_mov_b32 v22, v14\n"
"v_mov_b32 v23, v15\n"
"v_mov_b32 v24, v16\n"
"v_mov_b32 v25, v17\n"
"v_permlane32_swap_b32 v14, v22\n"
"v_permlane32_swap_b32 v15, v23\n"
"v_permlane32_swap_b32 v16, v24\n"
"v_permlane32_swap_b32 v17, v25\n"
"v_add_f32 v14, v14, v22\n"
"v_add_f32 v15, v15, v23\n"
"v_add_f32 v16, v16, v24\n"
"v_add_f32 v17, v17, v25\n"
"s_waitcnt lgkmcnt(4)\n"
"s_mov_b64 exec, 0\n"
"s_cmp_eq_u32 s38, 0\n"
"s_cbranch_scc1 L_pg12\n"
"s_cmp_eq_u32 s38, 3072\n"
"s_cbranch_scc1 L_pg13\n"
"s_cmp_eq_u32 s38, 6144\n"
"s_cbranch_scc1 L_pg14\n"
"s_branch L_pg15\n"
"L_bk3:\n"
"s_mov_b64 exec, -1\n"
"v_mfma_f32_16x16x32_bf16 a[0:3], a[240:243], v[128:131], a[0:3]\n"
"v_mov_b32 v22, v14\n"
"v_mov_b32 v23, v15\n"
"v_mfma_f32_16x16x32_bf16 a[0:3], a[244:247], v[144:147], a[0:3]\n"
"s_add_u32 m0, s38, 65536\n"
"s_mov_b32 s50, 1572864\n"
"ds_read_b64_tr_b16 a[216:217], v4 offset:0\n"
"v_mov_b32 v24, v16\n"
"v_mfma_f32_16x16x32_bf16 a[4:7], a[240:243], v[132:135], a[4:7]\n"
"buffer_load_dwordx4 v36, s[20:23], s50 offen offset:0 lds\n"
"v_mfma_f32_16x16x32_bf16 a[4:7], a[244:247], v[148:151], a[4:7]\n"
"ds_read_b64_tr_b16 a[218:219], v4 offset:6144\n"
"v_mov_b32 v25, v17\n"
"v_mfma_f32_16x16x32_bf16 a[8:11], a[240:243], v[136:139], a[8:11]\n"
"buffer_load_dwordx4 v37, s[20:23], s50 offen offset:1024 lds\n"
"v_mfma_f32_16x16x32_bf16 a[8:11], a[244:247], v[152:155], a[8:11]\n"
"ds_read_b64_tr_b16 a[220:221], v4 offset:12288\n"
"v_permlane16_swap_b32 v14, v22\n"
"v_mfma_f32_16x16x32_bf16 a[12:15], a[240:243], v[140:143], a[12:15]\n"
"buffer_load_dwordx4 v38, s[20:23], s50 offen offset:2048 lds\n"
"v_mfma_f32_16x16x32_bf16 a[12:15], a[244:247], v[156:159], a[12:15]\n"
"s_waitcnt lgkmcnt(3)\n"
"s_add_u32 m0, s38, 77824\n"
"s_mov_b32 s50, 1966080\n"
"ds_read_b64_tr_b16 a[222:223], v4 offset:18432\n"
"v_permlane16_swap_b32 v15, v23\n"
"v_mfma_f32_16x16x32_bf16 a[16:19], a[248:251], v[128:131], a[16:19]\n"
"buffer_load_dwordx4 v36, s[20:23], s50 offen offset:0 lds\n"
"v_mfma_f32_16x16x32_bf16 a[16:19], a[252:255], v[144:147], a[16:19]\n"
"ds_read_b64_tr_b16 a[224:225], v5 offset:0\n"
"v_permlane16_swap_b32 v16, v24\n"
"v_mfma_f32_16x16x32_bf16 a[20:23], a[248:251], v[132:135], a[20:23]\n"
"buffer_load_dwordx4 v37, s[20:23], s50 offen offset:1024 lds\n"
"v_mfma_f32_16x16x32_bf16 a[20:23], a[252:255], v[148:151], a[20:23]\n"
"ds_read_b64_tr_b16 a[226:227], v5 offset:6144\n"
"v_permlane16_swap_b32 v17, v25\n"
"v_mfma_f32_16x16x32_bf16 a[24:27], a[248:251], v[136:139], a[24:27]\n"
"buffer_load_dwordx4 v38, s[20:23], s50 offen offset:2048 lds\n"
"v_mfma_f32_16x16x32_bf16 a[24:27], a[252:255], v[152:155], a[24:27]\n"
"s_add_u32 m0, s39, 0\n"
"s_add_u32 s51, s68, 256\n"
"ds_read_b64_tr_b16 a[228:229], v5 offset:12288\n"
"v_add_f32 v14, v14, v22\n"
"v_mfma_f32_16x16x32_bf16 a[28:31], a[248:251], v[140:143], a[28:31]\n"
"buffer_load_dwordx4 v32, s[16:19], s51 offen offset:0 lds\n"
"v_mfma_f32_16x16x32_bf16 a[28:31], a[252:255], v[156:159], a[28:31]\n"
"s_waitcnt lgkmcnt(3)\n"
"ds_read_b64_tr_b16 a[230:231], v5 offset:18432\n"
"v_add_f32 v15, v15, v23\n"
"v_mfma_f32_16x16x32_bf16 a[32:35], a[216:219], v[128:131], a[32:35]\n"
"v_add_f32 v16, v16, v24\n"
"v_add_f32 v17, v17, v25\n"
"v_mfma_f32_16x16x32_bf16 a[32:35], a[220:223], v[144:147], a[32:35]\n"
"ds_read_b64_tr_b16 a[240:241], v2 offset:128\n"
"v_accvgpr_read_b32 v40, a0\n"
"v_mfma_f32_16x16x32_bf16 a[36:39], a[216:219], v[132:135], a[36:39]\n"
"buffer_load_dwordx4 v33, s[16:19], s51 offen offset:1024 lds\n"
"v_mfma_f32_16x16x32_bf16 a[36:39], a[220:223], v[148:151], a[36:39]\n"
"ds_read_b64_tr_b16 a[242:243], v2 offset:6272\n"
"v_accvgpr_read_b32 v41, a1\n"
"v_mfma_f32_16x16x32_bf16 a[40:43], a[216:219], v[136:139], a[40:43]\n"
"v_accvgpr_read_b32 v42, a2\n"
"v_accvgpr_read_b32 v43, a3\n"
"v_mfma_f32_16x16x32_bf16 a[40:43], a[220:223], v[152:155], a[40:43]\n"
"s_add_u32 m0, s39, 2048\n"
"s_add_u32 s51, s69, 256\n"
"ds_read_b64_tr_b16 a[244:245], v2 offset:12416\n"
"v_accvgpr_read_b32 v44, a16\n"
"v_mfma_f32_16x16x32_bf16 a[44:47], a[216:219], v[140:143], a[44:47]\n"
"buffer_load_dwordx4 v32, s[16:19], s51 offen offset:0 lds\n"
"v_mfma_f32_16x16x32_bf16 a[44:47], a[220:223], v[156:159], a[44:47]\n"
"s_waitcnt lgkmcnt(3)\n"
"ds_read_b64_tr_b16 a[246:247], v2 offset:18560\n"
"v_accvgpr_read_b32 v45, a17\n"
"v_mfma_f32_16x16x32_bf16 a[48:51], a[224:227], v[128:131], a[48:51]\n"
"v_accvgpr_read_b32 v46, a18\n"
"v_accvgpr_read_b32 v47, a19\n"
"v_mfma_f32_16x16x32_bf16 a[48:51], a[228:231], v[144:147], a[48:51]\n"
"ds_read_b64_tr_b16 a[248:249], v3 offset:128\n"
"v_accvgpr_read_b32 v48, a4\n"
"v_mfma_f32_16x16x32_bf16 a[52:55], a[224:227], v[132:135], a[52:55]\n"
"buffer_load_dwordx4 v33, s[16:19], s51 offen offset:1024 lds\n"
"v_mfma_f32_16x16x32_bf16 a[52:55], a[228:231], v[148:151], a[52:55]\n"
"ds_read_b64_tr_b16 a[250:251], v3 offset:6272\n"
"v_accvgpr_read_b32 v49, a5\n"
"v_mfma_f32_16x16x32_bf16 a[56:59], a[224:227], v[136:139], a[56:59]\n"
"v_accvgpr_read_b32 v50, a6\n"
"v_accvgpr_read_b32 v51, a7\n"
"v_mfma_f32_16x16x32_bf16 a[56:59], a[228:231], v[152:155], a[56:59]\n"
"s_add_u32 m0, s39, 4096\n"
"s_add_u32 s51, s70, 256\n"
"ds_read_b64_tr_b16 a[252:253], v3 offset:12416\n"
"v_accvgpr_read_b32 v52, a20\n"
"v_mfma_f32_16x16x32_bf16 a[60:63], a[224:227], v[140:143], a[60:63]\n"
"buffer_load_dwordx4 v32, s[16:19], s51 offen offset:0 lds\n"
"v_mfma_f32_16x16x32_bf16 a[60:63], a[228:231], v[156:159], a[60:63]\n"
"s_waitcnt lgkmcnt(3)\n"
"ds_read_b64_tr_b16 a[254:255], v3 offset:18560\n"
"v_accvgpr_read_b32 v53, a21\n"
"v_mfma_f32_16x16x32_bf16 a[64:67], a[240:243], v[128:131], a[64:67]\n"
"v_accvgpr_read_b32 v54, a22\n"
"v_accvgpr_read_b32 v55, a23\n"
"v_mfma_f32_16x16x32_bf16 a[64:67], a[244:247], v[144:147], a[64:67]\n"
"ds_read_b64_tr_b16 a[216:217], v4 offset:128\n"
"v_accvgpr_read_b32 v56, a8\n"
"v_mfma_f32_16x16x32_bf16 a[68:71], a[240:243], v[132:135], a[68:71]\n"
"buffer_load_dwordx4 v33, s[16:19], s51 offen offset:1024 lds\n"
"v_mfma_f32_16x16x32_bf16 a[68:71], a[244:247], v[148:151], a[68:71]\n"
"ds_read_b64_tr_b16 a[218:219], v4 offset:6272\n"
"v_accvgpr_read_b32 v57, a9\n"
"v_mfma_f32_16x16x32_bf16 a[72:75], a[240:243], v[136:139], a[72:75]\n"
"v_accvgpr_read_b32 v58, a10\n"
"v_accvgpr_read_b32 v59, a11\n"
"v_mfma_f32_16x16x32_bf16 a[72:75], a[244:247], v[152:155], a[72:75]\n"
"s_add_u32 m0, s39, 6144\n"
"s_add_u32 s51, s71, 256\n"
"ds_read_b64_tr_b16 a[220:221], v4 offset:12416\n"
"v_accvgpr_read_b32 v60, a24\n"
"v_mfma_f32_16x16x32_bf16 a[76:79], a[240:243], v[140:143], a[76:79]\n"
"buffer_load_dwordx4 v32, s[16:19], s51 offen offset:0 lds\n"
"v_mfma_f32_16x16x32_bf16 a[76:79], a[244:247], v[156:159], a[76:79]\n"
"s_waitcnt lgkmcnt(3)\n"
"ds_read_b64_tr_b16 a[222:223], v4 offset:18560\n"
"v_accvgpr_read_b32 v61, a25\n"
"v_mfma_f32_16x16x32_bf16 a[80:83], a[248:251], v[128:131], a[80:83]\n"
"v_rcp_f32 v14, v14\n"
"v_mfma_f32_16x16x32_bf16 a[80:83], a[252:255], v[144:147], a[80:83]\n"
"ds_read_b64_tr_b16 a[224:225], v5 offset:128\n"
"v_accvgpr_read_b32 v62, a26\n"
"v_mfma_f32_16x16x32_bf16 a[84:87], a[248:251], v[132:135], a[84:87]\n"
"buffer_load_dwordx4 v33, s[16:19], s51 offen offset:1024 lds\n"
"v_mfma_f32_16x16x32_bf16 a[84:87], a[252:255], v[148:151], a[84:87]\n"
"ds_read_b64_tr_b16 a[226:227], v5 offset:6272\n"
"v_accvgpr_read_b32 v63, a27\n"
"v_mfma_f32_16x16x32_bf16 a[88:91], a[248:251], v[136:139], a[88:91]\n"
"v_rcp_f32 v15, v15\n"
"v_mfma_f32_16x16x32_bf16 a[88:91], a[252:255], v[152:155], a[88:91]\n"
"ds_read_b64_tr_b16 a[228:229], v5 offset:12416\n"
"v_mfma_f32_16x16x32_bf16 a[92:95], a[248:251], v[140:143], a[92:95]\n"
"v_rcp_f32 v16, v16\n"
"v_mfma_f32_16x16x32_bf16 a[92:95], a[252:255], v[156:159], a[92:95]\n"
"s_waitcnt lgkmcnt(3)\n"
"ds_read_b64_tr_b16 a[230:231], v5 offset:18560\n"
"v_mfma_f32_16x16x32_bf16 a[96:99], a[216:219], v[128:131], a[96:99]\n"
"v_rcp_f32 v17, v17\n"
"v_mfma_f32_16x16x32_bf16 a[96:99], a[220:223], v[144:147], a[96:99]\n"
"ds_read_b64_tr_b16 a[240:241], v2 offset:256\n"
"v_accvgpr_read_b32 v18, a12\n"
"v_mfma_f32_16x16x32_bf16 a[100:103], a[216:219], v[132:135], a[100:103]\n"
"v_accvgpr_read_b32 v19, a13\n"
"v_accvgpr_read_b32 v20, a14\n"
"v_mfma_f32_16x16x32_bf16 a[100:103], a[220:223], v[148:151], a[100:103]\n"
"ds_read_b64_tr_b16 a[242:243], v2 offset:6400\n"
"v_accvgpr_read_b32 v21, a15\n"
"v_mfma_f32_16x16x32_bf16 a[104:107], a[216:219], v[136:139], a[104:107]\n"
"v_accvgpr_read_b32 v22, a28\n"
"v_accvgpr_read_b32 v23, a29\n"
"v_mfma_f32_16x16x32_bf16 a[104:107], a[220:223], v[152:155], a[104:107]\n"
"ds_read_b64_tr_b16 a[244:245], v2 offset:12544\n"
"v_accvgpr_read_b32 v24, a30\n"
"v_mfma_f32_16x16x32_bf16 a[108:111], a[216:219], v[140:143], a[108:111]\n"
"v_accvgpr_read_b32 v25, a31\n"
"v_accvgpr_read_b32 v224, a40\n"
"v_mfma_f32_16x16x32_bf16 a[108:111], a[220:223], v[156:159], a[108:111]\n"
"s_waitcnt lgkmcnt(3)\n"
"ds_read_b64_tr_b16 a[246:247], v2 offset:18688\n"
"v_accvgpr_read_b32 v225, a41\n"
"v_mfma_f32_16x16x32_bf16 a[112:115], a[224:227], v[128:131], a[112:115]\n"
"v_accvgpr_read_b32 v226, a42\n"
"v_accvgpr_read_b32 v227, a43\n"
"v_mfma_f32_16x16x32_bf16 a[112:115], a[228:231], v[144:147], a[112:115]\n"
"ds_read_b64_tr_b16 a[248:249], v3 offset:256\n"
"v_accvgpr_read_b32 v228, a56\n"
"v_mfma_f32_16x16x32_bf16 a[116:119], a[224:227], v[132:135], a[116:119]\n"
"v_accvgpr_read_b32 v229, a57\n"
"v_accvgpr_read_b32 v230, a58\n"
"v_mfma_f32_16x16x32_bf16 a[116:119], a[228:231], v[148:151], a[116:119]\n"
"ds_read_b64_tr_b16 a[250:251], v3 offset:6400\n"
"v_accvgpr_read_b32 v231, a59\n"
"v_mfma_f32_16x16x32_bf16 a[120:123], a[224:227], v[136:139], a[120:123]\n"
"v_accvgpr_read_b32 v232, a44\n"
"v_accvgpr_read_b32 v233, a45\n"
"v_mfma_f32_16x16x32_bf16 a[120:123], a[228:231], v[152:155], a[120:123]\n"
"ds_read_b64_tr_b16 a[252:253], v3 offset:12544\n"
"v_accvgpr_read_b32 v234, a46\n"
"v_mfma_f32_16x16x32_bf16 a[124:127], a[224:227], v[140:143], a[124:127]\n"
"v_accvgpr_read_b32 v235, a47\n"
"v_accvgpr_read_b32 v236, a60\n"
"v_mfma_f32_16x16x32_bf16 a[124:127], a[228:231], v[156:159], a[124:127]\n"
"s_waitcnt lgkmcnt(3)\n"
"ds_read_b64_tr_b16 a[254:255], v3 offset:18688\n"
"v_accvgpr_read_b32 v237, a61\n"
"v_mfma_f32_16x16x32_bf16 a[128:131], a[240:243], v[128:131], a[128:131]\n"
"v_accvgpr_read_b32 v238, a62\n"
"v_accvgpr_read_b32 v239, a63\n"
"v_mfma_f32_16x16x32_bf16 a[128:131], a[244:247], v[144:147], a[128:131]\n"
"ds_read_b64_tr_b16 a[216:217], v4 offset:256\n"
"v_fma_f32 v224, v16, v224, -v16\n"
"v_mfma_f32_16x16x32_bf16 a[132:135], a[240:243], v[132:135], a[132:135]\n"
"v_fma_f32 v225, v16, v225, -v16\n"
"v_fma_f32 v226, v16, v226, -v16\n"
"v_mfma_f32_16x16x32_bf16 a[132:135], a[244:247], v[148:151], a[132:135]\n"
"ds_read_b64_tr_b16 a[218:219], v4 offset:6400\n"
"v_fma_f32 v227, v16, v227, -v16\n"
"v_mfma_f32_16x16x32_bf16 a[136:139], a[240:243], v[136:139], a[136:139]\n"
"v_fma_f32 v228, v16, v228, -v16\n"
"v_fma_f32 v229, v16, v229, -v16\n"
"v_mfma_f32_16x16x32_bf16 a[136:139], a[244:247], v[152:155], a[136:139]\n"
"ds_read_b64_tr_b16 a[220:221], v4 offset:12544\n"
"v_fma_f32 v230, v16, v230, -v16\n"
"v_mfma_f32_16x16x32_bf16 a[140:143], a[240:243], v[140:143], a[140:143]\n"
"v_fma_f32 v231, v16, v231, -v16\n"
"v_cvt_pk_bf16_f32 v224, v224, v225\n"
"v_mfma_f32_16x16x32_bf16 a[140:143], a[244:247], v[156:159], a[140:143]\n"
"s_waitcnt lgkmcnt(3)\n"
"ds_read_b64_tr_b16 a[222:223], v4 offset:18688\n"
"v_cvt_pk_bf16_f32 v225, v226, v227\n"
"v_mfma_f32_16x16x32_bf16 a[144:147], a[248:251], v[128:131], a[144:147]\n"
"v_cvt_pk_bf16_f32 v226, v228, v229\n"
"v_cvt_pk_bf16_f32 v227, v230, v231\n"
"v_mfma_f32_16x16x32_bf16 a[144:147], a[252:255], v[144:147], a[144:147]\n"
"ds_read_b64_tr_b16 a[224:225], v5 offset:256\n"
"v_fma_f32 v232, v17, v232, -v17\n"
"v_mfma_f32_16x16x32_bf16 a[148:151], a[248:251], v[132:135], a[148:151]\n"
"buffer_store_dwordx4 v[224:227], v12, s[24:27], s3 offen offset:2048 sc1\n"
"v_mfma_f32_16x16x32_bf16 a[148:151], a[252:255], v[148:151], a[148:151]\n"
"ds_read_b64_tr_b16 a[226:227], v5 offset:6400\n"
"v_fma_f32 v233, v17, v233, -v17\n"
"v_mfma_f32_16x16x32_bf16 a[152:155], a[248:251], v[136:139], a[152:155]\n"
"v_fma_f32 v234, v17, v234, -v17\n"
"v_fma_f32 v235, v17, v235, -v17\n"
"v_mfma_f32_16x16x32_bf16 a[152:155], a[252:255], v[152:155], a[152:155]\n"
"ds_read_b64_tr_b16 a[228:229], v5 offset:12544\n"
"v_fma_f32 v236, v17, v236, -v17\n"
"v_mfma_f32_16x16x32_bf16 a[156:159], a[248:251], v[140:143], a[156:159]\n"
"v_fma_f32 v237, v17, v237, -v17\n"
"v_fma_f32 v238, v17, v238, -v17\n"
"v_mfma_f32_16x16x32_bf16 a[156:159], a[252:255], v[156:159], a[156:159]\n"
"s_waitcnt lgkmcnt(3)\n"
"ds_read_b64_tr_b16 a[230:231], v5 offset:18688\n"
"v_fma_f32 v239, v17, v239, -v17\n"
"v_mfma_f32_16x16x32_bf16 a[160:163], a[216:219], v[128:131], a[160:163]\n"
"v_cvt_pk_bf16_f32 v232, v232, v233\n"
"v_cvt_pk_bf16_f32 v233, v234, v235\n"
"v_mfma_f32_16x16x32_bf16 a[160:163], a[220:223], v[144:147], a[160:163]\n"
"v_cvt_pk_bf16_f32 v234, v236, v237\n"
"v_cvt_pk_bf16_f32 v235, v238, v239\n"
"v_mfma_f32_16x16x32_bf16 a[164:167], a[216:219], v[132:135], a[164:167]\n"
"buffer_store_dwordx4 v[232:235], v12, s[24:27], s3 offen offset:3072 sc1\n"
"v_mfma_f32_16x16x32_bf16 a[164:167], a[220:223], v[148:151], a[164:167]\n"
"ds_read_b64_tr_b16 a[240:241], v28 offset:0\n"
"v_fma_f32 v40, v14, v40, -v14\n"
"v_mfma_f32_16x16x32_bf16 a[168:171], a[216:219], v[136:139], a[168:171]\n"
"ds_read_b64_tr_b16 a[242:243], v28 offset:1536\n"
"v_fma_f32 v41, v14, v41, -v14\n"
"v_mfma_f32_16x16x32_bf16 a[168:171], a[220:223], v[152:155], a[168:171]\n"
"ds_read_b64_tr_b16 a[244:245], v28 offset:12288\n"
"v_fma_f32 v42, v14, v42, -v14\n"
"v_mfma_f32_16x16x32_bf16 a[172:175], a[216:219], v[140:143], a[172:175]\n"
"ds_read_b64_tr_b16 a[246:247], v28 offset:13824\n"
"v_fma_f32 v43, v14, v43, -v14\n"
"v_mfma_f32_16x16x32_bf16 a[172:175], a[220:223], v[156:159], a[172:175]\n"
"s_waitcnt lgkmcnt(4)\n"
"ds_read_b64_tr_b16 a[248:249], v29 offset:0\n"
"v_fma_f32 v44, v14, v44, -v14\n"
"v_mfma_f32_16x16x32_bf16 a[176:179], a[224:227], v[128:131], a[176:179]\n"
"ds_read_b64_tr_b16 a[250:251], v29 offset:1536\n"
"v_fma_f32 v45, v14, v45, -v14\n"
"v_mfma_f32_16x16x32_bf16 a[176:179], a[228:231], v[144:147], a[176:179]\n"
"ds_read_b64_tr_b16 a[252:253], v29 offset:12288\n"
"v_fma_f32 v46, v14, v46, -v14\n"
"v_mfma_f32_16x16x32_bf16 a[180:183], a[224:227], v[132:135], a[180:183]\n"
"ds_read_b64_tr_b16 a[254:255], v29 offset:13824\n"
"v_fma_f32 v47, v14, v47, -v14\n"
"v_mfma_f32_16x16x32_bf16 a[180:183], a[228:231], v[148:151], a[180:183]\n"
"ds_read_b64_tr_b16 a[216:217], v30 offset:0\n"
"v_cvt_pk_bf16_f32 v40, v40, v41\n"
"v_mfma_f32_16x16x32_bf16 a[184:187], a[224:227], v[136:139], a[184:187]\n"
"ds_read_b64_tr_b16 a[218:219], v30 offset:1536\n"
"v_cvt_pk_bf16_f32 v41, v42, v43\n"
"v_mfma_f32_16x16x32_bf16 a[184:187], a[228:231], v[152:155], a[184:187]\n"
"ds_read_b64_tr_b16 a[220:221], v30 offset:12288\n"
"v_cvt_pk_bf16_f32 v42, v44, v45\n"
"v_mfma_f32_16x16x32_bf16 a[188:191], a[224:227], v[140:143], a[188:191]\n"
"ds_read_b64_tr_b16 a[222:223], v30 offset:13824\n"
"v_cvt_pk_bf16_f32 v43, v46, v47\n"
"v_mfma_f32_16x16x32_bf16 a[188:191], a[228:231], v[156:159], a[188:191]\n"
"buffer_store_dwordx4 v[40:43], v12, s[24:27], s2 offen offset:0 sc1\n"
"s_waitcnt lgkmcnt(8)\n"
"s_mov_b32 s49, 1\n"
"s_min_u32 s47, s99, 1\n"
"s_mul_i32 s47, s47, s67\n"
"s_add_u32 s97, s97, s47\n"
"s_movk_i32 s79, 0\n"
"v_mfma_f32_16x16x32_bf16 v[64:67], a[240:243], a[192:195], 0\n"
"ds_read_b64_tr_b16 a[224:225], v31 offset:0\n"
"v_fma_f32 v48, v15, v48, -v15\n"
"v_mfma_f32_16x16x32_bf16 v[64:67], a[244:247], a[208:211], v[64:67]\n"
"ds_read_b64_tr_b16 a[226:227], v31 offset:1536\n"
"v_fma_f32 v49, v15, v49, -v15\n"
"v_mfma_f32_16x16x32_bf16 v[68:71], a[240:243], a[196:199], 0\n"
"ds_read_b64_tr_b16 a[228:229], v31 offset:12288\n"
"v_fma_f32 v50, v15, v50, -v15\n"
"v_mfma_f32_16x16x32_bf16 v[68:71], a[244:247], a[212:215], v[68:71]\n"
"ds_read_b64_tr_b16 a[230:231], v31 offset:13824\n"
"v_fma_f32 v51, v15, v51, -v15\n"
"v_mfma_f32_16x16x32_bf16 v[72:75], a[240:243], a[200:203], 0\n"
"v_fma_f32 v52, v15, v52, -v15\n"
"v_fma_f32 v53, v15, v53, -v15\n"
"L_pg12:\n"
"s_cbranch_execz L_bk3\n"
"v_mfma_f32_16x16x32_bf16 v[72:75], a[244:247], a[232:235], v[72:75]\n"
"v_max3_f32 v252, |v64|, |v65|, |v66|\n"
"v_fma_f32 v54, v15, v54, -v15\n"
"v_mfma_f32_16x16x32_bf16 v[76:79], a[240:243], a[204:207], 0\n"
"v_fma_f32 v55, v15, v55, -v15\n"
"v_cvt_pk_bf16_f32 v48, v48, v49\n"
"v_mfma_f32_16x16x32_bf16 v[76:79], a[244:247], a[236:239], v[76:79]\n"
"s_waitcnt lgkmcnt(8)\n"
"v_max3_f32 v253, |v68|, |v69|, |v70|\n"
"v_cvt_pk_bf16_f32 v49, v50, v51\n"
"v_mfma_f32_16x16x32_bf16 v[160:163], a[248:251], a[192:195], 0\n"
"ds_read_b64_tr_b16 a[240:241], v28 offset:128\n"
"v_cvt_pk_bf16_f32 v50, v52, v53\n"
"v_mfma_f32_16x16x32_bf16 v[160:163], a[252:255], a[208:211], v[160:163]\n"
"ds_read_b64_tr_b16 a[242:243], v28 offset:1664\n"
"v_max3_f32 v254, |v72|, |v73|, |v74|\n"
"v_mfma_f32_16x16x32_bf16 v[164:167], a[248:251], a[196:199], 0\n"
"ds_read_b64_tr_b16 a[244:245], v28 offset:12416\n"
"v_cvt_pk_bf16_f32 v51, v54, v55\n"
"v_mfma_f32_16x16x32_bf16 v[164:167], a[252:255], a[212:215], v[164:167]\n"
"ds_read_b64_tr_b16 a[246:247], v28 offset:13952\n"
"v_max3_f32 v255, |v76|, |v77|, |v78|\n"
"v_mfma_f32_16x16x32_bf16 v[168:171], a[248:251], a[200:203], 0\n"
"v_min3_f32 v252, v252, v253, v254\n"
"v_min_f32 v252, v252, v255\n"
"v_mfma_f32_16x16x32_bf16 v[168:171], a[252:255], a[232:235], v[168:171]\n"
"v_min_f32 v252, 0x71800000, v252\n"
"v_add_u32 v252, 0x1000000, v252\n"
"v_mfma_f32_16x16x32_bf16 v[172:175], a[248:251], a[204:207], 0\n"
"v_and_b32 v252, 0x7e000000, v252\n"
"v_mul_f32 v253, 0x42c00000, v252\n"
"v_mfma_f32_16x16x32_bf16 v[172:175], a[252:255], a[236:239], v[172:175]\n"
"s_waitcnt lgkmcnt(8)\n"
"v_mul_f32 v254, 0x42c00000, v252\n"
"v_mul_f32 v255, 0x42c00000, v252\n"
"v_mfma_f32_16x16x32_bf16 v[80:83], a[216:219], a[192:195], 0\n"
"ds_read_b64_tr_b16 a[248:249], v29 offset:128\n"
"v_mul_f32 v252, 0x42c00000, v252\n"
"v_mfma_f32_16x16x32_bf16 v[80:83], a[220:223], a[208:211], v[80:83]\n"
"ds_read_b64_tr_b16 a[250:251], v29 offset:1664\n"
"v_mul_f32 v39, 0xbdd53b95, v252\n"
"v_mfma_f32_16x16x32_bf16 v[84:87], a[216:219], a[196:199], 0\n"
"ds_read_b64_tr_b16 a[252:253], v29 offset:12416\n"
"v_accvgpr_read_b32 v40, a32\n"
"v_mfma_f32_16x16x32_bf16 v[84:87], a[220:223], a[212:215], v[84:87]\n"
"s_waitcnt vmcnt(17)\n"
"ds_read_b64_tr_b16 a[254:255], v29 offset:13952\n"
"v_accvgpr_read_b32 v41, a33\n"
"v_mfma_f32_16x16x32_bf16 v[88:91], a[216:219], a[200:203], 0\n"
"ds_read_b128 a[0:3], v34 offset:0\n"
"v_accvgpr_read_b32 v42, a34\n"
"v_mfma_f32_16x16x32_bf16 v[88:91], a[220:223], a[232:235], v[88:91]\n"
"ds_read_b128 a[4:7], v34 offset:2048\n"
"v_accvgpr_read_b32 v43, a35\n"
"v_mfma_f32_16x16x32_bf16 v[92:95], a[216:219], a[204:207], 0\n"
"ds_read_b128 a[8:11], v34 offset:4096\n"
"v_accvgpr_read_b32 v44, a48\n"
"v_mfma_f32_16x16x32_bf16 v[92:95], a[220:223], a[236:239], v[92:95]\n"
"s_waitcnt lgkmcnt(8)\n"
"buffer_store_dwordx4 v[48:51], v12, s[24:27], s2 offen offset:1024 sc1\n"
"v_mfma_f32_16x16x32_bf16 v[176:179], a[224:227], a[192:195], v[252:255]\n"
"ds_read_b64_tr_b16 a[216:217], v30 offset:128\n"
"v_accvgpr_read_b32 v45, a49\n"
"v_mfma_f32_16x16x32_bf16 v[176:179], a[228:231], a[208:211], v[176:179]\n"
"ds_read_b64_tr_b16 a[218:219], v30 offset:1664\n"
"v_accvgpr_read_b32 v46, a50\n"
"v_mfma_f32_16x16x32_bf16 v[180:183], a[224:227], a[196:199], v[252:255]\n"
"ds_read_b64_tr_b16 a[220:221], v30 offset:12416\n"
"v_accvgpr_read_b32 v47, a51\n"
"v_mfma_f32_16x16x32_bf16 v[180:183], a[228:231], a[212:215], v[180:183]\n"
"ds_read_b64_tr_b16 a[222:223], v30 offset:13952\n"
"v_accvgpr_read_b32 v48, a36\n"
"v_mfma_f32_16x16x32_bf16 v[184:187], a[224:227], a[200:203], v[252:255]\n"
"ds_read_b128 a[12:15], v34 offset:6144\n"
"v_sub_u32 v34, v34, s59\n"
"v_mfma_f32_16x16x32_bf16 v[184:187], a[228:231], a[232:235], v[184:187]\n"
"ds_read_b128 a[16:19], v35 offset:0\n"
"v_accvgpr_read_b32 v49, a37\n"
"v_mfma_f32_16x16x32_bf16 v[188:191], a[224:227], a[204:207], v[252:255]\n"
"ds_read_b128 a[20:23], v35 offset:2048\n"
"v_accvgpr_read_b32 v50, a38\n"
"v_mfma_f32_16x16x32_bf16 v[188:191], a[228:231], a[236:239], v[188:191]\n"
"s_waitcnt lgkmcnt(8)\n"
"v_accvgpr_read_b32 v51, a39\n"
"v_accvgpr_read_b32 v52, a52\n"
"v_mfma_f32_16x16x32_bf16 v[96:99], a[240:243], a[192:195], v[252:255]\n"
"ds_read_b64_tr_b16 a[224:225], v31 offset:128\n"
"v_accvgpr_read_b32 v53, a53\n"
"v_mfma_f32_16x16x32_bf16 v[96:99], a[244:247], a[208:211], v[96:99]\n"
"ds_read_b64_tr_b16 a[226:227], v31 offset:1664\n"
"v_accvgpr_read_b32 v54, a54\n"
"v_mfma_f32_16x16x32_bf16 v[100:103], a[240:243], a[196:199], v[252:255]\n"
"ds_read_b64_tr_b16 a[228:229], v31 offset:12416\n"
"v_accvgpr_read_b32 v55, a55\n"
"v_mfma_f32_16x16x32_bf16 v[100:103], a[244:247], a[212:215], v[100:103]\n"
"ds_read_b64_tr_b16 a[230:231], v31 offset:13952\n"
"v_fma_f32 v56, v16, v56, -v16\n"
"v_mfma_f32_16x16x32_bf16 v[104:107], a[240:243], a[200:203], v[252:255]\n"
"ds_read_b128 a[24:27], v35 offset:4096\n"
"v_fma_f32 v57, v16, v57, -v16\n"
"v_mfma_f32_16x16x32_bf16 v[104:107], a[244:247], a[232:235], v[104:107]\n"
"ds_read_b128 a[28:31], v35 offset:6144\n"
"v_sub_u32 v35, v35, s59\n"
"v_mfma_f32_16x16x32_bf16 v[108:111], a[240:243], a[204:207], v[252:255]\n"
"v_fma_f32 v58, v16, v58, -v16\n"
"v_fma_f32 v59, v16, v59, -v16\n"
"v_mfma_f32_16x16x32_bf16 v[108:111], a[244:247], a[236:239], v[108:111]\n"
"s_waitcnt lgkmcnt(8)\n"
"v_fma_f32 v60, v16, v60, -v16\n"
"v_fma_f32 v61, v16, v61, -v16\n"
"v_mfma_f32_16x16x32_bf16 v[192:195], a[248:251], a[192:195], v[252:255]\n"
"ds_read_b64_tr_b16 a[240:241], v28 offset:256\n"
"v_fma_f32 v62, v16, v62, -v16\n"
"v_mfma_f32_16x16x32_bf16 v[192:195], a[252:255], a[208:211], v[192:195]\n"
"ds_read_b64_tr_b16 a[242:243], v28 offset:1792\n"
"v_fma_f32 v63, v16, v63, -v16\n"
"v_mfma_f32_16x16x32_bf16 v[196:199], a[248:251], a[196:199], v[252:255]\n"
"s_waitcnt lgkmcnt(2)\n"
"ds_read_b64_tr_b16 a[244:245], v28 offset:12544\n"
"v_cvt_pk_bf16_f32 v56, v56, v57\n"
"v_mfma_f32_16x16x32_bf16 v[196:199], a[252:255], a[212:215], v[196:199]\n"
"ds_read_b64_tr_b16 a[246:247], v28 offset:14080\n"
"v_cvt_pk_bf16_f32 v57, v58, v59\n"
"v_mfma_f32_16x16x32_bf16 v[200:203], a[248:251], a[200:203], v[252:255]\n"
"s_add_u32 m0, s58, 0\n"
"s_add_u32 s51, s68, 384\n"
"v_add_u32 v28, 0xfffe8000, v28\n"
"v_cvt_pk_bf16_f32 v58, v60, v61\n"
"v_mfma_f32_16x16x32_bf16 v[200:203], a[252:255], a[232:235], v[200:203]\n"
"buffer_load_dwordx4 v32, s[16:19], s51 offen offset:0 lds\n"
"v_mfma_f32_16x16x32_bf16 v[204:207], a[248:251], a[204:207], v[252:255]\n"
"s_add_u32 m0, s58, 0\n"
"v_cvt_pk_bf16_f32 v59, v62, v63\n"
"v_fma_f32 v18, v17, v18, -v17\n"
"v_mfma_f32_16x16x32_bf16 v[204:207], a[252:255], a[236:239], v[204:207]\n"
"s_waitcnt lgkmcnt(4)\n"
"buffer_load_dwordx4 v33, s[16:19], s51 offen offset:1024 lds\n"
"v_mfma_f32_16x16x32_bf16 v[112:115], a[216:219], a[192:195], v[252:255]\n"
"ds_read_b64_tr_b16 a[248:249], v29 offset:256\n"
"v_fma_f32 v19, v17, v19, -v17\n"
"v_mfma_f32_16x16x32_bf16 v[112:115], a[220:223], a[208:211], v[112:115]\n"
"ds_read_b64_tr_b16 a[250:251], v29 offset:1792\n"
"v_fma_f32 v20, v17, v20, -v17\n"
"v_mfma_f32_16x16x32_bf16 v[116:119], a[216:219], a[196:199], v[252:255]\n"
"ds_read_b64_tr_b16 a[252:253], v29 offset:12544\n"
"v_fma_f32 v21, v17, v21, -v17\n"
"v_mfma_f32_16x16x32_bf16 v[116:119], a[220:223], a[212:215], v[116:119]\n"
"ds_read_b64_tr_b16 a[254:255], v29 offset:14080\n"
"v_fma_f32 v22, v17, v22, -v17\n"
"v_mfma_f32_16x16x32_bf16 v[120:123], a[216:219], a[200:203], v[252:255]\n"
"s_add_u32 m0, s58, 2048\n"
"s_add_u32 s51, s69, 384\n"
"v_add_u32 v29, 0xfffe8000, v29\n"
"v_fma_f32 v23, v17, v23, -v17\n"
"v_mfma_f32_16x16x32_bf16 v[120:123], a[220:223], a[232:235], v[120:123]\n"
"buffer_load_dwordx4 v32, s[16:19], s51 offen offset:0 lds\n"
"v_mfma_f32_16x16x32_bf16 v[124:127], a[216:219], a[204:207], v[252:255]\n"
"s_add_u32 m0, s58, 2048\n"
"buffer_store_dwordx4 v[56:59], v12, s[24:27], s2 offen offset:2048 sc1\n"
"v_mfma_f32_16x16x32_bf16 v[124:127], a[220:223], a[236:239], v[124:127]\n"
"s_waitcnt lgkmcnt(8)\n"
"buffer_load_dwordx4 v33, s[16:19], s51 offen offset:1024 lds\n"
"v_mfma_f32_16x16x32_bf16 v[208:211], a[224:227], a[192:195], v[252:255]\n"
"ds_read_b64_tr_b16 a[216:217], v30 offset:256\n"
"v_fma_f32 v24, v17, v24, -v17\n"
"v_mfma_f32_16x16x32_bf16 v[208:211], a[228:231], a[208:211], v[208:211]\n"
"ds_read_b64_tr_b16 a[218:219], v30 offset:1792\n"
"v_fma_f32 v25, v17, v25, -v17\n"
"v_mfma_f32_16x16x32_bf16 v[212:215], a[224:227], a[196:199], v[252:255]\n"
"ds_read_b64_tr_b16 a[220:221], v30 offset:12544\n"
"v_cvt_pk_bf16_f32 v18, v18, v19\n"
"v_mfma_f32_16x16x32_bf16 v[212:215], a[228:231], a[212:215], v[212:215]\n"
"ds_read_b64_tr_b16 a[222:223], v30 offset:14080\n"
"v_cvt_pk_bf16_f32 v19, v20, v21\n"
"v_mfma_f32_16x16x32_bf16 v[216:219], a[224:227], a[200:203], v[252:255]\n"
"s_add_u32 m0, s58, 4096\n"
"s_add_u32 s51, s70, 384\n"
"v_add_u32 v30, 0xfffe8000, v30\n"
"v_cvt_pk_bf16_f32 v20, v22, v23\n"
"v_mfma_f32_16x16x32_bf16 v[216:219], a[228:231], a[232:235], v[216:219]\n"
"buffer_load_dwordx4 v32, s[16:19], s51 offen offset:0 lds\n"
"v_mfma_f32_16x16x32_bf16 v[220:223], a[224:227], a[204:207], v[252:255]\n"
"s_add_u32 m0, s58, 4096\n"
"v_cvt_pk_bf16_f32 v21, v24, v25\n"
"v_fma_f32 v40, v14, v40, -v14\n"
"v_mfma_f32_16x16x32_bf16 v[220:223], a[228:231], a[236:239], v[220:223]\n"
"s_waitcnt lgkmcnt(8)\n"
"buffer_load_dwordx4 v33, s[16:19], s51 offen offset:1024 lds\n"
"v_mfma_f32_16x16x32_bf16 v[128:131], a[240:243], a[192:195], v[252:255]\n"
"ds_read_b64_tr_b16 a[224:225], v31 offset:256\n"
"v_fma_f32 v41, v14, v41, -v14\n"
"v_mfma_f32_16x16x32_bf16 v[128:131], a[244:247], a[208:211], v[128:131]\n"
"ds_read_b64_tr_b16 a[226:227], v31 offset:1792\n"
"v_fma_f32 v42, v14, v42, -v14\n"
"v_mfma_f32_16x16x32_bf16 v[132:135], a[240:243], a[196:199], v[252:255]\n"
"ds_read_b64_tr_b16 a[228:229], v31 offset:12544\n"
"v_fma_f32 v43, v14, v43, -v14\n"
"v_mfma_f32_16x16x32_bf16 v[132:135], a[244:247], a[212:215], v[132:135]\n"
"ds_read_b64_tr_b16 a[230:231], v31 offset:14080\n"
"v_fma_f32 v44, v14, v44, -v14\n"
"v_mfma_f32_16x16x32_bf16 v[136:139], a[240:243], a[200:203], v[252:255]\n"
"s_add_u32 m0, s58, 6144\n"
"s_add_u32 s51, s71, 384\n"
"v_add_u32 v31, 0xfffe8000, v31\n"
"v_fma_f32 v45, v14, v45, -v14\n"
"v_mfma_f32_16x16x32_bf16 v[136:139], a[244:247], a[232:235], v[136:139]\n"
"buffer_load_dwordx4 v32, s[16:19], s51 offen offset:0 lds\n"
"v_mfma_f32_16x16x32_bf16 v[140:143], a[240:243], a[204:207], v[252:255]\n"
"s_add_u32 m0, s58, 6144\n"
"buffer_store_dwordx4 v[18:21], v12, s[24:27], s2 offen offset:3072 sc1\n"
"v_mfma_f32_16x16x32_bf16 v[140:143], a[244:247], a[236:239], v[140:143]\n"
"s_waitcnt lgkmcnt(8)\n"
"buffer_load_dwordx4 v33, s[16:19], s51 offen offset:1024 lds\n"
"v_mfma_f32_16x16x32_bf16 v[224:227], a[248:251], a[192:195], v[252:255]\n"
"v_fma_f32 v46, v14, v46, -v14\n"
"v_fma_f32 v47, v14, v47, -v14\n"
"v_mfma_f32_16x16x32_bf16 v[224:227], a[252:255], a[208:211], v[224:227]\n"
"s_waitcnt vmcnt(38)\n"
"v_cvt_pk_bf16_f32 v40, v40, v41\n"
"v_cvt_pk_bf16_f32 v41, v42, v43\n"
"v_mfma_f32_16x16x32_bf16 v[228:231], a[248:251], a[196:199], v[252:255]\n"
"s_waitcnt lgkmcnt(0)\n"
"v_cvt_pk_bf16_f32 v42, v44, v45\n"
"v_cvt_pk_bf16_f32 v43, v46, v47\n"
"v_mfma_f32_16x16x32_bf16 v[228:231], a[252:255], a[212:215], v[228:231]\n"
"s_barrier\n"
"buffer_store_dwordx4 v[40:43], v12, s[24:27], s3 offen offset:0 sc1\n"
"v_mfma_f32_16x16x32_bf16 v[232:235], a[248:251], a[200:203], v[252:255]\n"
"ds_read_b64_tr_b16 a[240:241], v28 offset:0\n"
"v_fma_f32 v48, v15, v48, -v15\n"
"v_mfma_f32_16x16x32_bf16 v[232:235], a[252:255], a[232:235], v[232:235]\n"
"ds_read_b64_tr_b16 a[242:243], v28 offset:1536\n"
"v_fma_f32 v49, v15, v49, -v15\n"
"v_mfma_f32_16x16x32_bf16 v[236:239], a[248:251], a[204:207], v[252:255]\n"
"ds_read_b64_tr_b16 a[244:245], v28 offset:12288\n"
"v_fma_f32 v50, v15, v50, -v15\n"
"v_mfma_f32_16x16x32_bf16 v[236:239], a[252:255], a[236:239], v[236:239]\n"
"s_waitcnt lgkmcnt(3)\n"
"ds_read_b64_tr_b16 a[246:247], v28 offset:13824\n"
"v_fma_f32 v51, v15, v51, -v15\n"
"v_mfma_f32_16x16x32_bf16 v[144:147], a[216:219], a[192:195], v[252:255]\n"
"ds_read_b64_tr_b16 a[248:249], v29 offset:0\n"
"v_fma_f32 v52, v15, v52, -v15\n"
"v_mfma_f32_16x16x32_bf16 v[144:147], a[220:223], a[208:211], v[144:147]\n"
"ds_read_b64_tr_b16 a[250:251], v29 offset:1536\n"
"v_fma_f32 v53, v15, v53, -v15\n"
"v_mfma_f32_16x16x32_bf16 v[148:151], a[216:219], a[196:199], v[252:255]\n"
"ds_read_b64_tr_b16 a[252:253], v29 offset:12288\n"
"v_fma_f32 v54, v15, v54, -v15\n"
"v_mfma_f32_16x16x32_bf16 v[148:151], a[220:223], a[212:215], v[148:151]\n"
"ds_read_b64_tr_b16 a[254:255], v29 offset:13824\n"
"v_fma_f32 v55, v15, v55, -v15\n"
"v_mfma_f32_16x16x32_bf16 v[152:155], a[216:219], a[200:203], v[252:255]\n"
"v_cvt_pk_bf16_f32 v48, v48, v49\n"
"v_cvt_pk_bf16_f32 v49, v50, v51\n"
"v_mfma_f32_16x16x32_bf16 v[152:155], a[220:223], a[232:235], v[152:155]\n"
"v_cvt_pk_bf16_f32 v50, v52, v53\n"
"v_cvt_pk_bf16_f32 v51, v54, v55\n"
"v_mfma_f32_16x16x32_bf16 v[156:159], a[216:219], a[204:207], v[252:255]\n"
"buffer_store_dwordx4 v[48:51], v12, s[24:27], s3 offen offset:1024 sc1\n"
"v_mfma_f32_16x16x32_bf16 v[156:159], a[220:223], a[236:239], v[156:159]\n"
"s_waitcnt lgkmcnt(8)\n"
"v_accvgpr_read_b32 v56, a64\n"
"v_accvgpr_read_b32 v57, a65\n"
"v_mfma_f32_16x16x32_bf16 v[240:243], a[224:227], a[192:195], v[252:255]\n"
"ds_read_b64_tr_b16 a[216:217], v30 offset:0\n"
"v_accvgpr_read_b32 v58, a66\n"
"v_mfma_f32_16x16x32_bf16 v[240:243], a[228:231], a[208:211], v[240:243]\n"
"ds_read_b64_tr_b16 a[218:219], v30 offset:1536\n"
"v_accvgpr_read_b32 v59, a67\n"
"v_mfma_f32_16x16x32_bf16 v[244:247], a[224:227], a[196:199], v[252:255]\n"
"ds_read_b64_tr_b16 a[220:221], v30 offset:12288\n"
"v_accvgpr_read_b32 v60, a80\n"
"v_mfma_f32_16x16x32_bf16 v[244:247], a[228:231], a[212:215], v[244:247]\n"
"s_waitcnt lgkmcnt(8)\n"
"ds_read_b64_tr_b16 a[222:223], v30 offset:13824\n"
"v_accvgpr_read_b32 v61, a81\n"
"v_mfma_f32_16x16x32_bf16 v[248:251], a[224:227], a[200:203], v[252:255]\n"
"v_accvgpr_read_b32 v62, a82\n"
"v_accvgpr_read_b32 v63, a83\n"
"v_mfma_f32_16x16x32_bf16 v[248:251], a[228:231], a[232:235], v[248:251]\n"
"v_fma_f32 v56, v14, v56, -v14\n"
"v_fma_f32 v57, v14, v57, -v14\n"
"v_mfma_f32_16x16x32_bf16 v[252:255], a[224:227], a[204:207], v[252:255]\n"
"v_fma_f32 v58, v14, v58, -v14\n"
"v_fma_f32 v59, v14, v59, -v14\n"
"v_mfma_f32_16x16x32_bf16 v[252:255], a[228:231], a[236:239], v[252:255]\n"
"s_waitcnt lgkmcnt(8)\n"
"v_fma_f32 v60, v14, v60, -v14\n"
"v_fma_f32 v61, v14, v61, -v14\n"
"v_mfma_f32_16x16x32_bf16 v[64:67], a[240:243], a[0:3], v[64:67]\n"
"ds_read_b64_tr_b16 a[224:225], v31 offset:0\n"
"v_fma_f32 v62, v14, v62, -v14\n"
"v_mfma_f32_16x16x32_bf16 v[64:67], a[244:247], a[16:19], v[64:67]\n"
"ds_read_b64_tr_b16 a[226:227], v31 offset:1536\n"
"v_fma_f32 v63, v14, v63, -v14\n"
"v_mfma_f32_16x16x32_bf16 v[68:71], a[240:243], a[4:7], v[68:71]\n"
"ds_read_b64_tr_b16 a[228:229], v31 offset:12288\n"
"v_cvt_pk_bf16_f32 v56, v56, v57\n"
"v_mfma_f32_16x16x32_bf16 v[68:71], a[244:247], a[20:23], v[68:71]\n"
"ds_read_b64_tr_b16 a[230:231], v31 offset:13824\n"
"v_cvt_pk_bf16_f32 v57, v58, v59\n"
"v_mfma_f32_16x16x32_bf16 v[72:75], a[240:243], a[8:11], v[72:75]\n"
"v_cvt_pk_bf16_f32 v58, v60, v61\n"
"v_cvt_pk_bf16_f32 v59, v62, v63\n"
"v_mfma_f32_16x16x32_bf16 v[72:75], a[244:247], a[24:27], v[72:75]\n"
"buffer_store_dwordx4 v[56:59], v12, s[24:27], s4 offen offset:0 sc1\n"
"v_mfma_f32_16x16x32_bf16 v[76:79], a[240:243], a[12:15], v[76:79]\n"
"s_add_u32 m0, s38, 16384\n"
"s_mov_b32 s50, 2359296\n"
"v_accvgpr_read_b32 v18, a68\n"
"v_accvgpr_read_b32 v19, a69\n"
"v_mfma_f32_16x16x32_bf16 v[76:79], a[244:247], a[28:31], v[76:79]\n"
"s_waitcnt lgkmcnt(8)\n"
"buffer_load_dwordx4 v36, s[20:23], s50 offen offset:0 lds\n"
"v_mfma_f32_16x16x32_bf16 v[160:163], a[248:251], a[0:3], v[160:163]\n"
"ds_read_b64_tr_b16 a[240:241], v28 offset:128\n"
"v_accvgpr_read_b32 v20, a70\n"
"v_mfma_f32_16x16x32_bf16 v[160:163], a[252:255], a[16:19], v[160:163]\n"
"ds_read_b64_tr_b16 a[242:243], v28 offset:1664\n"
"v_accvgpr_read_b32 v21, a71\n"
"v_mfma_f32_16x16x32_bf16 v[164:167], a[248:251], a[4:7], v[164:167]\n"
"ds_read_b64_tr_b16 a[244:245], v28 offset:12416\n"
"v_accvgpr_read_b32 v22, a84\n"
"v_mfma_f32_16x16x32_bf16 v[164:167], a[252:255], a[20:23], v[164:167]\n"
"s_waitcnt vmcnt(18)\n"
"ds_read_b64_tr_b16 a[246:247], v28 offset:13952\n"
"v_accvgpr_read_b32 v23, a85\n"
"v_mfma_f32_16x16x32_bf16 v[168:171], a[248:251], a[8:11], v[168:171]\n"
"ds_read_b128 a[32:35], v34 offset:0\n"
"v_accvgpr_read_b32 v24, a86\n"
"v_mfma_f32_16x16x32_bf16 v[168:171], a[252:255], a[24:27], v[168:171]\n"
"ds_read_b128 a[36:39], v34 offset:2048\n"
"v_accvgpr_read_b32 v25, a87\n"
"v_mfma_f32_16x16x32_bf16 v[172:175], a[248:251], a[12:15], v[172:175]\n"
"s_add_u32 m0, s38, 16384\n"
"ds_read_b128 a[40:43], v34 offset:4096\n"
"v_fma_f32 v18, v15, v18, -v15\n"
"v_mfma_f32_16x16x32_bf16 v[172:175], a[252:255], a[28:31], v[172:175]\n"
"s_waitcnt lgkmcnt(8)\n"
"buffer_load_dwordx4 v37, s[20:23], s50 offen offset:1024 lds\n"
"v_mfma_f32_16x16x32_bf16 v[80:83], a[216:219], a[0:3], v[80:83]\n"
"ds_read_b64_tr_b16 a[248:249], v29 offset:128\n"
"v_fma_f32 v19, v15, v19, -v15\n"
"v_mfma_f32_16x16x32_bf16 v[80:83], a[220:223], a[16:19], v[80:83]\n"
"ds_read_b64_tr_b16 a[250:251], v29 offset:1664\n"
"v_fma_f32 v20, v15, v20, -v15\n"
"v_mfma_f32_16x16x32_bf16 v[84:87], a[216:219], a[4:7], v[84:87]\n"
"ds_read_b64_tr_b16 a[252:253], v29 offset:12416\n"
"v_fma_f32 v21, v15, v21, -v15\n"
"v_mfma_f32_16x16x32_bf16 v[84:87], a[220:223], a[20:23], v[84:87]\n"
"ds_read_b64_tr_b16 a[254:255], v29 offset:13952\n"
"v_fma_f32 v22, v15, v22, -v15\n"
"v_mfma_f32_16x16x32_bf16 v[88:91], a[216:219], a[8:11], v[88:91]\n"
"ds_read_b128 a[44:47], v34 offset:6144\n"
"v_add_u32 v34, v34, s59\n"
"v_mfma_f32_16x16x32_bf16 v[88:91], a[220:223], a[24:27], v[88:91]\n"
"ds_read_b128 a[48:51], v35 offset:0\n"
"v_fma_f32 v23, v15, v23, -v15\n"
"v_mfma_f32_16x16x32_bf16 v[92:95], a[216:219], a[12:15], v[92:95]\n"
"s_add_u32 m0, s38, 16384\n"
"ds_read_b128 a[52:55], v35 offset:2048\n"
"v_fma_f32 v24, v15, v24, -v15\n"
"v_mfma_f32_16x16x32_bf16 v[92:95], a[220:223], a[28:31], v[92:95]\n"
"s_waitcnt lgkmcnt(8)\n"
"buffer_load_dwordx4 v38, s[20:23], s50 offen offset:2048 lds\n"
"v_mfma_f32_16x16x32_bf16 v[176:179], a[224:227], a[0:3], v[176:179]\n"
"ds_read_b64_tr_b16 a[216:217], v30 offset:128\n"
"v_fma_f32 v25, v15, v25, -v15\n"
"v_mfma_f32_16x16x32_bf16 v[176:179], a[228:231], a[16:19], v[176:179]\n"
"ds_read_b64_tr_b16 a[218:219], v30 offset:1664\n"
"v_cvt_pk_bf16_f32 v18, v18, v19\n"
"v_mfma_f32_16x16x32_bf16 v[180:183], a[224:227], a[4:7], v[180:183]\n"
"ds_read_b64_tr_b16 a[220:221], v30 offset:12416\n"
"v_cvt_pk_bf16_f32 v19, v20, v21\n"
"v_mfma_f32_16x16x32_bf16 v[180:183], a[228:231], a[20:23], v[180:183]\n"
"ds_read_b64_tr_b16 a[222:223], v30 offset:13952\n"
"v_cvt_pk_bf16_f32 v20, v22, v23\n"
"v_mfma_f32_16x16x32_bf16 v[184:187], a[224:227], a[8:11], v[184:187]\n"
"ds_read_b128 a[56:59], v35 offset:4096\n"
"v_cvt_pk_bf16_f32 v21, v24, v25\n"
"v_mfma_f32_16x16x32_bf16 v[184:187], a[228:231], a[24:27], v[184:187]\n"
"ds_read_b128 a[60:63], v35 offset:6144\n"
"v_add_u32 v35, v35, s59\n"
"v_mfma_f32_16x16x32_bf16 v[188:191], a[224:227], a[12:15], v[188:191]\n"
"s_add_u32 m0, s38, 28672\n"
"s_mov_b32 s50, 2752512\n"
"buffer_store_dwordx4 v[18:21], v12, s[24:27], s4 offen offset:1024 sc1\n"
"v_mfma_f32_16x16x32_bf16 v[188:191], a[228:231], a[28:31], v[188:191]\n"
"s_waitcnt lgkmcnt(8)\n"
"buffer_load_dwordx4 v36, s[20:23], s50 offen offset:0 lds\n"
"v_mfma_f32_16x16x32_bf16 v[96:99], a[240:243], a[0:3], v[96:99]\n"
"ds_read_b64_tr_b16 a[224:225], v31 offset:128\n"
"v_accvgpr_read_b32 v40, a72\n"
"v_mfma_f32_16x16x32_bf16 v[96:99], a[244:247], a[16:19], v[96:99]\n"
"ds_read_b64_tr_b16 a[226:227], v31 offset:1664\n"
"v_accvgpr_read_b32 v41, a73\n"
"v_mfma_f32_16x16x32_bf16 v[100:103], a[240:243], a[4:7], v[100:103]\n"
"ds_read_b64_tr_b16 a[228:229], v31 offset:12416\n"
"v_accvgpr_read_b32 v42, a74\n"
"v_mfma_f32_16x16x32_bf16 v[100:103], a[244:247], a[20:23], v[100:103]\n"
"s_waitcnt lgkmcnt(3)\n"
"ds_read_b64_tr_b16 a[230:231], v31 offset:13952\n"
"v_accvgpr_read_b32 v43, a75\n"
"v_mfma_f32_16x16x32_bf16 v[104:107], a[240:243], a[8:11], v[104:107]\n"
"s_add_u32 m0, s39, 0\n"
"s_add_u32 s51, s68, 512\n"
"v_accvgpr_read_b32 v44, a88\n"
"v_accvgpr_read_b32 v45, a89\n"
"v_mfma_f32_16x16x32_bf16 v[104:107], a[244:247], a[24:27], v[104:107]\n"
"buffer_load_dwordx4 v32, s[16:19], s51 offen offset:0 lds\n"
"v_mfma_f32_16x16x32_bf16 v[108:111], a[240:243], a[12:15], v[108:111]\n"
"s_add_u32 m0, s38, 28672\n"
"v_accvgpr_read_b32 v46, a90\n"
"v_accvgpr_read_b32 v47, a91\n"
"v_mfma_f32_16x16x32_bf16 v[108:111], a[244:247], a[28:31], v[108:111]\n"
"s_waitcnt lgkmcnt(4)\n"
"buffer_load_dwordx4 v37, s[20:23], s50 offen offset:1024 lds\n"
"v_mfma_f32_16x16x32_bf16 v[192:195], a[248:251], a[0:3], v[192:195]\n"
"ds_read_b64_tr_b16 a[240:241], v28 offset:256\n"
"v_fma_f32 v40, v16, v40, -v16\n"
"v_mfma_f32_16x16x32_bf16 v[192:195], a[252:255], a[16:19], v[192:195]\n"
"ds_read_b64_tr_b16 a[242:243], v28 offset:1792\n"
"v_fma_f32 v41, v16, v41, -v16\n"
"v_mfma_f32_16x16x32_bf16 v[196:199], a[248:251], a[4:7], v[196:199]\n"
"ds_read_b64_tr_b16 a[244:245], v28 offset:12544\n"
"v_fma_f32 v42, v16, v42, -v16\n"
"v_mfma_f32_16x16x32_bf16 v[196:199], a[252:255], a[20:23], v[196:199]\n"
"ds_read_b64_tr_b16 a[246:247], v28 offset:14080\n"
"v_fma_f32 v43, v16, v43, -v16\n"
"v_mfma_f32_16x16x32_bf16 v[200:203], a[248:251], a[8:11], v[200:203]\n"
"s_add_u32 m0, s39, 0\n"
"v_add_u32 v28, 0x6000, v28\n"
"v_fma_f32 v44, v16, v44, -v16\n"
"v_mfma_f32_16x16x32_bf16 v[200:203], a[252:255], a[24:27], v[200:203]\n"
"buffer_load_dwordx4 v33, s[16:19], s51 offen offset:1024 lds\n"
"v_mfma_f32_16x16x32_bf16 v[204:207], a[248:251], a[12:15], v[204:207]\n"
"s_add_u32 m0, s38, 28672\n"
"v_fma_f32 v45, v16, v45, -v16\n"
"v_fma_f32 v46, v16, v46, -v16\n"
"v_mfma_f32_16x16x32_bf16 v[204:207], a[252:255], a[28:31], v[204:207]\n"
"s_waitcnt lgkmcnt(8)\n"
"buffer_load_dwordx4 v38, s[20:23], s50 offen offset:2048 lds\n"
"v_mfma_f32_16x16x32_bf16 v[112:115], a[216:219], a[0:3], v[112:115]\n"
"ds_read_b64_tr_b16 a[248:249], v29 offset:256\n"
"v_fma_f32 v47, v16, v47, -v16\n"
"v_mfma_f32_16x16x32_bf16 v[112:115], a[220:223], a[16:19], v[112:115]\n"
"ds_read_b64_tr_b16 a[250:251], v29 offset:1792\n"
"v_cvt_pk_bf16_f32 v40, v40, v41\n"
"v_mfma_f32_16x16x32_bf16 v[116:119], a[216:219], a[4:7], v[116:119]\n"
"ds_read_b64_tr_b16 a[252:253], v29 offset:12544\n"
"v_cvt_pk_bf16_f32 v41, v42, v43\n"
"v_mfma_f32_16x16x32_bf16 v[116:119], a[220:223], a[20:23], v[116:119]\n"
"ds_read_b64_tr_b16 a[254:255], v29 offset:14080\n"
"v_cvt_pk_bf16_f32 v42, v44, v45\n"
"v_mfma_f32_16x16x32_bf16 v[120:123], a[216:219], a[8:11], v[120:123]\n"
"s_add_u32 m0, s39, 2048\n"
"s_add_u32 s51, s69, 512\n"
"v_add_u32 v29, 0x6000, v29\n"
"v_cvt_pk_bf16_f32 v43, v46, v47\n"
"v_mfma_f32_16x16x32_bf16 v[120:123], a[220:223], a[24:27], v[120:123]\n"
"buffer_load_dwordx4 v32, s[16:19], s51 offen offset:0 lds\n"
"v_mfma_f32_16x16x32_bf16 v[124:127], a[216:219], a[12:15], v[124:127]\n"
"s_add_u32 m0, s39, 2048\n"
"buffer_store_dwordx4 v[40:43], v12, s[24:27], s4 offen offset:2048 sc1\n"
"v_mfma_f32_16x16x32_bf16 v[124:127], a[220:223], a[28:31], v[124:127]\n"
"s_waitcnt lgkmcnt(8)\n"
"buffer_load_dwordx4 v33, s[16:19], s51 offen offset:1024 lds\n"
"v_mfma_f32_16x16x32_bf16 v[208:211], a[224:227], a[0:3], v[208:211]\n"
"ds_read_b64_tr_b16 a[216:217], v30 offset:256\n"
"v_accvgpr_read_b32 v48, a76\n"
"v_mfma_f32_16x16x32_bf16 v[208:211], a[228:231], a[16:19], v[208:211]\n"
"ds_read_b64_tr_b16 a[218:219], v30 offset:1792\n"
"v_accvgpr_read_b32 v49, a77\n"
"v_mfma_f32_16x16x32_bf16 v[212:215], a[224:227], a[4:7], v[212:215]\n"
"ds_read_b64_tr_b16 a[220:221], v30 offset:12544\n"
"v_accvgpr_read_b32 v50, a78\n"
"v_mfma_f32_16x16x32_bf16 v[212:215], a[228:231], a[20:23], v[212:215]\n"
"ds_read_b64_tr_b16 a[222:223], v30 offset:14080\n"
"v_accvgpr_read_b32 v51, a79\n"
"v_mfma_f32_16x16x32_bf16 v[216:219], a[224:227], a[8:11], v[216:219]\n"
"s_add_u32 m0, s39, 4096\n"
"s_add_u32 s51, s70, 512\n"
"v_add_u32 v30, 0x6000, v30\n"
"v_accvgpr_read_b32 v52, a92\n"
"v_mfma_f32_16x16x32_bf16 v[216:219], a[228:231], a[24:27], v[216:219]\n"
"buffer_load_dwordx4 v32, s[16:19], s51 offen offset:0 lds\n"
"v_mfma_f32_16x16x32_bf16 v[220:223], a[224:227], a[12:15], v[220:223]\n"
"s_add_u32 m0, s39, 4096\n"
"v_accvgpr_read_b32 v53, a93\n"
"v_accvgpr_read_b32 v54, a94\n"
"v_mfma_f32_16x16x32_bf16 v[220:223], a[228:231], a[28:31], v[220:223]\n"
"s_waitcnt lgkmcnt(8)\n"
"buffer_load_dwordx4 v33, s[16:19], s51 offen offset:1024 lds\n"
"v_mfma_f32_16x16x32_bf16 v[128:131], a[240:243], a[0:3], v[128:131]\n"
"ds_read_b64_tr_b16 a[224:225], v31 offset:256\n"
"v_accvgpr_read_b32 v55, a95\n"
"v_mfma_f32_16x16x32_bf16 v[128:131], a[244:247], a[16:19], v[128:131]\n"
"ds_read_b64_tr_b16 a[226:227], v31 offset:1792\n"
"v_fma_f32 v48, v17, v48, -v17\n"
"v_mfma_f32_16x16x32_bf16 v[132:135], a[240:243], a[4:7], v[132:135]\n"
"ds_read_b64_tr_b16 a[228:229], v31 offset:12544\n"
"v_fma_f32 v49, v17, v49, -v17\n"
"v_mfma_f32_16x16x32_bf16 v[132:135], a[244:247], a[20:23], v[132:135]\n"
"ds_read_b64_tr_b16 a[230:231], v31 offset:14080\n"
"v_fma_f32 v50, v17, v50, -v17\n"
"v_mfma_f32_16x16x32_bf16 v[136:139], a[240:243], a[8:11], v[136:139]\n"
"s_add_u32 m0, s39, 6144\n"
"s_add_u32 s51, s71, 512\n"
"v_add_u32 v31, 0x6000, v31\n"
"v_fma_f32 v51, v17, v51, -v17\n"
"v_mfma_f32_16x16x32_bf16 v[136:139], a[244:247], a[24:27], v[136:139]\n"
"buffer_load_dwordx4 v32, s[16:19], s51 offen offset:0 lds\n"
"v_mfma_f32_16x16x32_bf16 v[140:143], a[240:243], a[12:15], v[140:143]\n"
"s_add_u32 m0, s39, 6144\n"
"v_fma_f32 v52, v17, v52, -v17\n"
"v_fma_f32 v53, v17, v53, -v17\n"
"v_mfma_f32_16x16x32_bf16 v[140:143], a[244:247], a[28:31], v[140:143]\n"
"s_waitcnt lgkmcnt(8)\n"
"buffer_load_dwordx4 v33, s[16:19], s51 offen offset:1024 lds\n"
"v_mfma_f32_16x16x32_bf16 v[224:227], a[248:251], a[0:3], v[224:227]\n"
"v_fma_f32 v54, v17, v54, -v17\n"
"v_fma_f32 v55, v17, v55, -v17\n"
"v_mfma_f32_16x16x32_bf16 v[224:227], a[252:255], a[16:19], v[224:227]\n"
"s_waitcnt vmcnt(41)\n"
"v_cvt_pk_bf16_f32 v48, v48, v49\n"
"v_cvt_pk_bf16_f32 v49, v50, v51\n"
"v_mfma_f32_16x16x32_bf16 v[228:231], a[248:251], a[4:7], v[228:231]\n"
"s_waitcnt lgkmcnt(0)\n"
"L_pg13:\n"
"s_cbranch_execz L_bk3\n"
"v_cvt_pk_bf16_f32 v50, v52, v53\n"
"v_cvt_pk_bf16_f32 v51, v54, v55\n"
"v_mfma_f32_16x16x32_bf16 v[228:231], a[252:255], a[20:23], v[228:231]\n"
"s_barrier\n"
"buffer_store_dwordx4 v[48:51], v12, s[24:27], s4 offen offset:3072 sc1\n"
"v_mfma_f32_16x16x32_bf16 v[232:235], a[248:251], a[8:11], v[232:235]\n"
"ds_read_b64_tr_b16 a[240:241], v28 offset:0\n"
"v_accvgpr_read_b32 v56, a96\n"
"v_mfma_f32_16x16x32_bf16 v[232:235], a[252:255], a[24:27], v[232:235]\n"
"ds_read_b64_tr_b16 a[242:243], v28 offset:1536\n"
"v_accvgpr_read_b32 v57, a97\n"
"v_mfma_f32_16x16x32_bf16 v[236:239], a[248:251], a[12:15], v[236:239]\n"
"ds_read_b64_tr_b16 a[244:245], v28 offset:12288\n"
"v_accvgpr_read_b32 v58, a98\n"
"v_mfma_f32_16x16x32_bf16 v[236:239], a[252:255], a[28:31], v[236:239]\n"
"s_waitcnt lgkmcnt(3)\n"
"ds_read_b64_tr_b16 a[246:247], v28 offset:13824\n"
"v_accvgpr_read_b32 v59, a99\n"
"v_mfma_f32_16x16x32_bf16 v[144:147], a[216:219], a[0:3], v[144:147]\n"
"ds_read_b64_tr_b16 a[248:249], v29 offset:0\n"
"v_accvgpr_read_b32 v60, a112\n"
"v_mfma_f32_16x16x32_bf16 v[144:147], a[220:223], a[16:19], v[144:147]\n"
"ds_read_b64_tr_b16 a[250:251], v29 offset:1536\n"
"v_accvgpr_read_b32 v61, a113\n"
"v_mfma_f32_16x16x32_bf16 v[148:151], a[216:219], a[4:7], v[148:151]\n"
"ds_read_b64_tr_b16 a[252:253], v29 offset:12288\n"
"v_accvgpr_read_b32 v62, a114\n"
"v_mfma_f32_16x16x32_bf16 v[148:151], a[220:223], a[20:23], v[148:151]\n"
"s_waitcnt vmcnt(20)\n"
"ds_read_b64_tr_b16 a[254:255], v29 offset:13824\n"
"v_accvgpr_read_b32 v63, a115\n"
"v_mfma_f32_16x16x32_bf16 v[152:155], a[216:219], a[8:11], v[152:155]\n"
"ds_read_b128 a[192:195], v34 offset:0\n"
"v_fma_f32 v56, v14, v56, -v14\n"
"v_mfma_f32_16x16x32_bf16 v[152:155], a[220:223], a[24:27], v[152:155]\n"
"ds_read_b128 a[196:199], v34 offset:2048\n"
"v_fma_f32 v57, v14, v57, -v14\n"
"v_mfma_f32_16x16x32_bf16 v[156:159], a[216:219], a[12:15], v[156:159]\n"
"ds_read_b128 a[200:203], v34 offset:4096\n"
"v_fma_f32 v58, v14, v58, -v14\n"
"v_mfma_f32_16x16x32_bf16 v[156:159], a[220:223], a[28:31], v[156:159]\n"
"s_waitcnt lgkmcnt(8)\n"
"ds_read_b128 a[204:207], v34 offset:6144\n"
"v_sub_u32 v34, v34, s59\n"
"v_mfma_f32_16x16x32_bf16 v[240:243], a[224:227], a[0:3], v[240:243]\n"
"ds_read_b64_tr_b16 a[216:217], v30 offset:0\n"
"v_fma_f32 v59, v14, v59, -v14\n"
"v_mfma_f32_16x16x32_bf16 v[240:243], a[228:231], a[16:19], v[240:243]\n"
"ds_read_b64_tr_b16 a[218:219], v30 offset:1536\n"
"v_fma_f32 v60, v14, v60, -v14\n"
"v_mfma_f32_16x16x32_bf16 v[244:247], a[224:227], a[4:7], v[244:247]\n"
"ds_read_b64_tr_b16 a[220:221], v30 offset:12288\n"
"v_fma_f32 v61, v14, v61, -v14\n"
"v_mfma_f32_16x16x32_bf16 v[244:247], a[228:231], a[20:23], v[244:247]\n"
"s_waitcnt lgkmcnt(8)\n"
"ds_read_b64_tr_b16 a[222:223], v30 offset:13824\n"
"v_fma_f32 v62, v14, v62, -v14\n"
"v_mfma_f32_16x16x32_bf16 v[248:251], a[224:227], a[8:11], v[248:251]\n"
"ds_read_b128 a[208:211], v35 offset:0\n"
"v_fma_f32 v63, v14, v63, -v14\n"
"v_mfma_f32_16x16x32_bf16 v[248:251], a[228:231], a[24:27], v[248:251]\n"
"ds_read_b128 a[212:215], v35 offset:2048\n"
"v_cvt_pk_bf16_f32 v56, v56, v57\n"
"v_mfma_f32_16x16x32_bf16 v[252:255], a[224:227], a[12:15], v[252:255]\n"
"ds_read_b128 a[232:235], v35 offset:4096\n"
"v_cvt_pk_bf16_f32 v57, v58, v59\n"
"v_mfma_f32_16x16x32_bf16 v[252:255], a[228:231], a[28:31], v[252:255]\n"
"s_waitcnt lgkmcnt(8)\n"
"ds_read_b128 a[236:239], v35 offset:6144\n"
"v_sub_u32 v35, v35, s59\n"
"v_mfma_f32_16x16x32_bf16 v[64:67], a[240:243], a[32:35], v[64:67]\n"
"ds_read_b64_tr_b16 a[224:225], v31 offset:0\n"
"v_cvt_pk_bf16_f32 v58, v60, v61\n"
"v_mfma_f32_16x16x32_bf16 v[64:67], a[244:247], a[48:51], v[64:67]\n"
"ds_read_b64_tr_b16 a[226:227], v31 offset:1536\n"
"v_cvt_pk_bf16_f32 v59, v62, v63\n"
"v_mfma_f32_16x16x32_bf16 v[68:71], a[240:243], a[36:39], v[68:71]\n"
"ds_read_b64_tr_b16 a[228:229], v31 offset:12288\n"
"v_accvgpr_read_b32 v18, a100\n"
"v_mfma_f32_16x16x32_bf16 v[68:71], a[244:247], a[52:55], v[68:71]\n"
"s_waitcnt lgkmcnt(3)\n"
"ds_read_b64_tr_b16 a[230:231], v31 offset:13824\n"
"v_accvgpr_read_b32 v19, a101\n"
"v_mfma_f32_16x16x32_bf16 v[72:75], a[240:243], a[40:43], v[72:75]\n"
"s_add_u32 m0, s58, 0\n"
"s_add_u32 s51, s68, 640\n"
"buffer_store_dwordx4 v[56:59], v12, s[24:27], s5 offen offset:0 sc1\n"
"v_mfma_f32_16x16x32_bf16 v[72:75], a[244:247], a[56:59], v[72:75]\n"
"buffer_load_dwordx4 v32, s[16:19], s51 offen offset:0 lds\n"
"v_mfma_f32_16x16x32_bf16 v[76:79], a[240:243], a[44:47], v[76:79]\n"
"s_add_u32 m0, s38, 139264\n"
"s_mov_b32 s50, 3145728\n"
"v_accvgpr_read_b32 v20, a102\n"
"v_accvgpr_read_b32 v21, a103\n"
"v_mfma_f32_16x16x32_bf16 v[76:79], a[244:247], a[60:63], v[76:79]\n"
"s_waitcnt lgkmcnt(4)\n"
"buffer_load_dwordx4 v36, s[20:23], s50 offen offset:0 lds\n"
"v_mfma_f32_16x16x32_bf16 v[160:163], a[248:251], a[32:35], v[160:163]\n"
"ds_read_b64_tr_b16 a[240:241], v28 offset:128\n"
"v_accvgpr_read_b32 v22, a116\n"
"v_mfma_f32_16x16x32_bf16 v[160:163], a[252:255], a[48:51], v[160:163]\n"
"ds_read_b64_tr_b16 a[242:243], v28 offset:1664\n"
"v_accvgpr_read_b32 v23, a117\n"
"v_mfma_f32_16x16x32_bf16 v[164:167], a[248:251], a[36:39], v[164:167]\n"
"ds_read_b64_tr_b16 a[244:245], v28 offset:12416\n"
"v_accvgpr_read_b32 v24, a118\n"
"v_mfma_f32_16x16x32_bf16 v[164:167], a[252:255], a[52:55], v[164:167]\n"
"ds_read_b64_tr_b16 a[246:247], v28 offset:13952\n"
"v_accvgpr_read_b32 v25, a119\n"
"v_mfma_f32_16x16x32_bf16 v[168:171], a[248:251], a[40:43], v[168:171]\n"
"s_add_u32 m0, s58, 0\n"
"v_fma_f32 v18, v15, v18, -v15\n"
"v_fma_f32 v19, v15, v19, -v15\n"
"v_mfma_f32_16x16x32_bf16 v[168:171], a[252:255], a[56:59], v[168:171]\n"
"buffer_load_dwordx4 v33, s[16:19], s51 offen offset:1024 lds\n"
"v_mfma_f32_16x16x32_bf16 v[172:175], a[248:251], a[44:47], v[172:175]\n"
"s_add_u32 m0, s38, 139264\n"
"v_fma_f32 v20, v15, v20, -v15\n"
"v_fma_f32 v21, v15, v21, -v15\n"
"v_mfma_f32_16x16x32_bf16 v[172:175], a[252:255], a[60:63], v[172:175]\n"
"s_waitcnt lgkmcnt(8)\n"
"buffer_load_dwordx4 v37, s[20:23], s50 offen offset:1024 lds\n"
"v_mfma_f32_16x16x32_bf16 v[80:83], a[216:219], a[32:35], v[80:83]\n"
"ds_read_b64_tr_b16 a[248:249], v29 offset:128\n"
"v_fma_f32 v22, v15, v22, -v15\n"
"v_mfma_f32_16x16x32_bf16 v[80:83], a[220:223], a[48:51], v[80:83]\n"
"ds_read_b64_tr_b16 a[250:251], v29 offset:1664\n"
"v_fma_f32 v23, v15, v23, -v15\n"
"v_mfma_f32_16x16x32_bf16 v[84:87], a[216:219], a[36:39], v[84:87]\n"
"ds_read_b64_tr_b16 a[252:253], v29 offset:12416\n"
"v_fma_f32 v24, v15, v24, -v15\n"
"v_mfma_f32_16x16x32_bf16 v[84:87], a[220:223], a[52:55], v[84:87]\n"
"ds_read_b64_tr_b16 a[254:255], v29 offset:13952\n"
"v_fma_f32 v25, v15, v25, -v15\n"
"v_mfma_f32_16x16x32_bf16 v[88:91], a[216:219], a[40:43], v[88:91]\n"
"s_add_u32 m0, s58, 2048\n"
"s_add_u32 s51, s69, 640\n"
"v_cvt_pk_bf16_f32 v18, v18, v19\n"
"v_cvt_pk_bf16_f32 v19, v20, v21\n"
"v_mfma_f32_16x16x32_bf16 v[88:91], a[220:223], a[56:59], v[88:91]\n"
"buffer_load_dwordx4 v32, s[16:19], s51 offen offset:0 lds\n"
"v_mfma_f32_16x16x32_bf16 v[92:95], a[216:219], a[44:47], v[92:95]\n"
"s_add_u32 m0, s38, 139264\n"
"v_cvt_pk_bf16_f32 v20, v22, v23\n"
"v_cvt_pk_bf16_f32 v21, v24, v25\n"
"v_mfma_f32_16x16x32_bf16 v[92:95], a[220:223], a[60:63], v[92:95]\n"
"s_waitcnt lgkmcnt(8)\n"
"buffer_load_dwordx4 v38, s[20:23], s50 offen offset:2048 lds\n"
"v_mfma_f32_16x16x32_bf16 v[176:179], a[224:227], a[32:35], v[176:179]\n"
"ds_read_b64_tr_b16 a[216:217], v30 offset:128\n"
"v_accvgpr_read_b32 v40, a104\n"
"v_mfma_f32_16x16x32_bf16 v[176:179], a[228:231], a[48:51], v[176:179]\n"
"ds_read_b64_tr_b16 a[218:219], v30 offset:1664\n"
"v_accvgpr_read_b32 v41, a105\n"
"v_mfma_f32_16x16x32_bf16 v[180:183], a[224:227], a[36:39], v[180:183]\n"
"ds_read_b64_tr_b16 a[220:221], v30 offset:12416\n"
"v_accvgpr_read_b32 v42, a106\n"
"v_mfma_f32_16x16x32_bf16 v[180:183], a[228:231], a[52:55], v[180:183]\n"
"ds_read_b64_tr_b16 a[222:223], v30 offset:13952\n"
"v_accvgpr_read_b32 v43, a107\n"
"v_mfma_f32_16x16x32_bf16 v[184:187], a[224:227], a[40:43], v[184:187]\n"
"s_add_u32 m0, s58, 2048\n"
"buffer_store_dwordx4 v[18:21], v12, s[24:27], s5 offen offset:1024 sc1\n"
"v_mfma_f32_16x16x32_bf16 v[184:187], a[228:231], a[56:59], v[184:187]\n"
"buffer_load_dwordx4 v33, s[16:19], s51 offen offset:1024 lds\n"
"v_mfma_f32_16x16x32_bf16 v[188:191], a[224:227], a[44:47], v[188:191]\n"
"s_add_u32 m0, s38, 151552\n"
"s_mov_b32 s50, 3538944\n"
"v_accvgpr_read_b32 v44, a120\n"
"v_accvgpr_read_b32 v45, a121\n"
"v_mfma_f32_16x16x32_bf16 v[188:191], a[228:231], a[60:63], v[188:191]\n"
"s_waitcnt lgkmcnt(8)\n"
"buffer_load_dwordx4 v36, s[20:23], s50 offen offset:0 lds\n"
"v_mfma_f32_16x16x32_bf16 v[96:99], a[240:243], a[32:35], v[96:99]\n"
"ds_read_b64_tr_b16 a[224:225], v31 offset:128\n"
"v_accvgpr_read_b32 v46, a122\n"
"v_mfma_f32_16x16x32_bf16 v[96:99], a[244:247], a[48:51], v[96:99]\n"
"ds_read_b64_tr_b16 a[226:227], v31 offset:1664\n"
"v_accvgpr_read_b32 v47, a123\n"
"v_mfma_f32_16x16x32_bf16 v[100:103], a[240:243], a[36:39], v[100:103]\n"
"ds_read_b64_tr_b16 a[228:229], v31 offset:12416\n"
"v_fma_f32 v40, v16, v40, -v16\n"
"v_mfma_f32_16x16x32_bf16 v[100:103], a[244:247], a[52:55], v[100:103]\n"
"ds_read_b64_tr_b16 a[230:231], v31 offset:13952\n"
"v_fma_f32 v41, v16, v41, -v16\n"
"v_mfma_f32_16x16x32_bf16 v[104:107], a[240:243], a[40:43], v[104:107]\n"
"s_add_u32 m0, s58, 4096\n"
"s_add_u32 s51, s70, 640\n"
"v_fma_f32 v42, v16, v42, -v16\n"
"v_fma_f32 v43, v16, v43, -v16\n"
"v_mfma_f32_16x16x32_bf16 v[104:107], a[244:247], a[56:59], v[104:107]\n"
"buffer_load_dwordx4 v32, s[16:19], s51 offen offset:0 lds\n"
"v_mfma_f32_16x16x32_bf16 v[108:111], a[240:243], a[44:47], v[108:111]\n"
"s_add_u32 m0, s38, 151552\n"
"v_fma_f32 v44, v16, v44, -v16\n"
"v_fma_f32 v45, v16, v45, -v16\n"
"v_mfma_f32_16x16x32_bf16 v[108:111], a[244:247], a[60:63], v[108:111]\n"
"s_waitcnt lgkmcnt(8)\n"
"buffer_load_dwordx4 v37, s[20:23], s50 offen offset:1024 lds\n"
"v_mfma_f32_16x16x32_bf16 v[192:195], a[248:251], a[32:35], v[192:195]\n"
"ds_read_b64_tr_b16 a[240:241], v28 offset:256\n"
"v_fma_f32 v46, v16, v46, -v16\n"
"v_mfma_f32_16x16x32_bf16 v[192:195], a[252:255], a[48:51], v[192:195]\n"
"ds_read_b64_tr_b16 a[242:243], v28 offset:1792\n"
"v_fma_f32 v47, v16, v47, -v16\n"
"v_mfma_f32_16x16x32_bf16 v[196:199], a[248:251], a[36:39], v[196:199]\n"
"ds_read_b64_tr_b16 a[244:245], v28 offset:12544\n"
"v_cvt_pk_bf16_f32 v40, v40, v41\n"
"v_mfma_f32_16x16x32_bf16 v[196:199], a[252:255], a[52:55], v[196:199]\n"
"ds_read_b64_tr_b16 a[246:247], v28 offset:14080\n"
"v_cvt_pk_bf16_f32 v41, v42, v43\n"
"v_mfma_f32_16x16x32_bf16 v[200:203], a[248:251], a[40:43], v[200:203]\n"
"s_add_u32 m0, s58, 4096\n"
"v_add_u32 v28, 0xffff4000, v28\n"
"v_cvt_pk_bf16_f32 v42, v44, v45\n"
"v_mfma_f32_16x16x32_bf16 v[200:203], a[252:255], a[56:59], v[200:203]\n"
"buffer_load_dwordx4 v33, s[16:19], s51 offen offset:1024 lds\n"
"v_mfma_f32_16x16x32_bf16 v[204:207], a[248:251], a[44:47], v[204:207]\n"
"s_add_u32 m0, s38, 151552\n"
"v_cvt_pk_bf16_f32 v43, v46, v47\n"
"v_accvgpr_read_b32 v48, a108\n"
"v_mfma_f32_16x16x32_bf16 v[204:207], a[252:255], a[60:63], v[204:207]\n"
"s_waitcnt lgkmcnt(8)\n"
"buffer_load_dwordx4 v38, s[20:23], s50 offen offset:2048 lds\n"
"v_mfma_f32_16x16x32_bf16 v[112:115], a[216:219], a[32:35], v[112:115]\n"
"ds_read_b64_tr_b16 a[248:249], v29 offset:256\n"
"v_accvgpr_read_b32 v49, a109\n"
"v_mfma_f32_16x16x32_bf16 v[112:115], a[220:223], a[48:51], v[112:115]\n"
"ds_read_b64_tr_b16 a[250:251], v29 offset:1792\n"
"v_accvgpr_read_b32 v50, a110\n"
"v_mfma_f32_16x16x32_bf16 v[116:119], a[216:219], a[36:39], v[116:119]\n"
"ds_read_b64_tr_b16 a[252:253], v29 offset:12544\n"
"v_accvgpr_read_b32 v51, a111\n"
"v_mfma_f32_16x16x32_bf16 v[116:119], a[220:223], a[52:55], v[116:119]\n"
"ds_read_b64_tr_b16 a[254:255], v29 offset:14080\n"
"v_accvgpr_read_b32 v52, a124\n"
"v_mfma_f32_16x16x32_bf16 v[120:123], a[216:219], a[40:43], v[120:123]\n"
"s_add_u32 m0, s58, 6144\n"
"s_add_u32 s51, s71, 640\n"
"v_add_u32 v29, 0xffff4000, v29\n"
"v_accvgpr_read_b32 v53, a125\n"
"v_mfma_f32_16x16x32_bf16 v[120:123], a[220:223], a[56:59], v[120:123]\n"
"buffer_load_dwordx4 v32, s[16:19], s51 offen offset:0 lds\n"
"v_mfma_f32_16x16x32_bf16 v[124:127], a[216:219], a[44:47], v[124:127]\n"
"buffer_store_dwordx4 v[40:43], v12, s[24:27], s5 offen offset:2048 sc1\n"
"v_mfma_f32_16x16x32_bf16 v[124:127], a[220:223], a[60:63], v[124:127]\n"
"s_waitcnt lgkmcnt(8)\n"
"v_accvgpr_read_b32 v54, a126\n"
"v_accvgpr_read_b32 v55, a127\n"
"v_mfma_f32_16x16x32_bf16 v[208:211], a[224:227], a[32:35], v[208:211]\n"
"ds_read_b64_tr_b16 a[216:217], v30 offset:256\n"
"v_fma_f32 v48, v17, v48, -v17\n"
"v_mfma_f32_16x16x32_bf16 v[208:211], a[228:231], a[48:51], v[208:211]\n"
"ds_read_b64_tr_b16 a[218:219], v30 offset:1792\n"
"v_fma_f32 v49, v17, v49, -v17\n"
"v_mfma_f32_16x16x32_bf16 v[212:215], a[224:227], a[36:39], v[212:215]\n"
"ds_read_b64_tr_b16 a[220:221], v30 offset:12544\n"
"v_fma_f32 v50, v17, v50, -v17\n"
"v_mfma_f32_16x16x32_bf16 v[212:215], a[228:231], a[52:55], v[212:215]\n"
"ds_read_b64_tr_b16 a[222:223], v30 offset:14080\n"
"v_fma_f32 v51, v17, v51, -v17\n"
"v_mfma_f32_16x16x32_bf16 v[216:219], a[224:227], a[40:43], v[216:219]\n"
"s_add_u32 m0, s58, 6144\n"
"v_add_u32 v30, 0xffff4000, v30\n"
"v_fma_f32 v52, v17, v52, -v17\n"
"v_mfma_f32_16x16x32_bf16 v[216:219], a[228:231], a[56:59], v[216:219]\n"
"buffer_load_dwordx4 v33, s[16:19], s51 offen offset:1024 lds\n"
"v_mfma_f32_16x16x32_bf16 v[220:223], a[224:227], a[44:47], v[220:223]\n"
"v_fma_f32 v53, v17, v53, -v17\n"
"v_fma_f32 v54, v17, v54, -v17\n"
"v_mfma_f32_16x16x32_bf16 v[220:223], a[228:231], a[60:63], v[220:223]\n"
"s_waitcnt lgkmcnt(8)\n"
"v_fma_f32 v55, v17, v55, -v17\n"
"v_cvt_pk_bf16_f32 v48, v48, v49\n"
"v_mfma_f32_16x16x32_bf16 v[128:131], a[240:243], a[32:35], v[128:131]\n"
"ds_read_b64_tr_b16 a[224:225], v31 offset:256\n"
"v_cvt_pk_bf16_f32 v49, v50, v51\n"
"v_mfma_f32_16x16x32_bf16 v[128:131], a[244:247], a[48:51], v[128:131]\n"
"ds_read_b64_tr_b16 a[226:227], v31 offset:1792\n"
"v_cvt_pk_bf16_f32 v50, v52, v53\n"
"v_mfma_f32_16x16x32_bf16 v[132:135], a[240:243], a[36:39], v[132:135]\n"
"ds_read_b64_tr_b16 a[228:229], v31 offset:12544\n"
"v_cvt_pk_bf16_f32 v51, v54, v55\n"
"v_mfma_f32_16x16x32_bf16 v[132:135], a[244:247], a[52:55], v[132:135]\n"
"ds_read_b64_tr_b16 a[230:231], v31 offset:14080\n"
"v_accvgpr_read_b32 v56, a128\n"
"v_mfma_f32_16x16x32_bf16 v[136:139], a[240:243], a[40:43], v[136:139]\n"
"v_add_u32 v31, 0xffff4000, v31\n"
"v_accvgpr_read_b32 v57, a129\n"
"v_mfma_f32_16x16x32_bf16 v[136:139], a[244:247], a[56:59], v[136:139]\n"
"buffer_store_dwordx4 v[48:51], v12, s[24:27], s5 offen offset:3072 sc1\n"
"v_mfma_f32_16x16x32_bf16 v[140:143], a[240:243], a[44:47], v[140:143]\n"
"v_accvgpr_read_b32 v58, a130\n"
"v_accvgpr_read_b32 v59, a131\n"
"v_mfma_f32_16x16x32_bf16 v[140:143], a[244:247], a[60:63], v[140:143]\n"
"s_waitcnt lgkmcnt(8)\n"
"v_accvgpr_read_b32 v60, a144\n"
"v_accvgpr_read_b32 v61, a145\n"
"v_mfma_f32_16x16x32_bf16 v[224:227], a[248:251], a[32:35], v[224:227]\n"
"v_accvgpr_read_b32 v62, a146\n"
"v_accvgpr_read_b32 v63, a147\n"
"v_mfma_f32_16x16x32_bf16 v[224:227], a[252:255], a[48:51], v[224:227]\n"
"s_waitcnt vmcnt(26)\n"
"v_fma_f32 v56, v14, v56, -v14\n"
"v_fma_f32 v57, v14, v57, -v14\n"
"v_mfma_f32_16x16x32_bf16 v[228:231], a[248:251], a[36:39], v[228:231]\n"
"s_waitcnt lgkmcnt(0)\n"
"v_fma_f32 v58, v14, v58, -v14\n"
"v_fma_f32 v59, v14, v59, -v14\n"
"v_mfma_f32_16x16x32_bf16 v[228:231], a[252:255], a[52:55], v[228:231]\n"
"s_barrier\n"
"v_fma_f32 v60, v14, v60, -v14\n"
"v_fma_f32 v61, v14, v61, -v14\n"
"v_mfma_f32_16x16x32_bf16 v[232:235], a[248:251], a[40:43], v[232:235]\n"
"ds_read_b64_tr_b16 a[240:241], v28 offset:0\n"
"v_fma_f32 v62, v14, v62, -v14\n"
"v_mfma_f32_16x16x32_bf16 v[232:235], a[252:255], a[56:59], v[232:235]\n"
"ds_read_b64_tr_b16 a[242:243], v28 offset:1536\n"
"v_fma_f32 v63, v14, v63, -v14\n"
"v_mfma_f32_16x16x32_bf16 v[236:239], a[248:251], a[44:47], v[236:239]\n"
"ds_read_b64_tr_b16 a[244:245], v28 offset:12288\n"
"v_cvt_pk_bf16_f32 v56, v56, v57\n"
"v_mfma_f32_16x16x32_bf16 v[236:239], a[252:255], a[60:63], v[236:239]\n"
"s_waitcnt lgkmcnt(3)\n"
"ds_read_b64_tr_b16 a[246:247], v28 offset:13824\n"
"v_cvt_pk_bf16_f32 v57, v58, v59\n"
"v_mfma_f32_16x16x32_bf16 v[144:147], a[216:219], a[32:35], v[144:147]\n"
"ds_read_b64_tr_b16 a[248:249], v29 offset:0\n"
"v_cvt_pk_bf16_f32 v58, v60, v61\n"
"v_mfma_f32_16x16x32_bf16 v[144:147], a[220:223], a[48:51], v[144:147]\n"
"ds_read_b64_tr_b16 a[250:251], v29 offset:1536\n"
"v_cvt_pk_bf16_f32 v59, v62, v63\n"
"v_mfma_f32_16x16x32_bf16 v[148:151], a[216:219], a[36:39], v[148:151]\n"
"ds_read_b64_tr_b16 a[252:253], v29 offset:12288\n"
"v_accvgpr_read_b32 v18, a132\n"
"v_mfma_f32_16x16x32_bf16 v[148:151], a[220:223], a[52:55], v[148:151]\n"
"s_waitcnt vmcnt(19)\n"
"ds_read_b64_tr_b16 a[254:255], v29 offset:13824\n"
"v_accvgpr_read_b32 v19, a133\n"
"v_mfma_f32_16x16x32_bf16 v[152:155], a[216:219], a[40:43], v[152:155]\n"
"ds_read_b128 a[0:3], v34 offset:0\n"
"v_accvgpr_read_b32 v20, a134\n"
"v_mfma_f32_16x16x32_bf16 v[152:155], a[220:223], a[56:59], v[152:155]\n"
"ds_read_b128 a[4:7], v34 offset:2048\n"
"v_accvgpr_read_b32 v21, a135\n"
"v_mfma_f32_16x16x32_bf16 v[156:159], a[216:219], a[44:47], v[156:159]\n"
"ds_read_b128 a[8:11], v34 offset:4096\n"
"v_accvgpr_read_b32 v22, a148\n"
"v_mfma_f32_16x16x32_bf16 v[156:159], a[220:223], a[60:63], v[156:159]\n"
"s_waitcnt lgkmcnt(8)\n"
"ds_read_b128 a[12:15], v34 offset:6144\n"
"v_add_u32 v34, v34, s59\n"
"v_mfma_f32_16x16x32_bf16 v[240:243], a[224:227], a[32:35], v[240:243]\n"
"ds_read_b64_tr_b16 a[216:217], v30 offset:0\n"
"v_accvgpr_read_b32 v23, a149\n"
"v_mfma_f32_16x16x32_bf16 v[240:243], a[228:231], a[48:51], v[240:243]\n"
"ds_read_b64_tr_b16 a[218:219], v30 offset:1536\n"
"v_accvgpr_read_b32 v24, a150\n"
"v_mfma_f32_16x16x32_bf16 v[244:247], a[224:227], a[36:39], v[244:247]\n"
"ds_read_b64_tr_b16 a[220:221], v30 offset:12288\n"
"v_accvgpr_read_b32 v25, a151\n"
"v_mfma_f32_16x16x32_bf16 v[244:247], a[228:231], a[52:55], v[244:247]\n"
"s_waitcnt lgkmcnt(8)\n"
"ds_read_b64_tr_b16 a[222:223], v30 offset:13824\n"
"v_fma_f32 v18, v15, v18, -v15\n"
"v_mfma_f32_16x16x32_bf16 v[248:251], a[224:227], a[40:43], v[248:251]\n"
"ds_read_b128 a[16:19], v35 offset:0\n"
"v_fma_f32 v19, v15, v19, -v15\n"
"v_mfma_f32_16x16x32_bf16 v[248:251], a[228:231], a[56:59], v[248:251]\n"
"ds_read_b128 a[20:23], v35 offset:2048\n"
"v_fma_f32 v20, v15, v20, -v15\n"
"v_mfma_f32_16x16x32_bf16 v[252:255], a[224:227], a[44:47], v[252:255]\n"
"ds_read_b128 a[24:27], v35 offset:4096\n"
"v_fma_f32 v21, v15, v21, -v15\n"
"v_mfma_f32_16x16x32_bf16 v[252:255], a[228:231], a[60:63], v[252:255]\n"
"s_waitcnt lgkmcnt(8)\n"
"ds_read_b128 a[28:31], v35 offset:6144\n"
"v_add_u32 v35, v35, s59\n"
"v_mfma_f32_16x16x32_bf16 v[64:67], a[240:243], a[192:195], v[64:67]\n"
"ds_read_b64_tr_b16 a[224:225], v31 offset:0\n"
"v_fma_f32 v22, v15, v22, -v15\n"
"v_mfma_f32_16x16x32_bf16 v[64:67], a[244:247], a[208:211], v[64:67]\n"
"ds_read_b64_tr_b16 a[226:227], v31 offset:1536\n"
"v_fma_f32 v23, v15, v23, -v15\n"
"v_mfma_f32_16x16x32_bf16 v[68:71], a[240:243], a[196:199], v[68:71]\n"
"ds_read_b64_tr_b16 a[228:229], v31 offset:12288\n"
"v_fma_f32 v24, v15, v24, -v15\n"
"v_mfma_f32_16x16x32_bf16 v[68:71], a[244:247], a[212:215], v[68:71]\n"
"s_waitcnt lgkmcnt(3)\n"
"ds_read_b64_tr_b16 a[230:231], v31 offset:13824\n"
"v_fma_f32 v25, v15, v25, -v15\n"
"v_mfma_f32_16x16x32_bf16 v[72:75], a[240:243], a[200:203], v[72:75]\n"
"s_add_u32 m0, s39, 0\n"
"s_add_u32 s51, s68, 768\n"
"buffer_store_dwordx4 v[56:59], v12, s[24:27], s6 offen offset:0 sc1\n"
"v_mfma_f32_16x16x32_bf16 v[72:75], a[244:247], a[232:235], v[72:75]\n"
"buffer_load_dwordx4 v32, s[16:19], s51 offen offset:0 lds\n"
"v_mfma_f32_16x16x32_bf16 v[76:79], a[240:243], a[204:207], v[76:79]\n"
"s_add_u32 m0, s38, 40960\n"
"s_mov_b32 s50, 3932160\n"
"v_cvt_pk_bf16_f32 v18, v18, v19\n"
"v_cvt_pk_bf16_f32 v19, v20, v21\n"
"v_mfma_f32_16x16x32_bf16 v[76:79], a[244:247], a[236:239], v[76:79]\n"
"s_waitcnt lgkmcnt(4)\n"
"buffer_load_dwordx4 v36, s[20:23], s50 offen offset:0 lds\n"
"v_mfma_f32_16x16x32_bf16 v[160:163], a[248:251], a[192:195], v[160:163]\n"
"ds_read_b64_tr_b16 a[240:241], v28 offset:128\n"
"v_cvt_pk_bf16_f32 v20, v22, v23\n"
"v_mfma_f32_16x16x32_bf16 v[160:163], a[252:255], a[208:211], v[160:163]\n"
"ds_read_b64_tr_b16 a[242:243], v28 offset:1664\n"
"v_cvt_pk_bf16_f32 v21, v24, v25\n"
"v_mfma_f32_16x16x32_bf16 v[164:167], a[248:251], a[196:199], v[164:167]\n"
"ds_read_b64_tr_b16 a[244:245], v28 offset:12416\n"
"v_accvgpr_read_b32 v40, a136\n"
"v_mfma_f32_16x16x32_bf16 v[164:167], a[252:255], a[212:215], v[164:167]\n"
"ds_read_b64_tr_b16 a[246:247], v28 offset:13952\n"
"v_accvgpr_read_b32 v41, a137\n"
"v_mfma_f32_16x16x32_bf16 v[168:171], a[248:251], a[200:203], v[168:171]\n"
"s_add_u32 m0, s39, 0\n"
"buffer_store_dwordx4 v[18:21], v12, s[24:27], s6 offen offset:1024 sc1\n"
"v_mfma_f32_16x16x32_bf16 v[168:171], a[252:255], a[232:235], v[168:171]\n"
"buffer_load_dwordx4 v33, s[16:19], s51 offen offset:1024 lds\n"
"v_mfma_f32_16x16x32_bf16 v[172:175], a[248:251], a[204:207], v[172:175]\n"
"s_add_u32 m0, s38, 40960\n"
"v_accvgpr_read_b32 v42, a138\n"
"v_accvgpr_read_b32 v43, a139\n"
"v_mfma_f32_16x16x32_bf16 v[172:175], a[252:255], a[236:239], v[172:175]\n"
"s_waitcnt lgkmcnt(8)\n"
"buffer_load_dwordx4 v37, s[20:23], s50 offen offset:1024 lds\n"
"v_mfma_f32_16x16x32_bf16 v[80:83], a[216:219], a[192:195], v[80:83]\n"
"ds_read_b64_tr_b16 a[248:249], v29 offset:128\n"
"v_accvgpr_read_b32 v44, a152\n"
"v_mfma_f32_16x16x32_bf16 v[80:83], a[220:223], a[208:211], v[80:83]\n"
"ds_read_b64_tr_b16 a[250:251], v29 offset:1664\n"
"v_accvgpr_read_b32 v45, a153\n"
"v_mfma_f32_16x16x32_bf16 v[84:87], a[216:219], a[196:199], v[84:87]\n"
"ds_read_b64_tr_b16 a[252:253], v29 offset:12416\n"
"v_accvgpr_read_b32 v46, a154\n"
"v_mfma_f32_16x16x32_bf16 v[84:87], a[220:223], a[212:215], v[84:87]\n"
"ds_read_b64_tr_b16 a[254:255], v29 offset:13952\n"
"v_accvgpr_read_b32 v47, a155\n"
"v_mfma_f32_16x16x32_bf16 v[88:91], a[216:219], a[200:203], v[88:91]\n"
"s_add_u32 m0, s39, 2048\n"
"s_add_u32 s51, s69, 768\n"
"v_fma_f32 v40, v16, v40, -v16\n"
"v_fma_f32 v41, v16, v41, -v16\n"
"v_mfma_f32_16x16x32_bf16 v[88:91], a[220:223], a[232:235], v[88:91]\n"
"buffer_load_dwordx4 v32, s[16:19], s51 offen offset:0 lds\n"
"v_mfma_f32_16x16x32_bf16 v[92:95], a[216:219], a[204:207], v[92:95]\n"
"s_add_u32 m0, s38, 40960\n"
"v_fma_f32 v42, v16, v42, -v16\n"
"v_fma_f32 v43, v16, v43, -v16\n"
"v_mfma_f32_16x16x32_bf16 v[92:95], a[220:223], a[236:239], v[92:95]\n"
"s_waitcnt lgkmcnt(8)\n"
"buffer_load_dwordx4 v38, s[20:23], s50 offen offset:2048 lds\n"
"v_mfma_f32_16x16x32_bf16 v[176:179], a[224:227], a[192:195], v[176:179]\n"
"ds_read_b64_tr_b16 a[216:217], v30 offset:128\n"
"v_fma_f32 v44, v16, v44, -v16\n"
"v_mfma_f32_16x16x32_bf16 v[176:179], a[228:231], a[208:211], v[176:179]\n"
"ds_read_b64_tr_b16 a[218:219], v30 offset:1664\n"
"v_fma_f32 v45, v16, v45, -v16\n"
"v_mfma_f32_16x16x32_bf16 v[180:183], a[224:227], a[196:199], v[180:183]\n"
"ds_read_b64_tr_b16 a[220:221], v30 offset:12416\n"
"v_fma_f32 v46, v16, v46, -v16\n"
"v_mfma_f32_16x16x32_bf16 v[180:183], a[228:231], a[212:215], v[180:183]\n"
"ds_read_b64_tr_b16 a[222:223], v30 offset:13952\n"
"v_fma_f32 v47, v16, v47, -v16\n"
"v_mfma_f32_16x16x32_bf16 v[184:187], a[224:227], a[200:203], v[184:187]\n"
"s_add_u32 m0, s39, 2048\n"
"v_cvt_pk_bf16_f32 v40, v40, v41\n"
"v_cvt_pk_bf16_f32 v41, v42, v43\n"
"v_mfma_f32_16x16x32_bf16 v[184:187], a[228:231], a[232:235], v[184:187]\n"
"buffer_load_dwordx4 v33, s[16:19], s51 offen offset:1024 lds\n"
"v_mfma_f32_16x16x32_bf16 v[188:191], a[224:227], a[204:207], v[188:191]\n"
"s_add_u32 m0, s38, 53248\n"
"s_mov_b32 s50, 4325376\n"
"v_cvt_pk_bf16_f32 v42, v44, v45\n"
"v_cvt_pk_bf16_f32 v43, v46, v47\n"
"v_mfma_f32_16x16x32_bf16 v[188:191], a[228:231], a[236:239], v[188:191]\n"
"s_waitcnt lgkmcnt(8)\n"
"buffer_load_dwordx4 v36, s[20:23], s50 offen offset:0 lds\n"
"v_mfma_f32_16x16x32_bf16 v[96:99], a[240:243], a[192:195], v[96:99]\n"
"ds_read_b64_tr_b16 a[224:225], v31 offset:128\n"
"v_accvgpr_read_b32 v48, a140\n"
"v_mfma_f32_16x16x32_bf16 v[96:99], a[244:247], a[208:211], v[96:99]\n"
"ds_read_b64_tr_b16 a[226:227], v31 offset:1664\n"
"v_accvgpr_read_b32 v49, a141\n"
"v_mfma_f32_16x16x32_bf16 v[100:103], a[240:243], a[196:199], v[100:103]\n"
"ds_read_b64_tr_b16 a[228:229], v31 offset:12416\n"
"v_accvgpr_read_b32 v50, a142\n"
"v_mfma_f32_16x16x32_bf16 v[100:103], a[244:247], a[212:215], v[100:103]\n"
"ds_read_b64_tr_b16 a[230:231], v31 offset:13952\n"
"v_accvgpr_read_b32 v51, a143\n"
"v_mfma_f32_16x16x32_bf16 v[104:107], a[240:243], a[200:203], v[104:107]\n"
"s_add_u32 m0, s39, 4096\n"
"s_add_u32 s51, s70, 768\n"
"buffer_store_dwordx4 v[40:43], v12, s[24:27], s6 offen offset:2048 sc1\n"
"v_mfma_f32_16x16x32_bf16 v[104:107], a[244:247], a[232:235], v[104:107]\n"
"buffer_load_dwordx4 v32, s[16:19], s51 offen offset:0 lds\n"
"v_mfma_f32_16x16x32_bf16 v[108:111], a[240:243], a[204:207], v[108:111]\n"
"s_add_u32 m0, s38, 53248\n"
"v_accvgpr_read_b32 v52, a156\n"
"v_accvgpr_read_b32 v53, a157\n"
"v_mfma_f32_16x16x32_bf16 v[108:111], a[244:247], a[236:239], v[108:111]\n"
"s_waitcnt lgkmcnt(8)\n"
"buffer_load_dwordx4 v37, s[20:23], s50 offen offset:1024 lds\n"
"v_mfma_f32_16x16x32_bf16 v[192:195], a[248:251], a[192:195], v[192:195]\n"
"ds_read_b64_tr_b16 a[240:241], v28 offset:256\n"
"v_accvgpr_read_b32 v54, a158\n"
"v_mfma_f32_16x16x32_bf16 v[192:195], a[252:255], a[208:211], v[192:195]\n"
"ds_read_b64_tr_b16 a[242:243], v28 offset:1792\n"
"v_accvgpr_read_b32 v55, a159\n"
"v_mfma_f32_16x16x32_bf16 v[196:199], a[248:251], a[196:199], v[196:199]\n"
"ds_read_b64_tr_b16 a[244:245], v28 offset:12544\n"
"v_fma_f32 v48, v17, v48, -v17\n"
"v_mfma_f32_16x16x32_bf16 v[196:199], a[252:255], a[212:215], v[196:199]\n"
"ds_read_b64_tr_b16 a[246:247], v28 offset:14080\n"
"v_fma_f32 v49, v17, v49, -v17\n"
"v_mfma_f32_16x16x32_bf16 v[200:203], a[248:251], a[200:203], v[200:203]\n"
"s_add_u32 m0, s39, 4096\n"
"v_add_u32 v28, 0x1e000, v28\n"
"v_fma_f32 v50, v17, v50, -v17\n"
"v_mfma_f32_16x16x32_bf16 v[200:203], a[252:255], a[232:235], v[200:203]\n"
"buffer_load_dwordx4 v33, s[16:19], s51 offen offset:1024 lds\n"
"v_mfma_f32_16x16x32_bf16 v[204:207], a[248:251], a[204:207], v[204:207]\n"
"s_add_u32 m0, s38, 53248\n"
"v_fma_f32 v51, v17, v51, -v17\n"
"v_fma_f32 v52, v17, v52, -v17\n"
"v_mfma_f32_16x16x32_bf16 v[204:207], a[252:255], a[236:239], v[204:207]\n"
"L_pg14:\n"
"s_cbranch_execz L_bk3\n"
"s_waitcnt lgkmcnt(8)\n"
"buffer_load_dwordx4 v38, s[20:23], s50 offen offset:2048 lds\n"
"v_mfma_f32_16x16x32_bf16 v[112:115], a[216:219], a[192:195], v[112:115]\n"
"ds_read_b64_tr_b16 a[248:249], v29 offset:256\n"
"v_fma_f32 v53, v17, v53, -v17\n"
"v_mfma_f32_16x16x32_bf16 v[112:115], a[220:223], a[208:211], v[112:115]\n"
"ds_read_b64_tr_b16 a[250:251], v29 offset:1792\n"
"v_fma_f32 v54, v17, v54, -v17\n"
"v_mfma_f32_16x16x32_bf16 v[116:119], a[216:219], a[196:199], v[116:119]\n"
"ds_read_b64_tr_b16 a[252:253], v29 offset:12544\n"
"v_fma_f32 v55, v17, v55, -v17\n"
"v_mfma_f32_16x16x32_bf16 v[116:119], a[220:223], a[212:215], v[116:119]\n"
"ds_read_b64_tr_b16 a[254:255], v29 offset:14080\n"
"v_cvt_pk_bf16_f32 v48, v48, v49\n"
"v_mfma_f32_16x16x32_bf16 v[120:123], a[216:219], a[200:203], v[120:123]\n"
"s_add_u32 m0, s39, 6144\n"
"s_add_u32 s51, s71, 768\n"
"v_add_u32 v29, 0x1e000, v29\n"
"v_cvt_pk_bf16_f32 v49, v50, v51\n"
"v_mfma_f32_16x16x32_bf16 v[120:123], a[220:223], a[232:235], v[120:123]\n"
"buffer_load_dwordx4 v32, s[16:19], s51 offen offset:0 lds\n"
"v_mfma_f32_16x16x32_bf16 v[124:127], a[216:219], a[204:207], v[124:127]\n"
"v_cvt_pk_bf16_f32 v50, v52, v53\n"
"v_cvt_pk_bf16_f32 v51, v54, v55\n"
"v_mfma_f32_16x16x32_bf16 v[124:127], a[220:223], a[236:239], v[124:127]\n"
"s_waitcnt lgkmcnt(8)\n"
"buffer_store_dwordx4 v[48:51], v12, s[24:27], s6 offen offset:3072 sc1\n"
"v_mfma_f32_16x16x32_bf16 v[208:211], a[224:227], a[192:195], v[208:211]\n"
"ds_read_b64_tr_b16 a[216:217], v30 offset:256\n"
"v_accvgpr_read_b32 v56, a160\n"
"v_mfma_f32_16x16x32_bf16 v[208:211], a[228:231], a[208:211], v[208:211]\n"
"ds_read_b64_tr_b16 a[218:219], v30 offset:1792\n"
"v_accvgpr_read_b32 v57, a161\n"
"v_mfma_f32_16x16x32_bf16 v[212:215], a[224:227], a[196:199], v[212:215]\n"
"ds_read_b64_tr_b16 a[220:221], v30 offset:12544\n"
"v_accvgpr_read_b32 v58, a162\n"
"v_mfma_f32_16x16x32_bf16 v[212:215], a[228:231], a[212:215], v[212:215]\n"
"ds_read_b64_tr_b16 a[222:223], v30 offset:14080\n"
"v_accvgpr_read_b32 v59, a163\n"
"v_mfma_f32_16x16x32_bf16 v[216:219], a[224:227], a[200:203], v[216:219]\n"
"s_add_u32 m0, s39, 6144\n"
"v_add_u32 v30, 0x1e000, v30\n"
"v_accvgpr_read_b32 v60, a176\n"
"v_mfma_f32_16x16x32_bf16 v[216:219], a[228:231], a[232:235], v[216:219]\n"
"buffer_load_dwordx4 v33, s[16:19], s51 offen offset:1024 lds\n"
"v_mfma_f32_16x16x32_bf16 v[220:223], a[224:227], a[204:207], v[220:223]\n"
"v_accvgpr_read_b32 v61, a177\n"
"v_accvgpr_read_b32 v62, a178\n"
"v_mfma_f32_16x16x32_bf16 v[220:223], a[228:231], a[236:239], v[220:223]\n"
"s_waitcnt lgkmcnt(8)\n"
"v_accvgpr_read_b32 v63, a179\n"
"v_fma_f32 v56, v14, v56, -v14\n"
"v_mfma_f32_16x16x32_bf16 v[128:131], a[240:243], a[192:195], v[128:131]\n"
"ds_read_b64_tr_b16 a[224:225], v31 offset:256\n"
"v_fma_f32 v57, v14, v57, -v14\n"
"v_mfma_f32_16x16x32_bf16 v[128:131], a[244:247], a[208:211], v[128:131]\n"
"ds_read_b64_tr_b16 a[226:227], v31 offset:1792\n"
"v_fma_f32 v58, v14, v58, -v14\n"
"v_mfma_f32_16x16x32_bf16 v[132:135], a[240:243], a[196:199], v[132:135]\n"
"ds_read_b64_tr_b16 a[228:229], v31 offset:12544\n"
"v_fma_f32 v59, v14, v59, -v14\n"
"v_mfma_f32_16x16x32_bf16 v[132:135], a[244:247], a[212:215], v[132:135]\n"
"ds_read_b64_tr_b16 a[230:231], v31 offset:14080\n"
"v_fma_f32 v60, v14, v60, -v14\n"
"v_mfma_f32_16x16x32_bf16 v[136:139], a[240:243], a[200:203], v[136:139]\n"
"v_add_u32 v31, 0x1e000, v31\n"
"v_fma_f32 v61, v14, v61, -v14\n"
"v_mfma_f32_16x16x32_bf16 v[136:139], a[244:247], a[232:235], v[136:139]\n"
"v_fma_f32 v62, v14, v62, -v14\n"
"v_fma_f32 v63, v14, v63, -v14\n"
"v_mfma_f32_16x16x32_bf16 v[140:143], a[240:243], a[204:207], v[140:143]\n"
"v_cvt_pk_bf16_f32 v56, v56, v57\n"
"v_cvt_pk_bf16_f32 v57, v58, v59\n"
"v_mfma_f32_16x16x32_bf16 v[140:143], a[244:247], a[236:239], v[140:143]\n"
"s_waitcnt lgkmcnt(8)\n"
"v_cvt_pk_bf16_f32 v58, v60, v61\n"
"v_cvt_pk_bf16_f32 v59, v62, v63\n"
"v_mfma_f32_16x16x32_bf16 v[224:227], a[248:251], a[192:195], v[224:227]\n"
"buffer_store_dwordx4 v[56:59], v12, s[24:27], s7 offen offset:0 sc1\n"
"v_mfma_f32_16x16x32_bf16 v[224:227], a[252:255], a[208:211], v[224:227]\n"
"s_waitcnt vmcnt(23)\n"
"v_accvgpr_read_b32 v18, a164\n"
"v_accvgpr_read_b32 v19, a165\n"
"v_mfma_f32_16x16x32_bf16 v[228:231], a[248:251], a[196:199], v[228:231]\n"
"s_waitcnt lgkmcnt(0)\n"
"v_accvgpr_read_b32 v20, a166\n"
"v_accvgpr_read_b32 v21, a167\n"
"v_mfma_f32_16x16x32_bf16 v[228:231], a[252:255], a[212:215], v[228:231]\n"
"s_barrier\n"
"v_accvgpr_read_b32 v22, a180\n"
"v_accvgpr_read_b32 v23, a181\n"
"v_mfma_f32_16x16x32_bf16 v[232:235], a[248:251], a[200:203], v[232:235]\n"
"ds_read_b64_tr_b16 a[240:241], v28 offset:0\n"
"v_accvgpr_read_b32 v24, a182\n"
"v_mfma_f32_16x16x32_bf16 v[232:235], a[252:255], a[232:235], v[232:235]\n"
"ds_read_b64_tr_b16 a[242:243], v28 offset:1536\n"
"v_accvgpr_read_b32 v25, a183\n"
"v_mfma_f32_16x16x32_bf16 v[236:239], a[248:251], a[204:207], v[236:239]\n"
"ds_read_b64_tr_b16 a[244:245], v28 offset:12288\n"
"v_fma_f32 v18, v15, v18, -v15\n"
"v_mfma_f32_16x16x32_bf16 v[236:239], a[252:255], a[236:239], v[236:239]\n"
"s_waitcnt lgkmcnt(3)\n"
"ds_read_b64_tr_b16 a[246:247], v28 offset:13824\n"
"v_fma_f32 v19, v15, v19, -v15\n"
"v_mfma_f32_16x16x32_bf16 v[144:147], a[216:219], a[192:195], v[144:147]\n"
"ds_read_b64_tr_b16 a[248:249], v29 offset:0\n"
"v_fma_f32 v20, v15, v20, -v15\n"
"v_mfma_f32_16x16x32_bf16 v[144:147], a[220:223], a[208:211], v[144:147]\n"
"ds_read_b64_tr_b16 a[250:251], v29 offset:1536\n"
"v_fma_f32 v21, v15, v21, -v15\n"
"v_mfma_f32_16x16x32_bf16 v[148:151], a[216:219], a[196:199], v[148:151]\n"
"ds_read_b64_tr_b16 a[252:253], v29 offset:12288\n"
"v_fma_f32 v22, v15, v22, -v15\n"
"v_mfma_f32_16x16x32_bf16 v[148:151], a[220:223], a[212:215], v[148:151]\n"
"s_waitcnt vmcnt(20)\n"
"ds_read_b64_tr_b16 a[254:255], v29 offset:13824\n"
"v_fma_f32 v23, v15, v23, -v15\n"
"v_mfma_f32_16x16x32_bf16 v[152:155], a[216:219], a[200:203], v[152:155]\n"
"ds_read_b128 a[32:35], v34 offset:0\n"
"v_fma_f32 v24, v15, v24, -v15\n"
"v_mfma_f32_16x16x32_bf16 v[152:155], a[220:223], a[232:235], v[152:155]\n"
"ds_read_b128 a[36:39], v34 offset:2048\n"
"v_fma_f32 v25, v15, v25, -v15\n"
"v_mfma_f32_16x16x32_bf16 v[156:159], a[216:219], a[204:207], v[156:159]\n"
"ds_read_b128 a[40:43], v34 offset:4096\n"
"v_cvt_pk_bf16_f32 v18, v18, v19\n"
"v_mfma_f32_16x16x32_bf16 v[156:159], a[220:223], a[236:239], v[156:159]\n"
"s_waitcnt lgkmcnt(8)\n"
"ds_read_b128 a[44:47], v34 offset:6144\n"
"v_sub_u32 v34, v34, s59\n"
"v_mfma_f32_16x16x32_bf16 v[240:243], a[224:227], a[192:195], v[240:243]\n"
"ds_read_b64_tr_b16 a[216:217], v30 offset:0\n"
"v_cvt_pk_bf16_f32 v19, v20, v21\n"
"v_mfma_f32_16x16x32_bf16 v[240:243], a[228:231], a[208:211], v[240:243]\n"
"ds_read_b64_tr_b16 a[218:219], v30 offset:1536\n"
"v_cvt_pk_bf16_f32 v20, v22, v23\n"
"v_mfma_f32_16x16x32_bf16 v[244:247], a[224:227], a[196:199], v[244:247]\n"
"ds_read_b64_tr_b16 a[220:221], v30 offset:12288\n"
"v_cvt_pk_bf16_f32 v21, v24, v25\n"
"v_mfma_f32_16x16x32_bf16 v[244:247], a[228:231], a[212:215], v[244:247]\n"
"s_waitcnt lgkmcnt(8)\n"
"ds_read_b64_tr_b16 a[222:223], v30 offset:13824\n"
"v_accvgpr_read_b32 v40, a168\n"
"v_mfma_f32_16x16x32_bf16 v[248:251], a[224:227], a[200:203], v[248:251]\n"
"ds_read_b128 a[48:51], v35 offset:0\n"
"v_accvgpr_read_b32 v41, a169\n"
"v_mfma_f32_16x16x32_bf16 v[248:251], a[228:231], a[232:235], v[248:251]\n"
"ds_read_b128 a[52:55], v35 offset:2048\n"
"v_accvgpr_read_b32 v42, a170\n"
"v_mfma_f32_16x16x32_bf16 v[252:255], a[224:227], a[204:207], v[252:255]\n"
"ds_read_b128 a[56:59], v35 offset:4096\n"
"v_accvgpr_read_b32 v43, a171\n"
"v_mfma_f32_16x16x32_bf16 v[252:255], a[228:231], a[236:239], v[252:255]\n"
"s_waitcnt lgkmcnt(8)\n"
"ds_read_b128 a[60:63], v35 offset:6144\n"
"v_sub_u32 v35, v35, s59\n"
"v_mfma_f32_16x16x32_bf16 v[64:67], a[240:243], a[0:3], v[64:67]\n"
"ds_read_b64_tr_b16 a[224:225], v31 offset:0\n"
"v_accvgpr_read_b32 v44, a184\n"
"v_mfma_f32_16x16x32_bf16 v[64:67], a[244:247], a[16:19], v[64:67]\n"
"ds_read_b64_tr_b16 a[226:227], v31 offset:1536\n"
"v_accvgpr_read_b32 v45, a185\n"
"v_mfma_f32_16x16x32_bf16 v[68:71], a[240:243], a[4:7], v[68:71]\n"
"s_mov_b32 s42, s88\n"
"ds_read_b64_tr_b16 a[228:229], v31 offset:12288\n"
"v_accvgpr_read_b32 v46, a186\n"
"v_mfma_f32_16x16x32_bf16 v[68:71], a[244:247], a[20:23], v[68:71]\n"
"s_waitcnt lgkmcnt(3)\n"
"ds_read_b64_tr_b16 a[230:231], v31 offset:13824\n"
"v_accvgpr_read_b32 v47, a187\n"
"v_mfma_f32_16x16x32_bf16 v[72:75], a[240:243], a[8:11], v[72:75]\n"
"s_add_u32 m0, s58, 0\n"
"s_add_u32 s51, s68, 896\n"
"buffer_store_dwordx4 v[18:21], v12, s[24:27], s7 offen offset:1024 sc1\n"
"v_mfma_f32_16x16x32_bf16 v[72:75], a[244:247], a[24:27], v[72:75]\n"
"buffer_load_dwordx4 v32, s[16:19], s51 offen offset:0 lds\n"
"v_mfma_f32_16x16x32_bf16 v[76:79], a[240:243], a[12:15], v[76:79]\n"
"s_add_u32 m0, s38, 65536\n"
"s_mov_b32 s50, 4718592\n"
"v_fma_f32 v40, v16, v40, -v16\n"
"v_fma_f32 v41, v16, v41, -v16\n"
"v_mfma_f32_16x16x32_bf16 v[76:79], a[244:247], a[28:31], v[76:79]\n"
"s_waitcnt lgkmcnt(4)\n"
"buffer_load_dwordx4 v36, s[20:23], s50 offen offset:0 lds\n"
"v_mfma_f32_16x16x32_bf16 v[160:163], a[248:251], a[0:3], v[160:163]\n"
"ds_read_b64_tr_b16 a[240:241], v28 offset:128\n"
"v_fma_f32 v42, v16, v42, -v16\n"
"v_mfma_f32_16x16x32_bf16 v[160:163], a[252:255], a[16:19], v[160:163]\n"
"ds_read_b64_tr_b16 a[242:243], v28 offset:1664\n"
"v_fma_f32 v43, v16, v43, -v16\n"
"v_mfma_f32_16x16x32_bf16 v[164:167], a[248:251], a[4:7], v[164:167]\n"
"s_mov_b32 s43, s88\n"
"ds_read_b64_tr_b16 a[244:245], v28 offset:12416\n"
"v_fma_f32 v44, v16, v44, -v16\n"
"v_mfma_f32_16x16x32_bf16 v[164:167], a[252:255], a[20:23], v[164:167]\n"
"ds_read_b64_tr_b16 a[246:247], v28 offset:13952\n"
"v_fma_f32 v45, v16, v45, -v16\n"
"v_mfma_f32_16x16x32_bf16 v[168:171], a[248:251], a[8:11], v[168:171]\n"
"s_add_u32 m0, s58, 0\n"
"v_fma_f32 v46, v16, v46, -v16\n"
"v_fma_f32 v47, v16, v47, -v16\n"
"v_mfma_f32_16x16x32_bf16 v[168:171], a[252:255], a[24:27], v[168:171]\n"
"buffer_load_dwordx4 v33, s[16:19], s51 offen offset:1024 lds\n"
"v_mfma_f32_16x16x32_bf16 v[172:175], a[248:251], a[12:15], v[172:175]\n"
"s_add_u32 m0, s38, 65536\n"
"v_cvt_pk_bf16_f32 v40, v40, v41\n"
"v_cvt_pk_bf16_f32 v41, v42, v43\n"
"v_mfma_f32_16x16x32_bf16 v[172:175], a[252:255], a[28:31], v[172:175]\n"
"s_waitcnt lgkmcnt(8)\n"
"buffer_load_dwordx4 v37, s[20:23], s50 offen offset:1024 lds\n"
"v_mfma_f32_16x16x32_bf16 v[80:83], a[216:219], a[0:3], v[80:83]\n"
"ds_read_b64_tr_b16 a[248:249], v29 offset:128\n"
"v_cvt_pk_bf16_f32 v42, v44, v45\n"
"v_mfma_f32_16x16x32_bf16 v[80:83], a[220:223], a[16:19], v[80:83]\n"
"ds_read_b64_tr_b16 a[250:251], v29 offset:1664\n"
"v_cvt_pk_bf16_f32 v43, v46, v47\n"
"v_mfma_f32_16x16x32_bf16 v[84:87], a[216:219], a[4:7], v[84:87]\n"
"s_mov_b32 s55, 0\n"
"ds_read_b64_tr_b16 a[252:253], v29 offset:12416\n"
"v_accvgpr_read_b32 v48, a172\n"
"v_mfma_f32_16x16x32_bf16 v[84:87], a[220:223], a[20:23], v[84:87]\n"
"ds_read_b64_tr_b16 a[254:255], v29 offset:13952\n"
"v_accvgpr_read_b32 v49, a173\n"
"v_mfma_f32_16x16x32_bf16 v[88:91], a[216:219], a[8:11], v[88:91]\n"
"s_add_u32 m0, s58, 2048\n"
"s_add_u32 s51, s69, 896\n"
"buffer_store_dwordx4 v[40:43], v12, s[24:27], s7 offen offset:2048 sc1\n"
"v_mfma_f32_16x16x32_bf16 v[88:91], a[220:223], a[24:27], v[88:91]\n"
"buffer_load_dwordx4 v32, s[16:19], s51 offen offset:0 lds\n"
"v_mfma_f32_16x16x32_bf16 v[92:95], a[216:219], a[12:15], v[92:95]\n"
"s_add_u32 m0, s38, 65536\n"
"v_accvgpr_read_b32 v50, a174\n"
"v_accvgpr_read_b32 v51, a175\n"
"v_mfma_f32_16x16x32_bf16 v[92:95], a[220:223], a[28:31], v[92:95]\n"
"s_waitcnt lgkmcnt(8)\n"
"buffer_load_dwordx4 v38, s[20:23], s50 offen offset:2048 lds\n"
"v_mfma_f32_16x16x32_bf16 v[176:179], a[224:227], a[0:3], v[176:179]\n"
"ds_read_b64_tr_b16 a[216:217], v30 offset:128\n"
"v_accvgpr_read_b32 v52, a188\n"
"v_mfma_f32_16x16x32_bf16 v[176:179], a[228:231], a[16:19], v[176:179]\n"
"ds_read_b64_tr_b16 a[218:219], v30 offset:1664\n"
"v_accvgpr_read_b32 v53, a189\n"
"v_mfma_f32_16x16x32_bf16 v[180:183], a[224:227], a[4:7], v[180:183]\n"
"s_add_u32 s40, s38, 90112\n"
"ds_read_b64_tr_b16 a[220:221], v30 offset:12416\n"
"v_accvgpr_read_b32 v54, a190\n"
"v_mfma_f32_16x16x32_bf16 v[180:183], a[228:231], a[20:23], v[180:183]\n"
"ds_read_b64_tr_b16 a[222:223], v30 offset:13952\n"
"v_accvgpr_read_b32 v55, a191\n"
"v_mfma_f32_16x16x32_bf16 v[184:187], a[224:227], a[8:11], v[184:187]\n"
"s_add_u32 m0, s58, 2048\n"
"v_fma_f32 v48, v17, v48, -v17\n"
"v_fma_f32 v49, v17, v49, -v17\n"
"v_mfma_f32_16x16x32_bf16 v[184:187], a[228:231], a[24:27], v[184:187]\n"
"buffer_load_dwordx4 v33, s[16:19], s51 offen offset:1024 lds\n"
"v_mfma_f32_16x16x32_bf16 v[188:191], a[224:227], a[12:15], v[188:191]\n"
"s_add_u32 m0, s38, 77824\n"
"s_mov_b32 s50, 5111808\n"
"v_fma_f32 v50, v17, v50, -v17\n"
"v_fma_f32 v51, v17, v51, -v17\n"
"v_mfma_f32_16x16x32_bf16 v[188:191], a[228:231], a[28:31], v[188:191]\n"
"s_waitcnt lgkmcnt(8)\n"
"buffer_load_dwordx4 v36, s[20:23], s50 offen offset:0 lds\n"
"v_mfma_f32_16x16x32_bf16 v[96:99], a[240:243], a[0:3], v[96:99]\n"
"ds_read_b64_tr_b16 a[224:225], v31 offset:128\n"
"v_fma_f32 v52, v17, v52, -v17\n"
"v_mfma_f32_16x16x32_bf16 v[96:99], a[244:247], a[16:19], v[96:99]\n"
"ds_read_b64_tr_b16 a[226:227], v31 offset:1664\n"
"v_fma_f32 v53, v17, v53, -v17\n"
"v_mfma_f32_16x16x32_bf16 v[100:103], a[240:243], a[4:7], v[100:103]\n"
"s_add_u32 s41, s38, 65536\n"
"ds_read_b64_tr_b16 a[228:229], v31 offset:12416\n"
"v_fma_f32 v54, v17, v54, -v17\n"
"v_mfma_f32_16x16x32_bf16 v[100:103], a[244:247], a[20:23], v[100:103]\n"
"ds_read_b64_tr_b16 a[230:231], v31 offset:13952\n"
"v_fma_f32 v55, v17, v55, -v17\n"
"v_mfma_f32_16x16x32_bf16 v[104:107], a[240:243], a[8:11], v[104:107]\n"
"s_add_u32 m0, s58, 4096\n"
"s_add_u32 s51, s70, 896\n"
"v_cvt_pk_bf16_f32 v48, v48, v49\n"
"v_cvt_pk_bf16_f32 v49, v50, v51\n"
"v_mfma_f32_16x16x32_bf16 v[104:107], a[244:247], a[24:27], v[104:107]\n"
"buffer_load_dwordx4 v32, s[16:19], s51 offen offset:0 lds\n"
"v_mfma_f32_16x16x32_bf16 v[108:111], a[240:243], a[12:15], v[108:111]\n"
"s_add_u32 m0, s38, 77824\n"
"v_cvt_pk_bf16_f32 v50, v52, v53\n"
"v_cvt_pk_bf16_f32 v51, v54, v55\n"
"v_mfma_f32_16x16x32_bf16 v[108:111], a[244:247], a[28:31], v[108:111]\n"
"s_waitcnt lgkmcnt(8)\n"
"buffer_load_dwordx4 v37, s[20:23], s50 offen offset:1024 lds\n"
"v_mfma_f32_16x16x32_bf16 v[192:195], a[248:251], a[0:3], v[192:195]\n"
"ds_read_b64_tr_b16 a[240:241], v28 offset:256\n"
"v_mfma_f32_16x16x32_bf16 v[192:195], a[252:255], a[16:19], v[192:195]\n"
"ds_read_b64_tr_b16 a[242:243], v28 offset:1792\n"
"v_mfma_f32_16x16x32_bf16 v[196:199], a[248:251], a[4:7], v[196:199]\n"
"s_mov_b32 s44, 24576\n"
"ds_read_b64_tr_b16 a[244:245], v28 offset:12544\n"
"v_mfma_f32_16x16x32_bf16 v[196:199], a[252:255], a[20:23], v[196:199]\n"
"ds_read_b64_tr_b16 a[246:247], v28 offset:14080\n"
"v_mfma_f32_16x16x32_bf16 v[200:203], a[248:251], a[8:11], v[200:203]\n"
"s_add_u32 m0, s58, 4096\n"
"v_add_u32 v28, 0xfffe8000, v28\n"
"v_mfma_f32_16x16x32_bf16 v[200:203], a[252:255], a[24:27], v[200:203]\n"
"buffer_load_dwordx4 v33, s[16:19], s51 offen offset:1024 lds\n"
"v_mfma_f32_16x16x32_bf16 v[204:207], a[248:251], a[12:15], v[204:207]\n"
"s_add_u32 m0, s38, 77824\n"
"buffer_store_dwordx4 v[48:51], v12, s[24:27], s7 offen offset:3072 sc1\n"
"v_mfma_f32_16x16x32_bf16 v[204:207], a[252:255], a[28:31], v[204:207]\n"
"s_waitcnt lgkmcnt(8)\n"
"buffer_load_dwordx4 v38, s[20:23], s50 offen offset:2048 lds\n"
"v_mfma_f32_16x16x32_bf16 v[112:115], a[216:219], a[0:3], v[112:115]\n"
"ds_read_b64_tr_b16 a[248:249], v29 offset:256\n"
"v_mfma_f32_16x16x32_bf16 v[112:115], a[220:223], a[16:19], v[112:115]\n"
"ds_read_b64_tr_b16 a[250:251], v29 offset:1792\n"
"v_mfma_f32_16x16x32_bf16 v[116:119], a[216:219], a[4:7], v[116:119]\n"
"s_mov_b32 s45, 24576\n"
"ds_read_b64_tr_b16 a[252:253], v29 offset:12544\n"
"v_mfma_f32_16x16x32_bf16 v[116:119], a[220:223], a[20:23], v[116:119]\n"
"ds_read_b64_tr_b16 a[254:255], v29 offset:14080\n"
"v_mfma_f32_16x16x32_bf16 v[120:123], a[216:219], a[8:11], v[120:123]\n"
"s_add_u32 m0, s58, 6144\n"
"s_add_u32 s51, s71, 896\n"
"v_add_u32 v29, 0xfffe8000, v29\n"
"v_mfma_f32_16x16x32_bf16 v[120:123], a[220:223], a[24:27], v[120:123]\n"
"buffer_load_dwordx4 v32, s[16:19], s51 offen offset:0 lds\n"
"v_mfma_f32_16x16x32_bf16 v[124:127], a[216:219], a[12:15], v[124:127]\n"
"v_mfma_f32_16x16x32_bf16 v[124:127], a[220:223], a[28:31], v[124:127]\n"
"s_waitcnt lgkmcnt(8)\n"
"v_mfma_f32_16x16x32_bf16 v[208:211], a[224:227], a[0:3], v[208:211]\n"
"ds_read_b64_tr_b16 a[216:217], v30 offset:256\n"
"v_mfma_f32_16x16x32_bf16 v[208:211], a[228:231], a[16:19], v[208:211]\n"
"ds_read_b64_tr_b16 a[218:219], v30 offset:1792\n"
"v_mfma_f32_16x16x32_bf16 v[212:215], a[224:227], a[4:7], v[212:215]\n"
"s_mov_b32 s46, -49152\n"
"ds_read_b64_tr_b16 a[220:221], v30 offset:12544\n"
"v_mfma_f32_16x16x32_bf16 v[212:215], a[228:231], a[20:23], v[212:215]\n"
"ds_read_b64_tr_b16 a[222:223], v30 offset:14080\n"
"v_mfma_f32_16x16x32_bf16 v[216:219], a[224:227], a[8:11], v[216:219]\n"
"s_add_u32 m0, s58, 6144\n"
"v_add_u32 v30, 0xfffe8000, v30\n"
"v_mfma_f32_16x16x32_bf16 v[216:219], a[228:231], a[24:27], v[216:219]\n"
"buffer_load_dwordx4 v33, s[16:19], s51 offen offset:1024 lds\n"
"v_mfma_f32_16x16x32_bf16 v[220:223], a[224:227], a[12:15], v[220:223]\n"
"v_mfma_f32_16x16x32_bf16 v[220:223], a[228:231], a[28:31], v[220:223]\n"
"s_waitcnt lgkmcnt(8)\n"
"v_mfma_f32_16x16x32_bf16 v[128:131], a[240:243], a[0:3], v[128:131]\n"
"ds_read_b64_tr_b16 a[224:225], v31 offset:256\n"
"v_mfma_f32_16x16x32_bf16 v[128:131], a[244:247], a[16:19], v[128:131]\n"
"ds_read_b64_tr_b16 a[226:227], v31 offset:1792\n"
"v_lshlrev_b32 v26, 3, v12\n"
"v_mfma_f32_16x16x32_bf16 v[132:135], a[240:243], a[4:7], v[132:135]\n"
"ds_read_b64_tr_b16 a[228:229], v31 offset:12544\n"
"v_mfma_f32_16x16x32_bf16 v[132:135], a[244:247], a[20:23], v[132:135]\n"
"ds_read_b64_tr_b16 a[230:231], v31 offset:14080\n"
"v_and_b32 v26, 0x780, v26\n"
"v_mfma_f32_16x16x32_bf16 v[136:139], a[240:243], a[8:11], v[136:139]\n"
"v_add_u32 v31, 0xfffe8000, v31\n"
"v_mfma_f32_16x16x32_bf16 v[136:139], a[244:247], a[24:27], v[136:139]\n"
"global_load_dword v27, v26, s[60:61]\n"
"v_mfma_f32_16x16x32_bf16 v[140:143], a[240:243], a[12:15], v[140:143]\n"
"v_mfma_f32_16x16x32_bf16 v[140:143], a[244:247], a[28:31], v[140:143]\n"
"s_waitcnt lgkmcnt(8)\n"
"v_mfma_f32_16x16x32_bf16 v[224:227], a[248:251], a[0:3], v[224:227]\n"
"v_mfma_f32_16x16x32_bf16 v[224:227], a[252:255], a[16:19], v[224:227]\n"
"s_waitcnt vmcnt(22)\n"
"v_mfma_f32_16x16x32_bf16 v[228:231], a[248:251], a[4:7], v[228:231]\n"
"s_waitcnt lgkmcnt(0)\n"
"v_mfma_f32_16x16x32_bf16 v[228:231], a[252:255], a[20:23], v[228:231]\n"
"s_barrier\n"
"v_mfma_f32_16x16x32_bf16 v[232:235], a[248:251], a[8:11], v[232:235]\n"
"ds_read_b64_tr_b16 a[240:241], v28 offset:0\n"
"v_mfma_f32_16x16x32_bf16 v[232:235], a[252:255], a[24:27], v[232:235]\n"
"ds_read_b64_tr_b16 a[242:243], v28 offset:1536\n"
"v_mfma_f32_16x16x32_bf16 v[236:239], a[248:251], a[12:15], v[236:239]\n"
"ds_read_b64_tr_b16 a[244:245], v28 offset:12288\n"
"v_mfma_f32_16x16x32_bf16 v[236:239], a[252:255], a[28:31], v[236:239]\n"
"s_waitcnt lgkmcnt(3)\n"
"ds_read_b64_tr_b16 a[246:247], v28 offset:13824\n"
"v_mfma_f32_16x16x32_bf16 v[144:147], a[216:219], a[0:3], v[144:147]\n"
"ds_read_b64_tr_b16 a[248:249], v29 offset:0\n"
"v_mfma_f32_16x16x32_bf16 v[144:147], a[220:223], a[16:19], v[144:147]\n"
"ds_read_b64_tr_b16 a[250:251], v29 offset:1536\n"
"v_mfma_f32_16x16x32_bf16 v[148:151], a[216:219], a[4:7], v[148:151]\n"
"ds_read_b64_tr_b16 a[252:253], v29 offset:12288\n"
"v_mfma_f32_16x16x32_bf16 v[148:151], a[220:223], a[20:23], v[148:151]\n"
"s_waitcnt vmcnt(19)\n"
"ds_read_b64_tr_b16 a[254:255], v29 offset:13824\n"
"v_mfma_f32_16x16x32_bf16 v[152:155], a[216:219], a[8:11], v[152:155]\n"
"ds_read_b128 a[192:195], v34 offset:0\n"
"v_mfma_f32_16x16x32_bf16 v[152:155], a[220:223], a[24:27], v[152:155]\n"
"ds_read_b128 a[196:199], v34 offset:2048\n"
"v_mfma_f32_16x16x32_bf16 v[156:159], a[216:219], a[12:15], v[156:159]\n"
"ds_read_b128 a[200:203], v34 offset:4096\n"
"v_mfma_f32_16x16x32_bf16 v[156:159], a[220:223], a[28:31], v[156:159]\n"
"s_waitcnt lgkmcnt(8)\n"
"ds_read_b128 a[204:207], v34 offset:6144\n"
"v_add_u32 v34, v34, s59\n"
"v_mfma_f32_16x16x32_bf16 v[240:243], a[224:227], a[0:3], v[240:243]\n"
"ds_read_b64_tr_b16 a[216:217], v30 offset:0\n"
"v_mfma_f32_16x16x32_bf16 v[240:243], a[228:231], a[16:19], v[240:243]\n"
"ds_read_b64_tr_b16 a[218:219], v30 offset:1536\n"
"v_mfma_f32_16x16x32_bf16 v[244:247], a[224:227], a[4:7], v[244:247]\n"
"ds_read_b64_tr_b16 a[220:221], v30 offset:12288\n"
"v_mfma_f32_16x16x32_bf16 v[244:247], a[228:231], a[20:23], v[244:247]\n"
"s_waitcnt lgkmcnt(8)\n"
"ds_read_b64_tr_b16 a[222:223], v30 offset:13824\n"
"v_mfma_f32_16x16x32_bf16 v[248:251], a[224:227], a[8:11], v[248:251]\n"
"ds_read_b128 a[208:211], v35 offset:0\n"
"v_mfma_f32_16x16x32_bf16 v[248:251], a[228:231], a[24:27], v[248:251]\n"
"ds_read_b128 a[212:215], v35 offset:2048\n"
"v_mfma_f32_16x16x32_bf16 v[252:255], a[224:227], a[12:15], v[252:255]\n"
"ds_read_b128 a[232:235], v35 offset:4096\n"
"v_mfma_f32_16x16x32_bf16 v[252:255], a[228:231], a[28:31], v[252:255]\n"
"s_waitcnt lgkmcnt(8)\n"
"ds_read_b128 a[236:239], v35 offset:6144\n"
"v_add_u32 v35, v35, s59\n"
"v_mov_b32 v22, v39\n"
"v_mov_b32 v23, v39\n"
"s_mov_b64 s[80:81], s[84:85]\n"
"s_mov_b32 s82, s86\n"
"s_mov_b32 s36, 491520\n"
"s_mov_b32 s96, 0\n"
"v_lshlrev_b32 v39, 3, v12\n"
"v_cmp_gt_u32 vcc, 3840, v39\n"
"s_nop 1\n"
"v_cndmask_b32 v39, -1, v39\n"
"s_add_u32 s2, s37, 0\n"
"s_add_u32 s3, s37, 4096\n"
"s_add_u32 s4, s37, 8192\n"
"s_add_u32 s5, s37, 12288\n"
"s_add_u32 s6, s37, 16384\n"
"s_add_u32 s7, s37, 20480\n"
"s_branch L_q4\n"
"L_lastB:\n"
"v_mfma_f32_16x16x32_bf16 v[128:131], a[192:195], v[160:163], v[48:51]\n"
"s_mov_b64 s[20:21], s[72:73]\n"
"s_mov_b32 s22, 1965120\n"
"s_mov_b32 s40, 0\n"
"v_add_f32 v14, v14, v18\n"
"v_add_f32 v15, v15, v19\n"
"v_mfma_f32_16x16x32_bf16 v[128:131], a[196:199], v[176:179], v[128:131]\n"
"s_add_u32 m0, s95, 122880\n"
"v_add_f32 v16, v16, v20\n"
"v_add_f32 v17, v17, v21\n"
"v_mfma_f32_16x16x32_bf16 v[128:131], a[200:203], v[192:195], v[128:131]\n"
"s_waitcnt lgkmcnt(0)\n"
"buffer_load_dwordx4 v30, s[20:23], s40 offen offset:0 lds\n"
"v_mfma_f32_16x16x32_bf16 v[128:131], a[204:207], v[208:211], v[128:131]\n"
"s_mul_i32 s41, s87, 245760\n"
"ds_read_b128 a[216:219], v0 offset:6144\n"
"v_add_u32 v2, s46, v2\n"
"v_mfma_f32_16x16x32_bf16 v[128:131], a[208:211], v[224:227], v[128:131]\n"
"s_add_u32 s55, s55, 18\n"
"s_add_u32 s41, s41, 983040\n"
"v_add_u32 v3, s46, v3\n"
"v_add_u32 v4, s46, v4\n"
"v_mfma_f32_16x16x32_bf16 v[128:131], a[212:215], v[240:243], v[128:131]\n"
"s_mov_b32 s29, s41\n"
"buffer_load_dwordx4 v31, s[20:23], s40 offen offset:1024 lds\n"
"v_mfma_f32_16x16x32_bf16 v[132:135], a[212:215], v[244:247], v[52:55]\n"
"s_add_u32 s28, s41, 245760\n"
"ds_read_b128 a[220:223], v1 offset:6144\n"
"v_add_u32 v5, s46, v5\n"
"v_mfma_f32_16x16x32_bf16 v[132:135], a[208:211], v[228:231], v[132:135]\n"
"s_add_u32 s42, s78, 98304\n"
"v_mfma_f32_16x16x32_bf16 v[132:135], a[204:207], v[212:215], v[132:135]\n"
"s_mov_b32 s31, s37\n"
"buffer_load_dwordx4 v32, s[20:23], s40 offen offset:2048 lds\n"
"v_mfma_f32_16x16x32_bf16 v[132:135], a[200:203], v[196:199], v[132:135]\n"
"s_add_u32 s30, s37, 24576\n"
"ds_read_b128 a[224:227], v0 offset:6272\n"
"v_mfma_f32_16x16x32_bf16 v[132:135], a[196:199], v[180:183], v[132:135]\n"
"v_exp_f32 v128, v128\n"
"v_mfma_f32_16x16x32_bf16 v[132:135], a[192:195], v[164:167], v[132:135]\n"
"buffer_load_dwordx4 v33, s[20:23], s40 offen offset:3072 lds\n"
"v_mfma_f32_16x16x32_bf16 v[136:139], a[192:195], v[168:171], v[56:59]\n"
"ds_read_b128 a[228:231], v1 offset:6272\n"
"v_mfma_f32_16x16x32_bf16 v[136:139], a[196:199], v[184:187], v[136:139]\n"
"s_add_u32 m0, s95, 126976\n"
"v_exp_f32 v129, v129\n"
"v_mfma_f32_16x16x32_bf16 v[136:139], a[200:203], v[200:203], v[136:139]\n"
"s_cmp_eq_u32 s42, s31\n"
"s_cselect_b32 s42, s30, s42\n"
"buffer_load_dwordx4 v34, s[20:23], s40 offen offset:0 lds\n"
"v_mfma_f32_16x16x32_bf16 v[136:139], a[204:207], v[216:219], v[136:139]\n"
"s_add_u32 s40, s40, 81920\n"
"ds_read_b128 a[232:235], v0 offset:6400\n"
"v_add_f32 v18, v128, v129\n"
"v_mfma_f32_16x16x32_bf16 v[136:139], a[208:211], v[232:235], v[136:139]\n"
"v_exp_f32 v130, v130\n"
"v_mfma_f32_16x16x32_bf16 v[136:139], a[212:215], v[248:251], v[136:139]\n"
"v_exp_f32 v131, v131\n"
"v_mfma_f32_16x16x32_bf16 v[140:143], a[212:215], v[252:255], v[60:63]\n"
"ds_read_b128 a[236:239], v1 offset:6400\n"
"v_cvt_pk_bf16_f32 v128, v128, v129\n"
"v_mfma_f32_16x16x32_bf16 v[140:143], a[208:211], v[236:239], v[140:143]\n"
"s_add_u32 m0, s95, 143360\n"
"v_exp_f32 v132, v132\n"
"v_mfma_f32_16x16x32_bf16 v[140:143], a[204:207], v[220:223], v[140:143]\n"
"buffer_load_dwordx4 v30, s[20:23], s41 offen offset:0 lds\n"
"v_mfma_f32_16x16x32_bf16 v[140:143], a[200:203], v[204:207], v[140:143]\n"
"v_exp_f32 v133, v133\n"
"v_mfma_f32_16x16x32_bf16 v[140:143], a[196:199], v[188:191], v[140:143]\n"
"v_add_f32 v18, v18, v130\n"
"v_add_f32 v18, v18, v131\n"
"v_mfma_f32_16x16x32_bf16 v[140:143], a[192:195], v[172:175], v[140:143]\n"
"s_waitcnt lgkmcnt(0)\n"
"buffer_load_dwordx4 v31, s[20:23], s41 offen offset:1024 lds\n"
"v_mfma_f32_16x16x32_bf16 v[64:67], a[216:219], v[160:163], v[48:51]\n"
"v_exp_f32 v134, v134\n"
"v_mfma_f32_16x16x32_bf16 v[64:67], a[220:223], v[176:179], v[64:67]\n"
"v_exp_f32 v135, v135\n"
"v_mfma_f32_16x16x32_bf16 v[64:67], a[224:227], v[192:195], v[64:67]\n"
"buffer_load_dwordx4 v32, s[20:23], s41 offen offset:2048 lds\n"
"v_mfma_f32_16x16x32_bf16 v[64:67], a[228:231], v[208:211], v[64:67]\n"
"ds_read_b128 a[192:195], v0 offset:12288\n"
"v_cvt_pk_bf16_f32 v129, v130, v131\n"
"v_mfma_f32_16x16x32_bf16 v[64:67], a[232:235], v[224:227], v[64:67]\n"
"v_add_f32 v19, v132, v133\n"
"v_cvt_pk_bf16_f32 v132, v132, v133\n"
"v_mfma_f32_16x16x32_bf16 v[64:67], a[236:239], v[240:243], v[64:67]\n"
"buffer_load_dwordx4 v33, s[20:23], s41 offen offset:3072 lds\n"
"v_mfma_f32_16x16x32_bf16 v[68:71], a[236:239], v[244:247], v[52:55]\n"
"ds_read_b128 a[196:199], v1 offset:12288\n"
"v_add_f32 v19, v19, v134\n"
"v_mfma_f32_16x16x32_bf16 v[68:71], a[232:235], v[228:231], v[68:71]\n"
"s_add_u32 m0, s95, 147456\n"
"v_exp_f32 v136, v136\n"
"v_mfma_f32_16x16x32_bf16 v[68:71], a[228:231], v[212:215], v[68:71]\n"
"buffer_load_dwordx4 v34, s[20:23], s41 offen offset:0 lds\n"
"v_mfma_f32_16x16x32_bf16 v[68:71], a[224:227], v[196:199], v[68:71]\n"
"s_add_u32 s41, s41, 81920\n"
"ds_read_b128 a[200:203], v0 offset:12416\n"
"v_add_f32 v19, v19, v135\n"
"v_mfma_f32_16x16x32_bf16 v[68:71], a[220:223], v[180:183], v[68:71]\n"
"v_exp_f32 v137, v137\n"
"v_mfma_f32_16x16x32_bf16 v[68:71], a[216:219], v[164:167], v[68:71]\n"
"v_exp_f32 v138, v138\n"
"v_mfma_f32_16x16x32_bf16 v[72:75], a[216:219], v[168:171], v[56:59]\n"
"ds_read_b128 a[204:207], v1 offset:12416\n"
"v_cvt_pk_bf16_f32 v133, v134, v135\n"
"v_mfma_f32_16x16x32_bf16 v[72:75], a[220:223], v[184:187], v[72:75]\n"
"v_exp_f32 v139, v139\n"
"v_mfma_f32_16x16x32_bf16 v[72:75], a[224:227], v[200:203], v[72:75]\n"
"v_add_f32 v20, v136, v137\n"
"v_cvt_pk_bf16_f32 v136, v136, v137\n"
"v_mfma_f32_16x16x32_bf16 v[72:75], a[228:231], v[216:219], v[72:75]\n"
"ds_read_b128 a[208:211], v0 offset:12544\n"
"v_add_f32 v20, v20, v138\n"
"v_mfma_f32_16x16x32_bf16 v[72:75], a[232:235], v[232:235], v[72:75]\n"
"v_exp_f32 v140, v140\n"
"v_mfma_f32_16x16x32_bf16 v[72:75], a[236:239], v[248:251], v[72:75]\n"
"v_exp_f32 v141, v141\n"
"v_mfma_f32_16x16x32_bf16 v[76:79], a[236:239], v[252:255], v[60:63]\n"
"ds_read_b128 a[212:215], v1 offset:12544\n"
"L_pg15:\n"
"s_cbranch_execz L_bk3\n"
"v_add_f32 v20, v20, v139\n"
"v_mfma_f32_16x16x32_bf16 v[76:79], a[232:235], v[236:239], v[76:79]\n"
"v_exp_f32 v142, v142\n"
"v_mfma_f32_16x16x32_bf16 v[76:79], a[228:231], v[220:223], v[76:79]\n"
"v_exp_f32 v143, v143\n"
"v_mfma_f32_16x16x32_bf16 v[76:79], a[224:227], v[204:207], v[76:79]\n"
"v_cvt_pk_bf16_f32 v137, v138, v139\n"
"v_add_f32 v21, v140, v141\n"
"v_mfma_f32_16x16x32_bf16 v[76:79], a[220:223], v[188:191], v[76:79]\n"
"v_exp_f32 v64, v64\n"
"v_mfma_f32_16x16x32_bf16 v[76:79], a[216:219], v[172:175], v[76:79]\n"
"s_waitcnt lgkmcnt(0)\n"
"buffer_load_dword v27, v39, s[80:83], s96 offen\n"
"v_mfma_f32_16x16x32_bf16 v[144:147], a[192:195], v[160:163], v[48:51]\n"
"s_add_u32 s96, s96, s36\n"
"v_cvt_pk_bf16_f32 v140, v140, v141\n"
"v_add_f32 v21, v21, v142\n"
"v_mfma_f32_16x16x32_bf16 v[144:147], a[196:199], v[176:179], v[144:147]\n"
"v_exp_f32 v65, v65\n"
"v_mfma_f32_16x16x32_bf16 v[144:147], a[200:203], v[192:195], v[144:147]\n"
"v_add_f32 v21, v21, v143\n"
"v_cvt_pk_bf16_f32 v141, v142, v143\n"
"v_mfma_f32_16x16x32_bf16 v[144:147], a[204:207], v[208:211], v[144:147]\n"
"ds_read_b128 a[216:219], v0 offset:18432\n"
"v_add_f32 v18, v18, v64\n"
"v_mfma_f32_16x16x32_bf16 v[144:147], a[208:211], v[224:227], v[144:147]\n"
"v_exp_f32 v66, v66\n"
"v_mfma_f32_16x16x32_bf16 v[144:147], a[212:215], v[240:243], v[144:147]\n"
"v_exp_f32 v67, v67\n"
"v_mfma_f32_16x16x32_bf16 v[148:151], a[212:215], v[244:247], v[52:55]\n"
"ds_read_b128 a[220:223], v1 offset:18432\n"
"v_add_f32 v18, v18, v65\n"
"v_mfma_f32_16x16x32_bf16 v[148:151], a[208:211], v[228:231], v[148:151]\n"
"v_exp_f32 v68, v68\n"
"v_mfma_f32_16x16x32_bf16 v[148:151], a[204:207], v[212:215], v[148:151]\n"
"v_cvt_pk_bf16_f32 v130, v64, v65\n"
"v_add_f32 v18, v18, v66\n"
"v_mfma_f32_16x16x32_bf16 v[148:151], a[200:203], v[196:199], v[148:151]\n"
"ds_read_b128 a[224:227], v0 offset:18560\n"
"v_add_f32 v18, v18, v67\n"
"v_mfma_f32_16x16x32_bf16 v[148:151], a[196:199], v[180:183], v[148:151]\n"
"v_exp_f32 v69, v69\n"
"v_mfma_f32_16x16x32_bf16 v[148:151], a[192:195], v[164:167], v[148:151]\n"
"v_exp_f32 v70, v70\n"
"v_mfma_f32_16x16x32_bf16 v[152:155], a[192:195], v[168:171], v[56:59]\n"
"ds_read_b128 a[228:231], v1 offset:18560\n"
"v_cvt_pk_bf16_f32 v131, v66, v67\n"
"v_mfma_f32_16x16x32_bf16 v[152:155], a[196:199], v[184:187], v[152:155]\n"
"v_exp_f32 v71, v71\n"
"v_mfma_f32_16x16x32_bf16 v[152:155], a[200:203], v[200:203], v[152:155]\n"
"v_add_f32 v19, v19, v68\n"
"v_add_f32 v19, v19, v69\n"
"v_mfma_f32_16x16x32_bf16 v[152:155], a[204:207], v[216:219], v[152:155]\n"
"ds_read_b128 a[232:235], v0 offset:18688\n"
"v_cvt_pk_bf16_f32 v134, v68, v69\n"
"v_mfma_f32_16x16x32_bf16 v[152:155], a[208:211], v[232:235], v[152:155]\n"
"v_exp_f32 v72, v72\n"
"v_mfma_f32_16x16x32_bf16 v[152:155], a[212:215], v[248:251], v[152:155]\n"
"v_exp_f32 v73, v73\n"
"v_mfma_f32_16x16x32_bf16 v[156:159], a[212:215], v[252:255], v[60:63]\n"
"ds_read_b128 a[236:239], v1 offset:18688\n"
"v_add_f32 v19, v19, v70\n"
"v_mfma_f32_16x16x32_bf16 v[156:159], a[208:211], v[236:239], v[156:159]\n"
"v_add_f32 v19, v19, v71\n"
"v_cvt_pk_bf16_f32 v135, v70, v71\n"
"v_mfma_f32_16x16x32_bf16 v[156:159], a[204:207], v[220:223], v[156:159]\n"
"v_exp_f32 v74, v74\n"
"v_mfma_f32_16x16x32_bf16 v[156:159], a[200:203], v[204:207], v[156:159]\n"
"v_exp_f32 v75, v75\n"
"v_mfma_f32_16x16x32_bf16 v[156:159], a[196:199], v[188:191], v[156:159]\n"
"v_add_u32 v0, 0x1e000, v28\n"
"v_add_f32 v20, v20, v72\n"
"v_mfma_f32_16x16x32_bf16 v[156:159], a[192:195], v[172:175], v[156:159]\n"
"s_waitcnt lgkmcnt(0)\n"
"v_add_f32 v20, v20, v73\n"
"v_cvt_pk_bf16_f32 v138, v72, v73\n"
"v_mfma_f32_16x16x32_bf16 v[80:83], a[216:219], v[160:163], v[48:51]\n"
"v_add_u32 v1, 0x1e000, v29\n"
"v_add_f32 v20, v20, v74\n"
"v_mfma_f32_16x16x32_bf16 v[80:83], a[220:223], v[176:179], v[80:83]\n"
"v_exp_f32 v76, v76\n"
"v_mfma_f32_16x16x32_bf16 v[80:83], a[224:227], v[192:195], v[80:83]\n"
"v_exp_f32 v77, v77\n"
"v_mfma_f32_16x16x32_bf16 v[80:83], a[228:231], v[208:211], v[80:83]\n"
"v_add_f32 v20, v20, v75\n"
"v_cvt_pk_bf16_f32 v139, v74, v75\n"
"v_mfma_f32_16x16x32_bf16 v[80:83], a[232:235], v[224:227], v[80:83]\n"
"v_exp_f32 v78, v78\n"
"v_mfma_f32_16x16x32_bf16 v[80:83], a[236:239], v[240:243], v[80:83]\n"
"v_exp_f32 v79, v79\n"
"v_mfma_f32_16x16x32_bf16 v[84:87], a[236:239], v[244:247], v[52:55]\n"
"s_waitcnt vmcnt(20)\n"
"v_add_f32 v21, v21, v76\n"
"v_add_f32 v21, v21, v77\n"
"v_mfma_f32_16x16x32_bf16 v[84:87], a[232:235], v[228:231], v[84:87]\n"
"s_barrier\n"
"v_exp_f32 v144, v144\n"
"v_mfma_f32_16x16x32_bf16 v[84:87], a[228:231], v[212:215], v[84:87]\n"
"v_cvt_pk_bf16_f32 v142, v76, v77\n"
"v_add_f32 v21, v21, v78\n"
"v_mfma_f32_16x16x32_bf16 v[84:87], a[224:227], v[196:199], v[84:87]\n"
"ds_read_b64_tr_b16 a[240:241], v2 offset:0\n"
"v_add_f32 v21, v21, v79\n"
"v_mfma_f32_16x16x32_bf16 v[84:87], a[220:223], v[180:183], v[84:87]\n"
"v_exp_f32 v145, v145\n"
"v_mfma_f32_16x16x32_bf16 v[84:87], a[216:219], v[164:167], v[84:87]\n"
"ds_read_b64_tr_b16 a[242:243], v2 offset:6144\n"
"v_cvt_pk_bf16_f32 v143, v78, v79\n"
"v_mfma_f32_16x16x32_bf16 v[88:91], a[216:219], v[168:171], v[56:59]\n"
"s_waitcnt vmcnt(11)\n"
"v_exp_f32 v146, v146\n"
"v_mfma_f32_16x16x32_bf16 v[88:91], a[220:223], v[184:187], v[88:91]\n"
"ds_read_b64_tr_b16 a[244:245], v2 offset:12288\n"
"v_cmp_eq_u64 vcc, s[76:77], v[46:47]\n"
"v_mfma_f32_16x16x32_bf16 v[88:91], a[224:227], v[200:203], v[88:91]\n"
"v_exp_f32 v147, v147\n"
"v_mfma_f32_16x16x32_bf16 v[88:91], a[228:231], v[216:219], v[88:91]\n"
"ds_read_b64_tr_b16 a[246:247], v2 offset:18432\n"
"v_add_f32 v18, v18, v144\n"
"v_mfma_f32_16x16x32_bf16 v[88:91], a[232:235], v[232:235], v[88:91]\n"
"s_cmp_eq_u64 vcc, -1\n"
"s_getpc_b64 s[52:53]\n"
"s_cbranch_scc0 L_poll\n"
"v_add_f32 v18, v18, v145\n"
"v_cvt_pk_bf16_f32 v144, v144, v145\n"
"v_mfma_f32_16x16x32_bf16 v[88:91], a[236:239], v[248:251], v[88:91]\n"
"ds_read_b64_tr_b16 a[248:249], v3 offset:0\n"
"v_add_f32 v18, v18, v146\n"
"v_mfma_f32_16x16x32_bf16 v[92:95], a[236:239], v[252:255], v[60:63]\n"
"s_xor_b32 s90, s90, 64\n"
"s_xor_b32 s89, s89, 64\n"
"v_exp_f32 v148, v148\n"
"v_mfma_f32_16x16x32_bf16 v[92:95], a[232:235], v[236:239], v[92:95]\n"
"s_lshl_b32 s98, s93, 7\n"
"s_and_b32 s98, s98, 128\n"
"ds_read_b64_tr_b16 a[250:251], v3 offset:6144\n"
"v_add_f32 v18, v18, v147\n"
"v_mfma_f32_16x16x32_bf16 v[92:95], a[228:231], v[220:223], v[92:95]\n"
"s_or_b32 s90, s90, s98\n"
"s_mov_b32 s98, 48\n"
"v_exp_f32 v149, v149\n"
"v_mfma_f32_16x16x32_bf16 v[92:95], a[224:227], v[204:207], v[92:95]\n"
"ds_read_b64_tr_b16 a[252:253], v3 offset:12288\n"
"v_cvt_pk_bf16_f32 v145, v146, v147\n"
"v_mfma_f32_16x16x32_bf16 v[92:95], a[220:223], v[188:191], v[92:95]\n"
"buffer_load_dword v27, v39, s[80:83], s96 offen\n"
"v_mfma_f32_16x16x32_bf16 v[92:95], a[216:219], v[172:175], v[92:95]\n"
"s_waitcnt lgkmcnt(3)\n"
"s_add_u32 s96, s96, s36\n"
"ds_read_b64_tr_b16 a[254:255], v3 offset:18432\n"
"v_add_f32 v19, v19, v148\n"
"v_mfma_f32_16x16x32_bf16 a[0:3], a[240:243], v[96:99], a[0:3]\n"
"v_exp_f32 v150, v150\n"
"v_mfma_f32_16x16x32_bf16 a[0:3], a[244:247], v[112:115], a[0:3]\n"
"ds_read_b64_tr_b16 a[216:217], v4 offset:0\n"
"v_add_f32 v19, v19, v149\n"
"v_mfma_f32_16x16x32_bf16 a[4:7], a[240:243], v[100:103], a[4:7]\n"
"buffer_load_dwordx4 a[192:195], v12, s[24:27], s78 offen offset:0 sc1\n"
"v_mfma_f32_16x16x32_bf16 a[4:7], a[244:247], v[116:119], a[4:7]\n"
"ds_read_b64_tr_b16 a[218:219], v4 offset:6144\n"
"v_cvt_pk_bf16_f32 v148, v148, v149\n"
"v_mfma_f32_16x16x32_bf16 a[8:11], a[240:243], v[104:107], a[8:11]\n"
"buffer_load_dwordx4 a[196:199], v12, s[24:27], s78 offen offset:1024 sc1\n"
"v_mfma_f32_16x16x32_bf16 a[8:11], a[244:247], v[120:123], a[8:11]\n"
"ds_read_b64_tr_b16 a[220:221], v4 offset:12288\n"
"v_add_f32 v19, v19, v150\n"
"v_mfma_f32_16x16x32_bf16 a[12:15], a[240:243], v[108:111], a[12:15]\n"
"buffer_load_dwordx4 a[200:203], v12, s[24:27], s78 offen offset:2048 sc1\n"
"v_mfma_f32_16x16x32_bf16 a[12:15], a[244:247], v[124:127], a[12:15]\n"
"s_waitcnt lgkmcnt(3)\n"
"ds_read_b64_tr_b16 a[222:223], v4 offset:18432\n"
"v_mfma_f32_16x16x32_bf16 a[16:19], a[248:251], v[96:99], a[16:19]\n"
"buffer_load_dwordx4 a[204:207], v12, s[24:27], s78 offen offset:3072 sc1\n"
"s_add_u32 s78, s78, 4096\n"
"v_mfma_f32_16x16x32_bf16 a[16:19], a[252:255], v[112:115], a[16:19]\n"
"ds_read_b64_tr_b16 a[224:225], v5 offset:0\n"
"v_mfma_f32_16x16x32_bf16 a[20:23], a[248:251], v[100:103], a[20:23]\n"
"buffer_load_dwordx4 a[208:211], v12, s[24:27], s78 offen offset:0 sc1\n"
"v_mfma_f32_16x16x32_bf16 a[20:23], a[252:255], v[116:119], a[20:23]\n"
"ds_read_b64_tr_b16 a[226:227], v5 offset:6144\n"
"v_mfma_f32_16x16x32_bf16 a[24:27], a[248:251], v[104:107], a[24:27]\n"
"buffer_load_dwordx4 a[212:215], v12, s[24:27], s78 offen offset:1024 sc1\n"
"v_mfma_f32_16x16x32_bf16 a[24:27], a[252:255], v[120:123], a[24:27]\n"
"ds_read_b64_tr_b16 a[228:229], v5 offset:12288\n"
"v_mfma_f32_16x16x32_bf16 a[28:31], a[248:251], v[108:111], a[28:31]\n"
"buffer_load_dwordx4 a[232:235], v12, s[24:27], s78 offen offset:2048 sc1\n"
"v_mfma_f32_16x16x32_bf16 a[28:31], a[252:255], v[124:127], a[28:31]\n"
"s_waitcnt lgkmcnt(3)\n"
"ds_read_b64_tr_b16 a[230:231], v5 offset:18432\n"
"v_mfma_f32_16x16x32_bf16 a[32:35], a[216:219], v[96:99], a[32:35]\n"
"buffer_load_dwordx4 a[236:239], v12, s[24:27], s78 offen offset:3072 sc1\n"
"s_add_u32 s78, s78, 4096\n"
"v_mfma_f32_16x16x32_bf16 a[32:35], a[220:223], v[112:115], a[32:35]\n"
"ds_read_b64_tr_b16 a[240:241], v2 offset:128\n"
"v_mfma_f32_16x16x32_bf16 a[36:39], a[216:219], v[100:103], a[36:39]\n"
"v_exp_f32 v151, v151\n"
"v_mfma_f32_16x16x32_bf16 a[36:39], a[220:223], v[116:119], a[36:39]\n"
"s_add_u32 m0, s95, 40960\n"
"ds_read_b64_tr_b16 a[242:243], v2 offset:6272\n"
"v_add_f32 v19, v19, v151\n"
"v_mfma_f32_16x16x32_bf16 a[40:43], a[216:219], v[104:107], a[40:43]\n"
"buffer_load_dwordx4 v30, s[20:23], s40 offen offset:0 lds\n"
"v_mfma_f32_16x16x32_bf16 a[40:43], a[220:223], v[120:123], a[40:43]\n"
"ds_read_b64_tr_b16 a[244:245], v2 offset:12416\n"
"v_cvt_pk_bf16_f32 v149, v150, v151\n"
"v_mfma_f32_16x16x32_bf16 a[44:47], a[216:219], v[108:111], a[44:47]\n"
"v_exp_f32 v152, v152\n"
"v_mfma_f32_16x16x32_bf16 a[44:47], a[220:223], v[124:127], a[44:47]\n"
"s_waitcnt lgkmcnt(3)\n"
"ds_read_b64_tr_b16 a[246:247], v2 offset:18560\n"
"v_add_f32 v20, v20, v152\n"
"v_mfma_f32_16x16x32_bf16 a[48:51], a[224:227], v[96:99], a[48:51]\n"
"buffer_load_dwordx4 v31, s[20:23], s40 offen offset:1024 lds\n"
"v_mfma_f32_16x16x32_bf16 a[48:51], a[228:231], v[112:115], a[48:51]\n"
"ds_read_b64_tr_b16 a[248:249], v3 offset:128\n"
"v_mfma_f32_16x16x32_bf16 a[52:55], a[224:227], v[100:103], a[52:55]\n"
"v_exp_f32 v153, v153\n"
"v_mfma_f32_16x16x32_bf16 a[52:55], a[228:231], v[116:119], a[52:55]\n"
"ds_read_b64_tr_b16 a[250:251], v3 offset:6272\n"
"v_add_f32 v20, v20, v153\n"
"v_mfma_f32_16x16x32_bf16 a[56:59], a[224:227], v[104:107], a[56:59]\n"
"buffer_load_dwordx4 v32, s[20:23], s40 offen offset:2048 lds\n"
"v_mfma_f32_16x16x32_bf16 a[56:59], a[228:231], v[120:123], a[56:59]\n"
"ds_read_b64_tr_b16 a[252:253], v3 offset:12416\n"
"v_cvt_pk_bf16_f32 v152, v152, v153\n"
"v_mfma_f32_16x16x32_bf16 a[60:63], a[224:227], v[108:111], a[60:63]\n"
"v_exp_f32 v154, v154\n"
"v_mfma_f32_16x16x32_bf16 a[60:63], a[228:231], v[124:127], a[60:63]\n"
"s_waitcnt lgkmcnt(3)\n"
"ds_read_b64_tr_b16 a[254:255], v3 offset:18560\n"
"v_add_f32 v20, v20, v154\n"
"v_mfma_f32_16x16x32_bf16 a[64:67], a[240:243], v[96:99], a[64:67]\n"
"buffer_load_dwordx4 v33, s[20:23], s40 offen offset:3072 lds\n"
"s_mov_b64 exec, 0\n"
"s_cmp_eq_u32 s38, 0\n"
"s_cbranch_scc1 L_pg16\n"
"s_cmp_eq_u32 s38, 3072\n"
"s_cbranch_scc1 L_pg17\n"
"s_cmp_eq_u32 s38, 6144\n"
"s_cbranch_scc1 L_pg18\n"
"s_branch L_pg19\n"
"L_bk4:\n"
"s_mov_b64 exec, -1\n"
"v_mfma_f32_16x16x32_bf16 a[64:67], a[244:247], v[112:115], a[64:67]\n"
"ds_read_b64_tr_b16 a[216:217], v4 offset:128\n"
"v_mfma_f32_16x16x32_bf16 a[68:71], a[240:243], v[100:103], a[68:71]\n"
"v_exp_f32 v155, v155\n"
"v_mfma_f32_16x16x32_bf16 a[68:71], a[244:247], v[116:119], a[68:71]\n"
"s_add_u32 m0, s95, 45056\n"
"ds_read_b64_tr_b16 a[218:219], v4 offset:6272\n"
"v_add_f32 v20, v20, v155\n"
"v_mfma_f32_16x16x32_bf16 a[72:75], a[240:243], v[104:107], a[72:75]\n"
"buffer_load_dwordx4 v34, s[20:23], s40 offen offset:0 lds\n"
"v_mfma_f32_16x16x32_bf16 a[72:75], a[244:247], v[120:123], a[72:75]\n"
"s_add_u32 s40, s40, 81920\n"
"ds_read_b64_tr_b16 a[220:221], v4 offset:12416\n"
"v_cvt_pk_bf16_f32 v153, v154, v155\n"
"v_mfma_f32_16x16x32_bf16 a[76:79], a[240:243], v[108:111], a[76:79]\n"
"v_exp_f32 v156, v156\n"
"v_mfma_f32_16x16x32_bf16 a[76:79], a[244:247], v[124:127], a[76:79]\n"
"s_waitcnt lgkmcnt(3)\n"
"s_add_u32 m0, s95, 102400\n"
"ds_read_b64_tr_b16 a[222:223], v4 offset:18560\n"
"v_add_f32 v21, v21, v156\n"
"v_mfma_f32_16x16x32_bf16 a[80:83], a[248:251], v[96:99], a[80:83]\n"
"buffer_load_dwordx4 v30, s[20:23], s41 offen offset:0 lds\n"
"v_mfma_f32_16x16x32_bf16 a[80:83], a[252:255], v[112:115], a[80:83]\n"
"ds_read_b64_tr_b16 a[224:225], v5 offset:128\n"
"v_mfma_f32_16x16x32_bf16 a[84:87], a[248:251], v[100:103], a[84:87]\n"
"v_exp_f32 v157, v157\n"
"v_mfma_f32_16x16x32_bf16 a[84:87], a[252:255], v[116:119], a[84:87]\n"
"ds_read_b64_tr_b16 a[226:227], v5 offset:6272\n"
"v_add_f32 v21, v21, v157\n"
"v_mfma_f32_16x16x32_bf16 a[88:91], a[248:251], v[104:107], a[88:91]\n"
"buffer_load_dwordx4 v31, s[20:23], s41 offen offset:1024 lds\n"
"v_mfma_f32_16x16x32_bf16 a[88:91], a[252:255], v[120:123], a[88:91]\n"
"ds_read_b64_tr_b16 a[228:229], v5 offset:12416\n"
"v_cvt_pk_bf16_f32 v156, v156, v157\n"
"v_mfma_f32_16x16x32_bf16 a[92:95], a[248:251], v[108:111], a[92:95]\n"
"v_exp_f32 v158, v158\n"
"v_mfma_f32_16x16x32_bf16 a[92:95], a[252:255], v[124:127], a[92:95]\n"
"s_waitcnt lgkmcnt(3)\n"
"ds_read_b64_tr_b16 a[230:231], v5 offset:18560\n"
"v_add_f32 v21, v21, v158\n"
"v_mfma_f32_16x16x32_bf16 a[96:99], a[216:219], v[96:99], a[96:99]\n"
"buffer_load_dwordx4 v32, s[20:23], s41 offen offset:2048 lds\n"
"v_mfma_f32_16x16x32_bf16 a[96:99], a[220:223], v[112:115], a[96:99]\n"
"ds_read_b64_tr_b16 a[240:241], v2 offset:256\n"
"v_mfma_f32_16x16x32_bf16 a[100:103], a[216:219], v[100:103], a[100:103]\n"
"v_exp_f32 v159, v159\n"
"v_mfma_f32_16x16x32_bf16 a[100:103], a[220:223], v[116:119], a[100:103]\n"
"ds_read_b64_tr_b16 a[242:243], v2 offset:6400\n"
"v_add_f32 v21, v21, v159\n"
"v_mfma_f32_16x16x32_bf16 a[104:107], a[216:219], v[104:107], a[104:107]\n"
"buffer_load_dwordx4 v33, s[20:23], s41 offen offset:3072 lds\n"
"v_mfma_f32_16x16x32_bf16 a[104:107], a[220:223], v[120:123], a[104:107]\n"
"ds_read_b64_tr_b16 a[244:245], v2 offset:12544\n"
"v_cvt_pk_bf16_f32 v157, v158, v159\n"
"v_mfma_f32_16x16x32_bf16 a[108:111], a[216:219], v[108:111], a[108:111]\n"
"v_exp_f32 v80, v80\n"
"v_mfma_f32_16x16x32_bf16 a[108:111], a[220:223], v[124:127], a[108:111]\n"
"s_waitcnt lgkmcnt(3)\n"
"s_add_u32 m0, s95, 106496\n"
"ds_read_b64_tr_b16 a[246:247], v2 offset:18688\n"
"v_add_f32 v18, v18, v80\n"
"v_mfma_f32_16x16x32_bf16 a[112:115], a[224:227], v[96:99], a[112:115]\n"
"buffer_load_dwordx4 v34, s[20:23], s41 offen offset:0 lds\n"
"v_mfma_f32_16x16x32_bf16 a[112:115], a[228:231], v[112:115], a[112:115]\n"
"s_add_u32 s41, s41, 81920\n"
"ds_read_b64_tr_b16 a[248:249], v3 offset:256\n"
"v_mfma_f32_16x16x32_bf16 a[116:119], a[224:227], v[100:103], a[116:119]\n"
"v_exp_f32 v81, v81\n"
"v_mfma_f32_16x16x32_bf16 a[116:119], a[228:231], v[116:119], a[116:119]\n"
"ds_read_b64_tr_b16 a[250:251], v3 offset:6400\n"
"v_add_f32 v18, v18, v81\n"
"v_mfma_f32_16x16x32_bf16 a[120:123], a[224:227], v[104:107], a[120:123]\n"
"v_exp_f32 v82, v82\n"
"v_mfma_f32_16x16x32_bf16 a[120:123], a[228:231], v[120:123], a[120:123]\n"
"ds_read_b64_tr_b16 a[252:253], v3 offset:12544\n"
"v_cvt_pk_bf16_f32 v146, v80, v81\n"
"v_mfma_f32_16x16x32_bf16 a[124:127], a[224:227], v[108:111], a[124:127]\n"
"v_exp_f32 v83, v83\n"
"v_mfma_f32_16x16x32_bf16 a[124:127], a[228:231], v[124:127], a[124:127]\n"
"s_waitcnt lgkmcnt(3)\n"
"ds_read_b64_tr_b16 a[254:255], v3 offset:18688\n"
"v_add_f32 v18, v18, v82\n"
"v_mfma_f32_16x16x32_bf16 a[128:131], a[240:243], v[96:99], a[128:131]\n"
"buffer_load_dword v27, v39, s[80:83], s96 offen\n"
"v_mfma_f32_16x16x32_bf16 a[128:131], a[244:247], v[112:115], a[128:131]\n"
"s_add_u32 s96, s96, s36\n"
"ds_read_b64_tr_b16 a[216:217], v4 offset:256\n"
"v_add_f32 v18, v18, v83\n"
"v_mfma_f32_16x16x32_bf16 a[132:135], a[240:243], v[100:103], a[132:135]\n"
"v_exp_f32 v84, v84\n"
"v_mfma_f32_16x16x32_bf16 a[132:135], a[244:247], v[116:119], a[132:135]\n"
"ds_read_b64_tr_b16 a[218:219], v4 offset:6400\n"
"v_cvt_pk_bf16_f32 v147, v82, v83\n"
"v_mfma_f32_16x16x32_bf16 a[136:139], a[240:243], v[104:107], a[136:139]\n"
"v_exp_f32 v85, v85\n"
"v_mfma_f32_16x16x32_bf16 a[136:139], a[244:247], v[120:123], a[136:139]\n"
"ds_read_b64_tr_b16 a[220:221], v4 offset:12544\n"
"v_add_f32 v19, v19, v84\n"
"v_mfma_f32_16x16x32_bf16 a[140:143], a[240:243], v[108:111], a[140:143]\n"
"v_exp_f32 v86, v86\n"
"v_mfma_f32_16x16x32_bf16 a[140:143], a[244:247], v[124:127], a[140:143]\n"
"s_waitcnt lgkmcnt(3)\n"
"ds_read_b64_tr_b16 a[222:223], v4 offset:18688\n"
"v_add_f32 v19, v19, v85\n"
"v_mfma_f32_16x16x32_bf16 a[144:147], a[248:251], v[96:99], a[144:147]\n"
"v_exp_f32 v87, v87\n"
"v_mfma_f32_16x16x32_bf16 a[144:147], a[252:255], v[112:115], a[144:147]\n"
"ds_read_b64_tr_b16 a[224:225], v5 offset:256\n"
"v_cvt_pk_bf16_f32 v150, v84, v85\n"
"v_mfma_f32_16x16x32_bf16 a[148:151], a[248:251], v[100:103], a[148:151]\n"
"v_exp_f32 v88, v88\n"
"v_mfma_f32_16x16x32_bf16 a[148:151], a[252:255], v[116:119], a[148:151]\n"
"ds_read_b64_tr_b16 a[226:227], v5 offset:6400\n"
"v_add_f32 v19, v19, v86\n"
"v_mfma_f32_16x16x32_bf16 a[152:155], a[248:251], v[104:107], a[152:155]\n"
"v_exp_f32 v89, v89\n"
"v_mfma_f32_16x16x32_bf16 a[152:155], a[252:255], v[120:123], a[152:155]\n"
"ds_read_b64_tr_b16 a[228:229], v5 offset:12544\n"
"v_add_f32 v19, v19, v87\n"
"v_mfma_f32_16x16x32_bf16 a[156:159], a[248:251], v[108:111], a[156:159]\n"
"v_exp_f32 v90, v90\n"
"v_mfma_f32_16x16x32_bf16 a[156:159], a[252:255], v[124:127], a[156:159]\n"
"s_waitcnt lgkmcnt(3)\n"
"ds_read_b64_tr_b16 a[230:231], v5 offset:18688\n"
"v_cvt_pk_bf16_f32 v151, v86, v87\n"
"v_mfma_f32_16x16x32_bf16 a[160:163], a[216:219], v[96:99], a[160:163]\n"
"v_exp_f32 v91, v91\n"
"v_mfma_f32_16x16x32_bf16 a[160:163], a[220:223], v[112:115], a[160:163]\n"
"v_add_f32 v20, v20, v88\n"
"v_add_f32 v20, v20, v89\n"
"v_mfma_f32_16x16x32_bf16 a[164:167], a[216:219], v[100:103], a[164:167]\n"
"v_exp_f32 v92, v92\n"
"v_mfma_f32_16x16x32_bf16 a[164:167], a[220:223], v[116:119], a[164:167]\n"
"v_cvt_pk_bf16_f32 v154, v88, v89\n"
"v_add_f32 v20, v20, v90\n"
"v_mfma_f32_16x16x32_bf16 a[168:171], a[216:219], v[104:107], a[168:171]\n"
"s_mov_b32 s47, s44\n"
"v_exp_f32 v93, v93\n"
"v_mfma_f32_16x16x32_bf16 a[168:171], a[220:223], v[120:123], a[168:171]\n"
"s_mov_b32 s44, s45\n"
"v_add_f32 v20, v20, v91\n"
"v_cvt_pk_bf16_f32 v155, v90, v91\n"
"v_mfma_f32_16x16x32_bf16 a[172:175], a[216:219], v[108:111], a[172:175]\n"
"s_mov_b32 s45, s46\n"
"v_exp_f32 v94, v94\n"
"v_mfma_f32_16x16x32_bf16 a[172:175], a[220:223], v[124:127], a[172:175]\n"
"s_waitcnt lgkmcnt(0)\n"
"s_mov_b32 s46, s47\n"
"v_exp_f32 v95, v95\n"
"v_mfma_f32_16x16x32_bf16 a[176:179], a[224:227], v[96:99], a[176:179]\n"
"v_add_f32 v21, v21, v92\n"
"v_add_f32 v21, v21, v93\n"
"v_mfma_f32_16x16x32_bf16 a[176:179], a[228:231], v[112:115], a[176:179]\n"
"v_cvt_pk_bf16_f32 v158, v92, v93\n"
"v_add_f32 v21, v21, v94\n"
"v_mfma_f32_16x16x32_bf16 a[180:183], a[224:227], v[100:103], a[180:183]\n"
"v_add_f32 v21, v21, v95\n"
"v_cvt_pk_bf16_f32 v159, v94, v95\n"
"v_mfma_f32_16x16x32_bf16 a[180:183], a[228:231], v[116:119], a[180:183]\n"
"v_max3_f32 v22, v18, v19, v20\n"
"v_max_f32 v22, v22, v21\n"
"v_mfma_f32_16x16x32_bf16 a[184:187], a[224:227], v[104:107], a[184:187]\n"
"v_cmp_lt_f32 vcc, s54, v22\n"
"v_mfma_f32_16x16x32_bf16 a[184:187], a[228:231], v[120:123], a[184:187]\n"
"v_mfma_f32_16x16x32_bf16 a[188:191], a[224:227], v[108:111], a[188:191]\n"
"v_mfma_f32_16x16x32_bf16 a[188:191], a[228:231], v[124:127], a[188:191]\n"
"s_cbranch_vccnz L_rc8\n"
"L_rr8:\n"
"v_add_f32 v14, v14, v18\n"
"v_add_f32 v15, v15, v19\n"
"v_add_f32 v16, v16, v20\n"
"v_add_f32 v17, v17, v21\n"
"v_add_u32 v2, s46, v2\n"
"v_add_u32 v3, s46, v3\n"
"v_add_u32 v4, s46, v4\n"
"v_add_u32 v5, s46, v5\n"
"s_waitcnt vmcnt(33)\n"
"s_waitcnt vmcnt(21)\n"
"s_barrier\n"
"ds_read_b64_tr_b16 a[240:241], v2 offset:0\n"
"ds_read_b64_tr_b16 a[242:243], v2 offset:6144\n"
"ds_read_b64_tr_b16 a[244:245], v2 offset:12288\n"
"ds_read_b64_tr_b16 a[246:247], v2 offset:18432\n"
"ds_read_b64_tr_b16 a[248:249], v3 offset:0\n"
"ds_read_b64_tr_b16 a[250:251], v3 offset:6144\n"
"ds_read_b64_tr_b16 a[252:253], v3 offset:12288\n"
"ds_read_b64_tr_b16 a[254:255], v3 offset:18432\n"
"v_mov_b32 v22, v14\n"
"v_mov_b32 v23, v15\n"
"v_mov_b32 v24, v16\n"
"v_mov_b32 v25, v17\n"
"v_permlane32_swap_b32 v14, v22\n"
"v_permlane32_swap_b32 v15, v23\n"
"v_permlane32_swap_b32 v16, v24\n"
"v_permlane32_swap_b32 v17, v25\n"
"v_add_f32 v14, v14, v22\n"
"v_add_f32 v15, v15, v23\n"
"v_add_f32 v16, v16, v24\n"
"v_add_f32 v17, v17, v25\n"
"s_waitcnt lgkmcnt(4)\n"
"v_mfma_f32_16x16x32_bf16 a[0:3], a[240:243], v[128:131], a[0:3]\n"
"v_mov_b32 v22, v14\n"
"v_mov_b32 v23, v15\n"
"v_mfma_f32_16x16x32_bf16 a[0:3], a[244:247], v[144:147], a[0:3]\n"
"s_add_u32 m0, s95, 61440\n"
"ds_read_b64_tr_b16 a[216:217], v4 offset:0\n"
"v_mov_b32 v24, v16\n"
"v_mfma_f32_16x16x32_bf16 a[4:7], a[240:243], v[132:135], a[4:7]\n"
"buffer_load_dwordx4 v30, s[20:23], s40 offen offset:0 lds\n"
"v_mfma_f32_16x16x32_bf16 a[4:7], a[244:247], v[148:151], a[4:7]\n"
"ds_read_b64_tr_b16 a[218:219], v4 offset:6144\n"
"v_mov_b32 v25, v17\n"
"v_mfma_f32_16x16x32_bf16 a[8:11], a[240:243], v[136:139], a[8:11]\n"
"buffer_load_dwordx4 v31, s[20:23], s40 offen offset:1024 lds\n"
"v_mfma_f32_16x16x32_bf16 a[8:11], a[244:247], v[152:155], a[8:11]\n"
"ds_read_b64_tr_b16 a[220:221], v4 offset:12288\n"
"v_permlane16_swap_b32 v14, v22\n"
"v_mfma_f32_16x16x32_bf16 a[12:15], a[240:243], v[140:143], a[12:15]\n"
"buffer_load_dwordx4 v32, s[20:23], s40 offen offset:2048 lds\n"
"v_mfma_f32_16x16x32_bf16 a[12:15], a[244:247], v[156:159], a[12:15]\n"
"s_waitcnt lgkmcnt(3)\n"
"ds_read_b64_tr_b16 a[222:223], v4 offset:18432\n"
"v_permlane16_swap_b32 v15, v23\n"
"v_mfma_f32_16x16x32_bf16 a[16:19], a[248:251], v[128:131], a[16:19]\n"
"buffer_load_dwordx4 v33, s[20:23], s40 offen offset:3072 lds\n"
"v_mfma_f32_16x16x32_bf16 a[16:19], a[252:255], v[144:147], a[16:19]\n"
"s_add_u32 m0, s95, 65536\n"
"ds_read_b64_tr_b16 a[224:225], v5 offset:0\n"
"v_permlane16_swap_b32 v16, v24\n"
"v_mfma_f32_16x16x32_bf16 a[20:23], a[248:251], v[132:135], a[20:23]\n"
"buffer_load_dwordx4 v34, s[20:23], s40 offen offset:0 lds\n"
"v_mfma_f32_16x16x32_bf16 a[20:23], a[252:255], v[148:151], a[20:23]\n"
"s_add_u32 s40, s40, 81920\n"
"s_add_u32 m0, s95, 81920\n"
"ds_read_b64_tr_b16 a[226:227], v5 offset:6144\n"
"v_permlane16_swap_b32 v17, v25\n"
"v_mfma_f32_16x16x32_bf16 a[24:27], a[248:251], v[136:139], a[24:27]\n"
"buffer_load_dwordx4 v30, s[20:23], s41 offen offset:0 lds\n"
"v_mfma_f32_16x16x32_bf16 a[24:27], a[252:255], v[152:155], a[24:27]\n"
"ds_read_b64_tr_b16 a[228:229], v5 offset:12288\n"
"v_add_f32 v14, v14, v22\n"
"v_mfma_f32_16x16x32_bf16 a[28:31], a[248:251], v[140:143], a[28:31]\n"
"v_add_f32 v15, v15, v23\n"
"v_add_f32 v16, v16, v24\n"
"v_mfma_f32_16x16x32_bf16 a[28:31], a[252:255], v[156:159], a[28:31]\n"
"s_waitcnt lgkmcnt(3)\n"
"ds_read_b64_tr_b16 a[230:231], v5 offset:18432\n"
"v_add_f32 v17, v17, v25\n"
"v_mfma_f32_16x16x32_bf16 a[32:35], a[216:219], v[128:131], a[32:35]\n"
"buffer_load_dwordx4 v31, s[20:23], s41 offen offset:1024 lds\n"
"v_mfma_f32_16x16x32_bf16 a[32:35], a[220:223], v[144:147], a[32:35]\n"
"ds_read_b64_tr_b16 a[240:241], v2 offset:128\n"
"v_accvgpr_read_b32 v40, a0\n"
"v_mfma_f32_16x16x32_bf16 a[36:39], a[216:219], v[132:135], a[36:39]\n"
"v_accvgpr_read_b32 v41, a1\n"
"v_accvgpr_read_b32 v42, a2\n"
"v_mfma_f32_16x16x32_bf16 a[36:39], a[220:223], v[148:151], a[36:39]\n"
"ds_read_b64_tr_b16 a[242:243], v2 offset:6272\n"
"v_accvgpr_read_b32 v43, a3\n"
"v_mfma_f32_16x16x32_bf16 a[40:43], a[216:219], v[136:139], a[40:43]\n"
"buffer_load_dwordx4 v32, s[20:23], s41 offen offset:2048 lds\n"
"v_mfma_f32_16x16x32_bf16 a[40:43], a[220:223], v[152:155], a[40:43]\n"
"ds_read_b64_tr_b16 a[244:245], v2 offset:12416\n"
"v_accvgpr_read_b32 v44, a16\n"
"v_mfma_f32_16x16x32_bf16 a[44:47], a[216:219], v[140:143], a[44:47]\n"
"buffer_load_dwordx4 a[0:3], v12, s[24:27], s78 offen offset:0 sc1\n"
"v_mfma_f32_16x16x32_bf16 a[44:47], a[220:223], v[156:159], a[44:47]\n"
"s_waitcnt lgkmcnt(3)\n"
"ds_read_b64_tr_b16 a[246:247], v2 offset:18560\n"
"v_accvgpr_read_b32 v45, a17\n"
"v_mfma_f32_16x16x32_bf16 a[48:51], a[224:227], v[128:131], a[48:51]\n"
"buffer_load_dwordx4 v33, s[20:23], s41 offen offset:3072 lds\n"
"v_mfma_f32_16x16x32_bf16 a[48:51], a[228:231], v[144:147], a[48:51]\n"
"ds_read_b64_tr_b16 a[248:249], v3 offset:128\n"
"v_accvgpr_read_b32 v10, a18\n"
"v_mfma_f32_16x16x32_bf16 a[52:55], a[224:227], v[132:135], a[52:55]\n"
"v_rcp_f32 v14, v14\n"
"v_mfma_f32_16x16x32_bf16 a[52:55], a[228:231], v[148:151], a[52:55]\n"
"s_add_u32 m0, s95, 86016\n"
"ds_read_b64_tr_b16 a[250:251], v3 offset:6272\n"
"v_accvgpr_read_b32 v11, a19\n"
"v_mfma_f32_16x16x32_bf16 a[56:59], a[224:227], v[136:139], a[56:59]\n"
"buffer_load_dwordx4 v34, s[20:23], s41 offen offset:0 lds\n"
"v_mfma_f32_16x16x32_bf16 a[56:59], a[228:231], v[152:155], a[56:59]\n"
"s_mov_b32 s41, 983040\n"
"s_cmp_eq_u32 s41, s29\n"
"s_cselect_b32 s41, s28, s41\n"
"ds_read_b64_tr_b16 a[252:253], v3 offset:12416\n"
"v_fma_f32 v40, v14, v40, -v14\n"
"v_mfma_f32_16x16x32_bf16 a[60:63], a[224:227], v[140:143], a[60:63]\n"
"v_fma_f32 v41, v14, v41, -v14\n"
"v_fma_f32 v42, v14, v42, -v14\n"
"v_mfma_f32_16x16x32_bf16 a[60:63], a[228:231], v[156:159], a[60:63]\n"
"s_waitcnt lgkmcnt(3)\n"
"ds_read_b64_tr_b16 a[254:255], v3 offset:18560\n"
"v_fma_f32 v43, v14, v43, -v14\n"
"v_mfma_f32_16x16x32_bf16 a[64:67], a[240:243], v[128:131], a[64:67]\n"
"v_fma_f32 v44, v14, v44, -v14\n"
"v_fma_f32 v45, v14, v45, -v14\n"
"v_mfma_f32_16x16x32_bf16 a[64:67], a[244:247], v[144:147], a[64:67]\n"
"ds_read_b64_tr_b16 a[216:217], v4 offset:128\n"
"v_fma_f32 v10, v14, v10, -v14\n"
"v_mfma_f32_16x16x32_bf16 a[68:71], a[240:243], v[132:135], a[68:71]\n"
"v_fma_f32 v11, v14, v11, -v14\n"
"v_cvt_pk_bf16_f32 v48, v40, v41\n"
"v_mfma_f32_16x16x32_bf16 a[68:71], a[244:247], v[148:151], a[68:71]\n"
"ds_read_b64_tr_b16 a[218:219], v4 offset:6272\n"
"v_cvt_pk_bf16_f32 v49, v42, v43\n"
"v_mfma_f32_16x16x32_bf16 a[72:75], a[240:243], v[136:139], a[72:75]\n"
"v_cvt_pk_bf16_f32 v50, v44, v45\n"
"v_cvt_pk_bf16_f32 v51, v10, v11\n"
"v_mfma_f32_16x16x32_bf16 a[72:75], a[244:247], v[152:155], a[72:75]\n"
"ds_read_b64_tr_b16 a[220:221], v4 offset:12416\n"
"v_accvgpr_read_b32 v40, a4\n"
"v_mfma_f32_16x16x32_bf16 a[76:79], a[240:243], v[140:143], a[76:79]\n"
"buffer_store_dwordx4 v[48:51], v12, s[24:27], s2 offen offset:0 sc1\n"
"v_mfma_f32_16x16x32_bf16 a[76:79], a[244:247], v[156:159], a[76:79]\n"
"s_waitcnt lgkmcnt(3)\n"
"ds_read_b64_tr_b16 a[222:223], v4 offset:18560\n"
"v_accvgpr_read_b32 v41, a5\n"
"v_mfma_f32_16x16x32_bf16 a[80:83], a[248:251], v[128:131], a[80:83]\n"
"v_accvgpr_read_b32 v42, a6\n"
"v_accvgpr_read_b32 v43, a7\n"
"v_mfma_f32_16x16x32_bf16 a[80:83], a[252:255], v[144:147], a[80:83]\n"
"ds_read_b64_tr_b16 a[224:225], v5 offset:128\n"
"v_accvgpr_read_b32 v44, a20\n"
"v_mfma_f32_16x16x32_bf16 a[84:87], a[248:251], v[132:135], a[84:87]\n"
"buffer_load_dwordx4 a[4:7], v12, s[24:27], s78 offen offset:1024 sc1\n"
"v_mfma_f32_16x16x32_bf16 a[84:87], a[252:255], v[148:151], a[84:87]\n"
"ds_read_b64_tr_b16 a[226:227], v5 offset:6272\n"
"v_accvgpr_read_b32 v45, a21\n"
"v_mfma_f32_16x16x32_bf16 a[88:91], a[248:251], v[136:139], a[88:91]\n"
"v_rcp_f32 v15, v15\n"
"v_mfma_f32_16x16x32_bf16 a[88:91], a[252:255], v[152:155], a[88:91]\n"
"ds_read_b64_tr_b16 a[228:229], v5 offset:12416\n"
"v_accvgpr_read_b32 v10, a22\n"
"v_mfma_f32_16x16x32_bf16 a[92:95], a[248:251], v[140:143], a[92:95]\n"
"v_accvgpr_read_b32 v11, a23\n"
"v_fma_f32 v40, v15, v40, -v15\n"
"v_mfma_f32_16x16x32_bf16 a[92:95], a[252:255], v[156:159], a[92:95]\n"
"s_waitcnt lgkmcnt(3)\n"
"ds_read_b64_tr_b16 a[230:231], v5 offset:18560\n"
"v_fma_f32 v41, v15, v41, -v15\n"
"v_mfma_f32_16x16x32_bf16 a[96:99], a[216:219], v[128:131], a[96:99]\n"
"v_fma_f32 v42, v15, v42, -v15\n"
"v_fma_f32 v43, v15, v43, -v15\n"
"v_mfma_f32_16x16x32_bf16 a[96:99], a[220:223], v[144:147], a[96:99]\n"
"ds_read_b64_tr_b16 a[240:241], v2 offset:256\n"
"v_fma_f32 v44, v15, v44, -v15\n"
"v_mfma_f32_16x16x32_bf16 a[100:103], a[216:219], v[132:135], a[100:103]\n"
"v_fma_f32 v45, v15, v45, -v15\n"
"v_fma_f32 v10, v15, v10, -v15\n"
"v_mfma_f32_16x16x32_bf16 a[100:103], a[220:223], v[148:151], a[100:103]\n"
"ds_read_b64_tr_b16 a[242:243], v2 offset:6400\n"
"v_fma_f32 v11, v15, v11, -v15\n"
"v_mfma_f32_16x16x32_bf16 a[104:107], a[216:219], v[136:139], a[104:107]\n"
"v_cvt_pk_bf16_f32 v52, v40, v41\n"
"v_cvt_pk_bf16_f32 v53, v42, v43\n"
"v_mfma_f32_16x16x32_bf16 a[104:107], a[220:223], v[152:155], a[104:107]\n"
"ds_read_b64_tr_b16 a[244:245], v2 offset:12544\n"
"v_cvt_pk_bf16_f32 v54, v44, v45\n"
"v_mfma_f32_16x16x32_bf16 a[108:111], a[216:219], v[140:143], a[108:111]\n"
"v_cvt_pk_bf16_f32 v55, v10, v11\n"
"v_accvgpr_read_b32 v40, a8\n"
"v_mfma_f32_16x16x32_bf16 a[108:111], a[220:223], v[156:159], a[108:111]\n"
"L_pg16:\n"
"s_cbranch_execz L_bk4\n"
"s_waitcnt lgkmcnt(3)\n"
"ds_read_b64_tr_b16 a[246:247], v2 offset:18688\n"
"v_accvgpr_read_b32 v41, a9\n"
"v_mfma_f32_16x16x32_bf16 a[112:115], a[224:227], v[128:131], a[112:115]\n"
"buffer_store_dwordx4 v[52:55], v12, s[24:27], s2 offen offset:1024 sc1\n"
"v_mfma_f32_16x16x32_bf16 a[112:115], a[228:231], v[144:147], a[112:115]\n"
"ds_read_b64_tr_b16 a[248:249], v3 offset:256\n"
"v_accvgpr_read_b32 v42, a10\n"
"v_mfma_f32_16x16x32_bf16 a[116:119], a[224:227], v[132:135], a[116:119]\n"
"v_accvgpr_read_b32 v43, a11\n"
"v_accvgpr_read_b32 v44, a24\n"
"v_mfma_f32_16x16x32_bf16 a[116:119], a[228:231], v[148:151], a[116:119]\n"
"ds_read_b64_tr_b16 a[250:251], v3 offset:6400\n"
"v_accvgpr_read_b32 v45, a25\n"
"v_mfma_f32_16x16x32_bf16 a[120:123], a[224:227], v[136:139], a[120:123]\n"
"buffer_load_dwordx4 a[8:11], v12, s[24:27], s78 offen offset:2048 sc1\n"
"v_mfma_f32_16x16x32_bf16 a[120:123], a[228:231], v[152:155], a[120:123]\n"
"ds_read_b64_tr_b16 a[252:253], v3 offset:12544\n"
"v_accvgpr_read_b32 v10, a26\n"
"v_mfma_f32_16x16x32_bf16 a[124:127], a[224:227], v[140:143], a[124:127]\n"
"v_rcp_f32 v16, v16\n"
"v_mfma_f32_16x16x32_bf16 a[124:127], a[228:231], v[156:159], a[124:127]\n"
"s_waitcnt lgkmcnt(3)\n"
"ds_read_b64_tr_b16 a[254:255], v3 offset:18688\n"
"v_accvgpr_read_b32 v11, a27\n"
"v_mfma_f32_16x16x32_bf16 a[128:131], a[240:243], v[128:131], a[128:131]\n"
"v_fma_f32 v40, v16, v40, -v16\n"
"v_fma_f32 v41, v16, v41, -v16\n"
"v_mfma_f32_16x16x32_bf16 a[128:131], a[244:247], v[144:147], a[128:131]\n"
"ds_read_b64_tr_b16 a[216:217], v4 offset:256\n"
"v_fma_f32 v42, v16, v42, -v16\n"
"v_mfma_f32_16x16x32_bf16 a[132:135], a[240:243], v[132:135], a[132:135]\n"
"v_fma_f32 v43, v16, v43, -v16\n"
"v_fma_f32 v44, v16, v44, -v16\n"
"v_mfma_f32_16x16x32_bf16 a[132:135], a[244:247], v[148:151], a[132:135]\n"
"ds_read_b64_tr_b16 a[218:219], v4 offset:6400\n"
"v_fma_f32 v45, v16, v45, -v16\n"
"v_mfma_f32_16x16x32_bf16 a[136:139], a[240:243], v[136:139], a[136:139]\n"
"v_fma_f32 v10, v16, v10, -v16\n"
"v_fma_f32 v11, v16, v11, -v16\n"
"v_mfma_f32_16x16x32_bf16 a[136:139], a[244:247], v[152:155], a[136:139]\n"
"ds_read_b64_tr_b16 a[220:221], v4 offset:12544\n"
"v_cvt_pk_bf16_f32 v56, v40, v41\n"
"v_mfma_f32_16x16x32_bf16 a[140:143], a[240:243], v[140:143], a[140:143]\n"
"v_cvt_pk_bf16_f32 v57, v42, v43\n"
"v_cvt_pk_bf16_f32 v58, v44, v45\n"
"v_mfma_f32_16x16x32_bf16 a[140:143], a[244:247], v[156:159], a[140:143]\n"
"s_waitcnt lgkmcnt(3)\n"
"ds_read_b64_tr_b16 a[222:223], v4 offset:18688\n"
"v_cvt_pk_bf16_f32 v59, v10, v11\n"
"v_mfma_f32_16x16x32_bf16 a[144:147], a[248:251], v[128:131], a[144:147]\n"
"buffer_store_dwordx4 v[56:59], v12, s[24:27], s2 offen offset:2048 sc1\n"
"v_mfma_f32_16x16x32_bf16 a[144:147], a[252:255], v[144:147], a[144:147]\n"
"ds_read_b64_tr_b16 a[224:225], v5 offset:256\n"
"v_accvgpr_read_b32 v40, a12\n"
"v_mfma_f32_16x16x32_bf16 a[148:151], a[248:251], v[132:135], a[148:151]\n"
"v_accvgpr_read_b32 v41, a13\n"
"v_accvgpr_read_b32 v42, a14\n"
"v_mfma_f32_16x16x32_bf16 a[148:151], a[252:255], v[148:151], a[148:151]\n"
"ds_read_b64_tr_b16 a[226:227], v5 offset:6400\n"
"v_accvgpr_read_b32 v43, a15\n"
"v_mfma_f32_16x16x32_bf16 a[152:155], a[248:251], v[136:139], a[152:155]\n"
"buffer_load_dwordx4 a[12:15], v12, s[24:27], s78 offen offset:3072 sc1\n"
"s_add_u32 s78, s78, 4096\n"
"v_mfma_f32_16x16x32_bf16 a[152:155], a[252:255], v[152:155], a[152:155]\n"
"ds_read_b64_tr_b16 a[228:229], v5 offset:12544\n"
"v_accvgpr_read_b32 v44, a28\n"
"v_mfma_f32_16x16x32_bf16 a[156:159], a[248:251], v[140:143], a[156:159]\n"
"buffer_load_dwordx4 a[16:19], v12, s[24:27], s78 offen offset:0 sc1\n"
"v_mfma_f32_16x16x32_bf16 a[156:159], a[252:255], v[156:159], a[156:159]\n"
"s_waitcnt lgkmcnt(3)\n"
"ds_read_b64_tr_b16 a[230:231], v5 offset:18688\n"
"v_accvgpr_read_b32 v45, a29\n"
"v_mfma_f32_16x16x32_bf16 a[160:163], a[216:219], v[128:131], a[160:163]\n"
"buffer_load_dwordx4 a[20:23], v12, s[24:27], s78 offen offset:1024 sc1\n"
"v_mfma_f32_16x16x32_bf16 a[160:163], a[220:223], v[144:147], a[160:163]\n"
"buffer_load_dwordx4 a[24:27], v12, s[24:27], s78 offen offset:2048 sc1\n"
"v_mfma_f32_16x16x32_bf16 a[164:167], a[216:219], v[132:135], a[164:167]\n"
"v_rcp_f32 v17, v17\n"
"v_mfma_f32_16x16x32_bf16 a[164:167], a[220:223], v[148:151], a[164:167]\n"
"ds_read_b64_tr_b16 a[240:241], v0 offset:0\n"
"v_accvgpr_read_b32 v10, a30\n"
"v_mfma_f32_16x16x32_bf16 a[168:171], a[216:219], v[136:139], a[168:171]\n"
"ds_read_b64_tr_b16 a[242:243], v0 offset:5120\n"
"v_accvgpr_read_b32 v11, a31\n"
"v_mfma_f32_16x16x32_bf16 a[168:171], a[220:223], v[152:155], a[168:171]\n"
"ds_read_b64_tr_b16 a[244:245], v0 offset:10240\n"
"v_fma_f32 v40, v17, v40, -v17\n"
"v_mfma_f32_16x16x32_bf16 a[172:175], a[216:219], v[140:143], a[172:175]\n"
"ds_read_b64_tr_b16 a[246:247], v0 offset:15360\n"
"v_fma_f32 v41, v17, v41, -v17\n"
"v_mfma_f32_16x16x32_bf16 a[172:175], a[220:223], v[156:159], a[172:175]\n"
"s_waitcnt lgkmcnt(4)\n"
"ds_read_b64_tr_b16 a[248:249], v1 offset:0\n"
"v_fma_f32 v42, v17, v42, -v17\n"
"v_mfma_f32_16x16x32_bf16 a[176:179], a[224:227], v[128:131], a[176:179]\n"
"ds_read_b64_tr_b16 a[250:251], v1 offset:5120\n"
"v_fma_f32 v43, v17, v43, -v17\n"
"v_mfma_f32_16x16x32_bf16 a[176:179], a[228:231], v[144:147], a[176:179]\n"
"ds_read_b64_tr_b16 a[252:253], v1 offset:10240\n"
"v_fma_f32 v44, v17, v44, -v17\n"
"v_mfma_f32_16x16x32_bf16 a[180:183], a[224:227], v[132:135], a[180:183]\n"
"ds_read_b64_tr_b16 a[254:255], v1 offset:15360\n"
"v_fma_f32 v45, v17, v45, -v17\n"
"v_mfma_f32_16x16x32_bf16 a[180:183], a[228:231], v[148:151], a[180:183]\n"
"ds_read_b64_tr_b16 a[216:217], v0 offset:64\n"
"v_fma_f32 v10, v17, v10, -v17\n"
"v_mfma_f32_16x16x32_bf16 a[184:187], a[224:227], v[136:139], a[184:187]\n"
"ds_read_b64_tr_b16 a[218:219], v0 offset:5184\n"
"v_fma_f32 v11, v17, v11, -v17\n"
"v_mfma_f32_16x16x32_bf16 a[184:187], a[228:231], v[152:155], a[184:187]\n"
"ds_read_b64_tr_b16 a[220:221], v0 offset:10304\n"
"v_cvt_pk_bf16_f32 v60, v40, v41\n"
"v_mfma_f32_16x16x32_bf16 a[188:191], a[224:227], v[140:143], a[188:191]\n"
"ds_read_b64_tr_b16 a[222:223], v0 offset:15424\n"
"v_cvt_pk_bf16_f32 v61, v42, v43\n"
"v_mfma_f32_16x16x32_bf16 a[188:191], a[228:231], v[156:159], a[188:191]\n"
"buffer_load_dwordx4 a[28:31], v12, s[24:27], s78 offen offset:3072 sc1\n"
"s_add_u32 s78, s78, 4096\n"
"s_waitcnt lgkmcnt(8)\n"
"s_waitcnt vmcnt(32)\n"
"v_mfma_f32_16x16x32_bf16 v[64:67], a[192:195], a[240:243], 0\n"
"ds_read_b64_tr_b16 a[224:225], v1 offset:64\n"
"v_add_u32 v2, 0xf000, v28\n"
"v_mfma_f32_16x16x32_bf16 v[64:67], a[208:211], a[244:247], v[64:67]\n"
"ds_read_b64_tr_b16 a[226:227], v1 offset:5184\n"
"v_cvt_pk_bf16_f32 v62, v44, v45\n"
"v_mfma_f32_16x16x32_bf16 v[68:71], a[196:199], a[240:243], 0\n"
"ds_read_b64_tr_b16 a[228:229], v1 offset:10304\n"
"v_cvt_pk_bf16_f32 v63, v10, v11\n"
"v_mfma_f32_16x16x32_bf16 v[68:71], a[212:215], a[244:247], v[68:71]\n"
"ds_read_b64_tr_b16 a[230:231], v1 offset:15424\n"
"v_accvgpr_read_b32 v40, a32\n"
"v_mfma_f32_16x16x32_bf16 v[72:75], a[200:203], a[240:243], 0\n"
"v_accvgpr_read_b32 v41, a33\n"
"v_accvgpr_read_b32 v42, a34\n"
"v_mfma_f32_16x16x32_bf16 v[72:75], a[232:235], a[244:247], v[72:75]\n"
"v_max3_f32 v252, |v64|, |v65|, |v66|\n"
"v_accvgpr_read_b32 v43, a35\n"
"v_mfma_f32_16x16x32_bf16 v[76:79], a[204:207], a[240:243], 0\n"
"v_accvgpr_read_b32 v44, a48\n"
"v_accvgpr_read_b32 v45, a49\n"
"v_mfma_f32_16x16x32_bf16 v[76:79], a[236:239], a[244:247], v[76:79]\n"
"s_waitcnt lgkmcnt(8)\n"
"v_max3_f32 v252, v252, |v67|, |v68|\n"
"v_max3_f32 v252, v252, |v69|, |v70|\n"
"v_mfma_f32_16x16x32_bf16 v[80:83], a[192:195], a[248:251], 0\n"
"ds_read_b64_tr_b16 a[240:241], v0 offset:128\n"
"v_add_u32 v3, 0xf000, v29\n"
"v_mfma_f32_16x16x32_bf16 v[80:83], a[208:211], a[252:255], v[80:83]\n"
"ds_read_b64_tr_b16 a[242:243], v0 offset:5248\n"
"v_min_f32 v252, 0x71800000, v252\n"
"v_mfma_f32_16x16x32_bf16 v[84:87], a[196:199], a[248:251], 0\n"
"ds_read_b64_tr_b16 a[244:245], v0 offset:10368\n"
"v_add_u32 v252, 0x1800000, v252\n"
"v_mfma_f32_16x16x32_bf16 v[84:87], a[212:215], a[252:255], v[84:87]\n"
"ds_read_b64_tr_b16 a[246:247], v0 offset:15488\n"
"v_and_b32 v252, 0x7e000000, v252\n"
"v_mfma_f32_16x16x32_bf16 v[88:91], a[200:203], a[248:251], 0\n"
"v_mul_f32 v253, 0x42400000, v252\n"
"v_mul_f32 v254, 0x42400000, v252\n"
"v_mfma_f32_16x16x32_bf16 v[88:91], a[232:235], a[252:255], v[88:91]\n"
"v_mul_f32 v255, 0x42400000, v252\n"
"v_mul_f32 v252, 0x42400000, v252\n"
"v_mfma_f32_16x16x32_bf16 v[92:95], a[204:207], a[248:251], 0\n"
"v_mov_b32 v13, v252\n"
"v_accvgpr_read_b32 v10, a50\n"
"v_mfma_f32_16x16x32_bf16 v[92:95], a[236:239], a[252:255], v[92:95]\n"
"s_waitcnt lgkmcnt(8)\n"
"buffer_store_dwordx4 v[60:63], v12, s[24:27], s2 offen offset:3072 sc1\n"
"v_mfma_f32_16x16x32_bf16 v[96:99], a[192:195], a[216:219], v[252:255]\n"
"ds_read_b64_tr_b16 a[248:249], v1 offset:128\n"
"v_accvgpr_read_b32 v11, a51\n"
"v_mfma_f32_16x16x32_bf16 v[96:99], a[208:211], a[220:223], v[96:99]\n"
"ds_read_b64_tr_b16 a[250:251], v1 offset:5248\n"
"v_fma_f32 v40, v14, v40, -v14\n"
"v_mfma_f32_16x16x32_bf16 v[100:103], a[196:199], a[216:219], v[252:255]\n"
"ds_read_b64_tr_b16 a[252:253], v1 offset:10368\n"
"v_fma_f32 v41, v14, v41, -v14\n"
"v_mfma_f32_16x16x32_bf16 v[100:103], a[212:215], a[220:223], v[100:103]\n"
"ds_read_b64_tr_b16 a[254:255], v1 offset:15488\n"
"v_fma_f32 v42, v14, v42, -v14\n"
"v_mfma_f32_16x16x32_bf16 v[104:107], a[200:203], a[216:219], v[252:255]\n"
"v_fma_f32 v43, v14, v43, -v14\n"
"v_fma_f32 v44, v14, v44, -v14\n"
"v_mfma_f32_16x16x32_bf16 v[104:107], a[232:235], a[220:223], v[104:107]\n"
"buffer_load_dwordx4 a[32:35], v12, s[24:27], s78 offen offset:0 sc1\n"
"v_mfma_f32_16x16x32_bf16 v[108:111], a[204:207], a[216:219], v[252:255]\n"
"v_fma_f32 v45, v14, v45, -v14\n"
"v_fma_f32 v10, v14, v10, -v14\n"
"v_mfma_f32_16x16x32_bf16 v[108:111], a[236:239], a[220:223], v[108:111]\n"
"s_waitcnt lgkmcnt(8)\n"
"v_fma_f32 v11, v14, v11, -v14\n"
"v_cvt_pk_bf16_f32 v18, v40, v41\n"
"v_mfma_f32_16x16x32_bf16 v[112:115], a[192:195], a[224:227], v[252:255]\n"
"ds_read_b64_tr_b16 a[216:217], v0 offset:192\n"
"v_cvt_pk_bf16_f32 v19, v42, v43\n"
"v_mfma_f32_16x16x32_bf16 v[112:115], a[208:211], a[228:231], v[112:115]\n"
"ds_read_b64_tr_b16 a[218:219], v0 offset:5312\n"
"v_cvt_pk_bf16_f32 v20, v44, v45\n"
"v_mfma_f32_16x16x32_bf16 v[116:119], a[196:199], a[224:227], v[252:255]\n"
"ds_read_b64_tr_b16 a[220:221], v0 offset:10432\n"
"v_cvt_pk_bf16_f32 v21, v10, v11\n"
"v_mfma_f32_16x16x32_bf16 v[116:119], a[212:215], a[228:231], v[116:119]\n"
"ds_read_b64_tr_b16 a[222:223], v0 offset:15552\n"
"v_accvgpr_read_b32 v40, a36\n"
"v_mfma_f32_16x16x32_bf16 v[120:123], a[200:203], a[224:227], v[252:255]\n"
"v_accvgpr_read_b32 v41, a37\n"
"v_accvgpr_read_b32 v42, a38\n"
"v_mfma_f32_16x16x32_bf16 v[120:123], a[232:235], a[228:231], v[120:123]\n"
"buffer_store_dwordx4 v[18:21], v12, s[24:27], s3 offen offset:0 sc1\n"
"v_mfma_f32_16x16x32_bf16 v[124:127], a[204:207], a[224:227], v[252:255]\n"
"s_waitcnt vmcnt(48)\n"
"v_accvgpr_read_b32 v43, a39\n"
"v_accvgpr_read_b32 v44, a52\n"
"v_mfma_f32_16x16x32_bf16 v[124:127], a[236:239], a[228:231], v[124:127]\n"
"s_waitcnt lgkmcnt(8)\n"
"buffer_load_dwordx4 a[36:39], v12, s[24:27], s78 offen offset:1024 sc1\n"
"v_mfma_f32_16x16x32_bf16 v[160:163], a[192:195], a[240:243], v[252:255]\n"
"ds_read_b64_tr_b16 a[224:225], v1 offset:192\n"
"v_accvgpr_read_b32 v45, a53\n"
"v_mfma_f32_16x16x32_bf16 v[160:163], a[208:211], a[244:247], v[160:163]\n"
"ds_read_b64_tr_b16 a[226:227], v1 offset:5312\n"
"v_accvgpr_read_b32 v10, a54\n"
"v_mfma_f32_16x16x32_bf16 v[164:167], a[196:199], a[240:243], v[252:255]\n"
"ds_read_b64_tr_b16 a[228:229], v1 offset:10432\n"
"v_accvgpr_read_b32 v11, a55\n"
"v_mfma_f32_16x16x32_bf16 v[164:167], a[212:215], a[244:247], v[164:167]\n"
"ds_read_b64_tr_b16 a[230:231], v1 offset:15552\n"
"v_fma_f32 v40, v15, v40, -v15\n"
"v_mfma_f32_16x16x32_bf16 v[168:171], a[200:203], a[240:243], v[252:255]\n"
"v_fma_f32 v41, v15, v41, -v15\n"
"v_fma_f32 v42, v15, v42, -v15\n"
"v_mfma_f32_16x16x32_bf16 v[168:171], a[232:235], a[244:247], v[168:171]\n"
"v_fma_f32 v43, v15, v43, -v15\n"
"v_fma_f32 v44, v15, v44, -v15\n"
"v_mfma_f32_16x16x32_bf16 v[172:175], a[204:207], a[240:243], v[252:255]\n"
"v_fma_f32 v45, v15, v45, -v15\n"
"v_fma_f32 v10, v15, v10, -v15\n"
"v_mfma_f32_16x16x32_bf16 v[172:175], a[236:239], a[244:247], v[172:175]\n"
"s_waitcnt lgkmcnt(8)\n"
"v_fma_f32 v11, v15, v11, -v15\n"
"v_cvt_pk_bf16_f32 v22, v40, v41\n"
"v_mfma_f32_16x16x32_bf16 v[176:179], a[192:195], a[248:251], v[252:255]\n"
"ds_read_b64_tr_b16 a[240:241], v0 offset:256\n"
"v_cvt_pk_bf16_f32 v23, v42, v43\n"
"v_mfma_f32_16x16x32_bf16 v[176:179], a[208:211], a[252:255], v[176:179]\n"
"ds_read_b64_tr_b16 a[242:243], v0 offset:5376\n"
"v_cvt_pk_bf16_f32 v24, v44, v45\n"
"v_mfma_f32_16x16x32_bf16 v[180:183], a[196:199], a[248:251], v[252:255]\n"
"ds_read_b64_tr_b16 a[244:245], v0 offset:10496\n"
"v_cvt_pk_bf16_f32 v25, v10, v11\n"
"v_mfma_f32_16x16x32_bf16 v[180:183], a[212:215], a[252:255], v[180:183]\n"
"ds_read_b64_tr_b16 a[246:247], v0 offset:15616\n"
"v_accvgpr_read_b32 v40, a40\n"
"v_mfma_f32_16x16x32_bf16 v[184:187], a[200:203], a[248:251], v[252:255]\n"
"v_accvgpr_read_b32 v41, a41\n"
"v_accvgpr_read_b32 v42, a42\n"
"v_mfma_f32_16x16x32_bf16 v[184:187], a[232:235], a[252:255], v[184:187]\n"
"buffer_store_dwordx4 v[22:25], v12, s[24:27], s3 offen offset:1024 sc1\n"
"v_mfma_f32_16x16x32_bf16 v[188:191], a[204:207], a[248:251], v[252:255]\n"
"v_accvgpr_read_b32 v43, a43\n"
"v_accvgpr_read_b32 v44, a56\n"
"v_mfma_f32_16x16x32_bf16 v[188:191], a[236:239], a[252:255], v[188:191]\n"
"s_waitcnt lgkmcnt(8)\n"
"buffer_load_dwordx4 a[40:43], v12, s[24:27], s78 offen offset:2048 sc1\n"
"v_mfma_f32_16x16x32_bf16 v[192:195], a[192:195], a[216:219], v[252:255]\n"
"ds_read_b64_tr_b16 a[248:249], v1 offset:256\n"
"v_accvgpr_read_b32 v45, a57\n"
"v_mfma_f32_16x16x32_bf16 v[192:195], a[208:211], a[220:223], v[192:195]\n"
"ds_read_b64_tr_b16 a[250:251], v1 offset:5376\n"
"v_accvgpr_read_b32 v10, a58\n"
"v_mfma_f32_16x16x32_bf16 v[196:199], a[196:199], a[216:219], v[252:255]\n"
"ds_read_b64_tr_b16 a[252:253], v1 offset:10496\n"
"v_accvgpr_read_b32 v11, a59\n"
"v_mfma_f32_16x16x32_bf16 v[196:199], a[212:215], a[220:223], v[196:199]\n"
"ds_read_b64_tr_b16 a[254:255], v1 offset:15616\n"
"v_fma_f32 v40, v16, v40, -v16\n"
"v_mfma_f32_16x16x32_bf16 v[200:203], a[200:203], a[216:219], v[252:255]\n"
"v_fma_f32 v41, v16, v41, -v16\n"
"v_fma_f32 v42, v16, v42, -v16\n"
"v_mfma_f32_16x16x32_bf16 v[200:203], a[232:235], a[220:223], v[200:203]\n"
"v_fma_f32 v43, v16, v43, -v16\n"
"v_fma_f32 v44, v16, v44, -v16\n"
"v_mfma_f32_16x16x32_bf16 v[204:207], a[204:207], a[216:219], v[252:255]\n"
"v_fma_f32 v45, v16, v45, -v16\n"
"v_fma_f32 v10, v16, v10, -v16\n"
"v_mfma_f32_16x16x32_bf16 v[204:207], a[236:239], a[220:223], v[204:207]\n"
"s_waitcnt lgkmcnt(8)\n"
"v_fma_f32 v11, v16, v11, -v16\n"
"v_cvt_pk_bf16_f32 v36, v40, v41\n"
"v_mfma_f32_16x16x32_bf16 v[208:211], a[192:195], a[224:227], v[252:255]\n"
"ds_read_b64_tr_b16 a[216:217], v0 offset:20480\n"
"v_cvt_pk_bf16_f32 v37, v42, v43\n"
"v_mfma_f32_16x16x32_bf16 v[208:211], a[208:211], a[228:231], v[208:211]\n"
"ds_read_b64_tr_b16 a[218:219], v0 offset:25600\n"
"v_cvt_pk_bf16_f32 v38, v44, v45\n"
"v_mfma_f32_16x16x32_bf16 v[212:215], a[196:199], a[224:227], v[252:255]\n"
"ds_read_b64_tr_b16 a[220:221], v0 offset:30720\n"
"v_cvt_pk_bf16_f32 v39, v10, v11\n"
"v_mfma_f32_16x16x32_bf16 v[212:215], a[212:215], a[228:231], v[212:215]\n"
"ds_read_b64_tr_b16 a[222:223], v0 offset:35840\n"
"v_accvgpr_read_b32 v40, a44\n"
"v_mfma_f32_16x16x32_bf16 v[216:219], a[200:203], a[224:227], v[252:255]\n"
"v_accvgpr_read_b32 v41, a45\n"
"v_accvgpr_read_b32 v42, a46\n"
"v_mfma_f32_16x16x32_bf16 v[216:219], a[232:235], a[228:231], v[216:219]\n"
"buffer_store_dwordx4 v[36:39], v12, s[24:27], s3 offen offset:2048 sc1\n"
"v_mfma_f32_16x16x32_bf16 v[220:223], a[204:207], a[224:227], v[252:255]\n"
"v_accvgpr_read_b32 v43, a47\n"
"v_accvgpr_read_b32 v44, a60\n"
"v_mfma_f32_16x16x32_bf16 v[220:223], a[236:239], a[228:231], v[220:223]\n"
"s_waitcnt lgkmcnt(8)\n"
"buffer_load_dwordx4 a[44:47], v12, s[24:27], s78 offen offset:3072 sc1\n"
"s_add_u32 s78, s78, 4096\n"
"v_mfma_f32_16x16x32_bf16 v[224:227], a[192:195], a[240:243], v[252:255]\n"
"ds_read_b64_tr_b16 a[224:225], v1 offset:20480\n"
"v_accvgpr_read_b32 v45, a61\n"
"v_mfma_f32_16x16x32_bf16 v[224:227], a[208:211], a[244:247], v[224:227]\n"
"ds_read_b64_tr_b16 a[226:227], v1 offset:25600\n"
"v_accvgpr_read_b32 v10, a62\n"
"v_mfma_f32_16x16x32_bf16 v[228:231], a[196:199], a[240:243], v[252:255]\n"
"ds_read_b64_tr_b16 a[228:229], v1 offset:30720\n"
"v_accvgpr_read_b32 v11, a63\n"
"v_mfma_f32_16x16x32_bf16 v[228:231], a[212:215], a[244:247], v[228:231]\n"
"ds_read_b64_tr_b16 a[230:231], v1 offset:35840\n"
"v_fma_f32 v40, v17, v40, -v17\n"
"v_mfma_f32_16x16x32_bf16 v[232:235], a[200:203], a[240:243], v[252:255]\n"
"v_fma_f32 v41, v17, v41, -v17\n"
"v_fma_f32 v42, v17, v42, -v17\n"
"v_mfma_f32_16x16x32_bf16 v[232:235], a[232:235], a[244:247], v[232:235]\n"
"v_fma_f32 v43, v17, v43, -v17\n"
"v_fma_f32 v44, v17, v44, -v17\n"
"v_mfma_f32_16x16x32_bf16 v[236:239], a[204:207], a[240:243], v[252:255]\n"
"v_fma_f32 v45, v17, v45, -v17\n"
"v_fma_f32 v10, v17, v10, -v17\n"
"v_mfma_f32_16x16x32_bf16 v[236:239], a[236:239], a[244:247], v[236:239]\n"
"s_waitcnt lgkmcnt(8)\n"
"v_fma_f32 v11, v17, v11, -v17\n"
"v_cvt_pk_bf16_f32 v6, v40, v41\n"
"v_mfma_f32_16x16x32_bf16 v[240:243], a[192:195], a[248:251], v[252:255]\n"
"ds_read_b64_tr_b16 a[240:241], v0 offset:20544\n"
"v_cvt_pk_bf16_f32 v7, v42, v43\n"
"v_mfma_f32_16x16x32_bf16 v[240:243], a[208:211], a[252:255], v[240:243]\n"
"ds_read_b64_tr_b16 a[242:243], v0 offset:25664\n"
"v_cvt_pk_bf16_f32 v8, v44, v45\n"
"v_mfma_f32_16x16x32_bf16 v[244:247], a[196:199], a[248:251], v[252:255]\n"
"ds_read_b64_tr_b16 a[244:245], v0 offset:30784\n"
"v_cvt_pk_bf16_f32 v9, v10, v11\n"
"v_mfma_f32_16x16x32_bf16 v[244:247], a[212:215], a[252:255], v[244:247]\n"
"s_waitcnt vmcnt(35)\n"
"s_waitcnt vmcnt(40)\n"
"ds_read_b64_tr_b16 a[246:247], v0 offset:35904\n"
"v_accvgpr_read_b32 v40, a64\n"
"v_mfma_f32_16x16x32_bf16 v[248:251], a[200:203], a[248:251], v[252:255]\n"
"s_barrier\n"
"v_accvgpr_read_b32 v41, a65\n"
"v_accvgpr_read_b32 v42, a66\n"
"v_mfma_f32_16x16x32_bf16 v[248:251], a[232:235], a[252:255], v[248:251]\n"
"buffer_store_dwordx4 v[6:9], v12, s[24:27], s3 offen offset:3072 sc1\n"
"v_mfma_f32_16x16x32_bf16 v[252:255], a[204:207], a[248:251], v[252:255]\n"
"v_accvgpr_read_b32 v43, a67\n"
"v_accvgpr_read_b32 v44, a80\n"
"v_mfma_f32_16x16x32_bf16 v[252:255], a[236:239], a[252:255], v[252:255]\n"
"s_waitcnt lgkmcnt(8)\n"
"v_accvgpr_read_b32 v45, a81\n"
"v_accvgpr_read_b32 v10, a82\n"
"v_mfma_f32_16x16x32_bf16 v[64:67], v[48:51], a[216:219], v[64:67]\n"
"ds_read_b64_tr_b16 a[248:249], v1 offset:20544\n"
"v_accvgpr_read_b32 v11, a83\n"
"v_mfma_f32_16x16x32_bf16 v[64:67], v[18:21], a[220:223], v[64:67]\n"
"ds_read_b64_tr_b16 a[250:251], v1 offset:25664\n"
"v_fma_f32 v40, v14, v40, -v14\n"
"v_mfma_f32_16x16x32_bf16 v[68:71], v[52:55], a[216:219], v[68:71]\n"
"ds_read_b64_tr_b16 a[252:253], v1 offset:30784\n"
"v_fma_f32 v41, v14, v41, -v14\n"
"v_mfma_f32_16x16x32_bf16 v[68:71], v[22:25], a[220:223], v[68:71]\n"
"ds_read_b64_tr_b16 a[254:255], v1 offset:35904\n"
"v_fma_f32 v42, v14, v42, -v14\n"
"v_mfma_f32_16x16x32_bf16 v[72:75], v[56:59], a[216:219], v[72:75]\n"
"v_fma_f32 v43, v14, v43, -v14\n"
"v_fma_f32 v44, v14, v44, -v14\n"
"v_mfma_f32_16x16x32_bf16 v[72:75], v[36:39], a[220:223], v[72:75]\n"
"buffer_load_dwordx4 a[48:51], v12, s[24:27], s78 offen offset:0 sc1\n"
"v_mfma_f32_16x16x32_bf16 v[76:79], v[60:63], a[216:219], v[76:79]\n"
"v_fma_f32 v45, v14, v45, -v14\n"
"v_fma_f32 v10, v14, v10, -v14\n"
"v_mfma_f32_16x16x32_bf16 v[76:79], v[6:9], a[220:223], v[76:79]\n"
"s_waitcnt lgkmcnt(8)\n"
"buffer_load_dwordx4 a[52:55], v12, s[24:27], s78 offen offset:1024 sc1\n"
"v_mfma_f32_16x16x32_bf16 v[80:83], v[48:51], a[224:227], v[80:83]\n"
"ds_read_b64_tr_b16 a[216:217], v0 offset:20608\n"
"v_fma_f32 v11, v14, v11, -v14\n"
"v_mfma_f32_16x16x32_bf16 v[80:83], v[18:21], a[228:231], v[80:83]\n"
"ds_read_b64_tr_b16 a[218:219], v0 offset:25728\n"
"v_cvt_pk_bf16_f32 v128, v40, v41\n"
"v_mfma_f32_16x16x32_bf16 v[84:87], v[52:55], a[224:227], v[84:87]\n"
"ds_read_b64_tr_b16 a[220:221], v0 offset:30848\n"
"v_cvt_pk_bf16_f32 v129, v42, v43\n"
"v_mfma_f32_16x16x32_bf16 v[84:87], v[22:25], a[228:231], v[84:87]\n"
"ds_read_b64_tr_b16 a[222:223], v0 offset:35968\n"
"v_cvt_pk_bf16_f32 v130, v44, v45\n"
"v_mfma_f32_16x16x32_bf16 v[88:91], v[56:59], a[224:227], v[88:91]\n"
"v_cvt_pk_bf16_f32 v131, v10, v11\n"
"v_accvgpr_read_b32 v40, a68\n"
"v_mfma_f32_16x16x32_bf16 v[88:91], v[36:39], a[228:231], v[88:91]\n"
"buffer_store_dwordx4 v[128:131], v12, s[24:27], s4 offen offset:0 sc1\n"
"v_mfma_f32_16x16x32_bf16 v[92:95], v[60:63], a[224:227], v[92:95]\n"
"v_accvgpr_read_b32 v41, a69\n"
"v_accvgpr_read_b32 v42, a70\n"
"v_mfma_f32_16x16x32_bf16 v[92:95], v[6:9], a[228:231], v[92:95]\n"
"s_waitcnt lgkmcnt(8)\n"
"buffer_load_dwordx4 a[56:59], v12, s[24:27], s78 offen offset:2048 sc1\n"
"v_mfma_f32_16x16x32_bf16 v[96:99], v[48:51], a[240:243], v[96:99]\n"
"ds_read_b64_tr_b16 a[224:225], v1 offset:20608\n"
"v_accvgpr_read_b32 v43, a71\n"
"v_mfma_f32_16x16x32_bf16 v[96:99], v[18:21], a[244:247], v[96:99]\n"
"ds_read_b64_tr_b16 a[226:227], v1 offset:25728\n"
"v_accvgpr_read_b32 v44, a84\n"
"v_mfma_f32_16x16x32_bf16 v[100:103], v[52:55], a[240:243], v[100:103]\n"
"ds_read_b64_tr_b16 a[228:229], v1 offset:30848\n"
"v_accvgpr_read_b32 v45, a85\n"
"v_mfma_f32_16x16x32_bf16 v[100:103], v[22:25], a[244:247], v[100:103]\n"
"ds_read_b64_tr_b16 a[230:231], v1 offset:35968\n"
"v_accvgpr_read_b32 v10, a86\n"
"v_mfma_f32_16x16x32_bf16 v[104:107], v[56:59], a[240:243], v[104:107]\n"
"v_accvgpr_read_b32 v11, a87\n"
"v_fma_f32 v40, v15, v40, -v15\n"
"v_mfma_f32_16x16x32_bf16 v[104:107], v[36:39], a[244:247], v[104:107]\n"
"buffer_load_dwordx4 a[60:63], v12, s[24:27], s78 offen offset:3072 sc1\n"
"s_add_u32 s78, s78, 4096\n"
"v_mfma_f32_16x16x32_bf16 v[108:111], v[60:63], a[240:243], v[108:111]\n"
"v_fma_f32 v41, v15, v41, -v15\n"
"v_fma_f32 v42, v15, v42, -v15\n"
"v_mfma_f32_16x16x32_bf16 v[108:111], v[6:9], a[244:247], v[108:111]\n"
"s_waitcnt lgkmcnt(8)\n"
"v_fma_f32 v43, v15, v43, -v15\n"
"v_fma_f32 v44, v15, v44, -v15\n"
"v_mfma_f32_16x16x32_bf16 v[112:115], v[48:51], a[248:251], v[112:115]\n"
"ds_read_b64_tr_b16 a[240:241], v0 offset:20672\n"
"v_fma_f32 v45, v15, v45, -v15\n"
"v_mfma_f32_16x16x32_bf16 v[112:115], v[18:21], a[252:255], v[112:115]\n"
"ds_read_b64_tr_b16 a[242:243], v0 offset:25792\n"
"v_fma_f32 v10, v15, v10, -v15\n"
"v_mfma_f32_16x16x32_bf16 v[116:119], v[52:55], a[248:251], v[116:119]\n"
"ds_read_b64_tr_b16 a[244:245], v0 offset:30912\n"
"v_fma_f32 v11, v15, v11, -v15\n"
"v_mfma_f32_16x16x32_bf16 v[116:119], v[22:25], a[252:255], v[116:119]\n"
"ds_read_b64_tr_b16 a[246:247], v0 offset:36032\n"
"v_cvt_pk_bf16_f32 v132, v40, v41\n"
"v_mfma_f32_16x16x32_bf16 v[120:123], v[56:59], a[248:251], v[120:123]\n"
"v_cvt_pk_bf16_f32 v133, v42, v43\n"
"v_cvt_pk_bf16_f32 v134, v44, v45\n"
"v_mfma_f32_16x16x32_bf16 v[120:123], v[36:39], a[252:255], v[120:123]\n"
"v_cvt_pk_bf16_f32 v135, v10, v11\n"
"v_accvgpr_read_b32 v40, a72\n"
"v_mfma_f32_16x16x32_bf16 v[124:127], v[60:63], a[248:251], v[124:127]\n"
"s_waitcnt vmcnt(48)\n"
"v_accvgpr_read_b32 v41, a73\n"
"v_accvgpr_read_b32 v42, a74\n"
"v_mfma_f32_16x16x32_bf16 v[124:127], v[6:9], a[252:255], v[124:127]\n"
"s_waitcnt lgkmcnt(8)\n"
"buffer_store_dwordx4 v[132:135], v12, s[24:27], s4 offen offset:1024 sc1\n"
"v_mfma_f32_16x16x32_bf16 v[160:163], v[48:51], a[216:219], v[160:163]\n"
"ds_read_b64_tr_b16 a[248:249], v1 offset:20672\n"
"v_accvgpr_read_b32 v43, a75\n"
"v_mfma_f32_16x16x32_bf16 v[160:163], v[18:21], a[220:223], v[160:163]\n"
"ds_read_b64_tr_b16 a[250:251], v1 offset:25792\n"
"v_accvgpr_read_b32 v44, a88\n"
"v_mfma_f32_16x16x32_bf16 v[164:167], v[52:55], a[216:219], v[164:167]\n"
"ds_read_b64_tr_b16 a[252:253], v1 offset:30912\n"
"v_accvgpr_read_b32 v45, a89\n"
"v_mfma_f32_16x16x32_bf16 v[164:167], v[22:25], a[220:223], v[164:167]\n"
"ds_read_b64_tr_b16 a[254:255], v1 offset:36032\n"
"v_accvgpr_read_b32 v10, a90\n"
"v_mfma_f32_16x16x32_bf16 v[168:171], v[56:59], a[216:219], v[168:171]\n"
"v_accvgpr_read_b32 v11, a91\n"
"v_fma_f32 v40, v16, v40, -v16\n"
"v_mfma_f32_16x16x32_bf16 v[168:171], v[36:39], a[220:223], v[168:171]\n"
"v_fma_f32 v41, v16, v41, -v16\n"
"v_fma_f32 v42, v16, v42, -v16\n"
"v_mfma_f32_16x16x32_bf16 v[172:175], v[60:63], a[216:219], v[172:175]\n"
"v_fma_f32 v43, v16, v43, -v16\n"
"v_fma_f32 v44, v16, v44, -v16\n"
"v_mfma_f32_16x16x32_bf16 v[172:175], v[6:9], a[220:223], v[172:175]\n"
"s_waitcnt lgkmcnt(8)\n"
"v_fma_f32 v45, v16, v45, -v16\n"
"v_fma_f32 v10, v16, v10, -v16\n"
"v_mfma_f32_16x16x32_bf16 v[176:179], v[48:51], a[224:227], v[176:179]\n"
"ds_read_b64_tr_b16 a[216:217], v0 offset:20736\n"
"v_fma_f32 v11, v16, v11, -v16\n"
"v_mfma_f32_16x16x32_bf16 v[176:179], v[18:21], a[228:231], v[176:179]\n"
"ds_read_b64_tr_b16 a[218:219], v0 offset:25856\n"
"v_cvt_pk_bf16_f32 v136, v40, v41\n"
"v_mfma_f32_16x16x32_bf16 v[180:183], v[52:55], a[224:227], v[180:183]\n"
"ds_read_b64_tr_b16 a[220:221], v0 offset:30976\n"
"v_cvt_pk_bf16_f32 v137, v42, v43\n"
"v_mfma_f32_16x16x32_bf16 v[180:183], v[22:25], a[228:231], v[180:183]\n"
"ds_read_b64_tr_b16 a[222:223], v0 offset:36096\n"
"v_cvt_pk_bf16_f32 v138, v44, v45\n"
"v_mfma_f32_16x16x32_bf16 v[184:187], v[56:59], a[224:227], v[184:187]\n"
"v_cvt_pk_bf16_f32 v139, v10, v11\n"
"v_accvgpr_read_b32 v40, a76\n"
"v_mfma_f32_16x16x32_bf16 v[184:187], v[36:39], a[228:231], v[184:187]\n"
"buffer_store_dwordx4 v[136:139], v12, s[24:27], s4 offen offset:2048 sc1\n"
"v_mfma_f32_16x16x32_bf16 v[188:191], v[60:63], a[224:227], v[188:191]\n"
"v_accvgpr_read_b32 v41, a77\n"
"v_accvgpr_read_b32 v42, a78\n"
"v_mfma_f32_16x16x32_bf16 v[188:191], v[6:9], a[228:231], v[188:191]\n"
"s_waitcnt lgkmcnt(8)\n"
"v_accvgpr_read_b32 v43, a79\n"
"v_accvgpr_read_b32 v44, a92\n"
"v_mfma_f32_16x16x32_bf16 v[192:195], v[48:51], a[240:243], v[192:195]\n"
"ds_read_b64_tr_b16 a[224:225], v1 offset:20736\n"
"v_accvgpr_read_b32 v45, a93\n"
"v_mfma_f32_16x16x32_bf16 v[192:195], v[18:21], a[244:247], v[192:195]\n"
"ds_read_b64_tr_b16 a[226:227], v1 offset:25856\n"
"v_accvgpr_read_b32 v10, a94\n"
"v_mfma_f32_16x16x32_bf16 v[196:199], v[52:55], a[240:243], v[196:199]\n"
"ds_read_b64_tr_b16 a[228:229], v1 offset:30976\n"
"v_accvgpr_read_b32 v11, a95\n"
"v_mfma_f32_16x16x32_bf16 v[196:199], v[22:25], a[244:247], v[196:199]\n"
"ds_read_b64_tr_b16 a[230:231], v1 offset:36096\n"
"v_fma_f32 v40, v17, v40, -v17\n"
"v_mfma_f32_16x16x32_bf16 v[200:203], v[56:59], a[240:243], v[200:203]\n"
"v_fma_f32 v41, v17, v41, -v17\n"
"v_fma_f32 v42, v17, v42, -v17\n"
"L_pg17:\n"
"s_cbranch_execz L_bk4\n"
"v_mfma_f32_16x16x32_bf16 v[200:203], v[36:39], a[244:247], v[200:203]\n"
"v_fma_f32 v43, v17, v43, -v17\n"
"v_fma_f32 v44, v17, v44, -v17\n"
"v_mfma_f32_16x16x32_bf16 v[204:207], v[60:63], a[240:243], v[204:207]\n"
"v_fma_f32 v45, v17, v45, -v17\n"
"v_fma_f32 v10, v17, v10, -v17\n"
"v_mfma_f32_16x16x32_bf16 v[204:207], v[6:9], a[244:247], v[204:207]\n"
"s_waitcnt lgkmcnt(8)\n"
"v_fma_f32 v11, v17, v11, -v17\n"
"v_cvt_pk_bf16_f32 v140, v40, v41\n"
"v_mfma_f32_16x16x32_bf16 v[208:211], v[48:51], a[248:251], v[208:211]\n"
"ds_read_b64_tr_b16 a[240:241], v28 offset:40960\n"
"v_cvt_pk_bf16_f32 v141, v42, v43\n"
"v_mfma_f32_16x16x32_bf16 v[208:211], v[18:21], a[252:255], v[208:211]\n"
"ds_read_b64_tr_b16 a[242:243], v28 offset:46080\n"
"v_cvt_pk_bf16_f32 v142, v44, v45\n"
"v_mfma_f32_16x16x32_bf16 v[212:215], v[52:55], a[248:251], v[212:215]\n"
"ds_read_b64_tr_b16 a[244:245], v28 offset:51200\n"
"v_cvt_pk_bf16_f32 v143, v10, v11\n"
"v_mfma_f32_16x16x32_bf16 v[212:215], v[22:25], a[252:255], v[212:215]\n"
"ds_read_b64_tr_b16 a[246:247], v28 offset:56320\n"
"v_accvgpr_read_b32 v40, a96\n"
"v_mfma_f32_16x16x32_bf16 v[216:219], v[56:59], a[248:251], v[216:219]\n"
"v_accvgpr_read_b32 v41, a97\n"
"v_accvgpr_read_b32 v42, a98\n"
"v_mfma_f32_16x16x32_bf16 v[216:219], v[36:39], a[252:255], v[216:219]\n"
"buffer_store_dwordx4 v[140:143], v12, s[24:27], s4 offen offset:3072 sc1\n"
"v_mfma_f32_16x16x32_bf16 v[220:223], v[60:63], a[248:251], v[220:223]\n"
"v_accvgpr_read_b32 v43, a99\n"
"v_accvgpr_read_b32 v44, a112\n"
"v_mfma_f32_16x16x32_bf16 v[220:223], v[6:9], a[252:255], v[220:223]\n"
"s_waitcnt lgkmcnt(8)\n"
"v_accvgpr_read_b32 v45, a113\n"
"v_accvgpr_read_b32 v10, a114\n"
"v_mfma_f32_16x16x32_bf16 v[224:227], v[48:51], a[216:219], v[224:227]\n"
"ds_read_b64_tr_b16 a[248:249], v29 offset:40960\n"
"v_accvgpr_read_b32 v11, a115\n"
"v_mfma_f32_16x16x32_bf16 v[224:227], v[18:21], a[220:223], v[224:227]\n"
"ds_read_b64_tr_b16 a[250:251], v29 offset:46080\n"
"v_fma_f32 v40, v14, v40, -v14\n"
"v_mfma_f32_16x16x32_bf16 v[228:231], v[52:55], a[216:219], v[228:231]\n"
"ds_read_b64_tr_b16 a[252:253], v29 offset:51200\n"
"v_fma_f32 v41, v14, v41, -v14\n"
"v_mfma_f32_16x16x32_bf16 v[228:231], v[22:25], a[220:223], v[228:231]\n"
"ds_read_b64_tr_b16 a[254:255], v29 offset:56320\n"
"v_fma_f32 v42, v14, v42, -v14\n"
"v_mfma_f32_16x16x32_bf16 v[232:235], v[56:59], a[216:219], v[232:235]\n"
"v_fma_f32 v43, v14, v43, -v14\n"
"v_fma_f32 v44, v14, v44, -v14\n"
"v_mfma_f32_16x16x32_bf16 v[232:235], v[36:39], a[220:223], v[232:235]\n"
"v_fma_f32 v45, v14, v45, -v14\n"
"v_fma_f32 v10, v14, v10, -v14\n"
"v_mfma_f32_16x16x32_bf16 v[236:239], v[60:63], a[216:219], v[236:239]\n"
"v_fma_f32 v11, v14, v11, -v14\n"
"v_cvt_pk_bf16_f32 v144, v40, v41\n"
"v_mfma_f32_16x16x32_bf16 v[236:239], v[6:9], a[220:223], v[236:239]\n"
"s_waitcnt lgkmcnt(8)\n"
"v_cvt_pk_bf16_f32 v145, v42, v43\n"
"v_cvt_pk_bf16_f32 v146, v44, v45\n"
"v_mfma_f32_16x16x32_bf16 v[240:243], v[48:51], a[224:227], v[240:243]\n"
"ds_read_b64_tr_b16 a[216:217], v28 offset:41024\n"
"v_cvt_pk_bf16_f32 v147, v10, v11\n"
"v_mfma_f32_16x16x32_bf16 v[240:243], v[18:21], a[228:231], v[240:243]\n"
"ds_read_b64_tr_b16 a[218:219], v28 offset:46144\n"
"v_accvgpr_read_b32 v40, a100\n"
"v_mfma_f32_16x16x32_bf16 v[244:247], v[52:55], a[224:227], v[244:247]\n"
"ds_read_b64_tr_b16 a[220:221], v28 offset:51264\n"
"v_accvgpr_read_b32 v41, a101\n"
"v_mfma_f32_16x16x32_bf16 v[244:247], v[22:25], a[228:231], v[244:247]\n"
"s_waitcnt vmcnt(39)\n"
"s_waitcnt vmcnt(40)\n"
"ds_read_b64_tr_b16 a[222:223], v28 offset:56384\n"
"v_accvgpr_read_b32 v42, a102\n"
"v_mfma_f32_16x16x32_bf16 v[248:251], v[56:59], a[224:227], v[248:251]\n"
"s_barrier\n"
"v_accvgpr_read_b32 v43, a103\n"
"v_accvgpr_read_b32 v44, a116\n"
"v_mfma_f32_16x16x32_bf16 v[248:251], v[36:39], a[228:231], v[248:251]\n"
"buffer_store_dwordx4 v[144:147], v12, s[24:27], s5 offen offset:0 sc1\n"
"v_mfma_f32_16x16x32_bf16 v[252:255], v[60:63], a[224:227], v[252:255]\n"
"v_accvgpr_read_b32 v45, a117\n"
"v_accvgpr_read_b32 v10, a118\n"
"v_mfma_f32_16x16x32_bf16 v[252:255], v[6:9], a[228:231], v[252:255]\n"
"s_waitcnt lgkmcnt(8)\n"
"s_waitcnt vmcnt(18)\n"
"v_accvgpr_read_b32 v11, a119\n"
"v_fma_f32 v40, v15, v40, -v15\n"
"v_mfma_f32_16x16x32_bf16 v[64:67], a[0:3], a[240:243], v[64:67]\n"
"ds_read_b64_tr_b16 a[224:225], v29 offset:41024\n"
"v_fma_f32 v41, v15, v41, -v15\n"
"v_mfma_f32_16x16x32_bf16 v[64:67], a[16:19], a[244:247], v[64:67]\n"
"ds_read_b64_tr_b16 a[226:227], v29 offset:46144\n"
"v_fma_f32 v42, v15, v42, -v15\n"
"v_mfma_f32_16x16x32_bf16 v[68:71], a[4:7], a[240:243], v[68:71]\n"
"ds_read_b64_tr_b16 a[228:229], v29 offset:51264\n"
"v_fma_f32 v43, v15, v43, -v15\n"
"v_mfma_f32_16x16x32_bf16 v[68:71], a[20:23], a[244:247], v[68:71]\n"
"ds_read_b64_tr_b16 a[230:231], v29 offset:56384\n"
"v_fma_f32 v44, v15, v44, -v15\n"
"v_mfma_f32_16x16x32_bf16 v[72:75], a[8:11], a[240:243], v[72:75]\n"
"v_fma_f32 v45, v15, v45, -v15\n"
"v_fma_f32 v10, v15, v10, -v15\n"
"v_mfma_f32_16x16x32_bf16 v[72:75], a[24:27], a[244:247], v[72:75]\n"
"buffer_load_dwordx4 a[64:67], v12, s[24:27], s78 offen offset:0 sc1\n"
"v_mfma_f32_16x16x32_bf16 v[76:79], a[12:15], a[240:243], v[76:79]\n"
"v_fma_f32 v11, v15, v11, -v15\n"
"v_cvt_pk_bf16_f32 v148, v40, v41\n"
"v_mfma_f32_16x16x32_bf16 v[76:79], a[28:31], a[244:247], v[76:79]\n"
"s_waitcnt lgkmcnt(8)\n"
"buffer_load_dwordx4 a[68:71], v12, s[24:27], s78 offen offset:1024 sc1\n"
"v_mfma_f32_16x16x32_bf16 v[80:83], a[0:3], a[248:251], v[80:83]\n"
"ds_read_b64_tr_b16 a[240:241], v28 offset:41088\n"
"v_cvt_pk_bf16_f32 v149, v42, v43\n"
"v_mfma_f32_16x16x32_bf16 v[80:83], a[16:19], a[252:255], v[80:83]\n"
"ds_read_b64_tr_b16 a[242:243], v28 offset:46208\n"
"v_cvt_pk_bf16_f32 v150, v44, v45\n"
"v_mfma_f32_16x16x32_bf16 v[84:87], a[4:7], a[248:251], v[84:87]\n"
"ds_read_b64_tr_b16 a[244:245], v28 offset:51328\n"
"v_cvt_pk_bf16_f32 v151, v10, v11\n"
"v_mfma_f32_16x16x32_bf16 v[84:87], a[20:23], a[252:255], v[84:87]\n"
"ds_read_b64_tr_b16 a[246:247], v28 offset:56448\n"
"v_accvgpr_read_b32 v40, a104\n"
"v_mfma_f32_16x16x32_bf16 v[88:91], a[8:11], a[248:251], v[88:91]\n"
"v_accvgpr_read_b32 v41, a105\n"
"v_accvgpr_read_b32 v42, a106\n"
"v_mfma_f32_16x16x32_bf16 v[88:91], a[24:27], a[252:255], v[88:91]\n"
"buffer_store_dwordx4 v[148:151], v12, s[24:27], s5 offen offset:1024 sc1\n"
"v_mfma_f32_16x16x32_bf16 v[92:95], a[12:15], a[248:251], v[92:95]\n"
"v_accvgpr_read_b32 v43, a107\n"
"v_accvgpr_read_b32 v44, a120\n"
"v_mfma_f32_16x16x32_bf16 v[92:95], a[28:31], a[252:255], v[92:95]\n"
"s_waitcnt lgkmcnt(8)\n"
"buffer_load_dwordx4 a[72:75], v12, s[24:27], s78 offen offset:2048 sc1\n"
"v_mfma_f32_16x16x32_bf16 v[96:99], a[0:3], a[216:219], v[96:99]\n"
"ds_read_b64_tr_b16 a[248:249], v29 offset:41088\n"
"v_accvgpr_read_b32 v45, a121\n"
"v_mfma_f32_16x16x32_bf16 v[96:99], a[16:19], a[220:223], v[96:99]\n"
"ds_read_b64_tr_b16 a[250:251], v29 offset:46208\n"
"v_accvgpr_read_b32 v10, a122\n"
"v_mfma_f32_16x16x32_bf16 v[100:103], a[4:7], a[216:219], v[100:103]\n"
"ds_read_b64_tr_b16 a[252:253], v29 offset:51328\n"
"v_accvgpr_read_b32 v11, a123\n"
"v_mfma_f32_16x16x32_bf16 v[100:103], a[20:23], a[220:223], v[100:103]\n"
"ds_read_b64_tr_b16 a[254:255], v29 offset:56448\n"
"v_fma_f32 v40, v16, v40, -v16\n"
"v_mfma_f32_16x16x32_bf16 v[104:107], a[8:11], a[216:219], v[104:107]\n"
"v_fma_f32 v41, v16, v41, -v16\n"
"v_fma_f32 v42, v16, v42, -v16\n"
"v_mfma_f32_16x16x32_bf16 v[104:107], a[24:27], a[220:223], v[104:107]\n"
"buffer_load_dwordx4 a[76:79], v12, s[24:27], s78 offen offset:3072 sc1\n"
"s_add_u32 s78, s78, 4096\n"
"v_mfma_f32_16x16x32_bf16 v[108:111], a[12:15], a[216:219], v[108:111]\n"
"v_fma_f32 v43, v16, v43, -v16\n"
"v_fma_f32 v44, v16, v44, -v16\n"
"v_mfma_f32_16x16x32_bf16 v[108:111], a[28:31], a[220:223], v[108:111]\n"
"s_waitcnt lgkmcnt(8)\n"
"v_fma_f32 v45, v16, v45, -v16\n"
"v_fma_f32 v10, v16, v10, -v16\n"
"v_mfma_f32_16x16x32_bf16 v[112:115], a[0:3], a[224:227], v[112:115]\n"
"ds_read_b64_tr_b16 a[216:217], v28 offset:41152\n"
"v_fma_f32 v11, v16, v11, -v16\n"
"v_mfma_f32_16x16x32_bf16 v[112:115], a[16:19], a[228:231], v[112:115]\n"
"ds_read_b64_tr_b16 a[218:219], v28 offset:46272\n"
"v_cvt_pk_bf16_f32 v152, v40, v41\n"
"v_mfma_f32_16x16x32_bf16 v[116:119], a[4:7], a[224:227], v[116:119]\n"
"ds_read_b64_tr_b16 a[220:221], v28 offset:51392\n"
"v_cvt_pk_bf16_f32 v153, v42, v43\n"
"v_mfma_f32_16x16x32_bf16 v[116:119], a[20:23], a[228:231], v[116:119]\n"
"ds_read_b64_tr_b16 a[222:223], v28 offset:56512\n"
"v_cvt_pk_bf16_f32 v154, v44, v45\n"
"v_mfma_f32_16x16x32_bf16 v[120:123], a[8:11], a[224:227], v[120:123]\n"
"v_cvt_pk_bf16_f32 v155, v10, v11\n"
"v_accvgpr_read_b32 v40, a108\n"
"v_mfma_f32_16x16x32_bf16 v[120:123], a[24:27], a[228:231], v[120:123]\n"
"buffer_store_dwordx4 v[152:155], v12, s[24:27], s5 offen offset:2048 sc1\n"
"v_mfma_f32_16x16x32_bf16 v[124:127], a[12:15], a[224:227], v[124:127]\n"
"s_waitcnt vmcnt(48)\n"
"v_accvgpr_read_b32 v41, a109\n"
"v_accvgpr_read_b32 v42, a110\n"
"v_mfma_f32_16x16x32_bf16 v[124:127], a[28:31], a[228:231], v[124:127]\n"
"s_waitcnt lgkmcnt(8)\n"
"v_accvgpr_read_b32 v43, a111\n"
"v_accvgpr_read_b32 v44, a124\n"
"v_mfma_f32_16x16x32_bf16 v[160:163], a[0:3], a[240:243], v[160:163]\n"
"ds_read_b64_tr_b16 a[224:225], v29 offset:41152\n"
"v_accvgpr_read_b32 v45, a125\n"
"v_mfma_f32_16x16x32_bf16 v[160:163], a[16:19], a[244:247], v[160:163]\n"
"ds_read_b64_tr_b16 a[226:227], v29 offset:46272\n"
"v_accvgpr_read_b32 v10, a126\n"
"v_mfma_f32_16x16x32_bf16 v[164:167], a[4:7], a[240:243], v[164:167]\n"
"ds_read_b64_tr_b16 a[228:229], v29 offset:51392\n"
"v_accvgpr_read_b32 v11, a127\n"
"v_mfma_f32_16x16x32_bf16 v[164:167], a[20:23], a[244:247], v[164:167]\n"
"ds_read_b64_tr_b16 a[230:231], v29 offset:56512\n"
"v_fma_f32 v40, v17, v40, -v17\n"
"v_mfma_f32_16x16x32_bf16 v[168:171], a[8:11], a[240:243], v[168:171]\n"
"v_fma_f32 v41, v17, v41, -v17\n"
"v_fma_f32 v42, v17, v42, -v17\n"
"v_mfma_f32_16x16x32_bf16 v[168:171], a[24:27], a[244:247], v[168:171]\n"
"v_fma_f32 v43, v17, v43, -v17\n"
"v_fma_f32 v44, v17, v44, -v17\n"
"v_mfma_f32_16x16x32_bf16 v[172:175], a[12:15], a[240:243], v[172:175]\n"
"v_fma_f32 v45, v17, v45, -v17\n"
"v_fma_f32 v10, v17, v10, -v17\n"
"v_mfma_f32_16x16x32_bf16 v[172:175], a[28:31], a[244:247], v[172:175]\n"
"s_waitcnt lgkmcnt(8)\n"
"v_fma_f32 v11, v17, v11, -v17\n"
"v_cvt_pk_bf16_f32 v156, v40, v41\n"
"v_mfma_f32_16x16x32_bf16 v[176:179], a[0:3], a[248:251], v[176:179]\n"
"ds_read_b64_tr_b16 a[240:241], v28 offset:41216\n"
"v_cvt_pk_bf16_f32 v157, v42, v43\n"
"v_mfma_f32_16x16x32_bf16 v[176:179], a[16:19], a[252:255], v[176:179]\n"
"ds_read_b64_tr_b16 a[242:243], v28 offset:46336\n"
"v_cvt_pk_bf16_f32 v158, v44, v45\n"
"v_mfma_f32_16x16x32_bf16 v[180:183], a[4:7], a[248:251], v[180:183]\n"
"ds_read_b64_tr_b16 a[244:245], v28 offset:51456\n"
"v_cvt_pk_bf16_f32 v159, v10, v11\n"
"v_mfma_f32_16x16x32_bf16 v[180:183], a[20:23], a[252:255], v[180:183]\n"
"ds_read_b64_tr_b16 a[246:247], v28 offset:56576\n"
"v_accvgpr_read_b32 v40, a128\n"
"v_mfma_f32_16x16x32_bf16 v[184:187], a[8:11], a[248:251], v[184:187]\n"
"v_accvgpr_read_b32 v41, a129\n"
"v_accvgpr_read_b32 v42, a130\n"
"v_mfma_f32_16x16x32_bf16 v[184:187], a[24:27], a[252:255], v[184:187]\n"
"buffer_store_dwordx4 v[156:159], v12, s[24:27], s5 offen offset:3072 sc1\n"
"v_mfma_f32_16x16x32_bf16 v[188:191], a[12:15], a[248:251], v[188:191]\n"
"v_accvgpr_read_b32 v43, a131\n"
"v_accvgpr_read_b32 v44, a144\n"
"v_mfma_f32_16x16x32_bf16 v[188:191], a[28:31], a[252:255], v[188:191]\n"
"s_waitcnt lgkmcnt(8)\n"
"v_accvgpr_read_b32 v45, a145\n"
"v_accvgpr_read_b32 v10, a146\n"
"v_mfma_f32_16x16x32_bf16 v[192:195], a[0:3], a[216:219], v[192:195]\n"
"ds_read_b64_tr_b16 a[248:249], v29 offset:41216\n"
"v_accvgpr_read_b32 v11, a147\n"
"v_mfma_f32_16x16x32_bf16 v[192:195], a[16:19], a[220:223], v[192:195]\n"
"ds_read_b64_tr_b16 a[250:251], v29 offset:46336\n"
"v_fma_f32 v40, v14, v40, -v14\n"
"v_mfma_f32_16x16x32_bf16 v[196:199], a[4:7], a[216:219], v[196:199]\n"
"ds_read_b64_tr_b16 a[252:253], v29 offset:51456\n"
"v_fma_f32 v41, v14, v41, -v14\n"
"v_mfma_f32_16x16x32_bf16 v[196:199], a[20:23], a[220:223], v[196:199]\n"
"ds_read_b64_tr_b16 a[254:255], v29 offset:56576\n"
"v_fma_f32 v42, v14, v42, -v14\n"
"v_mfma_f32_16x16x32_bf16 v[200:203], a[8:11], a[216:219], v[200:203]\n"
"v_fma_f32 v43, v14, v43, -v14\n"
"v_fma_f32 v44, v14, v44, -v14\n"
"v_mfma_f32_16x16x32_bf16 v[200:203], a[24:27], a[220:223], v[200:203]\n"
"v_fma_f32 v45, v14, v45, -v14\n"
"v_fma_f32 v10, v14, v10, -v14\n"
"v_mfma_f32_16x16x32_bf16 v[204:207], a[12:15], a[216:219], v[204:207]\n"
"v_fma_f32 v11, v14, v11, -v14\n"
"v_cvt_pk_bf16_f32 v48, v40, v41\n"
"v_mfma_f32_16x16x32_bf16 v[204:207], a[28:31], a[220:223], v[204:207]\n"
"s_waitcnt lgkmcnt(8)\n"
"v_cvt_pk_bf16_f32 v49, v42, v43\n"
"v_cvt_pk_bf16_f32 v50, v44, v45\n"
"v_mfma_f32_16x16x32_bf16 v[208:211], a[0:3], a[224:227], v[208:211]\n"
"ds_read_b64_tr_b16 a[216:217], v2 offset:40960\n"
"v_cvt_pk_bf16_f32 v51, v10, v11\n"
"v_mfma_f32_16x16x32_bf16 v[208:211], a[16:19], a[228:231], v[208:211]\n"
"ds_read_b64_tr_b16 a[218:219], v2 offset:46080\n"
"v_accvgpr_read_b32 v40, a132\n"
"v_mfma_f32_16x16x32_bf16 v[212:215], a[4:7], a[224:227], v[212:215]\n"
"ds_read_b64_tr_b16 a[220:221], v2 offset:51200\n"
"v_accvgpr_read_b32 v41, a133\n"
"v_mfma_f32_16x16x32_bf16 v[212:215], a[20:23], a[228:231], v[212:215]\n"
"ds_read_b64_tr_b16 a[222:223], v2 offset:56320\n"
"v_accvgpr_read_b32 v42, a134\n"
"v_mfma_f32_16x16x32_bf16 v[216:219], a[8:11], a[224:227], v[216:219]\n"
"v_accvgpr_read_b32 v43, a135\n"
"v_accvgpr_read_b32 v44, a148\n"
"v_mfma_f32_16x16x32_bf16 v[216:219], a[24:27], a[228:231], v[216:219]\n"
"buffer_store_dwordx4 v[48:51], v12, s[24:27], s6 offen offset:0 sc1\n"
"v_mfma_f32_16x16x32_bf16 v[220:223], a[12:15], a[224:227], v[220:223]\n"
"v_accvgpr_read_b32 v45, a149\n"
"v_accvgpr_read_b32 v10, a150\n"
"v_mfma_f32_16x16x32_bf16 v[220:223], a[28:31], a[228:231], v[220:223]\n"
"s_waitcnt lgkmcnt(8)\n"
"v_accvgpr_read_b32 v11, a151\n"
"v_fma_f32 v40, v15, v40, -v15\n"
"v_mfma_f32_16x16x32_bf16 v[224:227], a[0:3], a[240:243], v[224:227]\n"
"ds_read_b64_tr_b16 a[224:225], v3 offset:40960\n"
"v_fma_f32 v41, v15, v41, -v15\n"
"v_mfma_f32_16x16x32_bf16 v[224:227], a[16:19], a[244:247], v[224:227]\n"
"ds_read_b64_tr_b16 a[226:227], v3 offset:46080\n"
"v_fma_f32 v42, v15, v42, -v15\n"
"v_mfma_f32_16x16x32_bf16 v[228:231], a[4:7], a[240:243], v[228:231]\n"
"ds_read_b64_tr_b16 a[228:229], v3 offset:51200\n"
"v_fma_f32 v43, v15, v43, -v15\n"
"v_mfma_f32_16x16x32_bf16 v[228:231], a[20:23], a[244:247], v[228:231]\n"
"ds_read_b64_tr_b16 a[230:231], v3 offset:56320\n"
"v_fma_f32 v44, v15, v44, -v15\n"
"v_mfma_f32_16x16x32_bf16 v[232:235], a[8:11], a[240:243], v[232:235]\n"
"v_fma_f32 v45, v15, v45, -v15\n"
"v_fma_f32 v10, v15, v10, -v15\n"
"v_mfma_f32_16x16x32_bf16 v[232:235], a[24:27], a[244:247], v[232:235]\n"
"v_fma_f32 v11, v15, v11, -v15\n"
"v_cvt_pk_bf16_f32 v52, v40, v41\n"
"v_mfma_f32_16x16x32_bf16 v[236:239], a[12:15], a[240:243], v[236:239]\n"
"v_cvt_pk_bf16_f32 v53, v42, v43\n"
"v_cvt_pk_bf16_f32 v54, v44, v45\n"
"v_mfma_f32_16x16x32_bf16 v[236:239], a[28:31], a[244:247], v[236:239]\n"
"s_waitcnt lgkmcnt(8)\n"
"v_cvt_pk_bf16_f32 v55, v10, v11\n"
"v_accvgpr_read_b32 v40, a136\n"
"v_mfma_f32_16x16x32_bf16 v[240:243], a[0:3], a[248:251], v[240:243]\n"
"ds_read_b64_tr_b16 a[240:241], v2 offset:41024\n"
"v_accvgpr_read_b32 v41, a137\n"
"v_mfma_f32_16x16x32_bf16 v[240:243], a[16:19], a[252:255], v[240:243]\n"
"ds_read_b64_tr_b16 a[242:243], v2 offset:46144\n"
"v_accvgpr_read_b32 v42, a138\n"
"v_mfma_f32_16x16x32_bf16 v[244:247], a[4:7], a[248:251], v[244:247]\n"
"ds_read_b64_tr_b16 a[244:245], v2 offset:51264\n"
"v_accvgpr_read_b32 v43, a139\n"
"v_mfma_f32_16x16x32_bf16 v[244:247], a[20:23], a[252:255], v[244:247]\n"
"s_waitcnt vmcnt(42)\n"
"s_waitcnt vmcnt(40)\n"
"ds_read_b64_tr_b16 a[246:247], v2 offset:56384\n"
"v_accvgpr_read_b32 v44, a152\n"
"v_mfma_f32_16x16x32_bf16 v[248:251], a[8:11], a[248:251], v[248:251]\n"
"s_barrier\n"
"v_accvgpr_read_b32 v45, a153\n"
"v_accvgpr_read_b32 v10, a154\n"
"v_mfma_f32_16x16x32_bf16 v[248:251], a[24:27], a[252:255], v[248:251]\n"
"buffer_store_dwordx4 v[52:55], v12, s[24:27], s6 offen offset:1024 sc1\n"
"v_mfma_f32_16x16x32_bf16 v[252:255], a[12:15], a[248:251], v[252:255]\n"
"v_accvgpr_read_b32 v11, a155\n"
"v_fma_f32 v40, v16, v40, -v16\n"
"v_mfma_f32_16x16x32_bf16 v[252:255], a[28:31], a[252:255], v[252:255]\n"
"s_waitcnt lgkmcnt(8)\n"
"v_fma_f32 v41, v16, v41, -v16\n"
"v_fma_f32 v42, v16, v42, -v16\n"
"v_mfma_f32_16x16x32_bf16 v[64:67], v[128:131], a[216:219], v[64:67]\n"
"ds_read_b64_tr_b16 a[248:249], v3 offset:41024\n"
"v_fma_f32 v43, v16, v43, -v16\n"
"v_mfma_f32_16x16x32_bf16 v[64:67], v[144:147], a[220:223], v[64:67]\n"
"ds_read_b64_tr_b16 a[250:251], v3 offset:46144\n"
"v_fma_f32 v44, v16, v44, -v16\n"
"v_mfma_f32_16x16x32_bf16 v[68:71], v[132:135], a[216:219], v[68:71]\n"
"ds_read_b64_tr_b16 a[252:253], v3 offset:51264\n"
"v_fma_f32 v45, v16, v45, -v16\n"
"v_mfma_f32_16x16x32_bf16 v[68:71], v[148:151], a[220:223], v[68:71]\n"
"ds_read_b64_tr_b16 a[254:255], v3 offset:56384\n"
"v_fma_f32 v10, v16, v10, -v16\n"
"v_mfma_f32_16x16x32_bf16 v[72:75], v[136:139], a[216:219], v[72:75]\n"
"v_fma_f32 v11, v16, v11, -v16\n"
"v_cvt_pk_bf16_f32 v56, v40, v41\n"
"v_mfma_f32_16x16x32_bf16 v[72:75], v[152:155], a[220:223], v[72:75]\n"
"buffer_load_dwordx4 a[80:83], v12, s[24:27], s78 offen offset:0 sc1\n"
"v_mfma_f32_16x16x32_bf16 v[76:79], v[140:143], a[216:219], v[76:79]\n"
"s_add_u32 m0, s95, 0\n"
"v_cvt_pk_bf16_f32 v57, v42, v43\n"
"v_cvt_pk_bf16_f32 v58, v44, v45\n"
"v_mfma_f32_16x16x32_bf16 v[76:79], v[156:159], a[220:223], v[76:79]\n"
"s_waitcnt lgkmcnt(8)\n"
"buffer_load_dwordx4 v30, s[20:23], s40 offen offset:0 lds\n"
"v_mfma_f32_16x16x32_bf16 v[80:83], v[128:131], a[224:227], v[80:83]\n"
"ds_read_b64_tr_b16 a[216:217], v2 offset:41088\n"
"v_cvt_pk_bf16_f32 v59, v10, v11\n"
"v_mfma_f32_16x16x32_bf16 v[80:83], v[144:147], a[228:231], v[80:83]\n"
"ds_read_b64_tr_b16 a[218:219], v2 offset:46208\n"
"v_accvgpr_read_b32 v40, a140\n"
"v_mfma_f32_16x16x32_bf16 v[84:87], v[132:135], a[224:227], v[84:87]\n"
"ds_read_b64_tr_b16 a[220:221], v2 offset:51328\n"
"v_accvgpr_read_b32 v41, a141\n"
"v_mfma_f32_16x16x32_bf16 v[84:87], v[148:151], a[228:231], v[84:87]\n"
"ds_read_b64_tr_b16 a[222:223], v2 offset:56448\n"
"v_accvgpr_read_b32 v42, a142\n"
"v_mfma_f32_16x16x32_bf16 v[88:91], v[136:139], a[224:227], v[88:91]\n"
"v_accvgpr_read_b32 v43, a143\n"
"v_accvgpr_read_b32 v44, a156\n"
"v_mfma_f32_16x16x32_bf16 v[88:91], v[152:155], a[228:231], v[88:91]\n"
"buffer_store_dwordx4 v[56:59], v12, s[24:27], s6 offen offset:2048 sc1\n"
"v_mfma_f32_16x16x32_bf16 v[92:95], v[140:143], a[224:227], v[92:95]\n"
"v_accvgpr_read_b32 v45, a157\n"
"v_accvgpr_read_b32 v10, a158\n"
"v_mfma_f32_16x16x32_bf16 v[92:95], v[156:159], a[228:231], v[92:95]\n"
"s_waitcnt lgkmcnt(8)\n"
"buffer_load_dwordx4 v31, s[20:23], s40 offen offset:1024 lds\n"
"v_mfma_f32_16x16x32_bf16 v[96:99], v[128:131], a[240:243], v[96:99]\n"
"ds_read_b64_tr_b16 a[224:225], v3 offset:41088\n"
"v_accvgpr_read_b32 v11, a159\n"
"v_mfma_f32_16x16x32_bf16 v[96:99], v[144:147], a[244:247], v[96:99]\n"
"ds_read_b64_tr_b16 a[226:227], v3 offset:46208\n"
"v_fma_f32 v40, v17, v40, -v17\n"
"v_mfma_f32_16x16x32_bf16 v[100:103], v[132:135], a[240:243], v[100:103]\n"
"ds_read_b64_tr_b16 a[228:229], v3 offset:51328\n"
"v_fma_f32 v41, v17, v41, -v17\n"
"v_mfma_f32_16x16x32_bf16 v[100:103], v[148:151], a[244:247], v[100:103]\n"
"ds_read_b64_tr_b16 a[230:231], v3 offset:56448\n"
"v_fma_f32 v42, v17, v42, -v17\n"
"v_mfma_f32_16x16x32_bf16 v[104:107], v[136:139], a[240:243], v[104:107]\n"
"v_fma_f32 v43, v17, v43, -v17\n"
"v_fma_f32 v44, v17, v44, -v17\n"
"v_mfma_f32_16x16x32_bf16 v[104:107], v[152:155], a[244:247], v[104:107]\n"
"buffer_load_dwordx4 a[84:87], v12, s[24:27], s78 offen offset:1024 sc1\n"
"v_mfma_f32_16x16x32_bf16 v[108:111], v[140:143], a[240:243], v[108:111]\n"
"v_fma_f32 v45, v17, v45, -v17\n"
"v_fma_f32 v10, v17, v10, -v17\n"
"v_mfma_f32_16x16x32_bf16 v[108:111], v[156:159], a[244:247], v[108:111]\n"
"s_waitcnt lgkmcnt(8)\n"
"buffer_load_dwordx4 v32, s[20:23], s40 offen offset:2048 lds\n"
"v_mfma_f32_16x16x32_bf16 v[112:115], v[128:131], a[248:251], v[112:115]\n"
"ds_read_b64_tr_b16 a[240:241], v2 offset:41152\n"
"v_fma_f32 v11, v17, v11, -v17\n"
"v_mfma_f32_16x16x32_bf16 v[112:115], v[144:147], a[252:255], v[112:115]\n"
"ds_read_b64_tr_b16 a[242:243], v2 offset:46272\n"
"v_cvt_pk_bf16_f32 v60, v40, v41\n"
"v_mfma_f32_16x16x32_bf16 v[116:119], v[132:135], a[248:251], v[116:119]\n"
"ds_read_b64_tr_b16 a[244:245], v2 offset:51392\n"
"v_cvt_pk_bf16_f32 v61, v42, v43\n"
"v_mfma_f32_16x16x32_bf16 v[116:119], v[148:151], a[252:255], v[116:119]\n"
"ds_read_b64_tr_b16 a[246:247], v2 offset:56512\n"
"v_cvt_pk_bf16_f32 v62, v44, v45\n"
"v_mfma_f32_16x16x32_bf16 v[120:123], v[136:139], a[248:251], v[120:123]\n"
"v_cvt_pk_bf16_f32 v63, v10, v11\n"
"v_accvgpr_read_b32 v40, a160\n"
"v_mfma_f32_16x16x32_bf16 v[120:123], v[152:155], a[252:255], v[120:123]\n"
"buffer_store_dwordx4 v[60:63], v12, s[24:27], s6 offen offset:3072 sc1\n"
"v_mfma_f32_16x16x32_bf16 v[124:127], v[140:143], a[248:251], v[124:127]\n"
"s_waitcnt vmcnt(48)\n"
"v_accvgpr_read_b32 v41, a161\n"
"v_accvgpr_read_b32 v42, a162\n"
"v_mfma_f32_16x16x32_bf16 v[124:127], v[156:159], a[252:255], v[124:127]\n"
"s_waitcnt lgkmcnt(8)\n"
"buffer_load_dwordx4 v33, s[20:23], s40 offen offset:3072 lds\n"
"v_mfma_f32_16x16x32_bf16 v[160:163], v[128:131], a[216:219], v[160:163]\n"
"ds_read_b64_tr_b16 a[248:249], v3 offset:41152\n"
"v_accvgpr_read_b32 v43, a163\n"
"v_mfma_f32_16x16x32_bf16 v[160:163], v[144:147], a[220:223], v[160:163]\n"
"ds_read_b64_tr_b16 a[250:251], v3 offset:46272\n"
"v_accvgpr_read_b32 v44, a176\n"
"v_mfma_f32_16x16x32_bf16 v[164:167], v[132:135], a[216:219], v[164:167]\n"
"ds_read_b64_tr_b16 a[252:253], v3 offset:51392\n"
"v_accvgpr_read_b32 v45, a177\n"
"v_mfma_f32_16x16x32_bf16 v[164:167], v[148:151], a[220:223], v[164:167]\n"
"ds_read_b64_tr_b16 a[254:255], v3 offset:56512\n"
"v_accvgpr_read_b32 v10, a178\n"
"v_mfma_f32_16x16x32_bf16 v[168:171], v[136:139], a[216:219], v[168:171]\n"
"v_accvgpr_read_b32 v11, a179\n"
"v_fma_f32 v40, v14, v40, -v14\n"
"v_mfma_f32_16x16x32_bf16 v[168:171], v[152:155], a[220:223], v[168:171]\n"
"buffer_load_dwordx4 a[88:91], v12, s[24:27], s78 offen offset:2048 sc1\n"
"v_mfma_f32_16x16x32_bf16 v[172:175], v[140:143], a[216:219], v[172:175]\n"
"s_add_u32 m0, s95, 4096\n"
"v_fma_f32 v41, v14, v41, -v14\n"
"v_fma_f32 v42, v14, v42, -v14\n"
"v_mfma_f32_16x16x32_bf16 v[172:175], v[156:159], a[220:223], v[172:175]\n"
"s_waitcnt lgkmcnt(8)\n"
"buffer_load_dwordx4 v34, s[20:23], s40 offen offset:0 lds\n"
"v_mfma_f32_16x16x32_bf16 v[176:179], v[128:131], a[224:227], v[176:179]\n"
"s_add_u32 s40, s40, 81920\n"
"ds_read_b64_tr_b16 a[216:217], v2 offset:41216\n"
"v_fma_f32 v43, v14, v43, -v14\n"
"v_mfma_f32_16x16x32_bf16 v[176:179], v[144:147], a[228:231], v[176:179]\n"
"ds_read_b64_tr_b16 a[218:219], v2 offset:46336\n"
"v_fma_f32 v44, v14, v44, -v14\n"
"v_mfma_f32_16x16x32_bf16 v[180:183], v[132:135], a[224:227], v[180:183]\n"
"ds_read_b64_tr_b16 a[220:221], v2 offset:51456\n"
"v_fma_f32 v45, v14, v45, -v14\n"
"v_mfma_f32_16x16x32_bf16 v[180:183], v[148:151], a[228:231], v[180:183]\n"
"ds_read_b64_tr_b16 a[222:223], v2 offset:56576\n"
"v_fma_f32 v10, v14, v10, -v14\n"
"v_mfma_f32_16x16x32_bf16 v[184:187], v[136:139], a[224:227], v[184:187]\n"
"v_fma_f32 v11, v14, v11, -v14\n"
"v_cvt_pk_bf16_f32 v18, v40, v41\n"
"v_mfma_f32_16x16x32_bf16 v[184:187], v[152:155], a[228:231], v[184:187]\n"
"buffer_load_dwordx4 a[92:95], v12, s[24:27], s78 offen offset:3072 sc1\n"
"s_add_u32 s78, s78, 4096\n"
"v_mfma_f32_16x16x32_bf16 v[188:191], v[140:143], a[224:227], v[188:191]\n"
"v_cvt_pk_bf16_f32 v19, v42, v43\n"
"v_cvt_pk_bf16_f32 v20, v44, v45\n"
"v_mfma_f32_16x16x32_bf16 v[188:191], v[156:159], a[228:231], v[188:191]\n"
"s_waitcnt lgkmcnt(8)\n"
"v_cvt_pk_bf16_f32 v21, v10, v11\n"
"v_accvgpr_read_b32 v40, a164\n"
"v_mfma_f32_16x16x32_bf16 v[192:195], v[128:131], a[240:243], v[192:195]\n"
"ds_read_b64_tr_b16 a[224:225], v3 offset:41216\n"
"v_accvgpr_read_b32 v41, a165\n"
"v_mfma_f32_16x16x32_bf16 v[192:195], v[144:147], a[244:247], v[192:195]\n"
"ds_read_b64_tr_b16 a[226:227], v3 offset:46336\n"
"v_accvgpr_read_b32 v42, a166\n"
"v_mfma_f32_16x16x32_bf16 v[196:199], v[132:135], a[240:243], v[196:199]\n"
"ds_read_b64_tr_b16 a[228:229], v3 offset:51456\n"
"v_accvgpr_read_b32 v43, a167\n"
"v_mfma_f32_16x16x32_bf16 v[196:199], v[148:151], a[244:247], v[196:199]\n"
"ds_read_b64_tr_b16 a[230:231], v3 offset:56576\n"
"v_accvgpr_read_b32 v44, a180\n"
"v_mfma_f32_16x16x32_bf16 v[200:203], v[136:139], a[240:243], v[200:203]\n"
"v_accvgpr_read_b32 v45, a181\n"
"v_accvgpr_read_b32 v10, a182\n"
"v_mfma_f32_16x16x32_bf16 v[200:203], v[152:155], a[244:247], v[200:203]\n"
"buffer_store_dwordx4 v[18:21], v12, s[24:27], s7 offen offset:0 sc1\n"
"v_mfma_f32_16x16x32_bf16 v[204:207], v[140:143], a[240:243], v[204:207]\n"
"v_accvgpr_read_b32 v11, a183\n"
"v_fma_f32 v40, v15, v40, -v15\n"
"v_mfma_f32_16x16x32_bf16 v[204:207], v[156:159], a[244:247], v[204:207]\n"
"s_waitcnt lgkmcnt(8)\n"
"v_fma_f32 v41, v15, v41, -v15\n"
"v_fma_f32 v42, v15, v42, -v15\n"
"v_mfma_f32_16x16x32_bf16 v[208:211], v[128:131], a[248:251], v[208:211]\n"
"ds_read_b64_tr_b16 a[240:241], v2 offset:0\n"
"v_fma_f32 v43, v15, v43, -v15\n"
"v_mfma_f32_16x16x32_bf16 v[208:211], v[144:147], a[252:255], v[208:211]\n"
"ds_read_b64_tr_b16 a[242:243], v2 offset:5120\n"
"v_fma_f32 v44, v15, v44, -v15\n"
"v_mfma_f32_16x16x32_bf16 v[212:215], v[132:135], a[248:251], v[212:215]\n"
"ds_read_b64_tr_b16 a[244:245], v2 offset:10240\n"
"v_fma_f32 v45, v15, v45, -v15\n"
"v_mfma_f32_16x16x32_bf16 v[212:215], v[148:151], a[252:255], v[212:215]\n"
"ds_read_b64_tr_b16 a[246:247], v2 offset:15360\n"
"v_fma_f32 v10, v15, v10, -v15\n"
"v_mfma_f32_16x16x32_bf16 v[216:219], v[136:139], a[248:251], v[216:219]\n"
"v_fma_f32 v11, v15, v11, -v15\n"
"v_cvt_pk_bf16_f32 v22, v40, v41\n"
"v_mfma_f32_16x16x32_bf16 v[216:219], v[152:155], a[252:255], v[216:219]\n"
"v_cvt_pk_bf16_f32 v23, v42, v43\n"
"v_cvt_pk_bf16_f32 v24, v44, v45\n"
"v_mfma_f32_16x16x32_bf16 v[220:223], v[140:143], a[248:251], v[220:223]\n"
"v_cvt_pk_bf16_f32 v25, v10, v11\n"
"v_accvgpr_read_b32 v40, a168\n"
"v_mfma_f32_16x16x32_bf16 v[220:223], v[156:159], a[252:255], v[220:223]\n"
"s_waitcnt lgkmcnt(8)\n"
"buffer_store_dwordx4 v[22:25], v12, s[24:27], s7 offen offset:1024 sc1\n"
"v_mfma_f32_16x16x32_bf16 v[224:227], v[128:131], a[216:219], v[224:227]\n"
"ds_read_b64_tr_b16 a[248:249], v3 offset:0\n"
"L_pg18:\n"
"s_cbranch_execz L_bk4\n"
"v_accvgpr_read_b32 v41, a169\n"
"v_mfma_f32_16x16x32_bf16 v[224:227], v[144:147], a[220:223], v[224:227]\n"
"ds_read_b64_tr_b16 a[250:251], v3 offset:5120\n"
"v_accvgpr_read_b32 v42, a170\n"
"v_mfma_f32_16x16x32_bf16 v[228:231], v[132:135], a[216:219], v[228:231]\n"
"ds_read_b64_tr_b16 a[252:253], v3 offset:10240\n"
"v_accvgpr_read_b32 v43, a171\n"
"v_mfma_f32_16x16x32_bf16 v[228:231], v[148:151], a[220:223], v[228:231]\n"
"ds_read_b64_tr_b16 a[254:255], v3 offset:15360\n"
"v_accvgpr_read_b32 v44, a184\n"
"v_mfma_f32_16x16x32_bf16 v[232:235], v[136:139], a[216:219], v[232:235]\n"
"v_accvgpr_read_b32 v45, a185\n"
"v_accvgpr_read_b32 v10, a186\n"
"v_mfma_f32_16x16x32_bf16 v[232:235], v[152:155], a[220:223], v[232:235]\n"
"v_accvgpr_read_b32 v11, a187\n"
"v_fma_f32 v40, v16, v40, -v16\n"
"v_mfma_f32_16x16x32_bf16 v[236:239], v[140:143], a[216:219], v[236:239]\n"
"v_fma_f32 v41, v16, v41, -v16\n"
"v_fma_f32 v42, v16, v42, -v16\n"
"v_mfma_f32_16x16x32_bf16 v[236:239], v[156:159], a[220:223], v[236:239]\n"
"s_waitcnt lgkmcnt(8)\n"
"v_fma_f32 v43, v16, v43, -v16\n"
"v_fma_f32 v44, v16, v44, -v16\n"
"v_mfma_f32_16x16x32_bf16 v[240:243], v[128:131], a[224:227], v[240:243]\n"
"ds_read_b64_tr_b16 a[216:217], v2 offset:64\n"
"v_fma_f32 v45, v16, v45, -v16\n"
"v_mfma_f32_16x16x32_bf16 v[240:243], v[144:147], a[228:231], v[240:243]\n"
"ds_read_b64_tr_b16 a[218:219], v2 offset:5184\n"
"v_fma_f32 v10, v16, v10, -v16\n"
"v_mfma_f32_16x16x32_bf16 v[244:247], v[132:135], a[224:227], v[244:247]\n"
"ds_read_b64_tr_b16 a[220:221], v2 offset:10304\n"
"v_fma_f32 v11, v16, v11, -v16\n"
"v_mfma_f32_16x16x32_bf16 v[244:247], v[148:151], a[228:231], v[244:247]\n"
"s_waitcnt vmcnt(50)\n"
"s_waitcnt vmcnt(40)\n"
"ds_read_b64_tr_b16 a[222:223], v2 offset:15424\n"
"v_cvt_pk_bf16_f32 v36, v40, v41\n"
"v_mfma_f32_16x16x32_bf16 v[248:251], v[136:139], a[224:227], v[248:251]\n"
"s_barrier\n"
"v_cvt_pk_bf16_f32 v37, v42, v43\n"
"v_cvt_pk_bf16_f32 v38, v44, v45\n"
"v_mfma_f32_16x16x32_bf16 v[248:251], v[152:155], a[228:231], v[248:251]\n"
"v_cvt_pk_bf16_f32 v39, v10, v11\n"
"v_accvgpr_read_b32 v40, a172\n"
"v_mfma_f32_16x16x32_bf16 v[252:255], v[140:143], a[224:227], v[252:255]\n"
"v_accvgpr_read_b32 v41, a173\n"
"v_accvgpr_read_b32 v42, a174\n"
"v_mfma_f32_16x16x32_bf16 v[252:255], v[156:159], a[228:231], v[252:255]\n"
"s_waitcnt lgkmcnt(8)\n"
"s_waitcnt vmcnt(26)\n"
"buffer_store_dwordx4 v[36:39], v12, s[24:27], s7 offen offset:2048 sc1\n"
"v_mfma_f32_16x16x32_bf16 v[64:67], a[32:35], a[240:243], v[64:67]\n"
"ds_read_b64_tr_b16 a[224:225], v3 offset:64\n"
"v_accvgpr_read_b32 v43, a175\n"
"v_mfma_f32_16x16x32_bf16 v[64:67], a[48:51], a[244:247], v[64:67]\n"
"ds_read_b64_tr_b16 a[226:227], v3 offset:5184\n"
"v_accvgpr_read_b32 v44, a188\n"
"v_mfma_f32_16x16x32_bf16 v[68:71], a[36:39], a[240:243], v[68:71]\n"
"ds_read_b64_tr_b16 a[228:229], v3 offset:10304\n"
"v_accvgpr_read_b32 v45, a189\n"
"v_mfma_f32_16x16x32_bf16 v[68:71], a[52:55], a[244:247], v[68:71]\n"
"ds_read_b64_tr_b16 a[230:231], v3 offset:15424\n"
"v_accvgpr_read_b32 v10, a190\n"
"v_mfma_f32_16x16x32_bf16 v[72:75], a[40:43], a[240:243], v[72:75]\n"
"v_accvgpr_read_b32 v11, a191\n"
"v_fma_f32 v40, v17, v40, -v17\n"
"v_mfma_f32_16x16x32_bf16 v[72:75], a[56:59], a[244:247], v[72:75]\n"
"buffer_load_dwordx4 a[96:99], v12, s[24:27], s78 offen offset:0 sc1\n"
"v_mfma_f32_16x16x32_bf16 v[76:79], a[44:47], a[240:243], v[76:79]\n"
"s_add_u32 m0, s95, 20480\n"
"v_fma_f32 v41, v17, v41, -v17\n"
"v_fma_f32 v42, v17, v42, -v17\n"
"v_mfma_f32_16x16x32_bf16 v[76:79], a[60:63], a[244:247], v[76:79]\n"
"s_waitcnt lgkmcnt(8)\n"
"buffer_load_dwordx4 v30, s[20:23], s40 offen offset:0 lds\n"
"v_mfma_f32_16x16x32_bf16 v[80:83], a[32:35], a[248:251], v[80:83]\n"
"ds_read_b64_tr_b16 a[240:241], v2 offset:128\n"
"v_fma_f32 v43, v17, v43, -v17\n"
"v_mfma_f32_16x16x32_bf16 v[80:83], a[48:51], a[252:255], v[80:83]\n"
"ds_read_b64_tr_b16 a[242:243], v2 offset:5248\n"
"v_fma_f32 v44, v17, v44, -v17\n"
"v_mfma_f32_16x16x32_bf16 v[84:87], a[36:39], a[248:251], v[84:87]\n"
"ds_read_b64_tr_b16 a[244:245], v2 offset:10368\n"
"v_fma_f32 v45, v17, v45, -v17\n"
"v_mfma_f32_16x16x32_bf16 v[84:87], a[52:55], a[252:255], v[84:87]\n"
"ds_read_b64_tr_b16 a[246:247], v2 offset:15488\n"
"v_fma_f32 v10, v17, v10, -v17\n"
"v_mfma_f32_16x16x32_bf16 v[88:91], a[40:43], a[248:251], v[88:91]\n"
"v_fma_f32 v11, v17, v11, -v17\n"
"v_cvt_pk_bf16_f32 v6, v40, v41\n"
"v_mfma_f32_16x16x32_bf16 v[88:91], a[56:59], a[252:255], v[88:91]\n"
"buffer_load_dwordx4 a[100:103], v12, s[24:27], s78 offen offset:1024 sc1\n"
"v_mfma_f32_16x16x32_bf16 v[92:95], a[44:47], a[248:251], v[92:95]\n"
"v_cvt_pk_bf16_f32 v7, v42, v43\n"
"v_cvt_pk_bf16_f32 v8, v44, v45\n"
"v_mfma_f32_16x16x32_bf16 v[92:95], a[60:63], a[252:255], v[92:95]\n"
"s_waitcnt lgkmcnt(8)\n"
"buffer_load_dwordx4 v31, s[20:23], s40 offen offset:1024 lds\n"
"v_mfma_f32_16x16x32_bf16 v[96:99], a[32:35], a[216:219], v[96:99]\n"
"ds_read_b64_tr_b16 a[248:249], v3 offset:128\n"
"v_cvt_pk_bf16_f32 v9, v10, v11\n"
"v_mfma_f32_16x16x32_bf16 v[96:99], a[48:51], a[220:223], v[96:99]\n"
"ds_read_b64_tr_b16 a[250:251], v3 offset:5248\n"
"v_mfma_f32_16x16x32_bf16 v[100:103], a[36:39], a[216:219], v[100:103]\n"
"ds_read_b64_tr_b16 a[252:253], v3 offset:10368\n"
"v_mfma_f32_16x16x32_bf16 v[100:103], a[52:55], a[220:223], v[100:103]\n"
"ds_read_b64_tr_b16 a[254:255], v3 offset:15488\n"
"v_mfma_f32_16x16x32_bf16 v[104:107], a[40:43], a[216:219], v[104:107]\n"
"v_mfma_f32_16x16x32_bf16 v[104:107], a[56:59], a[220:223], v[104:107]\n"
"buffer_store_dwordx4 v[6:9], v12, s[24:27], s7 offen offset:3072 sc1\n"
"v_mfma_f32_16x16x32_bf16 v[108:111], a[44:47], a[216:219], v[108:111]\n"
"v_mfma_f32_16x16x32_bf16 v[108:111], a[60:63], a[220:223], v[108:111]\n"
"s_waitcnt lgkmcnt(8)\n"
"buffer_load_dwordx4 v32, s[20:23], s40 offen offset:2048 lds\n"
"v_mfma_f32_16x16x32_bf16 v[112:115], a[32:35], a[224:227], v[112:115]\n"
"ds_read_b64_tr_b16 a[216:217], v2 offset:192\n"
"v_mfma_f32_16x16x32_bf16 v[112:115], a[48:51], a[228:231], v[112:115]\n"
"ds_read_b64_tr_b16 a[218:219], v2 offset:5312\n"
"v_mfma_f32_16x16x32_bf16 v[116:119], a[36:39], a[224:227], v[116:119]\n"
"ds_read_b64_tr_b16 a[220:221], v2 offset:10432\n"
"v_mfma_f32_16x16x32_bf16 v[116:119], a[52:55], a[228:231], v[116:119]\n"
"ds_read_b64_tr_b16 a[222:223], v2 offset:15552\n"
"v_mfma_f32_16x16x32_bf16 v[120:123], a[40:43], a[224:227], v[120:123]\n"
"v_mfma_f32_16x16x32_bf16 v[120:123], a[56:59], a[228:231], v[120:123]\n"
"buffer_load_dwordx4 a[104:107], v12, s[24:27], s78 offen offset:2048 sc1\n"
"v_mfma_f32_16x16x32_bf16 v[124:127], a[44:47], a[224:227], v[124:127]\n"
"s_waitcnt vmcnt(48)\n"
"v_mfma_f32_16x16x32_bf16 v[124:127], a[60:63], a[228:231], v[124:127]\n"
"s_waitcnt lgkmcnt(8)\n"
"buffer_load_dwordx4 v33, s[20:23], s40 offen offset:3072 lds\n"
"v_mfma_f32_16x16x32_bf16 v[160:163], a[32:35], a[240:243], v[160:163]\n"
"ds_read_b64_tr_b16 a[224:225], v3 offset:192\n"
"v_mfma_f32_16x16x32_bf16 v[160:163], a[48:51], a[244:247], v[160:163]\n"
"ds_read_b64_tr_b16 a[226:227], v3 offset:5312\n"
"v_mfma_f32_16x16x32_bf16 v[164:167], a[36:39], a[240:243], v[164:167]\n"
"ds_read_b64_tr_b16 a[228:229], v3 offset:10432\n"
"v_mfma_f32_16x16x32_bf16 v[164:167], a[52:55], a[244:247], v[164:167]\n"
"ds_read_b64_tr_b16 a[230:231], v3 offset:15552\n"
"v_mfma_f32_16x16x32_bf16 v[168:171], a[40:43], a[240:243], v[168:171]\n"
"v_mfma_f32_16x16x32_bf16 v[168:171], a[56:59], a[244:247], v[168:171]\n"
"buffer_load_dwordx4 a[108:111], v12, s[24:27], s78 offen offset:3072 sc1\n"
"s_add_u32 s78, s78, 4096\n"
"v_mfma_f32_16x16x32_bf16 v[172:175], a[44:47], a[240:243], v[172:175]\n"
"s_add_u32 m0, s95, 24576\n"
"v_mfma_f32_16x16x32_bf16 v[172:175], a[60:63], a[244:247], v[172:175]\n"
"s_waitcnt lgkmcnt(8)\n"
"buffer_load_dwordx4 v34, s[20:23], s40 offen offset:0 lds\n"
"v_mfma_f32_16x16x32_bf16 v[176:179], a[32:35], a[248:251], v[176:179]\n"
"s_add_u32 s40, s40, 81920\n"
"ds_read_b64_tr_b16 a[240:241], v2 offset:256\n"
"v_mfma_f32_16x16x32_bf16 v[176:179], a[48:51], a[252:255], v[176:179]\n"
"ds_read_b64_tr_b16 a[242:243], v2 offset:5376\n"
"v_mfma_f32_16x16x32_bf16 v[180:183], a[36:39], a[248:251], v[180:183]\n"
"ds_read_b64_tr_b16 a[244:245], v2 offset:10496\n"
"v_mfma_f32_16x16x32_bf16 v[180:183], a[52:55], a[252:255], v[180:183]\n"
"ds_read_b64_tr_b16 a[246:247], v2 offset:15616\n"
"v_mfma_f32_16x16x32_bf16 v[184:187], a[40:43], a[248:251], v[184:187]\n"
"v_mfma_f32_16x16x32_bf16 v[184:187], a[56:59], a[252:255], v[184:187]\n"
"buffer_load_dwordx4 a[112:115], v12, s[24:27], s78 offen offset:0 sc1\n"
"v_mfma_f32_16x16x32_bf16 v[188:191], a[44:47], a[248:251], v[188:191]\n"
"v_mfma_f32_16x16x32_bf16 v[188:191], a[60:63], a[252:255], v[188:191]\n"
"s_waitcnt lgkmcnt(8)\n"
"buffer_load_dwordx4 a[116:119], v12, s[24:27], s78 offen offset:1024 sc1\n"
"v_mfma_f32_16x16x32_bf16 v[192:195], a[32:35], a[216:219], v[192:195]\n"
"ds_read_b64_tr_b16 a[248:249], v3 offset:256\n"
"v_mfma_f32_16x16x32_bf16 v[192:195], a[48:51], a[220:223], v[192:195]\n"
"ds_read_b64_tr_b16 a[250:251], v3 offset:5376\n"
"v_mfma_f32_16x16x32_bf16 v[196:199], a[36:39], a[216:219], v[196:199]\n"
"ds_read_b64_tr_b16 a[252:253], v3 offset:10496\n"
"v_mfma_f32_16x16x32_bf16 v[196:199], a[52:55], a[220:223], v[196:199]\n"
"ds_read_b64_tr_b16 a[254:255], v3 offset:15616\n"
"v_mfma_f32_16x16x32_bf16 v[200:203], a[40:43], a[216:219], v[200:203]\n"
"v_mfma_f32_16x16x32_bf16 v[200:203], a[56:59], a[220:223], v[200:203]\n"
"buffer_load_dwordx4 a[120:123], v12, s[24:27], s78 offen offset:2048 sc1\n"
"v_mfma_f32_16x16x32_bf16 v[204:207], a[44:47], a[216:219], v[204:207]\n"
"v_mfma_f32_16x16x32_bf16 v[204:207], a[60:63], a[220:223], v[204:207]\n"
"s_waitcnt lgkmcnt(8)\n"
"buffer_load_dwordx4 a[124:127], v12, s[24:27], s78 offen offset:3072 sc1\n"
"s_add_u32 s78, s78, 4096\n"
"v_mfma_f32_16x16x32_bf16 v[208:211], a[32:35], a[224:227], v[208:211]\n"
"ds_read_b64_tr_b16 a[216:217], v2 offset:20480\n"
"v_mfma_f32_16x16x32_bf16 v[208:211], a[48:51], a[228:231], v[208:211]\n"
"ds_read_b64_tr_b16 a[218:219], v2 offset:25600\n"
"v_mfma_f32_16x16x32_bf16 v[212:215], a[36:39], a[224:227], v[212:215]\n"
"ds_read_b64_tr_b16 a[220:221], v2 offset:30720\n"
"v_mfma_f32_16x16x32_bf16 v[212:215], a[52:55], a[228:231], v[212:215]\n"
"ds_read_b64_tr_b16 a[222:223], v2 offset:35840\n"
"v_mfma_f32_16x16x32_bf16 v[216:219], a[40:43], a[224:227], v[216:219]\n"
"v_mfma_f32_16x16x32_bf16 v[216:219], a[56:59], a[228:231], v[216:219]\n"
"v_mfma_f32_16x16x32_bf16 v[220:223], a[44:47], a[224:227], v[220:223]\n"
"v_mfma_f32_16x16x32_bf16 v[220:223], a[60:63], a[228:231], v[220:223]\n"
"s_waitcnt lgkmcnt(8)\n"
"v_mfma_f32_16x16x32_bf16 v[224:227], a[32:35], a[240:243], v[224:227]\n"
"ds_read_b64_tr_b16 a[224:225], v3 offset:20480\n"
"v_mfma_f32_16x16x32_bf16 v[224:227], a[48:51], a[244:247], v[224:227]\n"
"ds_read_b64_tr_b16 a[226:227], v3 offset:25600\n"
"v_mfma_f32_16x16x32_bf16 v[228:231], a[36:39], a[240:243], v[228:231]\n"
"ds_read_b64_tr_b16 a[228:229], v3 offset:30720\n"
"v_mfma_f32_16x16x32_bf16 v[228:231], a[52:55], a[244:247], v[228:231]\n"
"ds_read_b64_tr_b16 a[230:231], v3 offset:35840\n"
"v_mfma_f32_16x16x32_bf16 v[232:235], a[40:43], a[240:243], v[232:235]\n"
"v_mfma_f32_16x16x32_bf16 v[232:235], a[56:59], a[244:247], v[232:235]\n"
"v_mfma_f32_16x16x32_bf16 v[236:239], a[44:47], a[240:243], v[236:239]\n"
"v_mfma_f32_16x16x32_bf16 v[236:239], a[60:63], a[244:247], v[236:239]\n"
"s_waitcnt lgkmcnt(8)\n"
"v_mfma_f32_16x16x32_bf16 v[240:243], a[32:35], a[248:251], v[240:243]\n"
"ds_read_b64_tr_b16 a[240:241], v2 offset:20544\n"
"v_mfma_f32_16x16x32_bf16 v[240:243], a[48:51], a[252:255], v[240:243]\n"
"ds_read_b64_tr_b16 a[242:243], v2 offset:25664\n"
"v_mfma_f32_16x16x32_bf16 v[244:247], a[36:39], a[248:251], v[244:247]\n"
"ds_read_b64_tr_b16 a[244:245], v2 offset:30784\n"
"v_mfma_f32_16x16x32_bf16 v[244:247], a[52:55], a[252:255], v[244:247]\n"
"s_waitcnt vmcnt(18)\n"
"s_waitcnt vmcnt(40)\n"
"ds_read_b64_tr_b16 a[246:247], v2 offset:35904\n"
"v_mfma_f32_16x16x32_bf16 v[248:251], a[40:43], a[248:251], v[248:251]\n"
"s_barrier\n"
"v_mfma_f32_16x16x32_bf16 v[248:251], a[56:59], a[252:255], v[248:251]\n"
"v_mfma_f32_16x16x32_bf16 v[252:255], a[44:47], a[248:251], v[252:255]\n"
"v_mfma_f32_16x16x32_bf16 v[252:255], a[60:63], a[252:255], v[252:255]\n"
"s_waitcnt lgkmcnt(8)\n"
"v_mfma_f32_16x16x32_bf16 v[64:67], v[48:51], a[216:219], v[64:67]\n"
"ds_read_b64_tr_b16 a[248:249], v3 offset:20544\n"
"v_mfma_f32_16x16x32_bf16 v[64:67], v[18:21], a[220:223], v[64:67]\n"
"ds_read_b64_tr_b16 a[250:251], v3 offset:25664\n"
"v_mfma_f32_16x16x32_bf16 v[68:71], v[52:55], a[216:219], v[68:71]\n"
"ds_read_b64_tr_b16 a[252:253], v3 offset:30784\n"
"v_mfma_f32_16x16x32_bf16 v[68:71], v[22:25], a[220:223], v[68:71]\n"
"ds_read_b64_tr_b16 a[254:255], v3 offset:35904\n"
"v_mfma_f32_16x16x32_bf16 v[72:75], v[56:59], a[216:219], v[72:75]\n"
"v_mfma_f32_16x16x32_bf16 v[72:75], v[36:39], a[220:223], v[72:75]\n"
"buffer_load_dwordx4 a[128:131], v12, s[24:27], s78 offen offset:0 sc1\n"
"v_mfma_f32_16x16x32_bf16 v[76:79], v[60:63], a[216:219], v[76:79]\n"
"s_add_u32 m0, s95, 122880\n"
"v_mfma_f32_16x16x32_bf16 v[76:79], v[6:9], a[220:223], v[76:79]\n"
"s_waitcnt lgkmcnt(8)\n"
"buffer_load_dwordx4 v30, s[20:23], s40 offen offset:0 lds\n"
"v_mfma_f32_16x16x32_bf16 v[80:83], v[48:51], a[224:227], v[80:83]\n"
"ds_read_b64_tr_b16 a[216:217], v2 offset:20608\n"
"v_mfma_f32_16x16x32_bf16 v[80:83], v[18:21], a[228:231], v[80:83]\n"
"ds_read_b64_tr_b16 a[218:219], v2 offset:25728\n"
"v_mfma_f32_16x16x32_bf16 v[84:87], v[52:55], a[224:227], v[84:87]\n"
"ds_read_b64_tr_b16 a[220:221], v2 offset:30848\n"
"v_mfma_f32_16x16x32_bf16 v[84:87], v[22:25], a[228:231], v[84:87]\n"
"ds_read_b64_tr_b16 a[222:223], v2 offset:35968\n"
"v_mfma_f32_16x16x32_bf16 v[88:91], v[56:59], a[224:227], v[88:91]\n"
"v_mfma_f32_16x16x32_bf16 v[88:91], v[36:39], a[228:231], v[88:91]\n"
"buffer_load_dwordx4 a[132:135], v12, s[24:27], s78 offen offset:1024 sc1\n"
"v_mfma_f32_16x16x32_bf16 v[92:95], v[60:63], a[224:227], v[92:95]\n"
"v_mfma_f32_16x16x32_bf16 v[92:95], v[6:9], a[228:231], v[92:95]\n"
"s_waitcnt lgkmcnt(8)\n"
"buffer_load_dwordx4 v31, s[20:23], s40 offen offset:1024 lds\n"
"v_mfma_f32_16x16x32_bf16 v[96:99], v[48:51], a[240:243], v[96:99]\n"
"ds_read_b64_tr_b16 a[224:225], v3 offset:20608\n"
"v_mfma_f32_16x16x32_bf16 v[96:99], v[18:21], a[244:247], v[96:99]\n"
"ds_read_b64_tr_b16 a[226:227], v3 offset:25728\n"
"v_mfma_f32_16x16x32_bf16 v[100:103], v[52:55], a[240:243], v[100:103]\n"
"ds_read_b64_tr_b16 a[228:229], v3 offset:30848\n"
"v_mfma_f32_16x16x32_bf16 v[100:103], v[22:25], a[244:247], v[100:103]\n"
"ds_read_b64_tr_b16 a[230:231], v3 offset:35968\n"
"v_mfma_f32_16x16x32_bf16 v[104:107], v[56:59], a[240:243], v[104:107]\n"
"v_mfma_f32_16x16x32_bf16 v[104:107], v[36:39], a[244:247], v[104:107]\n"
"buffer_load_dwordx4 a[136:139], v12, s[24:27], s78 offen offset:2048 sc1\n"
"v_mfma_f32_16x16x32_bf16 v[108:111], v[60:63], a[240:243], v[108:111]\n"
"v_mfma_f32_16x16x32_bf16 v[108:111], v[6:9], a[244:247], v[108:111]\n"
"s_waitcnt lgkmcnt(8)\n"
"buffer_load_dwordx4 v32, s[20:23], s40 offen offset:2048 lds\n"
"v_mfma_f32_16x16x32_bf16 v[112:115], v[48:51], a[248:251], v[112:115]\n"
"ds_read_b64_tr_b16 a[240:241], v2 offset:20672\n"
"v_mfma_f32_16x16x32_bf16 v[112:115], v[18:21], a[252:255], v[112:115]\n"
"ds_read_b64_tr_b16 a[242:243], v2 offset:25792\n"
"v_mfma_f32_16x16x32_bf16 v[116:119], v[52:55], a[248:251], v[116:119]\n"
"ds_read_b64_tr_b16 a[244:245], v2 offset:30912\n"
"v_mfma_f32_16x16x32_bf16 v[116:119], v[22:25], a[252:255], v[116:119]\n"
"ds_read_b64_tr_b16 a[246:247], v2 offset:36032\n"
"v_mfma_f32_16x16x32_bf16 v[120:123], v[56:59], a[248:251], v[120:123]\n"
"v_mfma_f32_16x16x32_bf16 v[120:123], v[36:39], a[252:255], v[120:123]\n"
"buffer_load_dwordx4 a[140:143], v12, s[24:27], s78 offen offset:3072 sc1\n"
"s_add_u32 s78, s78, 4096\n"
"v_mfma_f32_16x16x32_bf16 v[124:127], v[60:63], a[248:251], v[124:127]\n"
"s_waitcnt vmcnt(48)\n"
"v_mfma_f32_16x16x32_bf16 v[124:127], v[6:9], a[252:255], v[124:127]\n"
"s_waitcnt lgkmcnt(8)\n"
"buffer_load_dwordx4 v33, s[20:23], s40 offen offset:3072 lds\n"
"v_mfma_f32_16x16x32_bf16 v[160:163], v[48:51], a[216:219], v[160:163]\n"
"ds_read_b64_tr_b16 a[248:249], v3 offset:20672\n"
"v_mfma_f32_16x16x32_bf16 v[160:163], v[18:21], a[220:223], v[160:163]\n"
"ds_read_b64_tr_b16 a[250:251], v3 offset:25792\n"
"v_mfma_f32_16x16x32_bf16 v[164:167], v[52:55], a[216:219], v[164:167]\n"
"ds_read_b64_tr_b16 a[252:253], v3 offset:30912\n"
"v_mfma_f32_16x16x32_bf16 v[164:167], v[22:25], a[220:223], v[164:167]\n"
"ds_read_b64_tr_b16 a[254:255], v3 offset:36032\n"
"v_mfma_f32_16x16x32_bf16 v[168:171], v[56:59], a[216:219], v[168:171]\n"
"v_mfma_f32_16x16x32_bf16 v[168:171], v[36:39], a[220:223], v[168:171]\n"
"buffer_load_dwordx4 a[144:147], v12, s[24:27], s78 offen offset:0 sc1\n"
"v_mfma_f32_16x16x32_bf16 v[172:175], v[60:63], a[216:219], v[172:175]\n"
"s_add_u32 m0, s95, 126976\n"
"v_mfma_f32_16x16x32_bf16 v[172:175], v[6:9], a[220:223], v[172:175]\n"
"s_waitcnt lgkmcnt(8)\n"
"buffer_load_dwordx4 v34, s[20:23], s40 offen offset:0 lds\n"
"v_mfma_f32_16x16x32_bf16 v[176:179], v[48:51], a[224:227], v[176:179]\n"
"s_add_u32 s40, s40, 81920\n"
"ds_read_b64_tr_b16 a[216:217], v2 offset:20736\n"
"v_mfma_f32_16x16x32_bf16 v[176:179], v[18:21], a[228:231], v[176:179]\n"
"ds_read_b64_tr_b16 a[218:219], v2 offset:25856\n"
"v_mfma_f32_16x16x32_bf16 v[180:183], v[52:55], a[224:227], v[180:183]\n"
"ds_read_b64_tr_b16 a[220:221], v2 offset:30976\n"
"v_mfma_f32_16x16x32_bf16 v[180:183], v[22:25], a[228:231], v[180:183]\n"
"ds_read_b64_tr_b16 a[222:223], v2 offset:36096\n"
"v_mfma_f32_16x16x32_bf16 v[184:187], v[56:59], a[224:227], v[184:187]\n"
"v_mfma_f32_16x16x32_bf16 v[184:187], v[36:39], a[228:231], v[184:187]\n"
"buffer_load_dwordx4 a[148:151], v12, s[24:27], s78 offen offset:1024 sc1\n"
"v_mfma_f32_16x16x32_bf16 v[188:191], v[60:63], a[224:227], v[188:191]\n"
"v_mfma_f32_16x16x32_bf16 v[188:191], v[6:9], a[228:231], v[188:191]\n"
"s_waitcnt lgkmcnt(8)\n"
"buffer_load_dwordx4 a[152:155], v12, s[24:27], s78 offen offset:2048 sc1\n"
"v_mfma_f32_16x16x32_bf16 v[192:195], v[48:51], a[240:243], v[192:195]\n"
"ds_read_b64_tr_b16 a[224:225], v3 offset:20736\n"
"v_mfma_f32_16x16x32_bf16 v[192:195], v[18:21], a[244:247], v[192:195]\n"
"ds_read_b64_tr_b16 a[226:227], v3 offset:25856\n"
"v_mfma_f32_16x16x32_bf16 v[196:199], v[52:55], a[240:243], v[196:199]\n"
"ds_read_b64_tr_b16 a[228:229], v3 offset:30976\n"
"v_mfma_f32_16x16x32_bf16 v[196:199], v[22:25], a[244:247], v[196:199]\n"
"ds_read_b64_tr_b16 a[230:231], v3 offset:36096\n"
"v_mfma_f32_16x16x32_bf16 v[200:203], v[56:59], a[240:243], v[200:203]\n"
"v_mfma_f32_16x16x32_bf16 v[200:203], v[36:39], a[244:247], v[200:203]\n"
"buffer_load_dwordx4 a[156:159], v12, s[24:27], s78 offen offset:3072 sc1\n"
"s_add_u32 s78, s78, 4096\n"
"v_mfma_f32_16x16x32_bf16 v[204:207], v[60:63], a[240:243], v[204:207]\n"
"v_mfma_f32_16x16x32_bf16 v[204:207], v[6:9], a[244:247], v[204:207]\n"
"s_waitcnt lgkmcnt(8)\n"
"v_mfma_f32_16x16x32_bf16 v[208:211], v[48:51], a[248:251], v[208:211]\n"
"ds_read_b64_tr_b16 a[240:241], v28 offset:0\n"
"v_mfma_f32_16x16x32_bf16 v[208:211], v[18:21], a[252:255], v[208:211]\n"
"ds_read_b64_tr_b16 a[242:243], v28 offset:5120\n"
"v_mfma_f32_16x16x32_bf16 v[212:215], v[52:55], a[248:251], v[212:215]\n"
"ds_read_b64_tr_b16 a[244:245], v28 offset:10240\n"
"v_mfma_f32_16x16x32_bf16 v[212:215], v[22:25], a[252:255], v[212:215]\n"
"ds_read_b64_tr_b16 a[246:247], v28 offset:15360\n"
"v_mfma_f32_16x16x32_bf16 v[216:219], v[56:59], a[248:251], v[216:219]\n"
"v_mfma_f32_16x16x32_bf16 v[216:219], v[36:39], a[252:255], v[216:219]\n"
"v_mfma_f32_16x16x32_bf16 v[220:223], v[60:63], a[248:251], v[220:223]\n"
"v_mfma_f32_16x16x32_bf16 v[220:223], v[6:9], a[252:255], v[220:223]\n"
"s_waitcnt lgkmcnt(8)\n"
"v_mfma_f32_16x16x32_bf16 v[224:227], v[48:51], a[216:219], v[224:227]\n"
"ds_read_b64_tr_b16 a[248:249], v29 offset:0\n"
"v_mfma_f32_16x16x32_bf16 v[224:227], v[18:21], a[220:223], v[224:227]\n"
"ds_read_b64_tr_b16 a[250:251], v29 offset:5120\n"
"v_mfma_f32_16x16x32_bf16 v[228:231], v[52:55], a[216:219], v[228:231]\n"
"ds_read_b64_tr_b16 a[252:253], v29 offset:10240\n"
"v_mfma_f32_16x16x32_bf16 v[228:231], v[22:25], a[220:223], v[228:231]\n"
"ds_read_b64_tr_b16 a[254:255], v29 offset:15360\n"
"v_mfma_f32_16x16x32_bf16 v[232:235], v[56:59], a[216:219], v[232:235]\n"
"v_mfma_f32_16x16x32_bf16 v[232:235], v[36:39], a[220:223], v[232:235]\n"
"v_mfma_f32_16x16x32_bf16 v[236:239], v[60:63], a[216:219], v[236:239]\n"
"v_mfma_f32_16x16x32_bf16 v[236:239], v[6:9], a[220:223], v[236:239]\n"
"s_waitcnt lgkmcnt(8)\n"
"v_mfma_f32_16x16x32_bf16 v[240:243], v[48:51], a[224:227], v[240:243]\n"
"ds_read_b64_tr_b16 a[216:217], v28 offset:64\n"
"v_mfma_f32_16x16x32_bf16 v[240:243], v[18:21], a[228:231], v[240:243]\n"
"ds_read_b64_tr_b16 a[218:219], v28 offset:5184\n"
"v_mfma_f32_16x16x32_bf16 v[244:247], v[52:55], a[224:227], v[244:247]\n"
"ds_read_b64_tr_b16 a[220:221], v28 offset:10304\n"
"v_mfma_f32_16x16x32_bf16 v[244:247], v[22:25], a[228:231], v[244:247]\n"
"s_waitcnt vmcnt(17)\n"
"s_waitcnt vmcnt(40)\n"
"s_waitcnt vmcnt(22)\n"
"ds_read_b64_tr_b16 a[222:223], v28 offset:15424\n"
"v_mfma_f32_16x16x32_bf16 v[248:251], v[56:59], a[224:227], v[248:251]\n"
"s_barrier\n"
"v_mfma_f32_16x16x32_bf16 v[248:251], v[36:39], a[228:231], v[248:251]\n"
"v_mfma_f32_16x16x32_bf16 v[252:255], v[60:63], a[224:227], v[252:255]\n"
"s_cmp_lg_u32 s38, 0\n"
"s_cbranch_scc1 L_pubo\n"
"v_mov_b32 v46, s76\n"
"v_mov_b32 v47, s77\n"
"s_mov_b64 exec, 1\n"
"buffer_store_dwordx2 v[46:47], off, s[24:27], s89 sc1\n"
"s_mov_b64 exec, -1\n"
"s_nop 1\n"
"L_pubo:\n"
"v_mfma_f32_16x16x32_bf16 v[252:255], v[6:9], a[228:231], v[252:255]\n"
"s_waitcnt lgkmcnt(8)\n"
"s_waitcnt vmcnt(30)\n"
"v_mfma_f32_16x16x32_bf16 v[64:67], a[64:67], a[240:243], v[64:67]\n"
"ds_read_b64_tr_b16 a[224:225], v29 offset:64\n"
"v_mfma_f32_16x16x32_bf16 v[64:67], a[80:83], a[244:247], v[64:67]\n"
"ds_read_b64_tr_b16 a[226:227], v29 offset:5184\n"
"v_mfma_f32_16x16x32_bf16 v[68:71], a[68:71], a[240:243], v[68:71]\n"
"s_bitset1_b32 s89, 3\n"
"ds_read_b64_tr_b16 a[228:229], v29 offset:10304\n"
"v_mfma_f32_16x16x32_bf16 v[68:71], a[84:87], a[244:247], v[68:71]\n"
"ds_read_b64_tr_b16 a[230:231], v29 offset:15424\n"
"v_mfma_f32_16x16x32_bf16 v[72:75], a[72:75], a[240:243], v[72:75]\n"
"v_mfma_f32_16x16x32_bf16 v[72:75], a[88:91], a[244:247], v[72:75]\n"
"buffer_load_dwordx4 a[160:163], v12, s[24:27], s78 offen offset:0 sc1\n"
"v_mfma_f32_16x16x32_bf16 v[76:79], a[76:79], a[240:243], v[76:79]\n"
"s_add_u32 m0, s95, 143360\n"
"v_mfma_f32_16x16x32_bf16 v[76:79], a[92:95], a[244:247], v[76:79]\n"
"s_waitcnt lgkmcnt(8)\n"
"buffer_load_dwordx4 v30, s[20:23], s40 offen offset:0 lds\n"
"v_mfma_f32_16x16x32_bf16 v[80:83], a[64:67], a[248:251], v[80:83]\n"
"ds_read_b64_tr_b16 a[240:241], v28 offset:128\n"
"v_mfma_f32_16x16x32_bf16 v[80:83], a[80:83], a[252:255], v[80:83]\n"
"ds_read_b64_tr_b16 a[242:243], v28 offset:5248\n"
"v_mfma_f32_16x16x32_bf16 v[84:87], a[68:71], a[248:251], v[84:87]\n"
"ds_read_b64_tr_b16 a[244:245], v28 offset:10368\n"
"v_mfma_f32_16x16x32_bf16 v[84:87], a[84:87], a[252:255], v[84:87]\n"
"ds_read_b64_tr_b16 a[246:247], v28 offset:15488\n"
"v_mfma_f32_16x16x32_bf16 v[88:91], a[72:75], a[248:251], v[88:91]\n"
"v_mfma_f32_16x16x32_bf16 v[88:91], a[88:91], a[252:255], v[88:91]\n"
"buffer_load_dwordx4 a[164:167], v12, s[24:27], s78 offen offset:1024 sc1\n"
"v_mfma_f32_16x16x32_bf16 v[92:95], a[76:79], a[248:251], v[92:95]\n"
"v_mfma_f32_16x16x32_bf16 v[92:95], a[92:95], a[252:255], v[92:95]\n"
"s_waitcnt lgkmcnt(8)\n"
"buffer_load_dwordx4 v31, s[20:23], s40 offen offset:1024 lds\n"
"v_mfma_f32_16x16x32_bf16 v[96:99], a[64:67], a[216:219], v[96:99]\n"
"ds_read_b64_tr_b16 a[248:249], v29 offset:128\n"
"v_mfma_f32_16x16x32_bf16 v[96:99], a[80:83], a[220:223], v[96:99]\n"
"ds_read_b64_tr_b16 a[250:251], v29 offset:5248\n"
"v_mfma_f32_16x16x32_bf16 v[100:103], a[68:71], a[216:219], v[100:103]\n"
"ds_read_b64_tr_b16 a[252:253], v29 offset:10368\n"
"v_mfma_f32_16x16x32_bf16 v[100:103], a[84:87], a[220:223], v[100:103]\n"
"ds_read_b64_tr_b16 a[254:255], v29 offset:15488\n"
"v_mfma_f32_16x16x32_bf16 v[104:107], a[72:75], a[216:219], v[104:107]\n"
"v_mfma_f32_16x16x32_bf16 v[104:107], a[88:91], a[220:223], v[104:107]\n"
"buffer_load_dwordx4 a[168:171], v12, s[24:27], s78 offen offset:2048 sc1\n"
"v_mfma_f32_16x16x32_bf16 v[108:111], a[76:79], a[216:219], v[108:111]\n"
"v_mfma_f32_16x16x32_bf16 v[108:111], a[92:95], a[220:223], v[108:111]\n"
"s_waitcnt lgkmcnt(8)\n"
"buffer_load_dwordx4 v32, s[20:23], s40 offen offset:2048 lds\n"
"v_mfma_f32_16x16x32_bf16 v[112:115], a[64:67], a[224:227], v[112:115]\n"
"ds_read_b64_tr_b16 a[216:217], v28 offset:192\n"
"v_mfma_f32_16x16x32_bf16 v[112:115], a[80:83], a[228:231], v[112:115]\n"
"ds_read_b64_tr_b16 a[218:219], v28 offset:5312\n"
"v_mfma_f32_16x16x32_bf16 v[116:119], a[68:71], a[224:227], v[116:119]\n"
"ds_read_b64_tr_b16 a[220:221], v28 offset:10432\n"
"v_mfma_f32_16x16x32_bf16 v[116:119], a[84:87], a[228:231], v[116:119]\n"
"ds_read_b64_tr_b16 a[222:223], v28 offset:15552\n"
"v_mfma_f32_16x16x32_bf16 v[120:123], a[72:75], a[224:227], v[120:123]\n"
"v_mfma_f32_16x16x32_bf16 v[120:123], a[88:91], a[228:231], v[120:123]\n"
"buffer_load_dwordx4 a[172:175], v12, s[24:27], s78 offen offset:3072 sc1\n"
"s_add_u32 s78, s78, 4096\n"
"v_mfma_f32_16x16x32_bf16 v[124:127], a[76:79], a[224:227], v[124:127]\n"
"s_waitcnt vmcnt(48)\n"
"v_mfma_f32_16x16x32_bf16 v[124:127], a[92:95], a[228:231], v[124:127]\n"
"s_waitcnt lgkmcnt(8)\n"
"buffer_load_dwordx4 v33, s[20:23], s40 offen offset:3072 lds\n"
"v_mfma_f32_16x16x32_bf16 v[160:163], a[64:67], a[240:243], v[160:163]\n"
"ds_read_b64_tr_b16 a[224:225], v29 offset:192\n"
"v_mfma_f32_16x16x32_bf16 v[160:163], a[80:83], a[244:247], v[160:163]\n"
"ds_read_b64_tr_b16 a[226:227], v29 offset:5312\n"
"v_mfma_f32_16x16x32_bf16 v[164:167], a[68:71], a[240:243], v[164:167]\n"
"ds_read_b64_tr_b16 a[228:229], v29 offset:10432\n"
"v_mfma_f32_16x16x32_bf16 v[164:167], a[84:87], a[244:247], v[164:167]\n"
"ds_read_b64_tr_b16 a[230:231], v29 offset:15552\n"
"v_mfma_f32_16x16x32_bf16 v[168:171], a[72:75], a[240:243], v[168:171]\n"
"v_mfma_f32_16x16x32_bf16 v[168:171], a[88:91], a[244:247], v[168:171]\n"
"buffer_load_dwordx4 a[176:179], v12, s[24:27], s78 offen offset:0 sc1\n"
"v_mfma_f32_16x16x32_bf16 v[172:175], a[76:79], a[240:243], v[172:175]\n"
"s_add_u32 m0, s95, 147456\n"
"v_mfma_f32_16x16x32_bf16 v[172:175], a[92:95], a[244:247], v[172:175]\n"
"s_waitcnt lgkmcnt(8)\n"
"buffer_load_dwordx4 v34, s[20:23], s40 offen offset:0 lds\n"
"v_mfma_f32_16x16x32_bf16 v[176:179], a[64:67], a[248:251], v[176:179]\n"
"s_add_u32 s40, s40, 81920\n"
"ds_read_b64_tr_b16 a[240:241], v28 offset:256\n"
"v_mfma_f32_16x16x32_bf16 v[176:179], a[80:83], a[252:255], v[176:179]\n"
"ds_read_b64_tr_b16 a[242:243], v28 offset:5376\n"
"v_mfma_f32_16x16x32_bf16 v[180:183], a[68:71], a[248:251], v[180:183]\n"
"ds_read_b64_tr_b16 a[244:245], v28 offset:10496\n"
"v_mfma_f32_16x16x32_bf16 v[180:183], a[84:87], a[252:255], v[180:183]\n"
"ds_read_b64_tr_b16 a[246:247], v28 offset:15616\n"
"v_mfma_f32_16x16x32_bf16 v[184:187], a[72:75], a[248:251], v[184:187]\n"
"v_mfma_f32_16x16x32_bf16 v[184:187], a[88:91], a[252:255], v[184:187]\n"
"buffer_load_dwordx4 a[180:183], v12, s[24:27], s78 offen offset:1024 sc1\n"
"v_mfma_f32_16x16x32_bf16 v[188:191], a[76:79], a[248:251], v[188:191]\n"
"v_mfma_f32_16x16x32_bf16 v[188:191], a[92:95], a[252:255], v[188:191]\n"
"s_waitcnt lgkmcnt(8)\n"
"buffer_load_dwordx4 a[184:187], v12, s[24:27], s78 offen offset:2048 sc1\n"
"v_mfma_f32_16x16x32_bf16 v[192:195], a[64:67], a[216:219], v[192:195]\n"
"ds_read_b64_tr_b16 a[248:249], v29 offset:256\n"
"v_mfma_f32_16x16x32_bf16 v[192:195], a[80:83], a[220:223], v[192:195]\n"
"ds_read_b64_tr_b16 a[250:251], v29 offset:5376\n"
"v_mfma_f32_16x16x32_bf16 v[196:199], a[68:71], a[216:219], v[196:199]\n"
"ds_read_b64_tr_b16 a[252:253], v29 offset:10496\n"
"v_mfma_f32_16x16x32_bf16 v[196:199], a[84:87], a[220:223], v[196:199]\n"
"ds_read_b64_tr_b16 a[254:255], v29 offset:15616\n"
"v_mfma_f32_16x16x32_bf16 v[200:203], a[72:75], a[216:219], v[200:203]\n"
"v_mfma_f32_16x16x32_bf16 v[200:203], a[88:91], a[220:223], v[200:203]\n"
"buffer_load_dwordx4 a[188:191], v12, s[24:27], s78 offen offset:3072 sc1\n"
"s_add_u32 s78, s78, 4096\n"
"v_mfma_f32_16x16x32_bf16 v[204:207], a[76:79], a[216:219], v[204:207]\n"
"v_mfma_f32_16x16x32_bf16 v[204:207], a[92:95], a[220:223], v[204:207]\n"
"s_waitcnt lgkmcnt(8)\n"
"v_mfma_f32_16x16x32_bf16 v[208:211], a[64:67], a[224:227], v[208:211]\n"
"ds_read_b64_tr_b16 a[216:217], v28 offset:20480\n"
"v_mfma_f32_16x16x32_bf16 v[208:211], a[80:83], a[228:231], v[208:211]\n"
"ds_read_b64_tr_b16 a[218:219], v28 offset:25600\n"
"v_mfma_f32_16x16x32_bf16 v[212:215], a[68:71], a[224:227], v[212:215]\n"
"ds_read_b64_tr_b16 a[220:221], v28 offset:30720\n"
"v_mfma_f32_16x16x32_bf16 v[212:215], a[84:87], a[228:231], v[212:215]\n"
"ds_read_b64_tr_b16 a[222:223], v28 offset:35840\n"
"v_mfma_f32_16x16x32_bf16 v[216:219], a[72:75], a[224:227], v[216:219]\n"
"v_mfma_f32_16x16x32_bf16 v[216:219], a[88:91], a[228:231], v[216:219]\n"
"v_mfma_f32_16x16x32_bf16 v[220:223], a[76:79], a[224:227], v[220:223]\n"
"v_mfma_f32_16x16x32_bf16 v[220:223], a[92:95], a[228:231], v[220:223]\n"
"s_waitcnt lgkmcnt(8)\n"
"v_mfma_f32_16x16x32_bf16 v[224:227], a[64:67], a[240:243], v[224:227]\n"
"ds_read_b64_tr_b16 a[224:225], v29 offset:20480\n"
"v_mfma_f32_16x16x32_bf16 v[224:227], a[80:83], a[244:247], v[224:227]\n"
"ds_read_b64_tr_b16 a[226:227], v29 offset:25600\n"
"v_mfma_f32_16x16x32_bf16 v[228:231], a[68:71], a[240:243], v[228:231]\n"
"ds_read_b64_tr_b16 a[228:229], v29 offset:30720\n"
"v_mfma_f32_16x16x32_bf16 v[228:231], a[84:87], a[244:247], v[228:231]\n"
"ds_read_b64_tr_b16 a[230:231], v29 offset:35840\n"
"v_mfma_f32_16x16x32_bf16 v[232:235], a[72:75], a[240:243], v[232:235]\n"
"v_mfma_f32_16x16x32_bf16 v[232:235], a[88:91], a[244:247], v[232:235]\n"
"v_mfma_f32_16x16x32_bf16 v[236:239], a[76:79], a[240:243], v[236:239]\n"
"v_mfma_f32_16x16x32_bf16 v[236:239], a[92:95], a[244:247], v[236:239]\n"
"s_waitcnt lgkmcnt(8)\n"
"v_mfma_f32_16x16x32_bf16 v[240:243], a[64:67], a[248:251], v[240:243]\n"
"ds_read_b64_tr_b16 a[240:241], v28 offset:20544\n"
"v_mfma_f32_16x16x32_bf16 v[240:243], a[80:83], a[252:255], v[240:243]\n"
"ds_read_b64_tr_b16 a[242:243], v28 offset:25664\n"
"v_mfma_f32_16x16x32_bf16 v[244:247], a[68:71], a[248:251], v[244:247]\n"
"ds_read_b64_tr_b16 a[244:245], v28 offset:30784\n"
"v_mfma_f32_16x16x32_bf16 v[244:247], a[84:87], a[252:255], v[244:247]\n"
"s_waitcnt vmcnt(16)\n"
"s_waitcnt vmcnt(40)\n"
"ds_read_b64_tr_b16 a[246:247], v28 offset:35904\n"
"v_mfma_f32_16x16x32_bf16 v[248:251], a[72:75], a[248:251], v[248:251]\n"
"s_barrier\n"
"v_mfma_f32_16x16x32_bf16 v[248:251], a[88:91], a[252:255], v[248:251]\n"
"v_mfma_f32_16x16x32_bf16 v[252:255], a[76:79], a[248:251], v[252:255]\n"
"v_mfma_f32_16x16x32_bf16 v[252:255], a[92:95], a[252:255], v[252:255]\n"
"s_waitcnt lgkmcnt(8)\n"
"s_waitcnt vmcnt(26)\n"
"v_mfma_f32_16x16x32_bf16 v[64:67], a[96:99], a[216:219], v[64:67]\n"
"ds_read_b64_tr_b16 a[248:249], v29 offset:20544\n"
"v_mfma_f32_16x16x32_bf16 v[64:67], a[112:115], a[220:223], v[64:67]\n"
"ds_read_b64_tr_b16 a[250:251], v29 offset:25664\n"
"L_pg19:\n"
"s_cbranch_execz L_bk4\n"
"v_mfma_f32_16x16x32_bf16 v[68:71], a[100:103], a[216:219], v[68:71]\n"
"ds_read_b64_tr_b16 a[252:253], v29 offset:30784\n"
"v_mfma_f32_16x16x32_bf16 v[68:71], a[116:119], a[220:223], v[68:71]\n"
"ds_read_b64_tr_b16 a[254:255], v29 offset:35904\n"
"v_mfma_f32_16x16x32_bf16 v[72:75], a[104:107], a[216:219], v[72:75]\n"
"v_mfma_f32_16x16x32_bf16 v[72:75], a[120:123], a[220:223], v[72:75]\n"
"buffer_load_dwordx4 a[0:3], v12, s[24:27], s78 offen offset:0 sc1\n"
"v_mfma_f32_16x16x32_bf16 v[76:79], a[108:111], a[216:219], v[76:79]\n"
"s_add_u32 m0, s95, 40960\n"
"v_mfma_f32_16x16x32_bf16 v[76:79], a[124:127], a[220:223], v[76:79]\n"
"s_waitcnt lgkmcnt(8)\n"
"buffer_load_dwordx4 v30, s[20:23], s40 offen offset:0 lds\n"
"v_mfma_f32_16x16x32_bf16 v[80:83], a[96:99], a[224:227], v[80:83]\n"
"ds_read_b64_tr_b16 a[216:217], v28 offset:20608\n"
"v_mfma_f32_16x16x32_bf16 v[80:83], a[112:115], a[228:231], v[80:83]\n"
"ds_read_b64_tr_b16 a[218:219], v28 offset:25728\n"
"v_mfma_f32_16x16x32_bf16 v[84:87], a[100:103], a[224:227], v[84:87]\n"
"ds_read_b64_tr_b16 a[220:221], v28 offset:30848\n"
"v_mfma_f32_16x16x32_bf16 v[84:87], a[116:119], a[228:231], v[84:87]\n"
"ds_read_b64_tr_b16 a[222:223], v28 offset:35968\n"
"v_mfma_f32_16x16x32_bf16 v[88:91], a[104:107], a[224:227], v[88:91]\n"
"v_mfma_f32_16x16x32_bf16 v[88:91], a[120:123], a[228:231], v[88:91]\n"
"buffer_load_dwordx4 a[4:7], v12, s[24:27], s78 offen offset:1024 sc1\n"
"v_mfma_f32_16x16x32_bf16 v[92:95], a[108:111], a[224:227], v[92:95]\n"
"v_mfma_f32_16x16x32_bf16 v[92:95], a[124:127], a[228:231], v[92:95]\n"
"s_waitcnt lgkmcnt(8)\n"
"buffer_load_dwordx4 v31, s[20:23], s40 offen offset:1024 lds\n"
"v_mfma_f32_16x16x32_bf16 v[96:99], a[96:99], a[240:243], v[96:99]\n"
"ds_read_b64_tr_b16 a[224:225], v29 offset:20608\n"
"v_mfma_f32_16x16x32_bf16 v[96:99], a[112:115], a[244:247], v[96:99]\n"
"ds_read_b64_tr_b16 a[226:227], v29 offset:25728\n"
"v_mfma_f32_16x16x32_bf16 v[100:103], a[100:103], a[240:243], v[100:103]\n"
"ds_read_b64_tr_b16 a[228:229], v29 offset:30848\n"
"v_mfma_f32_16x16x32_bf16 v[100:103], a[116:119], a[244:247], v[100:103]\n"
"ds_read_b64_tr_b16 a[230:231], v29 offset:35968\n"
"s_mov_b64 exec, 0\n"
"s_cmp_eq_u32 s38, 0\n"
"s_cbranch_scc1 L_pg20\n"
"s_cmp_eq_u32 s38, 3072\n"
"s_cbranch_scc1 L_pg21\n"
"s_cmp_eq_u32 s38, 6144\n"
"s_cbranch_scc1 L_pg22\n"
"s_branch L_pg23\n"
"L_bk5:\n"
"s_mov_b64 exec, -1\n"
"v_mfma_f32_16x16x32_bf16 v[104:107], a[104:107], a[240:243], v[104:107]\n"
"v_mfma_f32_16x16x32_bf16 v[104:107], a[120:123], a[244:247], v[104:107]\n"
"buffer_load_dwordx4 a[8:11], v12, s[24:27], s78 offen offset:2048 sc1\n"
"v_mfma_f32_16x16x32_bf16 v[108:111], a[108:111], a[240:243], v[108:111]\n"
"v_mfma_f32_16x16x32_bf16 v[108:111], a[124:127], a[244:247], v[108:111]\n"
"s_waitcnt lgkmcnt(8)\n"
"buffer_load_dwordx4 v32, s[20:23], s40 offen offset:2048 lds\n"
"v_mfma_f32_16x16x32_bf16 v[112:115], a[96:99], a[248:251], v[112:115]\n"
"ds_read_b64_tr_b16 a[240:241], v28 offset:20672\n"
"v_mfma_f32_16x16x32_bf16 v[112:115], a[112:115], a[252:255], v[112:115]\n"
"ds_read_b64_tr_b16 a[242:243], v28 offset:25792\n"
"v_mfma_f32_16x16x32_bf16 v[116:119], a[100:103], a[248:251], v[116:119]\n"
"ds_read_b64_tr_b16 a[244:245], v28 offset:30912\n"
"v_mfma_f32_16x16x32_bf16 v[116:119], a[116:119], a[252:255], v[116:119]\n"
"ds_read_b64_tr_b16 a[246:247], v28 offset:36032\n"
"v_mfma_f32_16x16x32_bf16 v[120:123], a[104:107], a[248:251], v[120:123]\n"
"v_mfma_f32_16x16x32_bf16 v[120:123], a[120:123], a[252:255], v[120:123]\n"
"buffer_load_dwordx4 a[12:15], v12, s[24:27], s78 offen offset:3072 sc1\n"
"s_add_u32 s78, s78, 4096\n"
"v_mfma_f32_16x16x32_bf16 v[124:127], a[108:111], a[248:251], v[124:127]\n"
"s_waitcnt vmcnt(48)\n"
"v_mfma_f32_16x16x32_bf16 v[124:127], a[124:127], a[252:255], v[124:127]\n"
"s_waitcnt lgkmcnt(8)\n"
"buffer_load_dwordx4 v33, s[20:23], s40 offen offset:3072 lds\n"
"v_mfma_f32_16x16x32_bf16 v[160:163], a[96:99], a[216:219], v[160:163]\n"
"ds_read_b64_tr_b16 a[248:249], v29 offset:20672\n"
"v_mfma_f32_16x16x32_bf16 v[160:163], a[112:115], a[220:223], v[160:163]\n"
"ds_read_b64_tr_b16 a[250:251], v29 offset:25792\n"
"v_mfma_f32_16x16x32_bf16 v[164:167], a[100:103], a[216:219], v[164:167]\n"
"ds_read_b64_tr_b16 a[252:253], v29 offset:30912\n"
"v_mfma_f32_16x16x32_bf16 v[164:167], a[116:119], a[220:223], v[164:167]\n"
"ds_read_b64_tr_b16 a[254:255], v29 offset:36032\n"
"v_mfma_f32_16x16x32_bf16 v[168:171], a[104:107], a[216:219], v[168:171]\n"
"v_mfma_f32_16x16x32_bf16 v[168:171], a[120:123], a[220:223], v[168:171]\n"
"buffer_load_dwordx4 a[16:19], v12, s[24:27], s78 offen offset:0 sc1\n"
"v_mfma_f32_16x16x32_bf16 v[172:175], a[108:111], a[216:219], v[172:175]\n"
"s_add_u32 m0, s95, 45056\n"
"v_mfma_f32_16x16x32_bf16 v[172:175], a[124:127], a[220:223], v[172:175]\n"
"s_waitcnt lgkmcnt(8)\n"
"buffer_load_dwordx4 v34, s[20:23], s40 offen offset:0 lds\n"
"v_mfma_f32_16x16x32_bf16 v[176:179], a[96:99], a[224:227], v[176:179]\n"
"s_add_u32 s40, s40, 81920\n"
"ds_read_b64_tr_b16 a[216:217], v28 offset:20736\n"
"v_mfma_f32_16x16x32_bf16 v[176:179], a[112:115], a[228:231], v[176:179]\n"
"ds_read_b64_tr_b16 a[218:219], v28 offset:25856\n"
"v_mfma_f32_16x16x32_bf16 v[180:183], a[100:103], a[224:227], v[180:183]\n"
"ds_read_b64_tr_b16 a[220:221], v28 offset:30976\n"
"v_mfma_f32_16x16x32_bf16 v[180:183], a[116:119], a[228:231], v[180:183]\n"
"ds_read_b64_tr_b16 a[222:223], v28 offset:36096\n"
"v_mfma_f32_16x16x32_bf16 v[184:187], a[104:107], a[224:227], v[184:187]\n"
"v_mfma_f32_16x16x32_bf16 v[184:187], a[120:123], a[228:231], v[184:187]\n"
"buffer_load_dwordx4 a[20:23], v12, s[24:27], s78 offen offset:1024 sc1\n"
"v_mfma_f32_16x16x32_bf16 v[188:191], a[108:111], a[224:227], v[188:191]\n"
"v_mfma_f32_16x16x32_bf16 v[188:191], a[124:127], a[228:231], v[188:191]\n"
"s_waitcnt lgkmcnt(8)\n"
"buffer_load_dwordx4 a[24:27], v12, s[24:27], s78 offen offset:2048 sc1\n"
"v_mfma_f32_16x16x32_bf16 v[192:195], a[96:99], a[240:243], v[192:195]\n"
"ds_read_b64_tr_b16 a[224:225], v29 offset:20736\n"
"v_mfma_f32_16x16x32_bf16 v[192:195], a[112:115], a[244:247], v[192:195]\n"
"ds_read_b64_tr_b16 a[226:227], v29 offset:25856\n"
"v_mfma_f32_16x16x32_bf16 v[196:199], a[100:103], a[240:243], v[196:199]\n"
"ds_read_b64_tr_b16 a[228:229], v29 offset:30976\n"
"v_mfma_f32_16x16x32_bf16 v[196:199], a[116:119], a[244:247], v[196:199]\n"
"ds_read_b64_tr_b16 a[230:231], v29 offset:36096\n"
"v_mfma_f32_16x16x32_bf16 v[200:203], a[104:107], a[240:243], v[200:203]\n"
"v_mfma_f32_16x16x32_bf16 v[200:203], a[120:123], a[244:247], v[200:203]\n"
"buffer_load_dwordx4 a[28:31], v12, s[24:27], s78 offen offset:3072 sc1\n"
"s_add_u32 s78, s78, 4096\n"
"v_mfma_f32_16x16x32_bf16 v[204:207], a[108:111], a[240:243], v[204:207]\n"
"v_mfma_f32_16x16x32_bf16 v[204:207], a[124:127], a[244:247], v[204:207]\n"
"s_waitcnt lgkmcnt(8)\n"
"v_mfma_f32_16x16x32_bf16 v[208:211], a[96:99], a[248:251], v[208:211]\n"
"ds_read_b64_tr_b16 a[240:241], v0 offset:0\n"
"v_mfma_f32_16x16x32_bf16 v[208:211], a[112:115], a[252:255], v[208:211]\n"
"ds_read_b64_tr_b16 a[242:243], v0 offset:5120\n"
"v_mfma_f32_16x16x32_bf16 v[212:215], a[100:103], a[248:251], v[212:215]\n"
"ds_read_b64_tr_b16 a[244:245], v0 offset:10240\n"
"v_mfma_f32_16x16x32_bf16 v[212:215], a[116:119], a[252:255], v[212:215]\n"
"ds_read_b64_tr_b16 a[246:247], v0 offset:15360\n"
"v_mfma_f32_16x16x32_bf16 v[216:219], a[104:107], a[248:251], v[216:219]\n"
"v_mfma_f32_16x16x32_bf16 v[216:219], a[120:123], a[252:255], v[216:219]\n"
"v_mfma_f32_16x16x32_bf16 v[220:223], a[108:111], a[248:251], v[220:223]\n"
"v_mfma_f32_16x16x32_bf16 v[220:223], a[124:127], a[252:255], v[220:223]\n"
"s_waitcnt lgkmcnt(8)\n"
"v_mfma_f32_16x16x32_bf16 v[224:227], a[96:99], a[216:219], v[224:227]\n"
"ds_read_b64_tr_b16 a[248:249], v1 offset:0\n"
"v_mfma_f32_16x16x32_bf16 v[224:227], a[112:115], a[220:223], v[224:227]\n"
"ds_read_b64_tr_b16 a[250:251], v1 offset:5120\n"
"v_mfma_f32_16x16x32_bf16 v[228:231], a[100:103], a[216:219], v[228:231]\n"
"ds_read_b64_tr_b16 a[252:253], v1 offset:10240\n"
"v_mfma_f32_16x16x32_bf16 v[228:231], a[116:119], a[220:223], v[228:231]\n"
"ds_read_b64_tr_b16 a[254:255], v1 offset:15360\n"
"v_lshlrev_b32 v26, 3, v12\n"
"v_mfma_f32_16x16x32_bf16 v[232:235], a[104:107], a[216:219], v[232:235]\n"
"v_mfma_f32_16x16x32_bf16 v[232:235], a[120:123], a[220:223], v[232:235]\n"
"v_and_b32 v26, 0x780, v26\n"
"v_mfma_f32_16x16x32_bf16 v[236:239], a[108:111], a[216:219], v[236:239]\n"
"v_mfma_f32_16x16x32_bf16 v[236:239], a[124:127], a[220:223], v[236:239]\n"
"s_waitcnt lgkmcnt(8)\n"
"global_load_dword v27, v26, s[60:61]\n"
"v_mfma_f32_16x16x32_bf16 v[240:243], a[96:99], a[224:227], v[240:243]\n"
"ds_read_b64_tr_b16 a[216:217], v0 offset:64\n"
"v_mfma_f32_16x16x32_bf16 v[240:243], a[112:115], a[228:231], v[240:243]\n"
"ds_read_b64_tr_b16 a[218:219], v0 offset:5184\n"
"v_mfma_f32_16x16x32_bf16 v[244:247], a[100:103], a[224:227], v[244:247]\n"
"ds_read_b64_tr_b16 a[220:221], v0 offset:10304\n"
"v_mfma_f32_16x16x32_bf16 v[244:247], a[116:119], a[228:231], v[244:247]\n"
"s_waitcnt vmcnt(17)\n"
"s_waitcnt vmcnt(40)\n"
"ds_read_b64_tr_b16 a[222:223], v0 offset:15424\n"
"v_mfma_f32_16x16x32_bf16 v[248:251], a[104:107], a[224:227], v[248:251]\n"
"s_barrier\n"
"v_mfma_f32_16x16x32_bf16 v[248:251], a[120:123], a[228:231], v[248:251]\n"
"v_mfma_f32_16x16x32_bf16 v[252:255], a[108:111], a[224:227], v[252:255]\n"
"v_mfma_f32_16x16x32_bf16 v[252:255], a[124:127], a[228:231], v[252:255]\n"
"s_waitcnt lgkmcnt(8)\n"
"s_waitcnt vmcnt(27)\n"
"s_bitcmp1_b32 s93, 0\n"
"s_cbranch_scc1 L_o2\n"
"L_ox:\n"
"v_mfma_f32_16x16x32_bf16 v[64:67], a[128:131], a[240:243], v[64:67]\n"
"s_mov_b32 s9, s78\n"
"s_add_u32 s78, s78, 16384\n"
"ds_read_b64_tr_b16 a[224:225], v1 offset:64\n"
"v_mfma_f32_16x16x32_bf16 v[64:67], a[144:147], a[244:247], v[64:67]\n"
"s_mov_b32 s8, s40\n"
"s_add_u32 s40, s40, 163840\n"
"ds_read_b64_tr_b16 a[226:227], v1 offset:5184\n"
"v_mfma_f32_16x16x32_bf16 v[68:71], a[132:135], a[240:243], v[68:71]\n"
"ds_read_b64_tr_b16 a[228:229], v1 offset:10304\n"
"v_mfma_f32_16x16x32_bf16 v[68:71], a[148:151], a[244:247], v[68:71]\n"
"ds_read_b64_tr_b16 a[230:231], v1 offset:15424\n"
"v_mfma_f32_16x16x32_bf16 v[72:75], a[136:139], a[240:243], v[72:75]\n"
"v_mfma_f32_16x16x32_bf16 v[72:75], a[152:155], a[244:247], v[72:75]\n"
"buffer_load_dwordx4 v[128:131], v12, s[24:27], s78 offen offset:0 sc1\n"
"v_mfma_f32_16x16x32_bf16 v[76:79], a[140:143], a[240:243], v[76:79]\n"
"s_add_u32 m0, s95, 102400\n"
"v_mfma_f32_16x16x32_bf16 v[76:79], a[156:159], a[244:247], v[76:79]\n"
"s_waitcnt lgkmcnt(8)\n"
"buffer_load_dwordx4 v30, s[20:23], s40 offen offset:0 lds\n"
"v_mfma_f32_16x16x32_bf16 v[80:83], a[128:131], a[248:251], v[80:83]\n"
"ds_read_b64_tr_b16 a[240:241], v0 offset:128\n"
"v_mfma_f32_16x16x32_bf16 v[80:83], a[144:147], a[252:255], v[80:83]\n"
"ds_read_b64_tr_b16 a[242:243], v0 offset:5248\n"
"v_mfma_f32_16x16x32_bf16 v[84:87], a[132:135], a[248:251], v[84:87]\n"
"ds_read_b64_tr_b16 a[244:245], v0 offset:10368\n"
"v_mfma_f32_16x16x32_bf16 v[84:87], a[148:151], a[252:255], v[84:87]\n"
"ds_read_b64_tr_b16 a[246:247], v0 offset:15488\n"
"v_mfma_f32_16x16x32_bf16 v[88:91], a[136:139], a[248:251], v[88:91]\n"
"v_mfma_f32_16x16x32_bf16 v[88:91], a[152:155], a[252:255], v[88:91]\n"
"buffer_load_dwordx4 v[132:135], v12, s[24:27], s78 offen offset:1024 sc1\n"
"v_mfma_f32_16x16x32_bf16 v[92:95], a[140:143], a[248:251], v[92:95]\n"
"v_mfma_f32_16x16x32_bf16 v[92:95], a[156:159], a[252:255], v[92:95]\n"
"s_waitcnt lgkmcnt(8)\n"
"buffer_load_dwordx4 v31, s[20:23], s40 offen offset:1024 lds\n"
"v_mfma_f32_16x16x32_bf16 v[96:99], a[128:131], a[216:219], v[96:99]\n"
"ds_read_b64_tr_b16 a[248:249], v1 offset:128\n"
"v_mfma_f32_16x16x32_bf16 v[96:99], a[144:147], a[220:223], v[96:99]\n"
"ds_read_b64_tr_b16 a[250:251], v1 offset:5248\n"
"v_mfma_f32_16x16x32_bf16 v[100:103], a[132:135], a[216:219], v[100:103]\n"
"ds_read_b64_tr_b16 a[252:253], v1 offset:10368\n"
"v_mfma_f32_16x16x32_bf16 v[100:103], a[148:151], a[220:223], v[100:103]\n"
"ds_read_b64_tr_b16 a[254:255], v1 offset:15488\n"
"v_mfma_f32_16x16x32_bf16 v[104:107], a[136:139], a[216:219], v[104:107]\n"
"v_mfma_f32_16x16x32_bf16 v[104:107], a[152:155], a[220:223], v[104:107]\n"
"buffer_load_dwordx4 v[136:139], v12, s[24:27], s78 offen offset:2048 sc1\n"
"v_mfma_f32_16x16x32_bf16 v[108:111], a[140:143], a[216:219], v[108:111]\n"
"v_mfma_f32_16x16x32_bf16 v[108:111], a[156:159], a[220:223], v[108:111]\n"
"s_waitcnt lgkmcnt(8)\n"
"buffer_load_dwordx4 v32, s[20:23], s40 offen offset:2048 lds\n"
"v_mfma_f32_16x16x32_bf16 v[112:115], a[128:131], a[224:227], v[112:115]\n"
"ds_read_b64_tr_b16 a[216:217], v0 offset:192\n"
"v_mfma_f32_16x16x32_bf16 v[112:115], a[144:147], a[228:231], v[112:115]\n"
"ds_read_b64_tr_b16 a[218:219], v0 offset:5312\n"
"v_mfma_f32_16x16x32_bf16 v[116:119], a[132:135], a[224:227], v[116:119]\n"
"ds_read_b64_tr_b16 a[220:221], v0 offset:10432\n"
"v_mfma_f32_16x16x32_bf16 v[116:119], a[148:151], a[228:231], v[116:119]\n"
"ds_read_b64_tr_b16 a[222:223], v0 offset:15552\n"
"v_mfma_f32_16x16x32_bf16 v[120:123], a[136:139], a[224:227], v[120:123]\n"
"v_mfma_f32_16x16x32_bf16 v[120:123], a[152:155], a[228:231], v[120:123]\n"
"buffer_load_dwordx4 v[140:143], v12, s[24:27], s78 offen offset:3072 sc1\n"
"s_add_u32 s78, s78, 4096\n"
"v_mfma_f32_16x16x32_bf16 v[124:127], a[140:143], a[224:227], v[124:127]\n"
"s_waitcnt vmcnt(48)\n"
"v_mfma_f32_16x16x32_bf16 v[124:127], a[156:159], a[228:231], v[124:127]\n"
"s_waitcnt lgkmcnt(8)\n"
"buffer_load_dwordx4 v33, s[20:23], s40 offen offset:3072 lds\n"
"v_mfma_f32_16x16x32_bf16 v[160:163], a[128:131], a[240:243], v[160:163]\n"
"ds_read_b64_tr_b16 a[224:225], v1 offset:192\n"
"v_mfma_f32_16x16x32_bf16 v[160:163], a[144:147], a[244:247], v[160:163]\n"
"ds_read_b64_tr_b16 a[226:227], v1 offset:5312\n"
"v_mfma_f32_16x16x32_bf16 v[164:167], a[132:135], a[240:243], v[164:167]\n"
"ds_read_b64_tr_b16 a[228:229], v1 offset:10432\n"
"v_mfma_f32_16x16x32_bf16 v[164:167], a[148:151], a[244:247], v[164:167]\n"
"ds_read_b64_tr_b16 a[230:231], v1 offset:15552\n"
"v_mfma_f32_16x16x32_bf16 v[168:171], a[136:139], a[240:243], v[168:171]\n"
"v_mfma_f32_16x16x32_bf16 v[168:171], a[152:155], a[244:247], v[168:171]\n"
"buffer_load_dwordx4 v[144:147], v12, s[24:27], s78 offen offset:0 sc1\n"
"v_mfma_f32_16x16x32_bf16 v[172:175], a[140:143], a[240:243], v[172:175]\n"
"s_add_u32 m0, s95, 106496\n"
"v_mfma_f32_16x16x32_bf16 v[172:175], a[156:159], a[244:247], v[172:175]\n"
"s_waitcnt lgkmcnt(8)\n"
"buffer_load_dwordx4 v34, s[20:23], s40 offen offset:0 lds\n"
"v_mfma_f32_16x16x32_bf16 v[176:179], a[128:131], a[248:251], v[176:179]\n"
"s_add_u32 s40, s40, 81920\n"
"ds_read_b64_tr_b16 a[240:241], v0 offset:256\n"
"v_mfma_f32_16x16x32_bf16 v[176:179], a[144:147], a[252:255], v[176:179]\n"
"ds_read_b64_tr_b16 a[242:243], v0 offset:5376\n"
"v_mfma_f32_16x16x32_bf16 v[180:183], a[132:135], a[248:251], v[180:183]\n"
"ds_read_b64_tr_b16 a[244:245], v0 offset:10496\n"
"v_mfma_f32_16x16x32_bf16 v[180:183], a[148:151], a[252:255], v[180:183]\n"
"ds_read_b64_tr_b16 a[246:247], v0 offset:15616\n"
"v_mfma_f32_16x16x32_bf16 v[184:187], a[136:139], a[248:251], v[184:187]\n"
"v_mfma_f32_16x16x32_bf16 v[184:187], a[152:155], a[252:255], v[184:187]\n"
"buffer_load_dwordx4 v[148:151], v12, s[24:27], s78 offen offset:1024 sc1\n"
"v_mfma_f32_16x16x32_bf16 v[188:191], a[140:143], a[248:251], v[188:191]\n"
"v_mfma_f32_16x16x32_bf16 v[188:191], a[156:159], a[252:255], v[188:191]\n"
"s_waitcnt lgkmcnt(8)\n"
"buffer_load_dwordx4 v[152:155], v12, s[24:27], s78 offen offset:2048 sc1\n"
"v_mfma_f32_16x16x32_bf16 v[192:195], a[128:131], a[216:219], v[192:195]\n"
"ds_read_b64_tr_b16 a[248:249], v1 offset:256\n"
"v_mfma_f32_16x16x32_bf16 v[192:195], a[144:147], a[220:223], v[192:195]\n"
"ds_read_b64_tr_b16 a[250:251], v1 offset:5376\n"
"v_mfma_f32_16x16x32_bf16 v[196:199], a[132:135], a[216:219], v[196:199]\n"
"ds_read_b64_tr_b16 a[252:253], v1 offset:10496\n"
"v_mfma_f32_16x16x32_bf16 v[196:199], a[148:151], a[220:223], v[196:199]\n"
"ds_read_b64_tr_b16 a[254:255], v1 offset:15616\n"
"v_mfma_f32_16x16x32_bf16 v[200:203], a[136:139], a[216:219], v[200:203]\n"
"v_mfma_f32_16x16x32_bf16 v[200:203], a[152:155], a[220:223], v[200:203]\n"
"buffer_load_dwordx4 v[156:159], v12, s[24:27], s78 offen offset:3072 sc1\n"
"s_add_u32 s78, s78, 4096\n"
"v_mfma_f32_16x16x32_bf16 v[204:207], a[140:143], a[216:219], v[204:207]\n"
"v_mfma_f32_16x16x32_bf16 v[204:207], a[156:159], a[220:223], v[204:207]\n"
"s_waitcnt lgkmcnt(8)\n"
"v_mfma_f32_16x16x32_bf16 v[208:211], a[128:131], a[224:227], v[208:211]\n"
"ds_read_b64_tr_b16 a[216:217], v0 offset:20480\n"
"v_mfma_f32_16x16x32_bf16 v[208:211], a[144:147], a[228:231], v[208:211]\n"
"ds_read_b64_tr_b16 a[218:219], v0 offset:25600\n"
"v_mfma_f32_16x16x32_bf16 v[212:215], a[132:135], a[224:227], v[212:215]\n"
"ds_read_b64_tr_b16 a[220:221], v0 offset:30720\n"
"v_mfma_f32_16x16x32_bf16 v[212:215], a[148:151], a[228:231], v[212:215]\n"
"ds_read_b64_tr_b16 a[222:223], v0 offset:35840\n"
"v_mfma_f32_16x16x32_bf16 v[216:219], a[136:139], a[224:227], v[216:219]\n"
"v_mfma_f32_16x16x32_bf16 v[216:219], a[152:155], a[228:231], v[216:219]\n"
"v_mfma_f32_16x16x32_bf16 v[220:223], a[140:143], a[224:227], v[220:223]\n"
"v_mfma_f32_16x16x32_bf16 v[220:223], a[156:159], a[228:231], v[220:223]\n"
"s_waitcnt lgkmcnt(8)\n"
"v_mfma_f32_16x16x32_bf16 v[224:227], a[128:131], a[240:243], v[224:227]\n"
"ds_read_b64_tr_b16 a[224:225], v1 offset:20480\n"
"v_mfma_f32_16x16x32_bf16 v[224:227], a[144:147], a[244:247], v[224:227]\n"
"ds_read_b64_tr_b16 a[226:227], v1 offset:25600\n"
"v_mfma_f32_16x16x32_bf16 v[228:231], a[132:135], a[240:243], v[228:231]\n"
"ds_read_b64_tr_b16 a[228:229], v1 offset:30720\n"
"v_mfma_f32_16x16x32_bf16 v[228:231], a[148:151], a[244:247], v[228:231]\n"
"ds_read_b64_tr_b16 a[230:231], v1 offset:35840\n"
"v_mfma_f32_16x16x32_bf16 v[232:235], a[136:139], a[240:243], v[232:235]\n"
"v_mfma_f32_16x16x32_bf16 v[232:235], a[152:155], a[244:247], v[232:235]\n"
"v_mfma_f32_16x16x32_bf16 v[236:239], a[140:143], a[240:243], v[236:239]\n"
"v_mfma_f32_16x16x32_bf16 v[236:239], a[156:159], a[244:247], v[236:239]\n"
"s_waitcnt lgkmcnt(8)\n"
"v_mfma_f32_16x16x32_bf16 v[240:243], a[128:131], a[248:251], v[240:243]\n"
"ds_read_b64_tr_b16 a[240:241], v0 offset:20544\n"
"v_mfma_f32_16x16x32_bf16 v[240:243], a[144:147], a[252:255], v[240:243]\n"
"ds_read_b64_tr_b16 a[242:243], v0 offset:25664\n"
"v_mfma_f32_16x16x32_bf16 v[244:247], a[132:135], a[248:251], v[244:247]\n"
"ds_read_b64_tr_b16 a[244:245], v0 offset:30784\n"
"v_mfma_f32_16x16x32_bf16 v[244:247], a[148:151], a[252:255], v[244:247]\n"
"s_waitcnt vmcnt(17)\n"
"s_waitcnt vmcnt(40)\n"
"ds_read_b64_tr_b16 a[246:247], v0 offset:35904\n"
"v_mfma_f32_16x16x32_bf16 v[248:251], a[136:139], a[248:251], v[248:251]\n"
"s_barrier\n"
"v_mfma_f32_16x16x32_bf16 v[248:251], a[152:155], a[252:255], v[248:251]\n"
"v_mfma_f32_16x16x32_bf16 v[252:255], a[140:143], a[248:251], v[252:255]\n"
"v_mfma_f32_16x16x32_bf16 v[252:255], a[156:159], a[252:255], v[252:255]\n"
"s_waitcnt lgkmcnt(8)\n"
"s_waitcnt vmcnt(27)\n"
"v_mfma_f32_16x16x32_bf16 v[64:67], a[160:163], a[216:219], v[64:67]\n"
"ds_read_b64_tr_b16 a[248:249], v1 offset:20544\n"
"v_mfma_f32_16x16x32_bf16 v[64:67], a[176:179], a[220:223], v[64:67]\n"
"ds_read_b64_tr_b16 a[250:251], v1 offset:25664\n"
"v_mfma_f32_16x16x32_bf16 v[68:71], a[164:167], a[216:219], v[68:71]\n"
"ds_read_b64_tr_b16 a[252:253], v1 offset:30784\n"
"v_mfma_f32_16x16x32_bf16 v[68:71], a[180:183], a[220:223], v[68:71]\n"
"ds_read_b64_tr_b16 a[254:255], v1 offset:35904\n"
"v_mfma_f32_16x16x32_bf16 v[72:75], a[168:171], a[216:219], v[72:75]\n"
"v_mfma_f32_16x16x32_bf16 v[72:75], a[184:187], a[220:223], v[72:75]\n"
"buffer_load_dwordx4 a[128:131], v12, s[24:27], s78 offen offset:0 sc1\n"
"v_mfma_f32_16x16x32_bf16 v[76:79], a[172:175], a[216:219], v[76:79]\n"
"s_add_u32 m0, s95, 122880\n"
"v_mfma_f32_16x16x32_bf16 v[76:79], a[188:191], a[220:223], v[76:79]\n"
"s_waitcnt lgkmcnt(8)\n"
"buffer_load_dwordx4 v30, s[20:23], s40 offen offset:0 lds\n"
"v_mfma_f32_16x16x32_bf16 v[80:83], a[160:163], a[224:227], v[80:83]\n"
"ds_read_b64_tr_b16 a[216:217], v0 offset:20608\n"
"v_mfma_f32_16x16x32_bf16 v[80:83], a[176:179], a[228:231], v[80:83]\n"
"ds_read_b64_tr_b16 a[218:219], v0 offset:25728\n"
"v_mfma_f32_16x16x32_bf16 v[84:87], a[164:167], a[224:227], v[84:87]\n"
"ds_read_b64_tr_b16 a[220:221], v0 offset:30848\n"
"v_mfma_f32_16x16x32_bf16 v[84:87], a[180:183], a[228:231], v[84:87]\n"
"ds_read_b64_tr_b16 a[222:223], v0 offset:35968\n"
"v_mfma_f32_16x16x32_bf16 v[88:91], a[168:171], a[224:227], v[88:91]\n"
"v_mfma_f32_16x16x32_bf16 v[88:91], a[184:187], a[228:231], v[88:91]\n"
"buffer_load_dwordx4 a[132:135], v12, s[24:27], s78 offen offset:1024 sc1\n"
"v_mfma_f32_16x16x32_bf16 v[92:95], a[172:175], a[224:227], v[92:95]\n"
"v_mfma_f32_16x16x32_bf16 v[92:95], a[188:191], a[228:231], v[92:95]\n"
"s_waitcnt lgkmcnt(8)\n"
"buffer_load_dwordx4 v31, s[20:23], s40 offen offset:1024 lds\n"
"v_mfma_f32_16x16x32_bf16 v[96:99], a[160:163], a[240:243], v[96:99]\n"
"ds_read_b64_tr_b16 a[224:225], v1 offset:20608\n"
"v_mfma_f32_16x16x32_bf16 v[96:99], a[176:179], a[244:247], v[96:99]\n"
"ds_read_b64_tr_b16 a[226:227], v1 offset:25728\n"
"v_mfma_f32_16x16x32_bf16 v[100:103], a[164:167], a[240:243], v[100:103]\n"
"ds_read_b64_tr_b16 a[228:229], v1 offset:30848\n"
"v_mfma_f32_16x16x32_bf16 v[100:103], a[180:183], a[244:247], v[100:103]\n"
"ds_read_b64_tr_b16 a[230:231], v1 offset:35968\n"
"v_mfma_f32_16x16x32_bf16 v[104:107], a[168:171], a[240:243], v[104:107]\n"
"v_mfma_f32_16x16x32_bf16 v[104:107], a[184:187], a[244:247], v[104:107]\n"
"buffer_load_dwordx4 a[136:139], v12, s[24:27], s78 offen offset:2048 sc1\n"
"v_mfma_f32_16x16x32_bf16 v[108:111], a[172:175], a[240:243], v[108:111]\n"
"v_mfma_f32_16x16x32_bf16 v[108:111], a[188:191], a[244:247], v[108:111]\n"
"s_waitcnt lgkmcnt(8)\n"
"buffer_load_dwordx4 v32, s[20:23], s40 offen offset:2048 lds\n"
"v_mfma_f32_16x16x32_bf16 v[112:115], a[160:163], a[248:251], v[112:115]\n"
"ds_read_b64_tr_b16 a[240:241], v0 offset:20672\n"
"v_mfma_f32_16x16x32_bf16 v[112:115], a[176:179], a[252:255], v[112:115]\n"
"ds_read_b64_tr_b16 a[242:243], v0 offset:25792\n"
"v_mfma_f32_16x16x32_bf16 v[116:119], a[164:167], a[248:251], v[116:119]\n"
"ds_read_b64_tr_b16 a[244:245], v0 offset:30912\n"
"v_mfma_f32_16x16x32_bf16 v[116:119], a[180:183], a[252:255], v[116:119]\n"
"ds_read_b64_tr_b16 a[246:247], v0 offset:36032\n"
"v_mfma_f32_16x16x32_bf16 v[120:123], a[168:171], a[248:251], v[120:123]\n"
"v_mfma_f32_16x16x32_bf16 v[120:123], a[184:187], a[252:255], v[120:123]\n"
"buffer_load_dwordx4 a[140:143], v12, s[24:27], s78 offen offset:3072 sc1\n"
"s_add_u32 s78, s78, 4096\n"
"v_mfma_f32_16x16x32_bf16 v[124:127], a[172:175], a[248:251], v[124:127]\n"
"s_waitcnt vmcnt(48)\n"
"v_mfma_f32_16x16x32_bf16 v[124:127], a[188:191], a[252:255], v[124:127]\n"
"s_waitcnt lgkmcnt(8)\n"
"buffer_load_dwordx4 v33, s[20:23], s40 offen offset:3072 lds\n"
"v_mfma_f32_16x16x32_bf16 v[160:163], a[160:163], a[216:219], v[160:163]\n"
"ds_read_b64_tr_b16 a[248:249], v1 offset:20672\n"
"v_mfma_f32_16x16x32_bf16 v[160:163], a[176:179], a[220:223], v[160:163]\n"
"ds_read_b64_tr_b16 a[250:251], v1 offset:25792\n"
"v_mfma_f32_16x16x32_bf16 v[164:167], a[164:167], a[216:219], v[164:167]\n"
"ds_read_b64_tr_b16 a[252:253], v1 offset:30912\n"
"v_mfma_f32_16x16x32_bf16 v[164:167], a[180:183], a[220:223], v[164:167]\n"
"ds_read_b64_tr_b16 a[254:255], v1 offset:36032\n"
"v_mfma_f32_16x16x32_bf16 v[168:171], a[168:171], a[216:219], v[168:171]\n"
"v_mfma_f32_16x16x32_bf16 v[168:171], a[184:187], a[220:223], v[168:171]\n"
"buffer_load_dwordx4 a[144:147], v12, s[24:27], s78 offen offset:0 sc1\n"
"v_mfma_f32_16x16x32_bf16 v[172:175], a[172:175], a[216:219], v[172:175]\n"
"s_add_u32 m0, s95, 126976\n"
"v_mfma_f32_16x16x32_bf16 v[172:175], a[188:191], a[220:223], v[172:175]\n"
"s_waitcnt lgkmcnt(8)\n"
"buffer_load_dwordx4 v34, s[20:23], s40 offen offset:0 lds\n"
"v_mfma_f32_16x16x32_bf16 v[176:179], a[160:163], a[224:227], v[176:179]\n"
"s_add_u32 s40, s40, 81920\n"
"s_sub_u32 s40, s40, 163840\n"
"s_add_u32 s20, s20, 1966080\n"
"s_addc_u32 s21, s21, 0\n"
"ds_read_b64_tr_b16 a[216:217], v0 offset:20736\n"
"v_mfma_f32_16x16x32_bf16 v[176:179], a[176:179], a[228:231], v[176:179]\n"
"ds_read_b64_tr_b16 a[218:219], v0 offset:25856\n"
"v_mfma_f32_16x16x32_bf16 v[180:183], a[164:167], a[224:227], v[180:183]\n"
"ds_read_b64_tr_b16 a[220:221], v0 offset:30976\n"
"v_mfma_f32_16x16x32_bf16 v[180:183], a[180:183], a[228:231], v[180:183]\n"
"ds_read_b64_tr_b16 a[222:223], v0 offset:36096\n"
"v_mfma_f32_16x16x32_bf16 v[184:187], a[168:171], a[224:227], v[184:187]\n"
"v_mfma_f32_16x16x32_bf16 v[184:187], a[184:187], a[228:231], v[184:187]\n"
"buffer_load_dwordx4 a[148:151], v12, s[24:27], s78 offen offset:1024 sc1\n"
"v_mfma_f32_16x16x32_bf16 v[188:191], a[172:175], a[224:227], v[188:191]\n"
"v_mfma_f32_16x16x32_bf16 v[188:191], a[188:191], a[228:231], v[188:191]\n"
"s_waitcnt lgkmcnt(8)\n"
"buffer_load_dwordx4 a[152:155], v12, s[24:27], s78 offen offset:2048 sc1\n"
"v_mfma_f32_16x16x32_bf16 v[192:195], a[160:163], a[240:243], v[192:195]\n"
"ds_read_b64_tr_b16 a[224:225], v1 offset:20736\n"
"v_mfma_f32_16x16x32_bf16 v[192:195], a[176:179], a[244:247], v[192:195]\n"
"ds_read_b64_tr_b16 a[226:227], v1 offset:25856\n"
"v_mfma_f32_16x16x32_bf16 v[196:199], a[164:167], a[240:243], v[196:199]\n"
"ds_read_b64_tr_b16 a[228:229], v1 offset:30976\n"
"v_mfma_f32_16x16x32_bf16 v[196:199], a[180:183], a[244:247], v[196:199]\n"
"ds_read_b64_tr_b16 a[230:231], v1 offset:36096\n"
"v_mfma_f32_16x16x32_bf16 v[200:203], a[168:171], a[240:243], v[200:203]\n"
"v_mfma_f32_16x16x32_bf16 v[200:203], a[184:187], a[244:247], v[200:203]\n"
"buffer_load_dwordx4 a[156:159], v12, s[24:27], s78 offen offset:3072 sc1\n"
"s_add_u32 s78, s78, 4096\n"
"s_add_u32 s78, s78, 180224\n"
"v_mfma_f32_16x16x32_bf16 v[204:207], a[172:175], a[240:243], v[204:207]\n"
"v_mfma_f32_16x16x32_bf16 v[204:207], a[188:191], a[244:247], v[204:207]\n"
"s_waitcnt lgkmcnt(8)\n"
"v_mfma_f32_16x16x32_bf16 v[208:211], a[160:163], a[248:251], v[208:211]\n"
"ds_read_b64_tr_b16 a[240:241], v28 offset:40960\n"
"v_mfma_f32_16x16x32_bf16 v[208:211], a[176:179], a[252:255], v[208:211]\n"
"ds_read_b64_tr_b16 a[242:243], v28 offset:46080\n"
"v_mfma_f32_16x16x32_bf16 v[212:215], a[164:167], a[248:251], v[212:215]\n"
"ds_read_b64_tr_b16 a[244:245], v28 offset:51200\n"
"v_mfma_f32_16x16x32_bf16 v[212:215], a[180:183], a[252:255], v[212:215]\n"
"ds_read_b64_tr_b16 a[246:247], v28 offset:56320\n"
"v_mfma_f32_16x16x32_bf16 v[216:219], a[168:171], a[248:251], v[216:219]\n"
"v_mfma_f32_16x16x32_bf16 v[216:219], a[184:187], a[252:255], v[216:219]\n"
"v_mfma_f32_16x16x32_bf16 v[220:223], a[172:175], a[248:251], v[220:223]\n"
"v_mfma_f32_16x16x32_bf16 v[220:223], a[188:191], a[252:255], v[220:223]\n"
"s_waitcnt lgkmcnt(8)\n"
"v_mfma_f32_16x16x32_bf16 v[224:227], a[160:163], a[216:219], v[224:227]\n"
"ds_read_b64_tr_b16 a[248:249], v29 offset:40960\n"
"v_mfma_f32_16x16x32_bf16 v[224:227], a[176:179], a[220:223], v[224:227]\n"
"ds_read_b64_tr_b16 a[250:251], v29 offset:46080\n"
"v_mfma_f32_16x16x32_bf16 v[228:231], a[164:167], a[216:219], v[228:231]\n"
"ds_read_b64_tr_b16 a[252:253], v29 offset:51200\n"
"v_mfma_f32_16x16x32_bf16 v[228:231], a[180:183], a[220:223], v[228:231]\n"
"ds_read_b64_tr_b16 a[254:255], v29 offset:56320\n"
"v_mfma_f32_16x16x32_bf16 v[232:235], a[168:171], a[216:219], v[232:235]\n"
"v_mfma_f32_16x16x32_bf16 v[232:235], a[184:187], a[220:223], v[232:235]\n"
"v_mfma_f32_16x16x32_bf16 v[236:239], a[172:175], a[216:219], v[236:239]\n"
"v_mfma_f32_16x16x32_bf16 v[236:239], a[188:191], a[220:223], v[236:239]\n"
"s_waitcnt lgkmcnt(8)\n"
"v_mfma_f32_16x16x32_bf16 v[240:243], a[160:163], a[224:227], v[240:243]\n"
"ds_read_b64_tr_b16 a[216:217], v28 offset:41024\n"
"v_mfma_f32_16x16x32_bf16 v[240:243], a[176:179], a[228:231], v[240:243]\n"
"ds_read_b64_tr_b16 a[218:219], v28 offset:46144\n"
"v_mfma_f32_16x16x32_bf16 v[244:247], a[164:167], a[224:227], v[244:247]\n"
"ds_read_b64_tr_b16 a[220:221], v28 offset:51264\n"
"v_mfma_f32_16x16x32_bf16 v[244:247], a[180:183], a[228:231], v[244:247]\n"
"s_waitcnt vmcnt(16)\n"
"s_waitcnt vmcnt(40)\n"
"ds_read_b64_tr_b16 a[222:223], v28 offset:56384\n"
"v_mfma_f32_16x16x32_bf16 v[248:251], a[168:171], a[224:227], v[248:251]\n"
"s_barrier\n"
"v_mfma_f32_16x16x32_bf16 v[248:251], a[184:187], a[228:231], v[248:251]\n"
"v_mfma_f32_16x16x32_bf16 v[252:255], a[172:175], a[224:227], v[252:255]\n"
"v_mfma_f32_16x16x32_bf16 v[252:255], a[188:191], a[228:231], v[252:255]\n"
"s_waitcnt lgkmcnt(8)\n"
"s_waitcnt vmcnt(27)\n"
"v_mfma_f32_16x16x32_bf16 v[64:67], a[0:3], a[240:243], v[64:67]\n"
"ds_read_b64_tr_b16 a[224:225], v29 offset:41024\n"
"v_mfma_f32_16x16x32_bf16 v[64:67], a[16:19], a[244:247], v[64:67]\n"
"ds_read_b64_tr_b16 a[226:227], v29 offset:46144\n"
"v_mfma_f32_16x16x32_bf16 v[68:71], a[4:7], a[240:243], v[68:71]\n"
"ds_read_b64_tr_b16 a[228:229], v29 offset:51264\n"
"v_mfma_f32_16x16x32_bf16 v[68:71], a[20:23], a[244:247], v[68:71]\n"
"ds_read_b64_tr_b16 a[230:231], v29 offset:56384\n"
"v_mfma_f32_16x16x32_bf16 v[72:75], a[8:11], a[240:243], v[72:75]\n"
"v_mfma_f32_16x16x32_bf16 v[72:75], a[24:27], a[244:247], v[72:75]\n"
"buffer_load_dwordx4 a[160:163], v12, s[24:27], s78 offen offset:1024 sc1\n"
"v_mfma_f32_16x16x32_bf16 v[76:79], a[12:15], a[240:243], v[76:79]\n"
"s_add_u32 m0, s95, 143360\n"
"v_mfma_f32_16x16x32_bf16 v[76:79], a[28:31], a[244:247], v[76:79]\n"
"s_waitcnt lgkmcnt(8)\n"
"buffer_load_dwordx4 v30, s[20:23], s40 offen offset:0 lds\n"
"v_mfma_f32_16x16x32_bf16 v[80:83], a[0:3], a[248:251], v[80:83]\n"
"ds_read_b64_tr_b16 a[240:241], v28 offset:41088\n"
"v_mfma_f32_16x16x32_bf16 v[80:83], a[16:19], a[252:255], v[80:83]\n"
"ds_read_b64_tr_b16 a[242:243], v28 offset:46208\n"
"v_mfma_f32_16x16x32_bf16 v[84:87], a[4:7], a[248:251], v[84:87]\n"
"ds_read_b64_tr_b16 a[244:245], v28 offset:51328\n"
"v_mfma_f32_16x16x32_bf16 v[84:87], a[20:23], a[252:255], v[84:87]\n"
"ds_read_b64_tr_b16 a[246:247], v28 offset:56448\n"
"v_mfma_f32_16x16x32_bf16 v[88:91], a[8:11], a[248:251], v[88:91]\n"
"v_mfma_f32_16x16x32_bf16 v[88:91], a[24:27], a[252:255], v[88:91]\n"
"buffer_load_dwordx4 a[164:167], v12, s[24:27], s78 offen offset:0 sc1\n"
"v_mfma_f32_16x16x32_bf16 v[92:95], a[12:15], a[248:251], v[92:95]\n"
"v_mfma_f32_16x16x32_bf16 v[92:95], a[28:31], a[252:255], v[92:95]\n"
"s_waitcnt lgkmcnt(8)\n"
"buffer_load_dwordx4 v31, s[20:23], s40 offen offset:1024 lds\n"
"v_mfma_f32_16x16x32_bf16 v[96:99], a[0:3], a[216:219], v[96:99]\n"
"ds_read_b64_tr_b16 a[248:249], v29 offset:41088\n"
"v_mfma_f32_16x16x32_bf16 v[96:99], a[16:19], a[220:223], v[96:99]\n"
"ds_read_b64_tr_b16 a[250:251], v29 offset:46208\n"
"v_mfma_f32_16x16x32_bf16 v[100:103], a[4:7], a[216:219], v[100:103]\n"
"ds_read_b64_tr_b16 a[252:253], v29 offset:51328\n"
"v_mfma_f32_16x16x32_bf16 v[100:103], a[20:23], a[220:223], v[100:103]\n"
"ds_read_b64_tr_b16 a[254:255], v29 offset:56448\n"
"v_mfma_f32_16x16x32_bf16 v[104:107], a[8:11], a[216:219], v[104:107]\n"
"v_mfma_f32_16x16x32_bf16 v[104:107], a[24:27], a[220:223], v[104:107]\n"
"buffer_load_dwordx4 a[168:171], v12, s[24:27], s78 offen offset:3072 sc1\n"
"v_mfma_f32_16x16x32_bf16 v[108:111], a[12:15], a[216:219], v[108:111]\n"
"v_mfma_f32_16x16x32_bf16 v[108:111], a[28:31], a[220:223], v[108:111]\n"
"s_waitcnt lgkmcnt(8)\n"
"buffer_load_dwordx4 v32, s[20:23], s40 offen offset:2048 lds\n"
"v_mfma_f32_16x16x32_bf16 v[112:115], a[0:3], a[224:227], v[112:115]\n"
"ds_read_b64_tr_b16 a[216:217], v28 offset:41152\n"
"v_mfma_f32_16x16x32_bf16 v[112:115], a[16:19], a[228:231], v[112:115]\n"
"ds_read_b64_tr_b16 a[218:219], v28 offset:46272\n"
"v_mfma_f32_16x16x32_bf16 v[116:119], a[4:7], a[224:227], v[116:119]\n"
"ds_read_b64_tr_b16 a[220:221], v28 offset:51392\n"
"v_mfma_f32_16x16x32_bf16 v[116:119], a[20:23], a[228:231], v[116:119]\n"
"ds_read_b64_tr_b16 a[222:223], v28 offset:56512\n"
"v_mfma_f32_16x16x32_bf16 v[120:123], a[8:11], a[224:227], v[120:123]\n"
"v_mfma_f32_16x16x32_bf16 v[120:123], a[24:27], a[228:231], v[120:123]\n"
"buffer_load_dwordx4 a[172:175], v12, s[24:27], s78 offen offset:2048 sc1\n"
"s_add_u32 s78, s78, 4096\n"
"v_mfma_f32_16x16x32_bf16 v[124:127], a[12:15], a[224:227], v[124:127]\n"
"s_waitcnt vmcnt(48)\n"
"v_mfma_f32_16x16x32_bf16 v[124:127], a[28:31], a[228:231], v[124:127]\n"
"s_waitcnt lgkmcnt(8)\n"
"buffer_load_dwordx4 v33, s[20:23], s40 offen offset:3072 lds\n"
"v_mfma_f32_16x16x32_bf16 v[160:163], a[0:3], a[240:243], v[160:163]\n"
"ds_read_b64_tr_b16 a[224:225], v29 offset:41152\n"
"v_mfma_f32_16x16x32_bf16 v[160:163], a[16:19], a[244:247], v[160:163]\n"
"L_pg20:\n"
"s_cbranch_execz L_bk5\n"
"ds_read_b64_tr_b16 a[226:227], v29 offset:46272\n"
"v_mfma_f32_16x16x32_bf16 v[164:167], a[4:7], a[240:243], v[164:167]\n"
"ds_read_b64_tr_b16 a[228:229], v29 offset:51392\n"
"v_mfma_f32_16x16x32_bf16 v[164:167], a[20:23], a[244:247], v[164:167]\n"
"ds_read_b64_tr_b16 a[230:231], v29 offset:56512\n"
"v_mfma_f32_16x16x32_bf16 v[168:171], a[8:11], a[240:243], v[168:171]\n"
"v_mfma_f32_16x16x32_bf16 v[168:171], a[24:27], a[244:247], v[168:171]\n"
"buffer_load_dwordx4 a[176:179], v12, s[24:27], s78 offen offset:1024 sc1\n"
"v_mfma_f32_16x16x32_bf16 v[172:175], a[12:15], a[240:243], v[172:175]\n"
"s_add_u32 m0, s95, 147456\n"
"v_mfma_f32_16x16x32_bf16 v[172:175], a[28:31], a[244:247], v[172:175]\n"
"s_waitcnt lgkmcnt(8)\n"
"buffer_load_dwordx4 v34, s[20:23], s40 offen offset:0 lds\n"
"v_mfma_f32_16x16x32_bf16 v[176:179], a[0:3], a[248:251], v[176:179]\n"
"s_add_u32 s40, s40, 81920\n"
"ds_read_b64_tr_b16 a[240:241], v28 offset:41216\n"
"v_mfma_f32_16x16x32_bf16 v[176:179], a[16:19], a[252:255], v[176:179]\n"
"ds_read_b64_tr_b16 a[242:243], v28 offset:46336\n"
"v_mfma_f32_16x16x32_bf16 v[180:183], a[4:7], a[248:251], v[180:183]\n"
"ds_read_b64_tr_b16 a[244:245], v28 offset:51456\n"
"v_mfma_f32_16x16x32_bf16 v[180:183], a[20:23], a[252:255], v[180:183]\n"
"ds_read_b64_tr_b16 a[246:247], v28 offset:56576\n"
"v_mfma_f32_16x16x32_bf16 v[184:187], a[8:11], a[248:251], v[184:187]\n"
"v_mfma_f32_16x16x32_bf16 v[184:187], a[24:27], a[252:255], v[184:187]\n"
"buffer_load_dwordx4 a[180:183], v12, s[24:27], s78 offen offset:0 sc1\n"
"v_mfma_f32_16x16x32_bf16 v[188:191], a[12:15], a[248:251], v[188:191]\n"
"v_mfma_f32_16x16x32_bf16 v[188:191], a[28:31], a[252:255], v[188:191]\n"
"s_waitcnt lgkmcnt(8)\n"
"buffer_load_dwordx4 a[184:187], v12, s[24:27], s78 offen offset:3072 sc1\n"
"v_mfma_f32_16x16x32_bf16 v[192:195], a[0:3], a[216:219], v[192:195]\n"
"ds_read_b64_tr_b16 a[248:249], v29 offset:41216\n"
"v_mfma_f32_16x16x32_bf16 v[192:195], a[16:19], a[220:223], v[192:195]\n"
"ds_read_b64_tr_b16 a[250:251], v29 offset:46336\n"
"v_mfma_f32_16x16x32_bf16 v[196:199], a[4:7], a[216:219], v[196:199]\n"
"ds_read_b64_tr_b16 a[252:253], v29 offset:51456\n"
"v_mfma_f32_16x16x32_bf16 v[196:199], a[20:23], a[220:223], v[196:199]\n"
"ds_read_b64_tr_b16 a[254:255], v29 offset:56576\n"
"v_mfma_f32_16x16x32_bf16 v[200:203], a[8:11], a[216:219], v[200:203]\n"
"v_mfma_f32_16x16x32_bf16 v[200:203], a[24:27], a[220:223], v[200:203]\n"
"buffer_load_dwordx4 a[188:191], v12, s[24:27], s78 offen offset:2048 sc1\n"
"s_add_u32 s78, s78, 4096\n"
"v_mfma_f32_16x16x32_bf16 v[204:207], a[12:15], a[216:219], v[204:207]\n"
"v_mfma_f32_16x16x32_bf16 v[204:207], a[28:31], a[220:223], v[204:207]\n"
"s_waitcnt lgkmcnt(8)\n"
"v_mfma_f32_16x16x32_bf16 v[208:211], a[0:3], a[224:227], v[208:211]\n"
"ds_read_b64_tr_b16 a[216:217], v2 offset:40960\n"
"v_mfma_f32_16x16x32_bf16 v[208:211], a[16:19], a[228:231], v[208:211]\n"
"ds_read_b64_tr_b16 a[218:219], v2 offset:46080\n"
"v_mfma_f32_16x16x32_bf16 v[212:215], a[4:7], a[224:227], v[212:215]\n"
"ds_read_b64_tr_b16 a[220:221], v2 offset:51200\n"
"v_mfma_f32_16x16x32_bf16 v[212:215], a[20:23], a[228:231], v[212:215]\n"
"ds_read_b64_tr_b16 a[222:223], v2 offset:56320\n"
"v_mfma_f32_16x16x32_bf16 v[216:219], a[8:11], a[224:227], v[216:219]\n"
"v_mfma_f32_16x16x32_bf16 v[216:219], a[24:27], a[228:231], v[216:219]\n"
"v_mfma_f32_16x16x32_bf16 v[220:223], a[12:15], a[224:227], v[220:223]\n"
"v_mfma_f32_16x16x32_bf16 v[220:223], a[28:31], a[228:231], v[220:223]\n"
"s_waitcnt lgkmcnt(8)\n"
"v_mfma_f32_16x16x32_bf16 v[224:227], a[0:3], a[240:243], v[224:227]\n"
"ds_read_b64_tr_b16 a[224:225], v3 offset:40960\n"
"v_mfma_f32_16x16x32_bf16 v[224:227], a[16:19], a[244:247], v[224:227]\n"
"ds_read_b64_tr_b16 a[226:227], v3 offset:46080\n"
"v_mfma_f32_16x16x32_bf16 v[228:231], a[4:7], a[240:243], v[228:231]\n"
"ds_read_b64_tr_b16 a[228:229], v3 offset:51200\n"
"v_mfma_f32_16x16x32_bf16 v[228:231], a[20:23], a[244:247], v[228:231]\n"
"ds_read_b64_tr_b16 a[230:231], v3 offset:56320\n"
"v_mfma_f32_16x16x32_bf16 v[232:235], a[8:11], a[240:243], v[232:235]\n"
"v_mfma_f32_16x16x32_bf16 v[232:235], a[24:27], a[244:247], v[232:235]\n"
"v_mfma_f32_16x16x32_bf16 v[236:239], a[12:15], a[240:243], v[236:239]\n"
"v_mfma_f32_16x16x32_bf16 v[236:239], a[28:31], a[244:247], v[236:239]\n"
"s_waitcnt lgkmcnt(8)\n"
"v_mfma_f32_16x16x32_bf16 v[240:243], a[0:3], a[248:251], v[240:243]\n"
"ds_read_b64_tr_b16 a[240:241], v2 offset:41024\n"
"v_mfma_f32_16x16x32_bf16 v[240:243], a[16:19], a[252:255], v[240:243]\n"
"ds_read_b64_tr_b16 a[242:243], v2 offset:46144\n"
"v_mfma_f32_16x16x32_bf16 v[244:247], a[4:7], a[248:251], v[244:247]\n"
"ds_read_b64_tr_b16 a[244:245], v2 offset:51264\n"
"v_mfma_f32_16x16x32_bf16 v[244:247], a[20:23], a[252:255], v[244:247]\n"
"s_waitcnt vmcnt(16)\n"
"s_waitcnt vmcnt(40)\n"
"ds_read_b64_tr_b16 a[246:247], v2 offset:56384\n"
"v_mfma_f32_16x16x32_bf16 v[248:251], a[8:11], a[248:251], v[248:251]\n"
"s_barrier\n"
"v_mfma_f32_16x16x32_bf16 v[248:251], a[24:27], a[252:255], v[248:251]\n"
"v_mfma_f32_16x16x32_bf16 v[252:255], a[12:15], a[248:251], v[252:255]\n"
"v_mfma_f32_16x16x32_bf16 v[252:255], a[28:31], a[252:255], v[252:255]\n"
"s_waitcnt lgkmcnt(8)\n"
"s_waitcnt vmcnt(26)\n"
"v_mfma_f32_16x16x32_bf16 v[64:67], v[128:131], a[216:219], v[64:67]\n"
"ds_read_b64_tr_b16 a[248:249], v3 offset:41024\n"
"v_mfma_f32_16x16x32_bf16 v[64:67], v[144:147], a[220:223], v[64:67]\n"
"ds_read_b64_tr_b16 a[250:251], v3 offset:46144\n"
"v_mfma_f32_16x16x32_bf16 v[68:71], v[132:135], a[216:219], v[68:71]\n"
"ds_read_b64_tr_b16 a[252:253], v3 offset:51264\n"
"v_mfma_f32_16x16x32_bf16 v[68:71], v[148:151], a[220:223], v[68:71]\n"
"ds_read_b64_tr_b16 a[254:255], v3 offset:56384\n"
"v_mfma_f32_16x16x32_bf16 v[72:75], v[136:139], a[216:219], v[72:75]\n"
"v_mfma_f32_16x16x32_bf16 v[72:75], v[152:155], a[220:223], v[72:75]\n"
"buffer_load_dwordx4 a[0:3], v12, s[24:27], s78 offen offset:1024 sc1\n"
"v_mfma_f32_16x16x32_bf16 v[76:79], v[140:143], a[216:219], v[76:79]\n"
"s_add_u32 m0, s95, 40960\n"
"v_mfma_f32_16x16x32_bf16 v[76:79], v[156:159], a[220:223], v[76:79]\n"
"s_waitcnt lgkmcnt(8)\n"
"buffer_load_dwordx4 v30, s[20:23], s40 offen offset:0 lds\n"
"v_mfma_f32_16x16x32_bf16 v[80:83], v[128:131], a[224:227], v[80:83]\n"
"ds_read_b64_tr_b16 a[216:217], v2 offset:41088\n"
"v_mfma_f32_16x16x32_bf16 v[80:83], v[144:147], a[228:231], v[80:83]\n"
"ds_read_b64_tr_b16 a[218:219], v2 offset:46208\n"
"v_mfma_f32_16x16x32_bf16 v[84:87], v[132:135], a[224:227], v[84:87]\n"
"ds_read_b64_tr_b16 a[220:221], v2 offset:51328\n"
"v_mfma_f32_16x16x32_bf16 v[84:87], v[148:151], a[228:231], v[84:87]\n"
"ds_read_b64_tr_b16 a[222:223], v2 offset:56448\n"
"v_mfma_f32_16x16x32_bf16 v[88:91], v[136:139], a[224:227], v[88:91]\n"
"v_mfma_f32_16x16x32_bf16 v[88:91], v[152:155], a[228:231], v[88:91]\n"
"buffer_load_dwordx4 a[4:7], v12, s[24:27], s78 offen offset:0 sc1\n"
"v_mfma_f32_16x16x32_bf16 v[92:95], v[140:143], a[224:227], v[92:95]\n"
"v_mfma_f32_16x16x32_bf16 v[92:95], v[156:159], a[228:231], v[92:95]\n"
"s_waitcnt lgkmcnt(8)\n"
"buffer_load_dwordx4 v31, s[20:23], s40 offen offset:1024 lds\n"
"v_mfma_f32_16x16x32_bf16 v[96:99], v[128:131], a[240:243], v[96:99]\n"
"ds_read_b64_tr_b16 a[224:225], v3 offset:41088\n"
"v_mfma_f32_16x16x32_bf16 v[96:99], v[144:147], a[244:247], v[96:99]\n"
"ds_read_b64_tr_b16 a[226:227], v3 offset:46208\n"
"v_mfma_f32_16x16x32_bf16 v[100:103], v[132:135], a[240:243], v[100:103]\n"
"ds_read_b64_tr_b16 a[228:229], v3 offset:51328\n"
"v_mfma_f32_16x16x32_bf16 v[100:103], v[148:151], a[244:247], v[100:103]\n"
"ds_read_b64_tr_b16 a[230:231], v3 offset:56448\n"
"v_mfma_f32_16x16x32_bf16 v[104:107], v[136:139], a[240:243], v[104:107]\n"
"v_mfma_f32_16x16x32_bf16 v[104:107], v[152:155], a[244:247], v[104:107]\n"
"buffer_load_dwordx4 a[8:11], v12, s[24:27], s78 offen offset:3072 sc1\n"
"v_mfma_f32_16x16x32_bf16 v[108:111], v[140:143], a[240:243], v[108:111]\n"
"v_mfma_f32_16x16x32_bf16 v[108:111], v[156:159], a[244:247], v[108:111]\n"
"s_waitcnt lgkmcnt(8)\n"
"buffer_load_dwordx4 v32, s[20:23], s40 offen offset:2048 lds\n"
"v_mfma_f32_16x16x32_bf16 v[112:115], v[128:131], a[248:251], v[112:115]\n"
"ds_read_b64_tr_b16 a[240:241], v2 offset:41152\n"
"v_mfma_f32_16x16x32_bf16 v[112:115], v[144:147], a[252:255], v[112:115]\n"
"ds_read_b64_tr_b16 a[242:243], v2 offset:46272\n"
"v_mfma_f32_16x16x32_bf16 v[116:119], v[132:135], a[248:251], v[116:119]\n"
"ds_read_b64_tr_b16 a[244:245], v2 offset:51392\n"
"v_mfma_f32_16x16x32_bf16 v[116:119], v[148:151], a[252:255], v[116:119]\n"
"ds_read_b64_tr_b16 a[246:247], v2 offset:56512\n"
"v_mfma_f32_16x16x32_bf16 v[120:123], v[136:139], a[248:251], v[120:123]\n"
"v_mfma_f32_16x16x32_bf16 v[120:123], v[152:155], a[252:255], v[120:123]\n"
"buffer_load_dwordx4 a[12:15], v12, s[24:27], s78 offen offset:2048 sc1\n"
"s_add_u32 s78, s78, 4096\n"
"v_mfma_f32_16x16x32_bf16 v[124:127], v[140:143], a[248:251], v[124:127]\n"
"s_waitcnt vmcnt(48)\n"
"v_mfma_f32_16x16x32_bf16 v[124:127], v[156:159], a[252:255], v[124:127]\n"
"s_waitcnt lgkmcnt(8)\n"
"buffer_load_dwordx4 v33, s[20:23], s40 offen offset:3072 lds\n"
"v_mfma_f32_16x16x32_bf16 v[160:163], v[128:131], a[216:219], v[160:163]\n"
"ds_read_b64_tr_b16 a[248:249], v3 offset:41152\n"
"v_mfma_f32_16x16x32_bf16 v[160:163], v[144:147], a[220:223], v[160:163]\n"
"ds_read_b64_tr_b16 a[250:251], v3 offset:46272\n"
"v_mfma_f32_16x16x32_bf16 v[164:167], v[132:135], a[216:219], v[164:167]\n"
"ds_read_b64_tr_b16 a[252:253], v3 offset:51392\n"
"v_mfma_f32_16x16x32_bf16 v[164:167], v[148:151], a[220:223], v[164:167]\n"
"ds_read_b64_tr_b16 a[254:255], v3 offset:56512\n"
"v_mfma_f32_16x16x32_bf16 v[168:171], v[136:139], a[216:219], v[168:171]\n"
"v_mfma_f32_16x16x32_bf16 v[168:171], v[152:155], a[220:223], v[168:171]\n"
"buffer_load_dwordx4 a[16:19], v12, s[24:27], s78 offen offset:1024 sc1\n"
"v_mfma_f32_16x16x32_bf16 v[172:175], v[140:143], a[216:219], v[172:175]\n"
"s_add_u32 m0, s95, 45056\n"
"v_mfma_f32_16x16x32_bf16 v[172:175], v[156:159], a[220:223], v[172:175]\n"
"s_waitcnt lgkmcnt(8)\n"
"buffer_load_dwordx4 v34, s[20:23], s40 offen offset:0 lds\n"
"v_mfma_f32_16x16x32_bf16 v[176:179], v[128:131], a[224:227], v[176:179]\n"
"s_add_u32 s40, s40, 81920\n"
"s_mov_b32 s40, s8\n"
"s_mov_b64 s[20:21], s[72:73]\n"
"ds_read_b64_tr_b16 a[216:217], v2 offset:41216\n"
"v_mfma_f32_16x16x32_bf16 v[176:179], v[144:147], a[228:231], v[176:179]\n"
"ds_read_b64_tr_b16 a[218:219], v2 offset:46336\n"
"v_mfma_f32_16x16x32_bf16 v[180:183], v[132:135], a[224:227], v[180:183]\n"
"ds_read_b64_tr_b16 a[220:221], v2 offset:51456\n"
"v_mfma_f32_16x16x32_bf16 v[180:183], v[148:151], a[228:231], v[180:183]\n"
"ds_read_b64_tr_b16 a[222:223], v2 offset:56576\n"
"v_mfma_f32_16x16x32_bf16 v[184:187], v[136:139], a[224:227], v[184:187]\n"
"v_mfma_f32_16x16x32_bf16 v[184:187], v[152:155], a[228:231], v[184:187]\n"
"buffer_load_dwordx4 a[20:23], v12, s[24:27], s78 offen offset:0 sc1\n"
"v_mfma_f32_16x16x32_bf16 v[188:191], v[140:143], a[224:227], v[188:191]\n"
"v_mfma_f32_16x16x32_bf16 v[188:191], v[156:159], a[228:231], v[188:191]\n"
"s_waitcnt lgkmcnt(8)\n"
"buffer_load_dwordx4 a[24:27], v12, s[24:27], s78 offen offset:3072 sc1\n"
"v_mfma_f32_16x16x32_bf16 v[192:195], v[128:131], a[240:243], v[192:195]\n"
"ds_read_b64_tr_b16 a[224:225], v3 offset:41216\n"
"v_mfma_f32_16x16x32_bf16 v[192:195], v[144:147], a[244:247], v[192:195]\n"
"ds_read_b64_tr_b16 a[226:227], v3 offset:46336\n"
"v_mfma_f32_16x16x32_bf16 v[196:199], v[132:135], a[240:243], v[196:199]\n"
"ds_read_b64_tr_b16 a[228:229], v3 offset:51456\n"
"v_mfma_f32_16x16x32_bf16 v[196:199], v[148:151], a[244:247], v[196:199]\n"
"ds_read_b64_tr_b16 a[230:231], v3 offset:56576\n"
"v_mfma_f32_16x16x32_bf16 v[200:203], v[136:139], a[240:243], v[200:203]\n"
"v_mfma_f32_16x16x32_bf16 v[200:203], v[152:155], a[244:247], v[200:203]\n"
"buffer_load_dwordx4 a[28:31], v12, s[24:27], s78 offen offset:2048 sc1\n"
"s_add_u32 s78, s78, 4096\n"
"s_mov_b32 s78, s9\n"
"v_mfma_f32_16x16x32_bf16 v[204:207], v[140:143], a[240:243], v[204:207]\n"
"v_mfma_f32_16x16x32_bf16 v[204:207], v[156:159], a[244:247], v[204:207]\n"
"s_waitcnt lgkmcnt(8)\n"
"v_mfma_f32_16x16x32_bf16 v[208:211], v[128:131], a[248:251], v[208:211]\n"
"ds_read_b64_tr_b16 a[240:241], v0 offset:0\n"
"v_mfma_f32_16x16x32_bf16 v[208:211], v[144:147], a[252:255], v[208:211]\n"
"ds_read_b64_tr_b16 a[242:243], v0 offset:5120\n"
"v_mfma_f32_16x16x32_bf16 v[212:215], v[132:135], a[248:251], v[212:215]\n"
"ds_read_b64_tr_b16 a[244:245], v0 offset:10240\n"
"v_mfma_f32_16x16x32_bf16 v[212:215], v[148:151], a[252:255], v[212:215]\n"
"ds_read_b64_tr_b16 a[246:247], v0 offset:15360\n"
"v_mfma_f32_16x16x32_bf16 v[216:219], v[136:139], a[248:251], v[216:219]\n"
"v_mfma_f32_16x16x32_bf16 v[216:219], v[152:155], a[252:255], v[216:219]\n"
"v_mfma_f32_16x16x32_bf16 v[220:223], v[140:143], a[248:251], v[220:223]\n"
"v_mfma_f32_16x16x32_bf16 v[220:223], v[156:159], a[252:255], v[220:223]\n"
"s_waitcnt lgkmcnt(8)\n"
"v_mfma_f32_16x16x32_bf16 v[224:227], v[128:131], a[216:219], v[224:227]\n"
"ds_read_b64_tr_b16 a[248:249], v1 offset:0\n"
"v_mfma_f32_16x16x32_bf16 v[224:227], v[144:147], a[220:223], v[224:227]\n"
"ds_read_b64_tr_b16 a[250:251], v1 offset:5120\n"
"v_mfma_f32_16x16x32_bf16 v[228:231], v[132:135], a[216:219], v[228:231]\n"
"ds_read_b64_tr_b16 a[252:253], v1 offset:10240\n"
"v_mfma_f32_16x16x32_bf16 v[228:231], v[148:151], a[220:223], v[228:231]\n"
"ds_read_b64_tr_b16 a[254:255], v1 offset:15360\n"
"v_mfma_f32_16x16x32_bf16 v[232:235], v[136:139], a[216:219], v[232:235]\n"
"v_mfma_f32_16x16x32_bf16 v[232:235], v[152:155], a[220:223], v[232:235]\n"
"v_mfma_f32_16x16x32_bf16 v[236:239], v[140:143], a[216:219], v[236:239]\n"
"v_mfma_f32_16x16x32_bf16 v[236:239], v[156:159], a[220:223], v[236:239]\n"
"s_waitcnt lgkmcnt(8)\n"
"v_mfma_f32_16x16x32_bf16 v[240:243], v[128:131], a[224:227], v[240:243]\n"
"ds_read_b64_tr_b16 a[216:217], v0 offset:64\n"
"v_mfma_f32_16x16x32_bf16 v[240:243], v[144:147], a[228:231], v[240:243]\n"
"ds_read_b64_tr_b16 a[218:219], v0 offset:5184\n"
"v_mfma_f32_16x16x32_bf16 v[244:247], v[132:135], a[224:227], v[244:247]\n"
"ds_read_b64_tr_b16 a[220:221], v0 offset:10304\n"
"v_mfma_f32_16x16x32_bf16 v[244:247], v[148:151], a[228:231], v[244:247]\n"
"s_waitcnt vmcnt(16)\n"
"s_waitcnt vmcnt(40)\n"
"ds_read_b64_tr_b16 a[222:223], v0 offset:15424\n"
"v_mfma_f32_16x16x32_bf16 v[248:251], v[136:139], a[224:227], v[248:251]\n"
"s_barrier\n"
"v_mfma_f32_16x16x32_bf16 v[248:251], v[152:155], a[228:231], v[248:251]\n"
"v_mfma_f32_16x16x32_bf16 v[252:255], v[140:143], a[224:227], v[252:255]\n"
"v_mfma_f32_16x16x32_bf16 v[252:255], v[156:159], a[228:231], v[252:255]\n"
"s_waitcnt lgkmcnt(8)\n"
"s_waitcnt vmcnt(26)\n"
"L_o2:\n"
"v_mfma_f32_16x16x32_bf16 v[64:67], a[128:131], a[240:243], v[64:67]\n"
"ds_read_b64_tr_b16 a[224:225], v1 offset:64\n"
"v_mfma_f32_16x16x32_bf16 v[64:67], a[144:147], a[244:247], v[64:67]\n"
"ds_read_b64_tr_b16 a[226:227], v1 offset:5184\n"
"v_mfma_f32_16x16x32_bf16 v[68:71], a[132:135], a[240:243], v[68:71]\n"
"ds_read_b64_tr_b16 a[228:229], v1 offset:10304\n"
"v_mfma_f32_16x16x32_bf16 v[68:71], a[148:151], a[244:247], v[68:71]\n"
"ds_read_b64_tr_b16 a[230:231], v1 offset:15424\n"
"v_mfma_f32_16x16x32_bf16 v[72:75], a[136:139], a[240:243], v[72:75]\n"
"v_mfma_f32_16x16x32_bf16 v[72:75], a[152:155], a[244:247], v[72:75]\n"
"buffer_load_dwordx4 a[32:35], v12, s[24:27], s78 offen offset:0 sc1\n"
"v_mfma_f32_16x16x32_bf16 v[76:79], a[140:143], a[240:243], v[76:79]\n"
"s_add_u32 m0, s95, 61440\n"
"v_mfma_f32_16x16x32_bf16 v[76:79], a[156:159], a[244:247], v[76:79]\n"
"s_waitcnt lgkmcnt(8)\n"
"buffer_load_dwordx4 v30, s[20:23], s40 offen offset:0 lds\n"
"v_mfma_f32_16x16x32_bf16 v[80:83], a[128:131], a[248:251], v[80:83]\n"
"ds_read_b64_tr_b16 a[240:241], v0 offset:128\n"
"v_mfma_f32_16x16x32_bf16 v[80:83], a[144:147], a[252:255], v[80:83]\n"
"ds_read_b64_tr_b16 a[242:243], v0 offset:5248\n"
"v_mfma_f32_16x16x32_bf16 v[84:87], a[132:135], a[248:251], v[84:87]\n"
"ds_read_b64_tr_b16 a[244:245], v0 offset:10368\n"
"v_mfma_f32_16x16x32_bf16 v[84:87], a[148:151], a[252:255], v[84:87]\n"
"ds_read_b64_tr_b16 a[246:247], v0 offset:15488\n"
"v_mfma_f32_16x16x32_bf16 v[88:91], a[136:139], a[248:251], v[88:91]\n"
"v_mfma_f32_16x16x32_bf16 v[88:91], a[152:155], a[252:255], v[88:91]\n"
"buffer_load_dwordx4 a[36:39], v12, s[24:27], s78 offen offset:1024 sc1\n"
"v_mfma_f32_16x16x32_bf16 v[92:95], a[140:143], a[248:251], v[92:95]\n"
"v_mfma_f32_16x16x32_bf16 v[92:95], a[156:159], a[252:255], v[92:95]\n"
"s_waitcnt lgkmcnt(8)\n"
"buffer_load_dwordx4 v31, s[20:23], s40 offen offset:1024 lds\n"
"v_mfma_f32_16x16x32_bf16 v[96:99], a[128:131], a[216:219], v[96:99]\n"
"ds_read_b64_tr_b16 a[248:249], v1 offset:128\n"
"v_mfma_f32_16x16x32_bf16 v[96:99], a[144:147], a[220:223], v[96:99]\n"
"ds_read_b64_tr_b16 a[250:251], v1 offset:5248\n"
"v_mfma_f32_16x16x32_bf16 v[100:103], a[132:135], a[216:219], v[100:103]\n"
"ds_read_b64_tr_b16 a[252:253], v1 offset:10368\n"
"v_mfma_f32_16x16x32_bf16 v[100:103], a[148:151], a[220:223], v[100:103]\n"
"ds_read_b64_tr_b16 a[254:255], v1 offset:15488\n"
"v_mfma_f32_16x16x32_bf16 v[104:107], a[136:139], a[216:219], v[104:107]\n"
"v_mfma_f32_16x16x32_bf16 v[104:107], a[152:155], a[220:223], v[104:107]\n"
"buffer_load_dwordx4 a[40:43], v12, s[24:27], s78 offen offset:2048 sc1\n"
"v_mfma_f32_16x16x32_bf16 v[108:111], a[140:143], a[216:219], v[108:111]\n"
"v_mfma_f32_16x16x32_bf16 v[108:111], a[156:159], a[220:223], v[108:111]\n"
"s_waitcnt lgkmcnt(8)\n"
"buffer_load_dwordx4 v32, s[20:23], s40 offen offset:2048 lds\n"
"v_mfma_f32_16x16x32_bf16 v[112:115], a[128:131], a[224:227], v[112:115]\n"
"ds_read_b64_tr_b16 a[216:217], v0 offset:192\n"
"v_mfma_f32_16x16x32_bf16 v[112:115], a[144:147], a[228:231], v[112:115]\n"
"ds_read_b64_tr_b16 a[218:219], v0 offset:5312\n"
"v_mfma_f32_16x16x32_bf16 v[116:119], a[132:135], a[224:227], v[116:119]\n"
"ds_read_b64_tr_b16 a[220:221], v0 offset:10432\n"
"v_mfma_f32_16x16x32_bf16 v[116:119], a[148:151], a[228:231], v[116:119]\n"
"ds_read_b64_tr_b16 a[222:223], v0 offset:15552\n"
"v_mfma_f32_16x16x32_bf16 v[120:123], a[136:139], a[224:227], v[120:123]\n"
"v_mfma_f32_16x16x32_bf16 v[120:123], a[152:155], a[228:231], v[120:123]\n"
"buffer_load_dwordx4 a[44:47], v12, s[24:27], s78 offen offset:3072 sc1\n"
"s_add_u32 s78, s78, 4096\n"
"v_mfma_f32_16x16x32_bf16 v[124:127], a[140:143], a[224:227], v[124:127]\n"
"s_waitcnt vmcnt(48)\n"
"v_mfma_f32_16x16x32_bf16 v[124:127], a[156:159], a[228:231], v[124:127]\n"
"s_waitcnt lgkmcnt(8)\n"
"buffer_load_dwordx4 v33, s[20:23], s40 offen offset:3072 lds\n"
"v_mfma_f32_16x16x32_bf16 v[160:163], a[128:131], a[240:243], v[160:163]\n"
"ds_read_b64_tr_b16 a[224:225], v1 offset:192\n"
"v_mfma_f32_16x16x32_bf16 v[160:163], a[144:147], a[244:247], v[160:163]\n"
"ds_read_b64_tr_b16 a[226:227], v1 offset:5312\n"
"v_mfma_f32_16x16x32_bf16 v[164:167], a[132:135], a[240:243], v[164:167]\n"
"ds_read_b64_tr_b16 a[228:229], v1 offset:10432\n"
"v_mfma_f32_16x16x32_bf16 v[164:167], a[148:151], a[244:247], v[164:167]\n"
"ds_read_b64_tr_b16 a[230:231], v1 offset:15552\n"
"v_mfma_f32_16x16x32_bf16 v[168:171], a[136:139], a[240:243], v[168:171]\n"
"v_mfma_f32_16x16x32_bf16 v[168:171], a[152:155], a[244:247], v[168:171]\n"
"buffer_load_dwordx4 a[48:51], v12, s[24:27], s78 offen offset:0 sc1\n"
"v_mfma_f32_16x16x32_bf16 v[172:175], a[140:143], a[240:243], v[172:175]\n"
"s_add_u32 m0, s95, 65536\n"
"v_mfma_f32_16x16x32_bf16 v[172:175], a[156:159], a[244:247], v[172:175]\n"
"s_waitcnt lgkmcnt(8)\n"
"buffer_load_dwordx4 v34, s[20:23], s40 offen offset:0 lds\n"
"v_mfma_f32_16x16x32_bf16 v[176:179], a[128:131], a[248:251], v[176:179]\n"
"s_add_u32 s40, s40, 81920\n"
"ds_read_b64_tr_b16 a[240:241], v0 offset:256\n"
"v_mfma_f32_16x16x32_bf16 v[176:179], a[144:147], a[252:255], v[176:179]\n"
"ds_read_b64_tr_b16 a[242:243], v0 offset:5376\n"
"v_mfma_f32_16x16x32_bf16 v[180:183], a[132:135], a[248:251], v[180:183]\n"
"ds_read_b64_tr_b16 a[244:245], v0 offset:10496\n"
"v_mfma_f32_16x16x32_bf16 v[180:183], a[148:151], a[252:255], v[180:183]\n"
"ds_read_b64_tr_b16 a[246:247], v0 offset:15616\n"
"v_mfma_f32_16x16x32_bf16 v[184:187], a[136:139], a[248:251], v[184:187]\n"
"v_mfma_f32_16x16x32_bf16 v[184:187], a[152:155], a[252:255], v[184:187]\n"
"buffer_load_dwordx4 a[52:55], v12, s[24:27], s78 offen offset:1024 sc1\n"
"v_mfma_f32_16x16x32_bf16 v[188:191], a[140:143], a[248:251], v[188:191]\n"
"v_mfma_f32_16x16x32_bf16 v[188:191], a[156:159], a[252:255], v[188:191]\n"
"s_waitcnt lgkmcnt(8)\n"
"buffer_load_dwordx4 a[56:59], v12, s[24:27], s78 offen offset:2048 sc1\n"
"v_mfma_f32_16x16x32_bf16 v[192:195], a[128:131], a[216:219], v[192:195]\n"
"ds_read_b64_tr_b16 a[248:249], v1 offset:256\n"
"v_mfma_f32_16x16x32_bf16 v[192:195], a[144:147], a[220:223], v[192:195]\n"
"ds_read_b64_tr_b16 a[250:251], v1 offset:5376\n"
"v_mfma_f32_16x16x32_bf16 v[196:199], a[132:135], a[216:219], v[196:199]\n"
"ds_read_b64_tr_b16 a[252:253], v1 offset:10496\n"
"v_mfma_f32_16x16x32_bf16 v[196:199], a[148:151], a[220:223], v[196:199]\n"
"ds_read_b64_tr_b16 a[254:255], v1 offset:15616\n"
"v_mfma_f32_16x16x32_bf16 v[200:203], a[136:139], a[216:219], v[200:203]\n"
"v_mfma_f32_16x16x32_bf16 v[200:203], a[152:155], a[220:223], v[200:203]\n"
"buffer_load_dwordx4 a[60:63], v12, s[24:27], s78 offen offset:3072 sc1\n"
"s_add_u32 s78, s78, 4096\n"
"v_mfma_f32_16x16x32_bf16 v[204:207], a[140:143], a[216:219], v[204:207]\n"
"v_mfma_f32_16x16x32_bf16 v[204:207], a[156:159], a[220:223], v[204:207]\n"
"s_waitcnt lgkmcnt(8)\n"
"v_mfma_f32_16x16x32_bf16 v[208:211], a[128:131], a[224:227], v[208:211]\n"
"ds_read_b64_tr_b16 a[216:217], v0 offset:20480\n"
"v_mfma_f32_16x16x32_bf16 v[208:211], a[144:147], a[228:231], v[208:211]\n"
"ds_read_b64_tr_b16 a[218:219], v0 offset:25600\n"
"v_mfma_f32_16x16x32_bf16 v[212:215], a[132:135], a[224:227], v[212:215]\n"
"ds_read_b64_tr_b16 a[220:221], v0 offset:30720\n"
"v_mfma_f32_16x16x32_bf16 v[212:215], a[148:151], a[228:231], v[212:215]\n"
"ds_read_b64_tr_b16 a[222:223], v0 offset:35840\n"
"v_mfma_f32_16x16x32_bf16 v[216:219], a[136:139], a[224:227], v[216:219]\n"
"v_and_b32 v46, s98, v12\n"
"v_mfma_f32_16x16x32_bf16 v[216:219], a[152:155], a[228:231], v[216:219]\n"
"buffer_load_dwordx2 v[46:47], v46, s[24:27], s90 offen sc1\n"
"v_mfma_f32_16x16x32_bf16 v[220:223], a[140:143], a[224:227], v[220:223]\n"
"v_mfma_f32_16x16x32_bf16 v[220:223], a[156:159], a[228:231], v[220:223]\n"
"s_waitcnt lgkmcnt(8)\n"
"v_mfma_f32_16x16x32_bf16 v[224:227], a[128:131], a[240:243], v[224:227]\n"
"ds_read_b64_tr_b16 a[224:225], v1 offset:20480\n"
"v_mfma_f32_16x16x32_bf16 v[224:227], a[144:147], a[244:247], v[224:227]\n"
"ds_read_b64_tr_b16 a[226:227], v1 offset:25600\n"
"v_mfma_f32_16x16x32_bf16 v[228:231], a[132:135], a[240:243], v[228:231]\n"
"ds_read_b64_tr_b16 a[228:229], v1 offset:30720\n"
"v_mfma_f32_16x16x32_bf16 v[228:231], a[148:151], a[244:247], v[228:231]\n"
"ds_read_b64_tr_b16 a[230:231], v1 offset:35840\n"
"v_mfma_f32_16x16x32_bf16 v[232:235], a[136:139], a[240:243], v[232:235]\n"
"v_mfma_f32_16x16x32_bf16 v[232:235], a[152:155], a[244:247], v[232:235]\n"
"v_mfma_f32_16x16x32_bf16 v[236:239], a[140:143], a[240:243], v[236:239]\n"
"v_mfma_f32_16x16x32_bf16 v[236:239], a[156:159], a[244:247], v[236:239]\n"
"s_waitcnt lgkmcnt(8)\n"
"v_mfma_f32_16x16x32_bf16 v[240:243], a[128:131], a[248:251], v[240:243]\n"
"ds_read_b64_tr_b16 a[240:241], v0 offset:20544\n"
"v_mfma_f32_16x16x32_bf16 v[240:243], a[144:147], a[252:255], v[240:243]\n"
"ds_read_b64_tr_b16 a[242:243], v0 offset:25664\n"
"v_mfma_f32_16x16x32_bf16 v[244:247], a[132:135], a[248:251], v[244:247]\n"
"ds_read_b64_tr_b16 a[244:245], v0 offset:30784\n"
"v_mfma_f32_16x16x32_bf16 v[244:247], a[148:151], a[252:255], v[244:247]\n"
"s_waitcnt vmcnt(17)\n"
"s_waitcnt vmcnt(40)\n"
"ds_read_b64_tr_b16 a[246:247], v0 offset:35904\n"
"v_mfma_f32_16x16x32_bf16 v[248:251], a[136:139], a[248:251], v[248:251]\n"
"s_barrier\n"
"v_mfma_f32_16x16x32_bf16 v[248:251], a[152:155], a[252:255], v[248:251]\n"
"v_mfma_f32_16x16x32_bf16 v[252:255], a[140:143], a[248:251], v[252:255]\n"
"v_mfma_f32_16x16x32_bf16 v[252:255], a[156:159], a[252:255], v[252:255]\n"
"s_waitcnt lgkmcnt(8)\n"
"s_waitcnt vmcnt(27)\n"
"v_mfma_f32_16x16x32_bf16 v[64:67], a[160:163], a[216:219], v[64:67]\n"
"ds_read_b64_tr_b16 a[248:249], v1 offset:20544\n"
"v_mfma_f32_16x16x32_bf16 v[64:67], a[176:179], a[220:223], v[64:67]\n"
"ds_read_b64_tr_b16 a[250:251], v1 offset:25664\n"
"v_mfma_f32_16x16x32_bf16 v[68:71], a[164:167], a[216:219], v[68:71]\n"
"ds_read_b64_tr_b16 a[252:253], v1 offset:30784\n"
"v_mfma_f32_16x16x32_bf16 v[68:71], a[180:183], a[220:223], v[68:71]\n"
"ds_read_b64_tr_b16 a[254:255], v1 offset:35904\n"
"v_mfma_f32_16x16x32_bf16 v[72:75], a[168:171], a[216:219], v[72:75]\n"
"v_mfma_f32_16x16x32_bf16 v[72:75], a[184:187], a[220:223], v[72:75]\n"
"buffer_load_dwordx4 v[48:51], v12, s[24:27], s78 offen offset:0 sc1\n"
"v_mfma_f32_16x16x32_bf16 v[76:79], a[172:175], a[216:219], v[76:79]\n"
"s_add_u32 m0, s95, 81920\n"
"v_mfma_f32_16x16x32_bf16 v[76:79], a[188:191], a[220:223], v[76:79]\n"
"s_waitcnt lgkmcnt(8)\n"
"buffer_load_dwordx4 v30, s[20:23], s40 offen offset:0 lds\n"
"v_mfma_f32_16x16x32_bf16 v[80:83], a[160:163], a[224:227], v[80:83]\n"
"ds_read_b64_tr_b16 a[216:217], v0 offset:20608\n"
"v_mfma_f32_16x16x32_bf16 v[80:83], a[176:179], a[228:231], v[80:83]\n"
"ds_read_b64_tr_b16 a[218:219], v0 offset:25728\n"
"v_mfma_f32_16x16x32_bf16 v[84:87], a[164:167], a[224:227], v[84:87]\n"
"ds_read_b64_tr_b16 a[220:221], v0 offset:30848\n"
"v_mfma_f32_16x16x32_bf16 v[84:87], a[180:183], a[228:231], v[84:87]\n"
"ds_read_b64_tr_b16 a[222:223], v0 offset:35968\n"
"v_mfma_f32_16x16x32_bf16 v[88:91], a[168:171], a[224:227], v[88:91]\n"
"v_mfma_f32_16x16x32_bf16 v[88:91], a[184:187], a[228:231], v[88:91]\n"
"buffer_load_dwordx4 v[52:55], v12, s[24:27], s78 offen offset:1024 sc1\n"
"v_mfma_f32_16x16x32_bf16 v[92:95], a[172:175], a[224:227], v[92:95]\n"
"v_mfma_f32_16x16x32_bf16 v[92:95], a[188:191], a[228:231], v[92:95]\n"
"s_waitcnt lgkmcnt(8)\n"
"buffer_load_dwordx4 v31, s[20:23], s40 offen offset:1024 lds\n"
"v_mfma_f32_16x16x32_bf16 v[96:99], a[160:163], a[240:243], v[96:99]\n"
"ds_read_b64_tr_b16 a[224:225], v1 offset:20608\n"
"v_mfma_f32_16x16x32_bf16 v[96:99], a[176:179], a[244:247], v[96:99]\n"
"ds_read_b64_tr_b16 a[226:227], v1 offset:25728\n"
"v_mfma_f32_16x16x32_bf16 v[100:103], a[164:167], a[240:243], v[100:103]\n"
"ds_read_b64_tr_b16 a[228:229], v1 offset:30848\n"
"v_mfma_f32_16x16x32_bf16 v[100:103], a[180:183], a[244:247], v[100:103]\n"
"ds_read_b64_tr_b16 a[230:231], v1 offset:35968\n"
"v_mfma_f32_16x16x32_bf16 v[104:107], a[168:171], a[240:243], v[104:107]\n"
"v_mfma_f32_16x16x32_bf16 v[104:107], a[184:187], a[244:247], v[104:107]\n"
"buffer_load_dwordx4 v[56:59], v12, s[24:27], s78 offen offset:2048 sc1\n"
"v_mfma_f32_16x16x32_bf16 v[108:111], a[172:175], a[240:243], v[108:111]\n"
"v_mfma_f32_16x16x32_bf16 v[108:111], a[188:191], a[244:247], v[108:111]\n"
"s_waitcnt lgkmcnt(8)\n"
"buffer_load_dwordx4 v32, s[20:23], s40 offen offset:2048 lds\n"
"v_mfma_f32_16x16x32_bf16 v[112:115], a[160:163], a[248:251], v[112:115]\n"
"ds_read_b64_tr_b16 a[240:241], v0 offset:20672\n"
"v_mfma_f32_16x16x32_bf16 v[112:115], a[176:179], a[252:255], v[112:115]\n"
"ds_read_b64_tr_b16 a[242:243], v0 offset:25792\n"
"v_mfma_f32_16x16x32_bf16 v[116:119], a[164:167], a[248:251], v[116:119]\n"
"ds_read_b64_tr_b16 a[244:245], v0 offset:30912\n"
"v_mfma_f32_16x16x32_bf16 v[116:119], a[180:183], a[252:255], v[116:119]\n"
"ds_read_b64_tr_b16 a[246:247], v0 offset:36032\n"
"v_mfma_f32_16x16x32_bf16 v[120:123], a[168:171], a[248:251], v[120:123]\n"
"v_mfma_f32_16x16x32_bf16 v[120:123], a[184:187], a[252:255], v[120:123]\n"
"buffer_load_dwordx4 v[60:63], v12, s[24:27], s78 offen offset:3072 sc1\n"
"s_add_u32 s78, s78, 4096\n"
"v_mfma_f32_16x16x32_bf16 v[124:127], a[172:175], a[248:251], v[124:127]\n"
"s_waitcnt vmcnt(48)\n"
"v_mfma_f32_16x16x32_bf16 v[124:127], a[188:191], a[252:255], v[124:127]\n"
"s_waitcnt lgkmcnt(8)\n"
"buffer_load_dwordx4 v33, s[20:23], s40 offen offset:3072 lds\n"
"v_mfma_f32_16x16x32_bf16 v[160:163], a[160:163], a[216:219], v[160:163]\n"
"ds_read_b64_tr_b16 a[248:249], v1 offset:20672\n"
"v_mfma_f32_16x16x32_bf16 v[160:163], a[176:179], a[220:223], v[160:163]\n"
"ds_read_b64_tr_b16 a[250:251], v1 offset:25792\n"
"v_mfma_f32_16x16x32_bf16 v[164:167], a[164:167], a[216:219], v[164:167]\n"
"ds_read_b64_tr_b16 a[252:253], v1 offset:30912\n"
"v_mfma_f32_16x16x32_bf16 v[164:167], a[180:183], a[220:223], v[164:167]\n"
"ds_read_b64_tr_b16 a[254:255], v1 offset:36032\n"
"v_mfma_f32_16x16x32_bf16 v[168:171], a[168:171], a[216:219], v[168:171]\n"
"v_mfma_f32_16x16x32_bf16 v[168:171], a[184:187], a[220:223], v[168:171]\n"
"buffer_load_dwordx4 v[18:21], v12, s[24:27], s78 offen offset:0 sc1\n"
"v_mfma_f32_16x16x32_bf16 v[172:175], a[172:175], a[216:219], v[172:175]\n"
"s_add_u32 m0, s95, 86016\n"
"v_mfma_f32_16x16x32_bf16 v[172:175], a[188:191], a[220:223], v[172:175]\n"
"s_waitcnt lgkmcnt(8)\n"
"buffer_load_dwordx4 v34, s[20:23], s40 offen offset:0 lds\n"
"v_mfma_f32_16x16x32_bf16 v[176:179], a[160:163], a[224:227], v[176:179]\n"
"s_add_u32 s40, s40, 81920\n"
"ds_read_b64_tr_b16 a[216:217], v0 offset:20736\n"
"v_mfma_f32_16x16x32_bf16 v[176:179], a[176:179], a[228:231], v[176:179]\n"
"ds_read_b64_tr_b16 a[218:219], v0 offset:25856\n"
"v_mfma_f32_16x16x32_bf16 v[180:183], a[164:167], a[224:227], v[180:183]\n"
"ds_read_b64_tr_b16 a[220:221], v0 offset:30976\n"
"v_mfma_f32_16x16x32_bf16 v[180:183], a[180:183], a[228:231], v[180:183]\n"
"ds_read_b64_tr_b16 a[222:223], v0 offset:36096\n"
"v_mfma_f32_16x16x32_bf16 v[184:187], a[168:171], a[224:227], v[184:187]\n"
"v_mfma_f32_16x16x32_bf16 v[184:187], a[184:187], a[228:231], v[184:187]\n"
"buffer_load_dwordx4 v[22:25], v12, s[24:27], s78 offen offset:1024 sc1\n"
"v_mfma_f32_16x16x32_bf16 v[188:191], a[172:175], a[224:227], v[188:191]\n"
"v_mfma_f32_16x16x32_bf16 v[188:191], a[188:191], a[228:231], v[188:191]\n"
"s_waitcnt lgkmcnt(8)\n"
"buffer_load_dwordx4 v[36:39], v12, s[24:27], s78 offen offset:2048 sc1\n"
"v_mfma_f32_16x16x32_bf16 v[192:195], a[160:163], a[240:243], v[192:195]\n"
"ds_read_b64_tr_b16 a[224:225], v1 offset:20736\n"
"v_mfma_f32_16x16x32_bf16 v[192:195], a[176:179], a[244:247], v[192:195]\n"
"ds_read_b64_tr_b16 a[226:227], v1 offset:25856\n"
"v_mfma_f32_16x16x32_bf16 v[196:199], a[164:167], a[240:243], v[196:199]\n"
"ds_read_b64_tr_b16 a[228:229], v1 offset:30976\n"
"v_mfma_f32_16x16x32_bf16 v[196:199], a[180:183], a[244:247], v[196:199]\n"
"ds_read_b64_tr_b16 a[230:231], v1 offset:36096\n"
"v_mfma_f32_16x16x32_bf16 v[200:203], a[168:171], a[240:243], v[200:203]\n"
"v_mfma_f32_16x16x32_bf16 v[200:203], a[184:187], a[244:247], v[200:203]\n"
"buffer_load_dwordx4 v[6:9], v12, s[24:27], s78 offen offset:3072 sc1\n"
"s_add_u32 s78, s78, 4096\n"
"v_mfma_f32_16x16x32_bf16 v[204:207], a[172:175], a[240:243], v[204:207]\n"
"v_mfma_f32_16x16x32_bf16 v[204:207], a[188:191], a[244:247], v[204:207]\n"
"s_waitcnt lgkmcnt(8)\n"
"v_mfma_f32_16x16x32_bf16 v[208:211], a[160:163], a[248:251], v[208:211]\n"
"ds_read_b64_tr_b16 a[240:241], v28 offset:40960\n"
"v_mfma_f32_16x16x32_bf16 v[208:211], a[176:179], a[252:255], v[208:211]\n"
"ds_read_b64_tr_b16 a[242:243], v28 offset:46080\n"
"v_mfma_f32_16x16x32_bf16 v[212:215], a[164:167], a[248:251], v[212:215]\n"
"ds_read_b64_tr_b16 a[244:245], v28 offset:51200\n"
"v_mfma_f32_16x16x32_bf16 v[212:215], a[180:183], a[252:255], v[212:215]\n"
"ds_read_b64_tr_b16 a[246:247], v28 offset:56320\n"
"v_mfma_f32_16x16x32_bf16 v[216:219], a[168:171], a[248:251], v[216:219]\n"
"v_mfma_f32_16x16x32_bf16 v[216:219], a[184:187], a[252:255], v[216:219]\n"
"v_mfma_f32_16x16x32_bf16 v[220:223], a[172:175], a[248:251], v[220:223]\n"
"v_mfma_f32_16x16x32_bf16 v[220:223], a[188:191], a[252:255], v[220:223]\n"
"s_waitcnt lgkmcnt(8)\n"
"v_mfma_f32_16x16x32_bf16 v[224:227], a[160:163], a[216:219], v[224:227]\n"
"ds_read_b64_tr_b16 a[248:249], v29 offset:40960\n"
"v_mfma_f32_16x16x32_bf16 v[224:227], a[176:179], a[220:223], v[224:227]\n"
"ds_read_b64_tr_b16 a[250:251], v29 offset:46080\n"
"v_mfma_f32_16x16x32_bf16 v[228:231], a[164:167], a[216:219], v[228:231]\n"
"ds_read_b64_tr_b16 a[252:253], v29 offset:51200\n"
"v_mfma_f32_16x16x32_bf16 v[228:231], a[180:183], a[220:223], v[228:231]\n"
"ds_read_b64_tr_b16 a[254:255], v29 offset:56320\n"
"v_mfma_f32_16x16x32_bf16 v[232:235], a[168:171], a[216:219], v[232:235]\n"
"v_mfma_f32_16x16x32_bf16 v[232:235], a[184:187], a[220:223], v[232:235]\n"
"v_mfma_f32_16x16x32_bf16 v[236:239], a[172:175], a[216:219], v[236:239]\n"
"v_mfma_f32_16x16x32_bf16 v[236:239], a[188:191], a[220:223], v[236:239]\n"
"s_waitcnt lgkmcnt(8)\n"
"v_mfma_f32_16x16x32_bf16 v[240:243], a[160:163], a[224:227], v[240:243]\n"
"ds_read_b64_tr_b16 a[216:217], v28 offset:41024\n"
"v_mfma_f32_16x16x32_bf16 v[240:243], a[176:179], a[228:231], v[240:243]\n"
"ds_read_b64_tr_b16 a[218:219], v28 offset:46144\n"
"v_mfma_f32_16x16x32_bf16 v[244:247], a[164:167], a[224:227], v[244:247]\n"
"L_pg21:\n"
"s_cbranch_execz L_bk5\n"
"ds_read_b64_tr_b16 a[220:221], v28 offset:51264\n"
"v_mfma_f32_16x16x32_bf16 v[244:247], a[180:183], a[228:231], v[244:247]\n"
"s_waitcnt vmcnt(17)\n"
"s_waitcnt vmcnt(40)\n"
"ds_read_b64_tr_b16 a[222:223], v28 offset:56384\n"
"v_mfma_f32_16x16x32_bf16 v[248:251], a[168:171], a[224:227], v[248:251]\n"
"s_barrier\n"
"v_mfma_f32_16x16x32_bf16 v[248:251], a[184:187], a[228:231], v[248:251]\n"
"s_waitcnt vmcnt(13)\n"
"v_cmp_eq_u64 vcc, s[76:77], v[46:47]\n"
"v_mfma_f32_16x16x32_bf16 v[252:255], a[172:175], a[224:227], v[252:255]\n"
"v_mfma_f32_16x16x32_bf16 v[252:255], a[188:191], a[228:231], v[252:255]\n"
"s_waitcnt lgkmcnt(8)\n"
"s_waitcnt vmcnt(27)\n"
"s_cmp_eq_u64 vcc, -1\n"
"s_getpc_b64 s[52:53]\n"
"s_cbranch_scc0 L_poll\n"
"v_mfma_f32_16x16x32_bf16 v[64:67], a[0:3], a[240:243], v[64:67]\n"
"ds_read_b64_tr_b16 a[224:225], v29 offset:41024\n"
"v_mfma_f32_16x16x32_bf16 v[64:67], a[16:19], a[244:247], v[64:67]\n"
"ds_read_b64_tr_b16 a[226:227], v29 offset:46144\n"
"v_mfma_f32_16x16x32_bf16 v[68:71], a[4:7], a[240:243], v[68:71]\n"
"s_bitset1_b32 s90, 3\n"
"ds_read_b64_tr_b16 a[228:229], v29 offset:51264\n"
"v_mfma_f32_16x16x32_bf16 v[68:71], a[20:23], a[244:247], v[68:71]\n"
"ds_read_b64_tr_b16 a[230:231], v29 offset:56384\n"
"v_mfma_f32_16x16x32_bf16 v[72:75], a[8:11], a[240:243], v[72:75]\n"
"v_mfma_f32_16x16x32_bf16 v[72:75], a[24:27], a[244:247], v[72:75]\n"
"buffer_load_dwordx4 a[192:195], v12, s[24:27], s42 offen offset:0 sc1\n"
"v_mfma_f32_16x16x32_bf16 v[76:79], a[12:15], a[240:243], v[76:79]\n"
"s_add_u32 m0, s95, 0\n"
"v_mfma_f32_16x16x32_bf16 v[76:79], a[28:31], a[244:247], v[76:79]\n"
"s_waitcnt lgkmcnt(8)\n"
"buffer_load_dwordx4 v30, s[20:23], s41 offen offset:0 lds\n"
"v_mfma_f32_16x16x32_bf16 v[80:83], a[0:3], a[248:251], v[80:83]\n"
"ds_read_b64_tr_b16 a[240:241], v28 offset:41088\n"
"v_mfma_f32_16x16x32_bf16 v[80:83], a[16:19], a[252:255], v[80:83]\n"
"ds_read_b64_tr_b16 a[242:243], v28 offset:46208\n"
"v_mfma_f32_16x16x32_bf16 v[84:87], a[4:7], a[248:251], v[84:87]\n"
"ds_read_b64_tr_b16 a[244:245], v28 offset:51328\n"
"v_mfma_f32_16x16x32_bf16 v[84:87], a[20:23], a[252:255], v[84:87]\n"
"ds_read_b64_tr_b16 a[246:247], v28 offset:56448\n"
"v_mfma_f32_16x16x32_bf16 v[88:91], a[8:11], a[248:251], v[88:91]\n"
"v_mfma_f32_16x16x32_bf16 v[88:91], a[24:27], a[252:255], v[88:91]\n"
"buffer_load_dwordx4 a[196:199], v12, s[24:27], s42 offen offset:1024 sc1\n"
"v_mfma_f32_16x16x32_bf16 v[92:95], a[12:15], a[248:251], v[92:95]\n"
"v_mfma_f32_16x16x32_bf16 v[92:95], a[28:31], a[252:255], v[92:95]\n"
"s_waitcnt lgkmcnt(8)\n"
"buffer_load_dwordx4 v31, s[20:23], s41 offen offset:1024 lds\n"
"v_mfma_f32_16x16x32_bf16 v[96:99], a[0:3], a[216:219], v[96:99]\n"
"ds_read_b64_tr_b16 a[248:249], v29 offset:41088\n"
"v_mfma_f32_16x16x32_bf16 v[96:99], a[16:19], a[220:223], v[96:99]\n"
"ds_read_b64_tr_b16 a[250:251], v29 offset:46208\n"
"v_mfma_f32_16x16x32_bf16 v[100:103], a[4:7], a[216:219], v[100:103]\n"
"ds_read_b64_tr_b16 a[252:253], v29 offset:51328\n"
"v_mfma_f32_16x16x32_bf16 v[100:103], a[20:23], a[220:223], v[100:103]\n"
"ds_read_b64_tr_b16 a[254:255], v29 offset:56448\n"
"v_mfma_f32_16x16x32_bf16 v[104:107], a[8:11], a[216:219], v[104:107]\n"
"v_mfma_f32_16x16x32_bf16 v[104:107], a[24:27], a[220:223], v[104:107]\n"
"buffer_load_dwordx4 a[200:203], v12, s[24:27], s42 offen offset:2048 sc1\n"
"v_mfma_f32_16x16x32_bf16 v[108:111], a[12:15], a[216:219], v[108:111]\n"
"v_mfma_f32_16x16x32_bf16 v[108:111], a[28:31], a[220:223], v[108:111]\n"
"s_waitcnt lgkmcnt(8)\n"
"buffer_load_dwordx4 v32, s[20:23], s41 offen offset:2048 lds\n"
"v_mfma_f32_16x16x32_bf16 v[112:115], a[0:3], a[224:227], v[112:115]\n"
"ds_read_b64_tr_b16 a[216:217], v28 offset:41152\n"
"v_mfma_f32_16x16x32_bf16 v[112:115], a[16:19], a[228:231], v[112:115]\n"
"ds_read_b64_tr_b16 a[218:219], v28 offset:46272\n"
"v_mfma_f32_16x16x32_bf16 v[116:119], a[4:7], a[224:227], v[116:119]\n"
"ds_read_b64_tr_b16 a[220:221], v28 offset:51392\n"
"v_mfma_f32_16x16x32_bf16 v[116:119], a[20:23], a[228:231], v[116:119]\n"
"ds_read_b64_tr_b16 a[222:223], v28 offset:56512\n"
"v_mfma_f32_16x16x32_bf16 v[120:123], a[8:11], a[224:227], v[120:123]\n"
"v_mfma_f32_16x16x32_bf16 v[120:123], a[24:27], a[228:231], v[120:123]\n"
"buffer_load_dwordx4 a[204:207], v12, s[24:27], s42 offen offset:3072 sc1\n"
"s_add_u32 s42, s42, 4096\n"
"v_mfma_f32_16x16x32_bf16 v[124:127], a[12:15], a[224:227], v[124:127]\n"
"s_waitcnt vmcnt(48)\n"
"v_mfma_f32_16x16x32_bf16 v[124:127], a[28:31], a[228:231], v[124:127]\n"
"s_waitcnt lgkmcnt(8)\n"
"buffer_load_dwordx4 v33, s[20:23], s41 offen offset:3072 lds\n"
"v_mfma_f32_16x16x32_bf16 v[160:163], a[0:3], a[240:243], v[160:163]\n"
"ds_read_b64_tr_b16 a[224:225], v29 offset:41152\n"
"v_mfma_f32_16x16x32_bf16 v[160:163], a[16:19], a[244:247], v[160:163]\n"
"ds_read_b64_tr_b16 a[226:227], v29 offset:46272\n"
"v_mfma_f32_16x16x32_bf16 v[164:167], a[4:7], a[240:243], v[164:167]\n"
"ds_read_b64_tr_b16 a[228:229], v29 offset:51392\n"
"v_mfma_f32_16x16x32_bf16 v[164:167], a[20:23], a[244:247], v[164:167]\n"
"ds_read_b64_tr_b16 a[230:231], v29 offset:56512\n"
"v_mfma_f32_16x16x32_bf16 v[168:171], a[8:11], a[240:243], v[168:171]\n"
"v_mfma_f32_16x16x32_bf16 v[168:171], a[24:27], a[244:247], v[168:171]\n"
"buffer_load_dwordx4 a[208:211], v12, s[24:27], s42 offen offset:0 sc1\n"
"v_mfma_f32_16x16x32_bf16 v[172:175], a[12:15], a[240:243], v[172:175]\n"
"s_add_u32 m0, s95, 4096\n"
"v_mfma_f32_16x16x32_bf16 v[172:175], a[28:31], a[244:247], v[172:175]\n"
"s_waitcnt lgkmcnt(8)\n"
"buffer_load_dwordx4 v34, s[20:23], s41 offen offset:0 lds\n"
"v_mfma_f32_16x16x32_bf16 v[176:179], a[0:3], a[248:251], v[176:179]\n"
"s_add_u32 s41, s41, 81920\n"
"ds_read_b64_tr_b16 a[240:241], v28 offset:41216\n"
"v_mfma_f32_16x16x32_bf16 v[176:179], a[16:19], a[252:255], v[176:179]\n"
"ds_read_b64_tr_b16 a[242:243], v28 offset:46336\n"
"v_mfma_f32_16x16x32_bf16 v[180:183], a[4:7], a[248:251], v[180:183]\n"
"ds_read_b64_tr_b16 a[244:245], v28 offset:51456\n"
"v_mfma_f32_16x16x32_bf16 v[180:183], a[20:23], a[252:255], v[180:183]\n"
"ds_read_b64_tr_b16 a[246:247], v28 offset:56576\n"
"v_mfma_f32_16x16x32_bf16 v[184:187], a[8:11], a[248:251], v[184:187]\n"
"v_mfma_f32_16x16x32_bf16 v[184:187], a[24:27], a[252:255], v[184:187]\n"
"buffer_load_dwordx4 a[212:215], v12, s[24:27], s42 offen offset:1024 sc1\n"
"v_mfma_f32_16x16x32_bf16 v[188:191], a[12:15], a[248:251], v[188:191]\n"
"v_mfma_f32_16x16x32_bf16 v[188:191], a[28:31], a[252:255], v[188:191]\n"
"s_waitcnt lgkmcnt(8)\n"
"buffer_load_dwordx4 a[232:235], v12, s[24:27], s42 offen offset:2048 sc1\n"
"v_mfma_f32_16x16x32_bf16 v[192:195], a[0:3], a[216:219], v[192:195]\n"
"ds_read_b64_tr_b16 a[248:249], v29 offset:41216\n"
"v_mfma_f32_16x16x32_bf16 v[192:195], a[16:19], a[220:223], v[192:195]\n"
"ds_read_b64_tr_b16 a[250:251], v29 offset:46336\n"
"v_mfma_f32_16x16x32_bf16 v[196:199], a[4:7], a[216:219], v[196:199]\n"
"ds_read_b64_tr_b16 a[252:253], v29 offset:51456\n"
"v_mfma_f32_16x16x32_bf16 v[196:199], a[20:23], a[220:223], v[196:199]\n"
"ds_read_b64_tr_b16 a[254:255], v29 offset:56576\n"
"v_mfma_f32_16x16x32_bf16 v[200:203], a[8:11], a[216:219], v[200:203]\n"
"v_mfma_f32_16x16x32_bf16 v[200:203], a[24:27], a[220:223], v[200:203]\n"
"buffer_load_dwordx4 a[236:239], v12, s[24:27], s42 offen offset:3072 sc1\n"
"s_add_u32 s42, s42, 4096\n"
"v_mfma_f32_16x16x32_bf16 v[204:207], a[12:15], a[216:219], v[204:207]\n"
"v_mfma_f32_16x16x32_bf16 v[204:207], a[28:31], a[220:223], v[204:207]\n"
"s_waitcnt lgkmcnt(8)\n"
"buffer_load_dwordx4 a[64:67], v12, s[24:27], s42 offen offset:0 sc1\n"
"v_mfma_f32_16x16x32_bf16 v[208:211], a[0:3], a[224:227], v[208:211]\n"
"ds_read_b64_tr_b16 a[216:217], v2 offset:0\n"
"v_mfma_f32_16x16x32_bf16 v[208:211], a[16:19], a[228:231], v[208:211]\n"
"ds_read_b64_tr_b16 a[218:219], v2 offset:5120\n"
"v_mfma_f32_16x16x32_bf16 v[212:215], a[4:7], a[224:227], v[212:215]\n"
"ds_read_b64_tr_b16 a[220:221], v2 offset:10240\n"
"v_mfma_f32_16x16x32_bf16 v[212:215], a[20:23], a[228:231], v[212:215]\n"
"ds_read_b64_tr_b16 a[222:223], v2 offset:15360\n"
"v_mfma_f32_16x16x32_bf16 v[216:219], a[8:11], a[224:227], v[216:219]\n"
"v_mfma_f32_16x16x32_bf16 v[216:219], a[24:27], a[228:231], v[216:219]\n"
"v_mfma_f32_16x16x32_bf16 v[220:223], a[12:15], a[224:227], v[220:223]\n"
"v_mfma_f32_16x16x32_bf16 v[220:223], a[28:31], a[228:231], v[220:223]\n"
"s_waitcnt lgkmcnt(8)\n"
"v_mfma_f32_16x16x32_bf16 v[224:227], a[0:3], a[240:243], v[224:227]\n"
"ds_read_b64_tr_b16 a[224:225], v3 offset:0\n"
"v_mfma_f32_16x16x32_bf16 v[224:227], a[16:19], a[244:247], v[224:227]\n"
"ds_read_b64_tr_b16 a[226:227], v3 offset:5120\n"
"v_mfma_f32_16x16x32_bf16 v[228:231], a[4:7], a[240:243], v[228:231]\n"
"ds_read_b64_tr_b16 a[228:229], v3 offset:10240\n"
"v_mfma_f32_16x16x32_bf16 v[228:231], a[20:23], a[244:247], v[228:231]\n"
"ds_read_b64_tr_b16 a[230:231], v3 offset:15360\n"
"v_mfma_f32_16x16x32_bf16 v[232:235], a[8:11], a[240:243], v[232:235]\n"
"v_mfma_f32_16x16x32_bf16 v[232:235], a[24:27], a[244:247], v[232:235]\n"
"v_mfma_f32_16x16x32_bf16 v[236:239], a[12:15], a[240:243], v[236:239]\n"
"v_mfma_f32_16x16x32_bf16 v[236:239], a[28:31], a[244:247], v[236:239]\n"
"s_waitcnt lgkmcnt(8)\n"
"v_mfma_f32_16x16x32_bf16 v[240:243], a[0:3], a[248:251], v[240:243]\n"
"ds_read_b64_tr_b16 a[240:241], v2 offset:64\n"
"v_mfma_f32_16x16x32_bf16 v[240:243], a[16:19], a[252:255], v[240:243]\n"
"ds_read_b64_tr_b16 a[242:243], v2 offset:5184\n"
"v_mfma_f32_16x16x32_bf16 v[244:247], a[4:7], a[248:251], v[244:247]\n"
"ds_read_b64_tr_b16 a[244:245], v2 offset:10304\n"
"v_mfma_f32_16x16x32_bf16 v[244:247], a[20:23], a[252:255], v[244:247]\n"
"s_waitcnt vmcnt(17)\n"
"s_waitcnt vmcnt(40)\n"
"ds_read_b64_tr_b16 a[246:247], v2 offset:15424\n"
"v_mfma_f32_16x16x32_bf16 v[248:251], a[8:11], a[248:251], v[248:251]\n"
"s_barrier\n"
"v_mfma_f32_16x16x32_bf16 v[248:251], a[24:27], a[252:255], v[248:251]\n"
"v_mfma_f32_16x16x32_bf16 v[252:255], a[12:15], a[248:251], v[252:255]\n"
"s_cmp_lg_u32 s38, 0\n"
"s_cbranch_scc1 L_puba\n"
"v_mov_b32 v46, s76\n"
"v_mov_b32 v47, s77\n"
"s_mov_b64 exec, 1\n"
"buffer_store_dwordx2 v[46:47], off, s[24:27], s89 sc1\n"
"s_mov_b64 exec, -1\n"
"s_nop 1\n"
"L_puba:\n"
"v_mfma_f32_16x16x32_bf16 v[252:255], a[28:31], a[252:255], v[252:255]\n"
"s_waitcnt lgkmcnt(8)\n"
"s_waitcnt vmcnt(28)\n"
"v_mfma_f32_16x16x32_bf16 v[64:67], a[32:35], a[216:219], v[64:67]\n"
"ds_read_b64_tr_b16 a[248:249], v3 offset:64\n"
"v_mfma_f32_16x16x32_bf16 v[64:67], a[48:51], a[220:223], v[64:67]\n"
"ds_read_b64_tr_b16 a[250:251], v3 offset:5184\n"
"v_mfma_f32_16x16x32_bf16 v[68:71], a[36:39], a[216:219], v[68:71]\n"
"ds_read_b64_tr_b16 a[252:253], v3 offset:10304\n"
"v_mfma_f32_16x16x32_bf16 v[68:71], a[52:55], a[220:223], v[68:71]\n"
"ds_read_b64_tr_b16 a[254:255], v3 offset:15424\n"
"v_mfma_f32_16x16x32_bf16 v[72:75], a[40:43], a[216:219], v[72:75]\n"
"v_mfma_f32_16x16x32_bf16 v[72:75], a[56:59], a[220:223], v[72:75]\n"
"buffer_load_dwordx4 a[68:71], v12, s[24:27], s42 offen offset:1024 sc1\n"
"v_mfma_f32_16x16x32_bf16 v[76:79], a[44:47], a[216:219], v[76:79]\n"
"s_add_u32 m0, s95, 20480\n"
"v_mfma_f32_16x16x32_bf16 v[76:79], a[60:63], a[220:223], v[76:79]\n"
"s_waitcnt lgkmcnt(8)\n"
"buffer_load_dwordx4 v30, s[20:23], s41 offen offset:0 lds\n"
"v_mfma_f32_16x16x32_bf16 v[80:83], a[32:35], a[224:227], v[80:83]\n"
"ds_read_b64_tr_b16 a[216:217], v2 offset:128\n"
"v_mfma_f32_16x16x32_bf16 v[80:83], a[48:51], a[228:231], v[80:83]\n"
"ds_read_b64_tr_b16 a[218:219], v2 offset:5248\n"
"v_mfma_f32_16x16x32_bf16 v[84:87], a[36:39], a[224:227], v[84:87]\n"
"ds_read_b64_tr_b16 a[220:221], v2 offset:10368\n"
"v_mfma_f32_16x16x32_bf16 v[84:87], a[52:55], a[228:231], v[84:87]\n"
"ds_read_b64_tr_b16 a[222:223], v2 offset:15488\n"
"v_mfma_f32_16x16x32_bf16 v[88:91], a[40:43], a[224:227], v[88:91]\n"
"v_mfma_f32_16x16x32_bf16 v[88:91], a[56:59], a[228:231], v[88:91]\n"
"buffer_load_dwordx4 a[72:75], v12, s[24:27], s42 offen offset:2048 sc1\n"
"v_mfma_f32_16x16x32_bf16 v[92:95], a[44:47], a[224:227], v[92:95]\n"
"v_mfma_f32_16x16x32_bf16 v[92:95], a[60:63], a[228:231], v[92:95]\n"
"s_waitcnt lgkmcnt(8)\n"
"buffer_load_dwordx4 v31, s[20:23], s41 offen offset:1024 lds\n"
"v_mfma_f32_16x16x32_bf16 v[96:99], a[32:35], a[240:243], v[96:99]\n"
"ds_read_b64_tr_b16 a[224:225], v3 offset:128\n"
"v_mfma_f32_16x16x32_bf16 v[96:99], a[48:51], a[244:247], v[96:99]\n"
"ds_read_b64_tr_b16 a[226:227], v3 offset:5248\n"
"v_mfma_f32_16x16x32_bf16 v[100:103], a[36:39], a[240:243], v[100:103]\n"
"ds_read_b64_tr_b16 a[228:229], v3 offset:10368\n"
"v_mfma_f32_16x16x32_bf16 v[100:103], a[52:55], a[244:247], v[100:103]\n"
"ds_read_b64_tr_b16 a[230:231], v3 offset:15488\n"
"v_mfma_f32_16x16x32_bf16 v[104:107], a[40:43], a[240:243], v[104:107]\n"
"v_mfma_f32_16x16x32_bf16 v[104:107], a[56:59], a[244:247], v[104:107]\n"
"buffer_load_dwordx4 a[76:79], v12, s[24:27], s42 offen offset:3072 sc1\n"
"s_add_u32 s42, s42, 4096\n"
"v_mfma_f32_16x16x32_bf16 v[108:111], a[44:47], a[240:243], v[108:111]\n"
"v_mfma_f32_16x16x32_bf16 v[108:111], a[60:63], a[244:247], v[108:111]\n"
"s_waitcnt lgkmcnt(8)\n"
"buffer_load_dwordx4 v32, s[20:23], s41 offen offset:2048 lds\n"
"v_mfma_f32_16x16x32_bf16 v[112:115], a[32:35], a[248:251], v[112:115]\n"
"ds_read_b64_tr_b16 a[240:241], v2 offset:192\n"
"v_mfma_f32_16x16x32_bf16 v[112:115], a[48:51], a[252:255], v[112:115]\n"
"ds_read_b64_tr_b16 a[242:243], v2 offset:5312\n"
"v_mfma_f32_16x16x32_bf16 v[116:119], a[36:39], a[248:251], v[116:119]\n"
"ds_read_b64_tr_b16 a[244:245], v2 offset:10432\n"
"v_mfma_f32_16x16x32_bf16 v[116:119], a[52:55], a[252:255], v[116:119]\n"
"ds_read_b64_tr_b16 a[246:247], v2 offset:15552\n"
"v_mfma_f32_16x16x32_bf16 v[120:123], a[40:43], a[248:251], v[120:123]\n"
"v_mfma_f32_16x16x32_bf16 v[120:123], a[56:59], a[252:255], v[120:123]\n"
"buffer_load_dwordx4 a[80:83], v12, s[24:27], s42 offen offset:0 sc1\n"
"v_mfma_f32_16x16x32_bf16 v[124:127], a[44:47], a[248:251], v[124:127]\n"
"s_waitcnt vmcnt(48)\n"
"v_mfma_f32_16x16x32_bf16 v[124:127], a[60:63], a[252:255], v[124:127]\n"
"s_waitcnt lgkmcnt(8)\n"
"buffer_load_dwordx4 v33, s[20:23], s41 offen offset:3072 lds\n"
"v_mfma_f32_16x16x32_bf16 v[160:163], a[32:35], a[216:219], v[160:163]\n"
"ds_read_b64_tr_b16 a[248:249], v3 offset:192\n"
"v_mfma_f32_16x16x32_bf16 v[160:163], a[48:51], a[220:223], v[160:163]\n"
"ds_read_b64_tr_b16 a[250:251], v3 offset:5312\n"
"v_mfma_f32_16x16x32_bf16 v[164:167], a[36:39], a[216:219], v[164:167]\n"
"ds_read_b64_tr_b16 a[252:253], v3 offset:10432\n"
"v_mfma_f32_16x16x32_bf16 v[164:167], a[52:55], a[220:223], v[164:167]\n"
"ds_read_b64_tr_b16 a[254:255], v3 offset:15552\n"
"v_mfma_f32_16x16x32_bf16 v[168:171], a[40:43], a[216:219], v[168:171]\n"
"v_mfma_f32_16x16x32_bf16 v[168:171], a[56:59], a[220:223], v[168:171]\n"
"buffer_load_dwordx4 a[84:87], v12, s[24:27], s42 offen offset:1024 sc1\n"
"v_mfma_f32_16x16x32_bf16 v[172:175], a[44:47], a[216:219], v[172:175]\n"
"s_add_u32 m0, s95, 24576\n"
"v_mfma_f32_16x16x32_bf16 v[172:175], a[60:63], a[220:223], v[172:175]\n"
"s_waitcnt lgkmcnt(8)\n"
"buffer_load_dwordx4 v34, s[20:23], s41 offen offset:0 lds\n"
"v_mfma_f32_16x16x32_bf16 v[176:179], a[32:35], a[224:227], v[176:179]\n"
"s_add_u32 s41, s41, 81920\n"
"ds_read_b64_tr_b16 a[216:217], v2 offset:256\n"
"v_mfma_f32_16x16x32_bf16 v[176:179], a[48:51], a[228:231], v[176:179]\n"
"ds_read_b64_tr_b16 a[218:219], v2 offset:5376\n"
"v_mfma_f32_16x16x32_bf16 v[180:183], a[36:39], a[224:227], v[180:183]\n"
"ds_read_b64_tr_b16 a[220:221], v2 offset:10496\n"
"v_mfma_f32_16x16x32_bf16 v[180:183], a[52:55], a[228:231], v[180:183]\n"
"ds_read_b64_tr_b16 a[222:223], v2 offset:15616\n"
"v_mfma_f32_16x16x32_bf16 v[184:187], a[40:43], a[224:227], v[184:187]\n"
"v_mfma_f32_16x16x32_bf16 v[184:187], a[56:59], a[228:231], v[184:187]\n"
"buffer_load_dwordx4 a[88:91], v12, s[24:27], s42 offen offset:2048 sc1\n"
"v_mfma_f32_16x16x32_bf16 v[188:191], a[44:47], a[224:227], v[188:191]\n"
"v_mfma_f32_16x16x32_bf16 v[188:191], a[60:63], a[228:231], v[188:191]\n"
"s_waitcnt lgkmcnt(8)\n"
"buffer_load_dwordx4 a[92:95], v12, s[24:27], s42 offen offset:3072 sc1\n"
"s_add_u32 s42, s42, 4096\n"
"v_mfma_f32_16x16x32_bf16 v[192:195], a[32:35], a[240:243], v[192:195]\n"
"ds_read_b64_tr_b16 a[224:225], v3 offset:256\n"
"v_mfma_f32_16x16x32_bf16 v[192:195], a[48:51], a[244:247], v[192:195]\n"
"ds_read_b64_tr_b16 a[226:227], v3 offset:5376\n"
"v_mfma_f32_16x16x32_bf16 v[196:199], a[36:39], a[240:243], v[196:199]\n"
"ds_read_b64_tr_b16 a[228:229], v3 offset:10496\n"
"v_mfma_f32_16x16x32_bf16 v[196:199], a[52:55], a[244:247], v[196:199]\n"
"ds_read_b64_tr_b16 a[230:231], v3 offset:15616\n"
"v_mfma_f32_16x16x32_bf16 v[200:203], a[40:43], a[240:243], v[200:203]\n"
"v_mfma_f32_16x16x32_bf16 v[200:203], a[56:59], a[244:247], v[200:203]\n"
"buffer_load_dwordx4 a[96:99], v12, s[24:27], s42 offen offset:0 sc1\n"
"v_mfma_f32_16x16x32_bf16 v[204:207], a[44:47], a[240:243], v[204:207]\n"
"v_mfma_f32_16x16x32_bf16 v[204:207], a[60:63], a[244:247], v[204:207]\n"
"s_waitcnt lgkmcnt(8)\n"
"buffer_load_dwordx4 a[100:103], v12, s[24:27], s42 offen offset:1024 sc1\n"
"v_mfma_f32_16x16x32_bf16 v[208:211], a[32:35], a[248:251], v[208:211]\n"
"ds_read_b64_tr_b16 a[240:241], v2 offset:20480\n"
"v_mfma_f32_16x16x32_bf16 v[208:211], a[48:51], a[252:255], v[208:211]\n"
"ds_read_b64_tr_b16 a[242:243], v2 offset:25600\n"
"v_mfma_f32_16x16x32_bf16 v[212:215], a[36:39], a[248:251], v[212:215]\n"
"ds_read_b64_tr_b16 a[244:245], v2 offset:30720\n"
"v_mfma_f32_16x16x32_bf16 v[212:215], a[52:55], a[252:255], v[212:215]\n"
"ds_read_b64_tr_b16 a[246:247], v2 offset:35840\n"
"v_mfma_f32_16x16x32_bf16 v[216:219], a[40:43], a[248:251], v[216:219]\n"
"v_mfma_f32_16x16x32_bf16 v[216:219], a[56:59], a[252:255], v[216:219]\n"
"v_mfma_f32_16x16x32_bf16 v[220:223], a[44:47], a[248:251], v[220:223]\n"
"v_mfma_f32_16x16x32_bf16 v[220:223], a[60:63], a[252:255], v[220:223]\n"
"s_waitcnt lgkmcnt(8)\n"
"v_mfma_f32_16x16x32_bf16 v[224:227], a[32:35], a[216:219], v[224:227]\n"
"ds_read_b64_tr_b16 a[248:249], v3 offset:20480\n"
"v_mfma_f32_16x16x32_bf16 v[224:227], a[48:51], a[220:223], v[224:227]\n"
"ds_read_b64_tr_b16 a[250:251], v3 offset:25600\n"
"v_mfma_f32_16x16x32_bf16 v[228:231], a[36:39], a[216:219], v[228:231]\n"
"ds_read_b64_tr_b16 a[252:253], v3 offset:30720\n"
"v_mfma_f32_16x16x32_bf16 v[228:231], a[52:55], a[220:223], v[228:231]\n"
"ds_read_b64_tr_b16 a[254:255], v3 offset:35840\n"
"v_mfma_f32_16x16x32_bf16 v[232:235], a[40:43], a[216:219], v[232:235]\n"
"v_mfma_f32_16x16x32_bf16 v[232:235], a[56:59], a[220:223], v[232:235]\n"
"v_mfma_f32_16x16x32_bf16 v[236:239], a[44:47], a[216:219], v[236:239]\n"
"v_mfma_f32_16x16x32_bf16 v[236:239], a[60:63], a[220:223], v[236:239]\n"
"s_waitcnt lgkmcnt(8)\n"
"v_mfma_f32_16x16x32_bf16 v[240:243], a[32:35], a[224:227], v[240:243]\n"
"ds_read_b64_tr_b16 a[216:217], v2 offset:20544\n"
"v_mfma_f32_16x16x32_bf16 v[240:243], a[48:51], a[228:231], v[240:243]\n"
"ds_read_b64_tr_b16 a[218:219], v2 offset:25664\n"
"v_mfma_f32_16x16x32_bf16 v[244:247], a[36:39], a[224:227], v[244:247]\n"
"ds_read_b64_tr_b16 a[220:221], v2 offset:30784\n"
"v_mfma_f32_16x16x32_bf16 v[244:247], a[52:55], a[228:231], v[244:247]\n"
"s_waitcnt vmcnt(18)\n"
"s_waitcnt vmcnt(40)\n"
"ds_read_b64_tr_b16 a[222:223], v2 offset:35904\n"
"v_mfma_f32_16x16x32_bf16 v[248:251], a[40:43], a[224:227], v[248:251]\n"
"s_barrier\n"
"v_mfma_f32_16x16x32_bf16 v[248:251], a[56:59], a[228:231], v[248:251]\n"
"v_mfma_f32_16x16x32_bf16 v[252:255], a[44:47], a[224:227], v[252:255]\n"
"v_mfma_f32_16x16x32_bf16 v[252:255], a[60:63], a[228:231], v[252:255]\n"
"s_waitcnt lgkmcnt(8)\n"
"s_waitcnt vmcnt(28)\n"
"v_mfma_f32_16x16x32_bf16 v[64:67], v[48:51], a[240:243], v[64:67]\n"
"ds_read_b64_tr_b16 a[224:225], v3 offset:20544\n"
"v_mfma_f32_16x16x32_bf16 v[64:67], v[18:21], a[244:247], v[64:67]\n"
"ds_read_b64_tr_b16 a[226:227], v3 offset:25664\n"
"v_mfma_f32_16x16x32_bf16 v[68:71], v[52:55], a[240:243], v[68:71]\n"
"ds_read_b64_tr_b16 a[228:229], v3 offset:30784\n"
"v_mfma_f32_16x16x32_bf16 v[68:71], v[22:25], a[244:247], v[68:71]\n"
"ds_read_b64_tr_b16 a[230:231], v3 offset:35904\n"
"v_mfma_f32_16x16x32_bf16 v[72:75], v[56:59], a[240:243], v[72:75]\n"
"v_mfma_f32_16x16x32_bf16 v[72:75], v[36:39], a[244:247], v[72:75]\n"
"buffer_load_dwordx4 a[104:107], v12, s[24:27], s42 offen offset:2048 sc1\n"
"v_mfma_f32_16x16x32_bf16 v[76:79], v[60:63], a[240:243], v[76:79]\n"
"s_add_u32 m0, s95, 102400\n"
"v_mfma_f32_16x16x32_bf16 v[76:79], v[6:9], a[244:247], v[76:79]\n"
"s_waitcnt lgkmcnt(8)\n"
"buffer_load_dwordx4 v30, s[20:23], s41 offen offset:0 lds\n"
"v_mfma_f32_16x16x32_bf16 v[80:83], v[48:51], a[248:251], v[80:83]\n"
"ds_read_b64_tr_b16 a[240:241], v2 offset:20608\n"
"v_mfma_f32_16x16x32_bf16 v[80:83], v[18:21], a[252:255], v[80:83]\n"
"ds_read_b64_tr_b16 a[242:243], v2 offset:25728\n"
"v_mfma_f32_16x16x32_bf16 v[84:87], v[52:55], a[248:251], v[84:87]\n"
"ds_read_b64_tr_b16 a[244:245], v2 offset:30848\n"
"v_mfma_f32_16x16x32_bf16 v[84:87], v[22:25], a[252:255], v[84:87]\n"
"ds_read_b64_tr_b16 a[246:247], v2 offset:35968\n"
"v_mfma_f32_16x16x32_bf16 v[88:91], v[56:59], a[248:251], v[88:91]\n"
"v_mfma_f32_16x16x32_bf16 v[88:91], v[36:39], a[252:255], v[88:91]\n"
"buffer_load_dwordx4 a[108:111], v12, s[24:27], s42 offen offset:3072 sc1\n"
"s_add_u32 s42, s42, 4096\n"
"v_mfma_f32_16x16x32_bf16 v[92:95], v[60:63], a[248:251], v[92:95]\n"
"v_mfma_f32_16x16x32_bf16 v[92:95], v[6:9], a[252:255], v[92:95]\n"
"s_waitcnt lgkmcnt(8)\n"
"buffer_load_dwordx4 v31, s[20:23], s41 offen offset:1024 lds\n"
"v_mfma_f32_16x16x32_bf16 v[96:99], v[48:51], a[216:219], v[96:99]\n"
"ds_read_b64_tr_b16 a[248:249], v3 offset:20608\n"
"v_mfma_f32_16x16x32_bf16 v[96:99], v[18:21], a[220:223], v[96:99]\n"
"ds_read_b64_tr_b16 a[250:251], v3 offset:25728\n"
"v_mfma_f32_16x16x32_bf16 v[100:103], v[52:55], a[216:219], v[100:103]\n"
"ds_read_b64_tr_b16 a[252:253], v3 offset:30848\n"
"v_mfma_f32_16x16x32_bf16 v[100:103], v[22:25], a[220:223], v[100:103]\n"
"ds_read_b64_tr_b16 a[254:255], v3 offset:35968\n"
"v_mfma_f32_16x16x32_bf16 v[104:107], v[56:59], a[216:219], v[104:107]\n"
"v_mfma_f32_16x16x32_bf16 v[104:107], v[36:39], a[220:223], v[104:107]\n"
"buffer_load_dwordx4 a[112:115], v12, s[24:27], s42 offen offset:0 sc1\n"
"v_mfma_f32_16x16x32_bf16 v[108:111], v[60:63], a[216:219], v[108:111]\n"
"v_mfma_f32_16x16x32_bf16 v[108:111], v[6:9], a[220:223], v[108:111]\n"
"s_waitcnt lgkmcnt(8)\n"
"buffer_load_dwordx4 v32, s[20:23], s41 offen offset:2048 lds\n"
"v_mfma_f32_16x16x32_bf16 v[112:115], v[48:51], a[224:227], v[112:115]\n"
"ds_read_b64_tr_b16 a[216:217], v2 offset:20672\n"
"v_mfma_f32_16x16x32_bf16 v[112:115], v[18:21], a[228:231], v[112:115]\n"
"ds_read_b64_tr_b16 a[218:219], v2 offset:25792\n"
"v_mfma_f32_16x16x32_bf16 v[116:119], v[52:55], a[224:227], v[116:119]\n"
"ds_read_b64_tr_b16 a[220:221], v2 offset:30912\n"
"v_mfma_f32_16x16x32_bf16 v[116:119], v[22:25], a[228:231], v[116:119]\n"
"ds_read_b64_tr_b16 a[222:223], v2 offset:36032\n"
"v_mfma_f32_16x16x32_bf16 v[120:123], v[56:59], a[224:227], v[120:123]\n"
"v_mfma_f32_16x16x32_bf16 v[120:123], v[36:39], a[228:231], v[120:123]\n"
"buffer_load_dwordx4 a[116:119], v12, s[24:27], s42 offen offset:1024 sc1\n"
"v_mfma_f32_16x16x32_bf16 v[124:127], v[60:63], a[224:227], v[124:127]\n"
"s_waitcnt vmcnt(48)\n"
"v_mfma_f32_16x16x32_bf16 v[124:127], v[6:9], a[228:231], v[124:127]\n"
"s_waitcnt lgkmcnt(8)\n"
"buffer_load_dwordx4 v33, s[20:23], s41 offen offset:3072 lds\n"
"v_mfma_f32_16x16x32_bf16 v[160:163], v[48:51], a[240:243], v[160:163]\n"
"ds_read_b64_tr_b16 a[224:225], v3 offset:20672\n"
"v_mfma_f32_16x16x32_bf16 v[160:163], v[18:21], a[244:247], v[160:163]\n"
"ds_read_b64_tr_b16 a[226:227], v3 offset:25792\n"
"v_mfma_f32_16x16x32_bf16 v[164:167], v[52:55], a[240:243], v[164:167]\n"
"ds_read_b64_tr_b16 a[228:229], v3 offset:30912\n"
"v_mfma_f32_16x16x32_bf16 v[164:167], v[22:25], a[244:247], v[164:167]\n"
"ds_read_b64_tr_b16 a[230:231], v3 offset:36032\n"
"v_mfma_f32_16x16x32_bf16 v[168:171], v[56:59], a[240:243], v[168:171]\n"
"v_mfma_f32_16x16x32_bf16 v[168:171], v[36:39], a[244:247], v[168:171]\n"
"buffer_load_dwordx4 a[120:123], v12, s[24:27], s42 offen offset:2048 sc1\n"
"v_mfma_f32_16x16x32_bf16 v[172:175], v[60:63], a[240:243], v[172:175]\n"
"s_add_u32 m0, s95, 106496\n"
"v_mfma_f32_16x16x32_bf16 v[172:175], v[6:9], a[244:247], v[172:175]\n"
"s_waitcnt lgkmcnt(8)\n"
"buffer_load_dwordx4 v34, s[20:23], s41 offen offset:0 lds\n"
"v_mfma_f32_16x16x32_bf16 v[176:179], v[48:51], a[248:251], v[176:179]\n"
"s_add_u32 s41, s41, 81920\n"
"s_cmp_eq_u32 s41, s29\n"
"s_cselect_b32 s41, s28, s41\n"
"ds_read_b64_tr_b16 a[240:241], v2 offset:20736\n"
"v_mfma_f32_16x16x32_bf16 v[176:179], v[18:21], a[252:255], v[176:179]\n"
"ds_read_b64_tr_b16 a[242:243], v2 offset:25856\n"
"v_mfma_f32_16x16x32_bf16 v[180:183], v[52:55], a[248:251], v[180:183]\n"
"ds_read_b64_tr_b16 a[244:245], v2 offset:30976\n"
"v_mfma_f32_16x16x32_bf16 v[180:183], v[22:25], a[252:255], v[180:183]\n"
"ds_read_b64_tr_b16 a[246:247], v2 offset:36096\n"
"v_mfma_f32_16x16x32_bf16 v[184:187], v[56:59], a[248:251], v[184:187]\n"
"v_mfma_f32_16x16x32_bf16 v[184:187], v[36:39], a[252:255], v[184:187]\n"
"buffer_load_dwordx4 a[124:127], v12, s[24:27], s42 offen offset:3072 sc1\n"
"s_add_u32 s42, s42, 4096\n"
"s_cmp_eq_u32 s42, s31\n"
"s_cselect_b32 s42, s30, s42\n"
"v_mfma_f32_16x16x32_bf16 v[188:191], v[60:63], a[248:251], v[188:191]\n"
"v_mfma_f32_16x16x32_bf16 v[188:191], v[6:9], a[252:255], v[188:191]\n"
"s_waitcnt lgkmcnt(8)\n"
"buffer_load_dwordx4 v[128:131], v12, s[24:27], s42 offen offset:0 sc1\n"
"v_mfma_f32_16x16x32_bf16 v[192:195], v[48:51], a[216:219], v[192:195]\n"
"ds_read_b64_tr_b16 a[248:249], v3 offset:20736\n"
"v_mfma_f32_16x16x32_bf16 v[192:195], v[18:21], a[220:223], v[192:195]\n"
"ds_read_b64_tr_b16 a[250:251], v3 offset:25856\n"
"v_mfma_f32_16x16x32_bf16 v[196:199], v[52:55], a[216:219], v[196:199]\n"
"ds_read_b64_tr_b16 a[252:253], v3 offset:30976\n"
"v_mfma_f32_16x16x32_bf16 v[196:199], v[22:25], a[220:223], v[196:199]\n"
"ds_read_b64_tr_b16 a[254:255], v3 offset:36096\n"
"v_mfma_f32_16x16x32_bf16 v[200:203], v[56:59], a[216:219], v[200:203]\n"
"v_mfma_f32_16x16x32_bf16 v[200:203], v[36:39], a[220:223], v[200:203]\n"
"buffer_load_dwordx4 v[132:135], v12, s[24:27], s42 offen offset:1024 sc1\n"
"v_mfma_f32_16x16x32_bf16 v[204:207], v[60:63], a[216:219], v[204:207]\n"
"v_mfma_f32_16x16x32_bf16 v[204:207], v[6:9], a[220:223], v[204:207]\n"
"s_waitcnt lgkmcnt(8)\n"
"buffer_load_dwordx4 v[136:139], v12, s[24:27], s42 offen offset:2048 sc1\n"
"v_mfma_f32_16x16x32_bf16 v[208:211], v[48:51], a[224:227], v[208:211]\n"
"ds_read_b64_tr_b16 a[216:217], v28 offset:0\n"
"v_mfma_f32_16x16x32_bf16 v[208:211], v[18:21], a[228:231], v[208:211]\n"
"ds_read_b64_tr_b16 a[218:219], v28 offset:5120\n"
"v_mfma_f32_16x16x32_bf16 v[212:215], v[52:55], a[224:227], v[212:215]\n"
"ds_read_b64_tr_b16 a[220:221], v28 offset:10240\n"
"v_mfma_f32_16x16x32_bf16 v[212:215], v[22:25], a[228:231], v[212:215]\n"
"ds_read_b64_tr_b16 a[222:223], v28 offset:15360\n"
"v_mfma_f32_16x16x32_bf16 v[216:219], v[56:59], a[224:227], v[216:219]\n"
"v_mfma_f32_16x16x32_bf16 v[216:219], v[36:39], a[228:231], v[216:219]\n"
"v_mfma_f32_16x16x32_bf16 v[220:223], v[60:63], a[224:227], v[220:223]\n"
"v_mfma_f32_16x16x32_bf16 v[220:223], v[6:9], a[228:231], v[220:223]\n"
"s_waitcnt lgkmcnt(8)\n"
"v_mfma_f32_16x16x32_bf16 v[224:227], v[48:51], a[240:243], v[224:227]\n"
"ds_read_b64_tr_b16 a[224:225], v29 offset:0\n"
"v_mfma_f32_16x16x32_bf16 v[224:227], v[18:21], a[244:247], v[224:227]\n"
"ds_read_b64_tr_b16 a[226:227], v29 offset:5120\n"
"v_mfma_f32_16x16x32_bf16 v[228:231], v[52:55], a[240:243], v[228:231]\n"
"ds_read_b64_tr_b16 a[228:229], v29 offset:10240\n"
"v_mfma_f32_16x16x32_bf16 v[228:231], v[22:25], a[244:247], v[228:231]\n"
"ds_read_b64_tr_b16 a[230:231], v29 offset:15360\n"
"v_mfma_f32_16x16x32_bf16 v[232:235], v[56:59], a[240:243], v[232:235]\n"
"v_mfma_f32_16x16x32_bf16 v[232:235], v[36:39], a[244:247], v[232:235]\n"
"v_mfma_f32_16x16x32_bf16 v[236:239], v[60:63], a[240:243], v[236:239]\n"
"v_mfma_f32_16x16x32_bf16 v[236:239], v[6:9], a[244:247], v[236:239]\n"
"s_waitcnt lgkmcnt(8)\n"
"v_mfma_f32_16x16x32_bf16 v[240:243], v[48:51], a[248:251], v[240:243]\n"
"ds_read_b64_tr_b16 a[240:241], v28 offset:64\n"
"v_mfma_f32_16x16x32_bf16 v[240:243], v[18:21], a[252:255], v[240:243]\n"
"ds_read_b64_tr_b16 a[242:243], v28 offset:5184\n"
"v_mfma_f32_16x16x32_bf16 v[244:247], v[52:55], a[248:251], v[244:247]\n"
"ds_read_b64_tr_b16 a[244:245], v28 offset:10304\n"
"v_mfma_f32_16x16x32_bf16 v[244:247], v[22:25], a[252:255], v[244:247]\n"
"s_waitcnt vmcnt(18)\n"
"s_waitcnt vmcnt(40)\n"
"ds_read_b64_tr_b16 a[246:247], v28 offset:15424\n"
"v_mfma_f32_16x16x32_bf16 v[248:251], v[56:59], a[248:251], v[248:251]\n"
"s_barrier\n"
"v_mfma_f32_16x16x32_bf16 v[248:251], v[36:39], a[252:255], v[248:251]\n"
"v_mfma_f32_16x16x32_bf16 v[252:255], v[60:63], a[248:251], v[252:255]\n"
"v_mfma_f32_16x16x32_bf16 v[252:255], v[6:9], a[252:255], v[252:255]\n"
"s_waitcnt lgkmcnt(8)\n"
"s_waitcnt vmcnt(29)\n"
"v_mfma_f32_16x16x32_bf16 v[64:67], a[192:195], a[216:219], v[64:67]\n"
"ds_read_b64_tr_b16 a[248:249], v29 offset:64\n"
"v_mfma_f32_16x16x32_bf16 v[64:67], a[208:211], a[220:223], v[64:67]\n"
"ds_read_b64_tr_b16 a[250:251], v29 offset:5184\n"
"v_mfma_f32_16x16x32_bf16 v[68:71], a[196:199], a[216:219], v[68:71]\n"
"ds_read_b64_tr_b16 a[252:253], v29 offset:10304\n"
"v_mfma_f32_16x16x32_bf16 v[68:71], a[212:215], a[220:223], v[68:71]\n"
"ds_read_b64_tr_b16 a[254:255], v29 offset:15424\n"
"v_mfma_f32_16x16x32_bf16 v[72:75], a[200:203], a[216:219], v[72:75]\n"
"v_mfma_f32_16x16x32_bf16 v[72:75], a[232:235], a[220:223], v[72:75]\n"
"buffer_load_dwordx4 v[140:143], v12, s[24:27], s42 offen offset:3072 sc1\n"
"s_add_u32 s42, s42, 4096\n"
"v_mfma_f32_16x16x32_bf16 v[76:79], a[204:207], a[216:219], v[76:79]\n"
"s_add_u32 m0, s95, 122880\n"
"v_mfma_f32_16x16x32_bf16 v[76:79], a[236:239], a[220:223], v[76:79]\n"
"s_waitcnt lgkmcnt(8)\n"
"buffer_load_dwordx4 v30, s[20:23], s41 offen offset:0 lds\n"
"v_mfma_f32_16x16x32_bf16 v[80:83], a[192:195], a[224:227], v[80:83]\n"
"ds_read_b64_tr_b16 a[216:217], v28 offset:128\n"
"v_mfma_f32_16x16x32_bf16 v[80:83], a[208:211], a[228:231], v[80:83]\n"
"ds_read_b64_tr_b16 a[218:219], v28 offset:5248\n"
"v_mfma_f32_16x16x32_bf16 v[84:87], a[196:199], a[224:227], v[84:87]\n"
"ds_read_b64_tr_b16 a[220:221], v28 offset:10368\n"
"v_mfma_f32_16x16x32_bf16 v[84:87], a[212:215], a[228:231], v[84:87]\n"
"ds_read_b64_tr_b16 a[222:223], v28 offset:15488\n"
"v_mfma_f32_16x16x32_bf16 v[88:91], a[200:203], a[224:227], v[88:91]\n"
"v_mfma_f32_16x16x32_bf16 v[88:91], a[232:235], a[228:231], v[88:91]\n"
"buffer_load_dwordx4 v[144:147], v12, s[24:27], s42 offen offset:0 sc1\n"
"v_mfma_f32_16x16x32_bf16 v[92:95], a[204:207], a[224:227], v[92:95]\n"
"v_mfma_f32_16x16x32_bf16 v[92:95], a[236:239], a[228:231], v[92:95]\n"
"s_waitcnt lgkmcnt(8)\n"
"buffer_load_dwordx4 v31, s[20:23], s41 offen offset:1024 lds\n"
"v_mfma_f32_16x16x32_bf16 v[96:99], a[192:195], a[240:243], v[96:99]\n"
"ds_read_b64_tr_b16 a[224:225], v29 offset:128\n"
"v_mfma_f32_16x16x32_bf16 v[96:99], a[208:211], a[244:247], v[96:99]\n"
"ds_read_b64_tr_b16 a[226:227], v29 offset:5248\n"
"v_mfma_f32_16x16x32_bf16 v[100:103], a[196:199], a[240:243], v[100:103]\n"
"ds_read_b64_tr_b16 a[228:229], v29 offset:10368\n"
"v_mfma_f32_16x16x32_bf16 v[100:103], a[212:215], a[244:247], v[100:103]\n"
"ds_read_b64_tr_b16 a[230:231], v29 offset:15488\n"
"v_mfma_f32_16x16x32_bf16 v[104:107], a[200:203], a[240:243], v[104:107]\n"
"v_mfma_f32_16x16x32_bf16 v[104:107], a[232:235], a[244:247], v[104:107]\n"
"buffer_load_dwordx4 v[148:151], v12, s[24:27], s42 offen offset:1024 sc1\n"
"v_mfma_f32_16x16x32_bf16 v[108:111], a[204:207], a[240:243], v[108:111]\n"
"v_mfma_f32_16x16x32_bf16 v[108:111], a[236:239], a[244:247], v[108:111]\n"
"s_waitcnt lgkmcnt(8)\n"
"buffer_load_dwordx4 v32, s[20:23], s41 offen offset:2048 lds\n"
"v_mfma_f32_16x16x32_bf16 v[112:115], a[192:195], a[248:251], v[112:115]\n"
"ds_read_b64_tr_b16 a[240:241], v28 offset:192\n"
"v_mfma_f32_16x16x32_bf16 v[112:115], a[208:211], a[252:255], v[112:115]\n"
"ds_read_b64_tr_b16 a[242:243], v28 offset:5312\n"
"v_mfma_f32_16x16x32_bf16 v[116:119], a[196:199], a[248:251], v[116:119]\n"
"ds_read_b64_tr_b16 a[244:245], v28 offset:10432\n"
"v_mfma_f32_16x16x32_bf16 v[116:119], a[212:215], a[252:255], v[116:119]\n"
"ds_read_b64_tr_b16 a[246:247], v28 offset:15552\n"
"L_pg22:\n"
"s_cbranch_execz L_bk5\n"
"v_mfma_f32_16x16x32_bf16 v[120:123], a[200:203], a[248:251], v[120:123]\n"
"v_mfma_f32_16x16x32_bf16 v[120:123], a[232:235], a[252:255], v[120:123]\n"
"buffer_load_dwordx4 v[152:155], v12, s[24:27], s42 offen offset:2048 sc1\n"
"v_mfma_f32_16x16x32_bf16 v[124:127], a[204:207], a[248:251], v[124:127]\n"
"s_waitcnt vmcnt(48)\n"
"v_mfma_f32_16x16x32_bf16 v[124:127], a[236:239], a[252:255], v[124:127]\n"
"s_waitcnt lgkmcnt(8)\n"
"buffer_load_dwordx4 v33, s[20:23], s41 offen offset:3072 lds\n"
"v_mfma_f32_16x16x32_bf16 v[160:163], a[192:195], a[216:219], v[160:163]\n"
"ds_read_b64_tr_b16 a[248:249], v29 offset:192\n"
"v_mfma_f32_16x16x32_bf16 v[160:163], a[208:211], a[220:223], v[160:163]\n"
"ds_read_b64_tr_b16 a[250:251], v29 offset:5312\n"
"v_mfma_f32_16x16x32_bf16 v[164:167], a[196:199], a[216:219], v[164:167]\n"
"ds_read_b64_tr_b16 a[252:253], v29 offset:10432\n"
"v_mfma_f32_16x16x32_bf16 v[164:167], a[212:215], a[220:223], v[164:167]\n"
"ds_read_b64_tr_b16 a[254:255], v29 offset:15552\n"
"v_mfma_f32_16x16x32_bf16 v[168:171], a[200:203], a[216:219], v[168:171]\n"
"v_mfma_f32_16x16x32_bf16 v[168:171], a[232:235], a[220:223], v[168:171]\n"
"buffer_load_dwordx4 v[156:159], v12, s[24:27], s42 offen offset:3072 sc1\n"
"s_add_u32 s42, s42, 4096\n"
"v_mfma_f32_16x16x32_bf16 v[172:175], a[204:207], a[216:219], v[172:175]\n"
"s_add_u32 m0, s95, 126976\n"
"v_mfma_f32_16x16x32_bf16 v[172:175], a[236:239], a[220:223], v[172:175]\n"
"s_waitcnt lgkmcnt(8)\n"
"buffer_load_dwordx4 v34, s[20:23], s41 offen offset:0 lds\n"
"v_mfma_f32_16x16x32_bf16 v[176:179], a[192:195], a[224:227], v[176:179]\n"
"s_add_u32 s41, s41, 81920\n"
"ds_read_b64_tr_b16 a[216:217], v28 offset:256\n"
"v_mfma_f32_16x16x32_bf16 v[176:179], a[208:211], a[228:231], v[176:179]\n"
"ds_read_b64_tr_b16 a[218:219], v28 offset:5376\n"
"v_mfma_f32_16x16x32_bf16 v[180:183], a[196:199], a[224:227], v[180:183]\n"
"ds_read_b64_tr_b16 a[220:221], v28 offset:10496\n"
"v_mfma_f32_16x16x32_bf16 v[180:183], a[212:215], a[228:231], v[180:183]\n"
"ds_read_b64_tr_b16 a[222:223], v28 offset:15616\n"
"v_mfma_f32_16x16x32_bf16 v[184:187], a[200:203], a[224:227], v[184:187]\n"
"v_mfma_f32_16x16x32_bf16 v[184:187], a[232:235], a[228:231], v[184:187]\n"
"v_mfma_f32_16x16x32_bf16 v[188:191], a[204:207], a[224:227], v[188:191]\n"
"s_add_u32 m0, s95, 143360\n"
"v_mfma_f32_16x16x32_bf16 v[188:191], a[236:239], a[228:231], v[188:191]\n"
"s_waitcnt lgkmcnt(8)\n"
"buffer_load_dwordx4 v30, s[20:23], s41 offen offset:0 lds\n"
"v_mfma_f32_16x16x32_bf16 v[192:195], a[192:195], a[240:243], v[192:195]\n"
"ds_read_b64_tr_b16 a[224:225], v29 offset:256\n"
"v_mfma_f32_16x16x32_bf16 v[192:195], a[208:211], a[244:247], v[192:195]\n"
"ds_read_b64_tr_b16 a[226:227], v29 offset:5376\n"
"v_mfma_f32_16x16x32_bf16 v[196:199], a[196:199], a[240:243], v[196:199]\n"
"ds_read_b64_tr_b16 a[228:229], v29 offset:10496\n"
"v_mfma_f32_16x16x32_bf16 v[196:199], a[212:215], a[244:247], v[196:199]\n"
"ds_read_b64_tr_b16 a[230:231], v29 offset:15616\n"
"v_mfma_f32_16x16x32_bf16 v[200:203], a[200:203], a[240:243], v[200:203]\n"
"v_mfma_f32_16x16x32_bf16 v[200:203], a[232:235], a[244:247], v[200:203]\n"
"v_mfma_f32_16x16x32_bf16 v[204:207], a[204:207], a[240:243], v[204:207]\n"
"v_mfma_f32_16x16x32_bf16 v[204:207], a[236:239], a[244:247], v[204:207]\n"
"s_waitcnt lgkmcnt(8)\n"
"buffer_load_dwordx4 v31, s[20:23], s41 offen offset:1024 lds\n"
"v_mfma_f32_16x16x32_bf16 v[208:211], a[192:195], a[248:251], v[208:211]\n"
"ds_read_b64_tr_b16 a[240:241], v28 offset:20480\n"
"v_mfma_f32_16x16x32_bf16 v[208:211], a[208:211], a[252:255], v[208:211]\n"
"ds_read_b64_tr_b16 a[242:243], v28 offset:25600\n"
"v_mfma_f32_16x16x32_bf16 v[212:215], a[196:199], a[248:251], v[212:215]\n"
"ds_read_b64_tr_b16 a[244:245], v28 offset:30720\n"
"v_mfma_f32_16x16x32_bf16 v[212:215], a[212:215], a[252:255], v[212:215]\n"
"ds_read_b64_tr_b16 a[246:247], v28 offset:35840\n"
"v_mfma_f32_16x16x32_bf16 v[216:219], a[200:203], a[248:251], v[216:219]\n"
"v_mfma_f32_16x16x32_bf16 v[216:219], a[232:235], a[252:255], v[216:219]\n"
"v_mfma_f32_16x16x32_bf16 v[220:223], a[204:207], a[248:251], v[220:223]\n"
"v_mfma_f32_16x16x32_bf16 v[220:223], a[236:239], a[252:255], v[220:223]\n"
"s_waitcnt lgkmcnt(8)\n"
"buffer_load_dwordx4 v32, s[20:23], s41 offen offset:2048 lds\n"
"v_mfma_f32_16x16x32_bf16 v[224:227], a[192:195], a[216:219], v[224:227]\n"
"ds_read_b64_tr_b16 a[248:249], v29 offset:20480\n"
"v_mfma_f32_16x16x32_bf16 v[224:227], a[208:211], a[220:223], v[224:227]\n"
"ds_read_b64_tr_b16 a[250:251], v29 offset:25600\n"
"v_mfma_f32_16x16x32_bf16 v[228:231], a[196:199], a[216:219], v[228:231]\n"
"ds_read_b64_tr_b16 a[252:253], v29 offset:30720\n"
"v_mfma_f32_16x16x32_bf16 v[228:231], a[212:215], a[220:223], v[228:231]\n"
"ds_read_b64_tr_b16 a[254:255], v29 offset:35840\n"
"v_mfma_f32_16x16x32_bf16 v[232:235], a[200:203], a[216:219], v[232:235]\n"
"v_mfma_f32_16x16x32_bf16 v[232:235], a[232:235], a[220:223], v[232:235]\n"
"buffer_load_dwordx4 v33, s[20:23], s41 offen offset:3072 lds\n"
"v_mfma_f32_16x16x32_bf16 v[236:239], a[204:207], a[216:219], v[236:239]\n"
"s_add_u32 m0, s95, 147456\n"
"v_mfma_f32_16x16x32_bf16 v[236:239], a[236:239], a[220:223], v[236:239]\n"
"s_waitcnt lgkmcnt(8)\n"
"buffer_load_dwordx4 v34, s[20:23], s41 offen offset:0 lds\n"
"v_mfma_f32_16x16x32_bf16 v[240:243], a[192:195], a[224:227], v[240:243]\n"
"s_add_u32 s41, s41, 81920\n"
"ds_read_b64_tr_b16 a[216:217], v28 offset:20544\n"
"v_mfma_f32_16x16x32_bf16 v[240:243], a[208:211], a[228:231], v[240:243]\n"
"ds_read_b64_tr_b16 a[218:219], v28 offset:25664\n"
"v_mfma_f32_16x16x32_bf16 v[244:247], a[196:199], a[224:227], v[244:247]\n"
"ds_read_b64_tr_b16 a[220:221], v28 offset:30784\n"
"v_mfma_f32_16x16x32_bf16 v[244:247], a[212:215], a[228:231], v[244:247]\n"
"s_waitcnt vmcnt(19)\n"
"s_waitcnt vmcnt(40)\n"
"ds_read_b64_tr_b16 a[222:223], v28 offset:35904\n"
"v_mfma_f32_16x16x32_bf16 v[248:251], a[200:203], a[224:227], v[248:251]\n"
"s_barrier\n"
"v_mfma_f32_16x16x32_bf16 v[248:251], a[232:235], a[228:231], v[248:251]\n"
"v_mfma_f32_16x16x32_bf16 v[252:255], a[204:207], a[224:227], v[252:255]\n"
"v_mfma_f32_16x16x32_bf16 v[252:255], a[236:239], a[228:231], v[252:255]\n"
"s_waitcnt lgkmcnt(8)\n"
"s_waitcnt vmcnt(31)\n"
"v_mfma_f32_16x16x32_bf16 v[64:67], a[64:67], a[240:243], v[64:67]\n"
"ds_read_b64_tr_b16 a[224:225], v29 offset:20544\n"
"v_lshrrev_b32 v4, 1, v12\n"
"v_mfma_f32_16x16x32_bf16 v[64:67], a[80:83], a[244:247], v[64:67]\n"
"ds_read_b64_tr_b16 a[226:227], v29 offset:25664\n"
"v_and_b32 v5, 0x200, v12\n"
"v_mfma_f32_16x16x32_bf16 v[68:71], a[68:71], a[240:243], v[68:71]\n"
"ds_read_b64_tr_b16 a[228:229], v29 offset:30784\n"
"v_add_u32 v4, s95, v4\n"
"v_mfma_f32_16x16x32_bf16 v[68:71], a[84:87], a[244:247], v[68:71]\n"
"ds_read_b64_tr_b16 a[230:231], v29 offset:35904\n"
"v_add_u32 v5, s95, v5\n"
"v_mfma_f32_16x16x32_bf16 v[72:75], a[72:75], a[240:243], v[72:75]\n"
"v_bfe_u32 v14, v12, 4, 2\n"
"v_add_u32 v4, 0x5000, v4\n"
"v_mfma_f32_16x16x32_bf16 v[72:75], a[88:91], a[244:247], v[72:75]\n"
"buffer_load_dwordx4 a[128:131], v12, s[24:27], s42 offen offset:0 sc1\n"
"v_mfma_f32_16x16x32_bf16 v[76:79], a[76:79], a[240:243], v[76:79]\n"
"s_add_u32 m0, s95, 40960\n"
"v_lshl_add_u32 v5, v14, 7, v5\n"
"v_bfe_u32 v14, v12, 8, 1\n"
"v_mfma_f32_16x16x32_bf16 v[76:79], a[92:95], a[244:247], v[76:79]\n"
"s_waitcnt lgkmcnt(8)\n"
"buffer_load_dwordx4 v30, s[20:23], s41 offen offset:0 lds\n"
"v_mfma_f32_16x16x32_bf16 v[80:83], a[64:67], a[248:251], v[80:83]\n"
"ds_read_b64_tr_b16 a[240:241], v28 offset:20608\n"
"v_lshl_add_u32 v5, v14, 6, v5\n"
"v_mfma_f32_16x16x32_bf16 v[80:83], a[80:83], a[252:255], v[80:83]\n"
"ds_read_b64_tr_b16 a[242:243], v28 offset:25728\n"
"v_bfe_u32 v14, v12, 6, 2\n"
"v_mfma_f32_16x16x32_bf16 v[84:87], a[68:71], a[248:251], v[84:87]\n"
"ds_read_b64_tr_b16 a[244:245], v28 offset:30848\n"
"v_lshl_add_u32 v5, v14, 3, v5\n"
"v_mfma_f32_16x16x32_bf16 v[84:87], a[84:87], a[252:255], v[84:87]\n"
"ds_read_b64_tr_b16 a[246:247], v28 offset:35968\n"
"v_add_u32 v5, 0x5000, v5\n"
"v_mfma_f32_16x16x32_bf16 v[88:91], a[72:75], a[248:251], v[88:91]\n"
"v_mfma_f32_16x16x32_bf16 v[88:91], a[88:91], a[252:255], v[88:91]\n"
"buffer_load_dwordx4 a[132:135], v12, s[24:27], s42 offen offset:1024 sc1\n"
"v_mfma_f32_16x16x32_bf16 v[92:95], a[76:79], a[248:251], v[92:95]\n"
"v_mfma_f32_16x16x32_bf16 v[92:95], a[92:95], a[252:255], v[92:95]\n"
"s_waitcnt lgkmcnt(8)\n"
"buffer_load_dwordx4 v31, s[20:23], s41 offen offset:1024 lds\n"
"v_mfma_f32_16x16x32_bf16 v[96:99], a[64:67], a[216:219], v[96:99]\n"
"ds_read_b64_tr_b16 a[248:249], v29 offset:20608\n"
"v_mfma_f32_16x16x32_bf16 v[96:99], a[80:83], a[220:223], v[96:99]\n"
"ds_read_b64_tr_b16 a[250:251], v29 offset:25728\n"
"v_mfma_f32_16x16x32_bf16 v[100:103], a[68:71], a[216:219], v[100:103]\n"
"ds_read_b64_tr_b16 a[252:253], v29 offset:30848\n"
"v_mfma_f32_16x16x32_bf16 v[100:103], a[84:87], a[220:223], v[100:103]\n"
"ds_read_b64_tr_b16 a[254:255], v29 offset:35968\n"
"v_mfma_f32_16x16x32_bf16 v[104:107], a[72:75], a[216:219], v[104:107]\n"
"v_mfma_f32_16x16x32_bf16 v[104:107], a[88:91], a[220:223], v[104:107]\n"
"buffer_load_dwordx4 a[136:139], v12, s[24:27], s42 offen offset:2048 sc1\n"
"v_mfma_f32_16x16x32_bf16 v[108:111], a[76:79], a[216:219], v[108:111]\n"
"v_mfma_f32_16x16x32_bf16 v[108:111], a[92:95], a[220:223], v[108:111]\n"
"s_waitcnt lgkmcnt(8)\n"
"buffer_load_dwordx4 v32, s[20:23], s41 offen offset:2048 lds\n"
"v_mfma_f32_16x16x32_bf16 v[112:115], a[64:67], a[224:227], v[112:115]\n"
"ds_read_b64_tr_b16 a[216:217], v28 offset:20672\n"
"v_mfma_f32_16x16x32_bf16 v[112:115], a[80:83], a[228:231], v[112:115]\n"
"ds_read_b64_tr_b16 a[218:219], v28 offset:25792\n"
"v_mfma_f32_16x16x32_bf16 v[116:119], a[68:71], a[224:227], v[116:119]\n"
"ds_read_b64_tr_b16 a[220:221], v28 offset:30912\n"
"v_mfma_f32_16x16x32_bf16 v[116:119], a[84:87], a[228:231], v[116:119]\n"
"ds_read_b64_tr_b16 a[222:223], v28 offset:36032\n"
"v_mfma_f32_16x16x32_bf16 v[120:123], a[72:75], a[224:227], v[120:123]\n"
"v_mfma_f32_16x16x32_bf16 v[120:123], a[88:91], a[228:231], v[120:123]\n"
"buffer_load_dwordx4 a[140:143], v12, s[24:27], s42 offen offset:3072 sc1\n"
"s_add_u32 s42, s42, 4096\n"
"v_mfma_f32_16x16x32_bf16 v[124:127], a[76:79], a[224:227], v[124:127]\n"
"s_waitcnt vmcnt(48)\n"
"v_mfma_f32_16x16x32_bf16 v[124:127], a[92:95], a[228:231], v[124:127]\n"
"s_waitcnt lgkmcnt(8)\n"
"buffer_load_dwordx4 v33, s[20:23], s41 offen offset:3072 lds\n"
"v_mfma_f32_16x16x32_bf16 v[160:163], a[64:67], a[240:243], v[160:163]\n"
"ds_read_b64_tr_b16 a[224:225], v29 offset:20672\n"
"v_mfma_f32_16x16x32_bf16 v[160:163], a[80:83], a[244:247], v[160:163]\n"
"ds_read_b64_tr_b16 a[226:227], v29 offset:25792\n"
"v_mfma_f32_16x16x32_bf16 v[164:167], a[68:71], a[240:243], v[164:167]\n"
"ds_read_b64_tr_b16 a[228:229], v29 offset:30912\n"
"v_mfma_f32_16x16x32_bf16 v[164:167], a[84:87], a[244:247], v[164:167]\n"
"ds_read_b64_tr_b16 a[230:231], v29 offset:36032\n"
"v_mfma_f32_16x16x32_bf16 v[168:171], a[72:75], a[240:243], v[168:171]\n"
"v_mfma_f32_16x16x32_bf16 v[168:171], a[88:91], a[244:247], v[168:171]\n"
"buffer_load_dwordx4 a[144:147], v12, s[24:27], s42 offen offset:0 sc1\n"
"v_mfma_f32_16x16x32_bf16 v[172:175], a[76:79], a[240:243], v[172:175]\n"
"s_add_u32 m0, s95, 45056\n"
"v_mfma_f32_16x16x32_bf16 v[172:175], a[92:95], a[244:247], v[172:175]\n"
"s_waitcnt lgkmcnt(8)\n"
"buffer_load_dwordx4 v34, s[20:23], s41 offen offset:0 lds\n"
"v_mfma_f32_16x16x32_bf16 v[176:179], a[64:67], a[248:251], v[176:179]\n"
"s_add_u32 s41, s41, 81920\n"
"s_cmp_eq_u32 s41, s29\n"
"s_cselect_b32 s41, s28, s41\n"
"ds_read_b64_tr_b16 a[240:241], v28 offset:20736\n"
"v_mfma_f32_16x16x32_bf16 v[176:179], a[80:83], a[252:255], v[176:179]\n"
"ds_read_b64_tr_b16 a[242:243], v28 offset:25856\n"
"v_mfma_f32_16x16x32_bf16 v[180:183], a[68:71], a[248:251], v[180:183]\n"
"ds_read_b64_tr_b16 a[244:245], v28 offset:30976\n"
"v_mfma_f32_16x16x32_bf16 v[180:183], a[84:87], a[252:255], v[180:183]\n"
"ds_read_b64_tr_b16 a[246:247], v28 offset:36096\n"
"v_mfma_f32_16x16x32_bf16 v[184:187], a[72:75], a[248:251], v[184:187]\n"
"v_mfma_f32_16x16x32_bf16 v[184:187], a[88:91], a[252:255], v[184:187]\n"
"buffer_load_dwordx4 a[148:151], v12, s[24:27], s42 offen offset:1024 sc1\n"
"v_mfma_f32_16x16x32_bf16 v[188:191], a[76:79], a[248:251], v[188:191]\n"
"v_mfma_f32_16x16x32_bf16 v[188:191], a[92:95], a[252:255], v[188:191]\n"
"s_waitcnt lgkmcnt(8)\n"
"buffer_load_dwordx4 a[152:155], v12, s[24:27], s42 offen offset:2048 sc1\n"
"v_mfma_f32_16x16x32_bf16 v[192:195], a[64:67], a[216:219], v[192:195]\n"
"ds_read_b64_tr_b16 a[248:249], v29 offset:20736\n"
"v_mfma_f32_16x16x32_bf16 v[192:195], a[80:83], a[220:223], v[192:195]\n"
"ds_read_b64_tr_b16 a[250:251], v29 offset:25856\n"
"v_mfma_f32_16x16x32_bf16 v[196:199], a[68:71], a[216:219], v[196:199]\n"
"ds_read_b64_tr_b16 a[252:253], v29 offset:30976\n"
"v_mfma_f32_16x16x32_bf16 v[196:199], a[84:87], a[220:223], v[196:199]\n"
"ds_read_b64_tr_b16 a[254:255], v29 offset:36096\n"
"v_mfma_f32_16x16x32_bf16 v[200:203], a[72:75], a[216:219], v[200:203]\n"
"v_mfma_f32_16x16x32_bf16 v[200:203], a[88:91], a[220:223], v[200:203]\n"
"buffer_load_dwordx4 a[156:159], v12, s[24:27], s42 offen offset:3072 sc1\n"
"s_add_u32 s42, s42, 4096\n"
"v_mfma_f32_16x16x32_bf16 v[204:207], a[76:79], a[216:219], v[204:207]\n"
"v_mfma_f32_16x16x32_bf16 v[204:207], a[92:95], a[220:223], v[204:207]\n"
"s_waitcnt lgkmcnt(8)\n"
"buffer_load_dwordx4 a[160:163], v12, s[24:27], s42 offen offset:0 sc1\n"
"v_mfma_f32_16x16x32_bf16 v[208:211], a[64:67], a[224:227], v[208:211]\n"
"ds_read_b64_tr_b16 a[216:217], v2 offset:41024\n"
"v_mfma_f32_16x16x32_bf16 v[208:211], a[80:83], a[228:231], v[208:211]\n"
"ds_read_b64_tr_b16 a[218:219], v2 offset:46144\n"
"v_mfma_f32_16x16x32_bf16 v[212:215], a[68:71], a[224:227], v[212:215]\n"
"ds_read_b64_tr_b16 a[220:221], v2 offset:51264\n"
"v_mfma_f32_16x16x32_bf16 v[212:215], a[84:87], a[228:231], v[212:215]\n"
"ds_read_b64_tr_b16 a[222:223], v2 offset:56384\n"
"v_mfma_f32_16x16x32_bf16 v[216:219], a[72:75], a[224:227], v[216:219]\n"
"v_mfma_f32_16x16x32_bf16 v[216:219], a[88:91], a[228:231], v[216:219]\n"
"v_mfma_f32_16x16x32_bf16 v[220:223], a[76:79], a[224:227], v[220:223]\n"
"v_mfma_f32_16x16x32_bf16 v[220:223], a[92:95], a[228:231], v[220:223]\n"
"s_waitcnt lgkmcnt(8)\n"
"v_mfma_f32_16x16x32_bf16 v[224:227], a[64:67], a[240:243], v[224:227]\n"
"ds_read_b64_tr_b16 a[224:225], v3 offset:41024\n"
"v_mfma_f32_16x16x32_bf16 v[224:227], a[80:83], a[244:247], v[224:227]\n"
"ds_read_b64_tr_b16 a[226:227], v3 offset:46144\n"
"v_mfma_f32_16x16x32_bf16 v[228:231], a[68:71], a[240:243], v[228:231]\n"
"ds_read_b64_tr_b16 a[228:229], v3 offset:51264\n"
"v_mfma_f32_16x16x32_bf16 v[228:231], a[84:87], a[244:247], v[228:231]\n"
"ds_read_b64_tr_b16 a[230:231], v3 offset:56384\n"
"v_mfma_f32_16x16x32_bf16 v[232:235], a[72:75], a[240:243], v[232:235]\n"
"v_mfma_f32_16x16x32_bf16 v[232:235], a[88:91], a[244:247], v[232:235]\n"
"v_mfma_f32_16x16x32_bf16 v[236:239], a[76:79], a[240:243], v[236:239]\n"
"v_mfma_f32_16x16x32_bf16 v[236:239], a[92:95], a[244:247], v[236:239]\n"
"s_waitcnt lgkmcnt(8)\n"
"v_mfma_f32_16x16x32_bf16 v[240:243], a[64:67], a[248:251], v[240:243]\n"
"ds_read_b64_tr_b16 a[240:241], v2 offset:41088\n"
"v_mfma_f32_16x16x32_bf16 v[240:243], a[80:83], a[252:255], v[240:243]\n"
"ds_read_b64_tr_b16 a[242:243], v2 offset:46208\n"
"v_mfma_f32_16x16x32_bf16 v[244:247], a[68:71], a[248:251], v[244:247]\n"
"ds_read_b64_tr_b16 a[244:245], v2 offset:51328\n"
"v_mfma_f32_16x16x32_bf16 v[244:247], a[84:87], a[252:255], v[244:247]\n"
"s_waitcnt vmcnt(19)\n"
"s_waitcnt vmcnt(40)\n"
"ds_read_b64_tr_b16 a[246:247], v2 offset:56448\n"
"v_mfma_f32_16x16x32_bf16 v[248:251], a[72:75], a[248:251], v[248:251]\n"
"s_barrier\n"
"v_mfma_f32_16x16x32_bf16 v[248:251], a[88:91], a[252:255], v[248:251]\n"
"v_mfma_f32_16x16x32_bf16 v[252:255], a[76:79], a[248:251], v[252:255]\n"
"v_mfma_f32_16x16x32_bf16 v[252:255], a[92:95], a[252:255], v[252:255]\n"
"s_waitcnt lgkmcnt(8)\n"
"s_waitcnt vmcnt(32)\n"
"v_mfma_f32_16x16x32_bf16 v[96:99], a[96:99], a[216:219], v[96:99]\n"
"ds_read_b64_tr_b16 a[248:249], v3 offset:41088\n"
"v_mfma_f32_16x16x32_bf16 v[96:99], a[112:115], a[220:223], v[96:99]\n"
"ds_read_b64_tr_b16 a[250:251], v3 offset:46208\n"
"v_mfma_f32_16x16x32_bf16 v[100:103], a[100:103], a[216:219], v[100:103]\n"
"ds_read_b64_tr_b16 a[252:253], v3 offset:51328\n"
"v_mfma_f32_16x16x32_bf16 v[100:103], a[116:119], a[220:223], v[100:103]\n"
"ds_read_b64_tr_b16 a[254:255], v3 offset:56448\n"
"v_mfma_f32_16x16x32_bf16 v[104:107], a[104:107], a[216:219], v[104:107]\n"
"v_mfma_f32_16x16x32_bf16 v[104:107], a[120:123], a[220:223], v[104:107]\n"
"buffer_load_dwordx4 a[164:167], v12, s[24:27], s42 offen offset:1024 sc1\n"
"v_mfma_f32_16x16x32_bf16 v[108:111], a[108:111], a[216:219], v[108:111]\n"
"s_add_u32 m0, s95, 61440\n"
"v_mfma_f32_16x16x32_bf16 v[108:111], a[124:127], a[220:223], v[108:111]\n"
"s_waitcnt lgkmcnt(8)\n"
"buffer_load_dwordx4 v30, s[20:23], s41 offen offset:0 lds\n"
"v_mfma_f32_16x16x32_bf16 v[112:115], a[96:99], a[224:227], v[112:115]\n"
"ds_read_b64_tr_b16 a[216:217], v2 offset:41152\n"
"v_mfma_f32_16x16x32_bf16 v[112:115], a[112:115], a[228:231], v[112:115]\n"
"ds_read_b64_tr_b16 a[218:219], v2 offset:46272\n"
"v_mfma_f32_16x16x32_bf16 v[116:119], a[100:103], a[224:227], v[116:119]\n"
"ds_read_b64_tr_b16 a[220:221], v2 offset:51392\n"
"v_mfma_f32_16x16x32_bf16 v[116:119], a[116:119], a[228:231], v[116:119]\n"
"ds_read_b64_tr_b16 a[222:223], v2 offset:56512\n"
"v_mfma_f32_16x16x32_bf16 v[120:123], a[104:107], a[224:227], v[120:123]\n"
"v_mfma_f32_16x16x32_bf16 v[120:123], a[120:123], a[228:231], v[120:123]\n"
"buffer_load_dwordx4 a[168:171], v12, s[24:27], s42 offen offset:2048 sc1\n"
"v_mfma_f32_16x16x32_bf16 v[124:127], a[108:111], a[224:227], v[124:127]\n"
"v_mfma_f32_16x16x32_bf16 v[124:127], a[124:127], a[228:231], v[124:127]\n"
"s_waitcnt lgkmcnt(8)\n"
"buffer_load_dwordx4 v31, s[20:23], s41 offen offset:1024 lds\n"
"v_mfma_f32_16x16x32_bf16 v[160:163], a[96:99], a[240:243], v[160:163]\n"
"ds_read_b64_tr_b16 a[224:225], v3 offset:41152\n"
"v_mfma_f32_16x16x32_bf16 v[160:163], a[112:115], a[244:247], v[160:163]\n"
"ds_read_b64_tr_b16 a[226:227], v3 offset:46272\n"
"v_mfma_f32_16x16x32_bf16 v[164:167], a[100:103], a[240:243], v[164:167]\n"
"ds_read_b64_tr_b16 a[228:229], v3 offset:51392\n"
"v_mfma_f32_16x16x32_bf16 v[164:167], a[116:119], a[244:247], v[164:167]\n"
"ds_read_b64_tr_b16 a[230:231], v3 offset:56512\n"
"v_mfma_f32_16x16x32_bf16 v[168:171], a[104:107], a[240:243], v[168:171]\n"
"v_mfma_f32_16x16x32_bf16 v[168:171], a[120:123], a[244:247], v[168:171]\n"
"buffer_load_dwordx4 a[172:175], v12, s[24:27], s42 offen offset:3072 sc1\n"
"s_add_u32 s42, s42, 4096\n"
"v_mfma_f32_16x16x32_bf16 v[172:175], a[108:111], a[240:243], v[172:175]\n"
"v_mfma_f32_16x16x32_bf16 v[172:175], a[124:127], a[244:247], v[172:175]\n"
"s_waitcnt lgkmcnt(8)\n"
"buffer_load_dwordx4 v32, s[20:23], s41 offen offset:2048 lds\n"
"v_mfma_f32_16x16x32_bf16 v[176:179], a[96:99], a[248:251], v[176:179]\n"
"ds_read_b64_tr_b16 a[240:241], v2 offset:41216\n"
"v_mfma_f32_16x16x32_bf16 v[176:179], a[112:115], a[252:255], v[176:179]\n"
"ds_read_b64_tr_b16 a[242:243], v2 offset:46336\n"
"v_mfma_f32_16x16x32_bf16 v[180:183], a[100:103], a[248:251], v[180:183]\n"
"ds_read_b64_tr_b16 a[244:245], v2 offset:51456\n"
"v_mfma_f32_16x16x32_bf16 v[180:183], a[116:119], a[252:255], v[180:183]\n"
"ds_read_b64_tr_b16 a[246:247], v2 offset:56576\n"
"v_mfma_f32_16x16x32_bf16 v[184:187], a[104:107], a[248:251], v[184:187]\n"
"v_mfma_f32_16x16x32_bf16 v[184:187], a[120:123], a[252:255], v[184:187]\n"
"buffer_load_dwordx4 a[176:179], v12, s[24:27], s42 offen offset:0 sc1\n"
"v_mfma_f32_16x16x32_bf16 v[188:191], a[108:111], a[248:251], v[188:191]\n"
"s_waitcnt vmcnt(48)\n"
"v_mfma_f32_16x16x32_bf16 v[188:191], a[124:127], a[252:255], v[188:191]\n"
"s_waitcnt lgkmcnt(8)\n"
"buffer_load_dwordx4 v33, s[20:23], s41 offen offset:3072 lds\n"
"v_mfma_f32_16x16x32_bf16 v[192:195], a[96:99], a[216:219], v[192:195]\n"
"ds_read_b64_tr_b16 a[248:249], v3 offset:41216\n"
"v_mfma_f32_16x16x32_bf16 v[192:195], a[112:115], a[220:223], v[192:195]\n"
"ds_read_b64_tr_b16 a[250:251], v3 offset:46336\n"
"v_mfma_f32_16x16x32_bf16 v[196:199], a[100:103], a[216:219], v[196:199]\n"
"ds_read_b64_tr_b16 a[252:253], v3 offset:51456\n"
"v_mfma_f32_16x16x32_bf16 v[196:199], a[116:119], a[220:223], v[196:199]\n"
"ds_read_b64_tr_b16 a[254:255], v3 offset:56576\n"
"v_mfma_f32_16x16x32_bf16 v[200:203], a[104:107], a[216:219], v[200:203]\n"
"v_mfma_f32_16x16x32_bf16 v[200:203], a[120:123], a[220:223], v[200:203]\n"
"buffer_load_dwordx4 a[180:183], v12, s[24:27], s42 offen offset:1024 sc1\n"
"v_mfma_f32_16x16x32_bf16 v[204:207], a[108:111], a[216:219], v[204:207]\n"
"s_add_u32 m0, s95, 65536\n"
"v_mfma_f32_16x16x32_bf16 v[204:207], a[124:127], a[220:223], v[204:207]\n"
"s_waitcnt lgkmcnt(8)\n"
"buffer_load_dwordx4 v34, s[20:23], s41 offen offset:0 lds\n"
"v_mfma_f32_16x16x32_bf16 v[208:211], a[96:99], a[224:227], v[208:211]\n"
"s_add_u32 s41, s41, 81920\n"
"ds_read_b64_tr_b16 a[216:217], v0 offset:64\n"
"v_mfma_f32_16x16x32_bf16 v[208:211], a[112:115], a[228:231], v[208:211]\n"
"ds_read_b64_tr_b16 a[218:219], v0 offset:5184\n"
"v_mfma_f32_16x16x32_bf16 v[212:215], a[100:103], a[224:227], v[212:215]\n"
"ds_read_b64_tr_b16 a[220:221], v0 offset:10304\n"
"v_mfma_f32_16x16x32_bf16 v[212:215], a[116:119], a[228:231], v[212:215]\n"
"ds_read_b64_tr_b16 a[222:223], v0 offset:15424\n"
"v_mfma_f32_16x16x32_bf16 v[216:219], a[104:107], a[224:227], v[216:219]\n"
"v_mfma_f32_16x16x32_bf16 v[216:219], a[120:123], a[228:231], v[216:219]\n"
"buffer_load_dwordx4 a[184:187], v12, s[24:27], s42 offen offset:2048 sc1\n"
"v_mfma_f32_16x16x32_bf16 v[220:223], a[108:111], a[224:227], v[220:223]\n"
"v_mfma_f32_16x16x32_bf16 v[220:223], a[124:127], a[228:231], v[220:223]\n"
"s_waitcnt lgkmcnt(8)\n"
"buffer_load_dwordx4 a[188:191], v12, s[24:27], s42 offen offset:3072 sc1\n"
"s_add_u32 s42, s42, 4096\n"
"s_cmp_eq_u32 s42, s31\n"
"s_cselect_b32 s42, s30, s42\n"
"v_mfma_f32_16x16x32_bf16 v[224:227], a[96:99], a[240:243], v[224:227]\n"
"ds_read_b64_tr_b16 a[224:225], v1 offset:64\n"
"v_mfma_f32_16x16x32_bf16 v[224:227], a[112:115], a[244:247], v[224:227]\n"
"ds_read_b64_tr_b16 a[226:227], v1 offset:5184\n"
"v_mfma_f32_16x16x32_bf16 v[228:231], a[100:103], a[240:243], v[228:231]\n"
"ds_read_b64_tr_b16 a[228:229], v1 offset:10304\n"
"v_mfma_f32_16x16x32_bf16 v[228:231], a[116:119], a[244:247], v[228:231]\n"
"ds_read_b64_tr_b16 a[230:231], v1 offset:15424\n"
"v_mfma_f32_16x16x32_bf16 v[232:235], a[104:107], a[240:243], v[232:235]\n"
"v_mfma_f32_16x16x32_bf16 v[232:235], a[120:123], a[244:247], v[232:235]\n"
"v_mfma_f32_16x16x32_bf16 v[236:239], a[108:111], a[240:243], v[236:239]\n"
"v_mfma_f32_16x16x32_bf16 v[236:239], a[124:127], a[244:247], v[236:239]\n"
"s_waitcnt lgkmcnt(8)\n"
"v_mfma_f32_16x16x32_bf16 v[240:243], a[96:99], a[248:251], v[240:243]\n"
"ds_read_b64_tr_b16 a[240:241], v0 offset:128\n"
"v_mfma_f32_16x16x32_bf16 v[240:243], a[112:115], a[252:255], v[240:243]\n"
"ds_read_b64_tr_b16 a[242:243], v0 offset:5248\n"
"v_mfma_f32_16x16x32_bf16 v[244:247], a[100:103], a[248:251], v[244:247]\n"
"ds_read_b64_tr_b16 a[244:245], v0 offset:10368\n"
"v_mfma_f32_16x16x32_bf16 v[244:247], a[116:119], a[252:255], v[244:247]\n"
"s_waitcnt vmcnt(26)\n"
"s_waitcnt vmcnt(40)\n"
"ds_read_b64_tr_b16 a[246:247], v0 offset:15488\n"
"v_mfma_f32_16x16x32_bf16 v[248:251], a[104:107], a[248:251], v[248:251]\n"
"s_barrier\n"
"v_mfma_f32_16x16x32_bf16 v[248:251], a[120:123], a[252:255], v[248:251]\n"
"v_mfma_f32_16x16x32_bf16 v[252:255], a[108:111], a[248:251], v[252:255]\n"
"v_mfma_f32_16x16x32_bf16 v[252:255], a[124:127], a[252:255], v[252:255]\n"
"s_waitcnt lgkmcnt(8)\n"
"s_waitcnt vmcnt(32)\n"
"v_mfma_f32_16x16x32_bf16 v[96:99], v[128:131], a[216:219], v[96:99]\n"
"ds_read_b64_tr_b16 a[248:249], v1 offset:128\n"
"v_mfma_f32_16x16x32_bf16 v[96:99], v[144:147], a[220:223], v[96:99]\n"
"ds_read_b64_tr_b16 a[250:251], v1 offset:5248\n"
"v_mfma_f32_16x16x32_bf16 v[100:103], v[132:135], a[216:219], v[100:103]\n"
"ds_read_b64_tr_b16 a[252:253], v1 offset:10368\n"
"v_mfma_f32_16x16x32_bf16 v[100:103], v[148:151], a[220:223], v[100:103]\n"
"ds_read_b64_tr_b16 a[254:255], v1 offset:15488\n"
"v_mfma_f32_16x16x32_bf16 v[104:107], v[136:139], a[216:219], v[104:107]\n"
"v_mfma_f32_16x16x32_bf16 v[104:107], v[152:155], a[220:223], v[104:107]\n"
"buffer_load_dwordx4 a[0:3], v12, s[24:27], s42 offen offset:0 sc1\n"
"v_mfma_f32_16x16x32_bf16 v[108:111], v[140:143], a[216:219], v[108:111]\n"
"s_add_u32 m0, s95, 81920\n"
"v_mfma_f32_16x16x32_bf16 v[108:111], v[156:159], a[220:223], v[108:111]\n"
"s_waitcnt lgkmcnt(8)\n"
"buffer_load_dwordx4 v30, s[20:23], s41 offen offset:0 lds\n"
"v_mfma_f32_16x16x32_bf16 v[112:115], v[128:131], a[224:227], v[112:115]\n"
"ds_read_b64_tr_b16 a[216:217], v0 offset:192\n"
"v_mfma_f32_16x16x32_bf16 v[112:115], v[144:147], a[228:231], v[112:115]\n"
"ds_read_b64_tr_b16 a[218:219], v0 offset:5312\n"
"v_mfma_f32_16x16x32_bf16 v[116:119], v[132:135], a[224:227], v[116:119]\n"
"ds_read_b64_tr_b16 a[220:221], v0 offset:10432\n"
"v_mfma_f32_16x16x32_bf16 v[116:119], v[148:151], a[228:231], v[116:119]\n"
"ds_read_b64_tr_b16 a[222:223], v0 offset:15552\n"
"v_mfma_f32_16x16x32_bf16 v[120:123], v[136:139], a[224:227], v[120:123]\n"
"v_mfma_f32_16x16x32_bf16 v[120:123], v[152:155], a[228:231], v[120:123]\n"
"buffer_load_dwordx4 a[4:7], v12, s[24:27], s42 offen offset:1024 sc1\n"
"v_mfma_f32_16x16x32_bf16 v[124:127], v[140:143], a[224:227], v[124:127]\n"
"v_mfma_f32_16x16x32_bf16 v[124:127], v[156:159], a[228:231], v[124:127]\n"
"s_waitcnt lgkmcnt(8)\n"
"buffer_load_dwordx4 v31, s[20:23], s41 offen offset:1024 lds\n"
"v_mfma_f32_16x16x32_bf16 v[160:163], v[128:131], a[240:243], v[160:163]\n"
"ds_read_b64_tr_b16 a[224:225], v1 offset:192\n"
"v_mfma_f32_16x16x32_bf16 v[160:163], v[144:147], a[244:247], v[160:163]\n"
"ds_read_b64_tr_b16 a[226:227], v1 offset:5312\n"
"v_mfma_f32_16x16x32_bf16 v[164:167], v[132:135], a[240:243], v[164:167]\n"
"ds_read_b64_tr_b16 a[228:229], v1 offset:10432\n"
"v_mfma_f32_16x16x32_bf16 v[164:167], v[148:151], a[244:247], v[164:167]\n"
"ds_read_b64_tr_b16 a[230:231], v1 offset:15552\n"
"v_mfma_f32_16x16x32_bf16 v[168:171], v[136:139], a[240:243], v[168:171]\n"
"v_mfma_f32_16x16x32_bf16 v[168:171], v[152:155], a[244:247], v[168:171]\n"
"buffer_load_dwordx4 a[8:11], v12, s[24:27], s42 offen offset:2048 sc1\n"
"v_mfma_f32_16x16x32_bf16 v[172:175], v[140:143], a[240:243], v[172:175]\n"
"v_mfma_f32_16x16x32_bf16 v[172:175], v[156:159], a[244:247], v[172:175]\n"
"s_waitcnt lgkmcnt(8)\n"
"buffer_load_dwordx4 v32, s[20:23], s41 offen offset:2048 lds\n"
"v_mfma_f32_16x16x32_bf16 v[176:179], v[128:131], a[248:251], v[176:179]\n"
"ds_read_b64_tr_b16 a[240:241], v0 offset:256\n"
"v_mfma_f32_16x16x32_bf16 v[176:179], v[144:147], a[252:255], v[176:179]\n"
"ds_read_b64_tr_b16 a[242:243], v0 offset:5376\n"
"v_mfma_f32_16x16x32_bf16 v[180:183], v[132:135], a[248:251], v[180:183]\n"
"ds_read_b64_tr_b16 a[244:245], v0 offset:10496\n"
"v_mfma_f32_16x16x32_bf16 v[180:183], v[148:151], a[252:255], v[180:183]\n"
"ds_read_b64_tr_b16 a[246:247], v0 offset:15616\n"
"v_mfma_f32_16x16x32_bf16 v[184:187], v[136:139], a[248:251], v[184:187]\n"
"v_mfma_f32_16x16x32_bf16 v[184:187], v[152:155], a[252:255], v[184:187]\n"
"buffer_load_dwordx4 a[12:15], v12, s[24:27], s42 offen offset:3072 sc1\n"
"s_add_u32 s42, s42, 4096\n"
"v_mfma_f32_16x16x32_bf16 v[188:191], v[140:143], a[248:251], v[188:191]\n"
"s_waitcnt vmcnt(48)\n"
"v_mfma_f32_16x16x32_bf16 v[188:191], v[156:159], a[252:255], v[188:191]\n"
"s_waitcnt lgkmcnt(8)\n"
"buffer_load_dwordx4 v33, s[20:23], s41 offen offset:3072 lds\n"
"v_mfma_f32_16x16x32_bf16 v[192:195], v[128:131], a[216:219], v[192:195]\n"
"ds_read_b64_tr_b16 a[248:249], v1 offset:256\n"
"v_mfma_f32_16x16x32_bf16 v[192:195], v[144:147], a[220:223], v[192:195]\n"
"ds_read_b64_tr_b16 a[250:251], v1 offset:5376\n"
"v_mfma_f32_16x16x32_bf16 v[196:199], v[132:135], a[216:219], v[196:199]\n"
"ds_read_b64_tr_b16 a[252:253], v1 offset:10496\n"
"v_mfma_f32_16x16x32_bf16 v[196:199], v[148:151], a[220:223], v[196:199]\n"
"ds_read_b64_tr_b16 a[254:255], v1 offset:15616\n"
"v_mfma_f32_16x16x32_bf16 v[200:203], v[136:139], a[216:219], v[200:203]\n"
"v_mfma_f32_16x16x32_bf16 v[200:203], v[152:155], a[220:223], v[200:203]\n"
"buffer_load_dwordx4 a[16:19], v12, s[24:27], s42 offen offset:0 sc1\n"
"v_mfma_f32_16x16x32_bf16 v[204:207], v[140:143], a[216:219], v[204:207]\n"
"s_add_u32 m0, s95, 86016\n"
"v_mfma_f32_16x16x32_bf16 v[204:207], v[156:159], a[220:223], v[204:207]\n"
"s_waitcnt lgkmcnt(8)\n"
"buffer_load_dwordx4 v34, s[20:23], s41 offen offset:0 lds\n"
"v_mfma_f32_16x16x32_bf16 v[208:211], v[128:131], a[224:227], v[208:211]\n"
"s_add_u32 s41, s41, 81920\n"
"ds_read_b64_tr_b16 a[216:217], v0 offset:20544\n"
"v_mfma_f32_16x16x32_bf16 v[208:211], v[144:147], a[228:231], v[208:211]\n"
"ds_read_b64_tr_b16 a[218:219], v0 offset:25664\n"
"v_mfma_f32_16x16x32_bf16 v[212:215], v[132:135], a[224:227], v[212:215]\n"
"ds_read_b64_tr_b16 a[220:221], v0 offset:30784\n"
"v_mfma_f32_16x16x32_bf16 v[212:215], v[148:151], a[228:231], v[212:215]\n"
"ds_read_b64_tr_b16 a[222:223], v0 offset:35904\n"
"v_mfma_f32_16x16x32_bf16 v[216:219], v[136:139], a[224:227], v[216:219]\n"
"v_mfma_f32_16x16x32_bf16 v[216:219], v[152:155], a[228:231], v[216:219]\n"
"buffer_load_dwordx4 a[20:23], v12, s[24:27], s42 offen offset:1024 sc1\n"
"v_mfma_f32_16x16x32_bf16 v[220:223], v[140:143], a[224:227], v[220:223]\n"
"v_mfma_f32_16x16x32_bf16 v[220:223], v[156:159], a[228:231], v[220:223]\n"
"s_waitcnt lgkmcnt(8)\n"
"buffer_load_dwordx4 a[24:27], v12, s[24:27], s42 offen offset:2048 sc1\n"
"v_mfma_f32_16x16x32_bf16 v[224:227], v[128:131], a[240:243], v[224:227]\n"
"ds_read_b64_tr_b16 a[224:225], v1 offset:20544\n"
"v_mfma_f32_16x16x32_bf16 v[224:227], v[144:147], a[244:247], v[224:227]\n"
"ds_read_b64_tr_b16 a[226:227], v1 offset:25664\n"
"v_mfma_f32_16x16x32_bf16 v[228:231], v[132:135], a[240:243], v[228:231]\n"
"ds_read_b64_tr_b16 a[228:229], v1 offset:30784\n"
"v_mfma_f32_16x16x32_bf16 v[228:231], v[148:151], a[244:247], v[228:231]\n"
"ds_read_b64_tr_b16 a[230:231], v1 offset:35904\n"
"v_mfma_f32_16x16x32_bf16 v[232:235], v[136:139], a[240:243], v[232:235]\n"
"v_mfma_f32_16x16x32_bf16 v[232:235], v[152:155], a[244:247], v[232:235]\n"
"v_mfma_f32_16x16x32_bf16 v[236:239], v[140:143], a[240:243], v[236:239]\n"
"v_mfma_f32_16x16x32_bf16 v[236:239], v[156:159], a[244:247], v[236:239]\n"
"s_waitcnt lgkmcnt(8)\n"
"v_mfma_f32_16x16x32_bf16 v[240:243], v[128:131], a[248:251], v[240:243]\n"
"ds_read_b64_tr_b16 a[240:241], v0 offset:20608\n"
"v_mfma_f32_16x16x32_bf16 v[240:243], v[144:147], a[252:255], v[240:243]\n"
"ds_read_b64_tr_b16 a[242:243], v0 offset:25728\n"
"v_mfma_f32_16x16x32_bf16 v[244:247], v[132:135], a[248:251], v[244:247]\n"
"ds_read_b64_tr_b16 a[244:245], v0 offset:30848\n"
"v_mfma_f32_16x16x32_bf16 v[244:247], v[148:151], a[252:255], v[244:247]\n"
"s_waitcnt vmcnt(28)\n"
"s_waitcnt vmcnt(40)\n"
"ds_read_b64_tr_b16 a[246:247], v0 offset:35968\n"
"v_mfma_f32_16x16x32_bf16 v[248:251], v[136:139], a[248:251], v[248:251]\n"
"s_barrier\n"
"v_mfma_f32_16x16x32_bf16 v[248:251], v[152:155], a[252:255], v[248:251]\n"
"v_mfma_f32_16x16x32_bf16 v[252:255], v[140:143], a[248:251], v[252:255]\n"
"v_mfma_f32_16x16x32_bf16 v[252:255], v[156:159], a[252:255], v[252:255]\n"
"s_waitcnt lgkmcnt(8)\n"
"s_waitcnt vmcnt(25)\n"
"v_mfma_f32_16x16x32_bf16 v[96:99], a[128:131], a[216:219], v[96:99]\n"
"ds_read_b64_tr_b16 a[248:249], v1 offset:20608\n"
"v_mfma_f32_16x16x32_bf16 v[96:99], a[144:147], a[220:223], v[96:99]\n"
"ds_read_b64_tr_b16 a[250:251], v1 offset:25728\n"
"v_mfma_f32_16x16x32_bf16 v[100:103], a[132:135], a[216:219], v[100:103]\n"
"ds_read_b64_tr_b16 a[252:253], v1 offset:30848\n"
"v_mfma_f32_16x16x32_bf16 v[100:103], a[148:151], a[220:223], v[100:103]\n"
"ds_read_b64_tr_b16 a[254:255], v1 offset:35968\n"
"v_mfma_f32_16x16x32_bf16 v[104:107], a[136:139], a[216:219], v[104:107]\n"
"v_mfma_f32_16x16x32_bf16 v[104:107], a[152:155], a[220:223], v[104:107]\n"
"buffer_load_dwordx4 a[28:31], v12, s[24:27], s42 offen offset:3072 sc1\n"
"s_add_u32 s42, s42, 4096\n"
"v_mfma_f32_16x16x32_bf16 v[108:111], a[140:143], a[216:219], v[108:111]\n"
"s_add_u32 m0, s95, 0\n"
"v_mfma_f32_16x16x32_bf16 v[108:111], a[156:159], a[220:223], v[108:111]\n"
"s_waitcnt lgkmcnt(8)\n"
"buffer_load_dwordx4 v30, s[20:23], s41 offen offset:0 lds\n"
"v_mfma_f32_16x16x32_bf16 v[112:115], a[128:131], a[224:227], v[112:115]\n"
"L_pg23:\n"
"s_cbranch_execz L_bk5\n"
"ds_read_b64_tr_b16 a[216:217], v0 offset:20672\n"
"v_mfma_f32_16x16x32_bf16 v[112:115], a[144:147], a[228:231], v[112:115]\n"
"ds_read_b64_tr_b16 a[218:219], v0 offset:25792\n"
"v_mfma_f32_16x16x32_bf16 v[116:119], a[132:135], a[224:227], v[116:119]\n"
"ds_read_b64_tr_b16 a[220:221], v0 offset:30912\n"
"v_mfma_f32_16x16x32_bf16 v[116:119], a[148:151], a[228:231], v[116:119]\n"
"ds_read_b64_tr_b16 a[222:223], v0 offset:36032\n"
"v_mfma_f32_16x16x32_bf16 v[120:123], a[136:139], a[224:227], v[120:123]\n"
"v_mfma_f32_16x16x32_bf16 v[120:123], a[152:155], a[228:231], v[120:123]\n"
"buffer_load_dwordx4 a[32:35], v12, s[24:27], s42 offen offset:0 sc1\n"
"v_mfma_f32_16x16x32_bf16 v[124:127], a[140:143], a[224:227], v[124:127]\n"
"v_mfma_f32_16x16x32_bf16 v[124:127], a[156:159], a[228:231], v[124:127]\n"
"s_waitcnt lgkmcnt(8)\n"
"buffer_load_dwordx4 v31, s[20:23], s41 offen offset:1024 lds\n"
"v_mfma_f32_16x16x32_bf16 v[160:163], a[128:131], a[240:243], v[160:163]\n"
"ds_read_b64_tr_b16 a[224:225], v1 offset:20672\n"
"v_mfma_f32_16x16x32_bf16 v[160:163], a[144:147], a[244:247], v[160:163]\n"
"ds_read_b64_tr_b16 a[226:227], v1 offset:25792\n"
"v_mfma_f32_16x16x32_bf16 v[164:167], a[132:135], a[240:243], v[164:167]\n"
"ds_read_b64_tr_b16 a[228:229], v1 offset:30912\n"
"v_mfma_f32_16x16x32_bf16 v[164:167], a[148:151], a[244:247], v[164:167]\n"
"ds_read_b64_tr_b16 a[230:231], v1 offset:36032\n"
"v_mfma_f32_16x16x32_bf16 v[168:171], a[136:139], a[240:243], v[168:171]\n"
"v_mfma_f32_16x16x32_bf16 v[168:171], a[152:155], a[244:247], v[168:171]\n"
"buffer_load_dwordx4 a[36:39], v12, s[24:27], s42 offen offset:1024 sc1\n"
"v_mfma_f32_16x16x32_bf16 v[172:175], a[140:143], a[240:243], v[172:175]\n"
"v_mfma_f32_16x16x32_bf16 v[172:175], a[156:159], a[244:247], v[172:175]\n"
"s_waitcnt lgkmcnt(8)\n"
"buffer_load_dwordx4 v32, s[20:23], s41 offen offset:2048 lds\n"
"v_mfma_f32_16x16x32_bf16 v[176:179], a[128:131], a[248:251], v[176:179]\n"
"ds_read_b64_tr_b16 a[240:241], v0 offset:20736\n"
"v_mfma_f32_16x16x32_bf16 v[176:179], a[144:147], a[252:255], v[176:179]\n"
"ds_read_b64_tr_b16 a[242:243], v0 offset:25856\n"
"v_mfma_f32_16x16x32_bf16 v[180:183], a[132:135], a[248:251], v[180:183]\n"
"ds_read_b64_tr_b16 a[244:245], v0 offset:30976\n"
"v_mfma_f32_16x16x32_bf16 v[180:183], a[148:151], a[252:255], v[180:183]\n"
"ds_read_b64_tr_b16 a[246:247], v0 offset:36096\n"
"v_mfma_f32_16x16x32_bf16 v[184:187], a[136:139], a[248:251], v[184:187]\n"
"v_mfma_f32_16x16x32_bf16 v[184:187], a[152:155], a[252:255], v[184:187]\n"
"buffer_load_dwordx4 a[40:43], v12, s[24:27], s42 offen offset:2048 sc1\n"
"v_mfma_f32_16x16x32_bf16 v[188:191], a[140:143], a[248:251], v[188:191]\n"
"s_waitcnt vmcnt(48)\n"
"v_mfma_f32_16x16x32_bf16 v[188:191], a[156:159], a[252:255], v[188:191]\n"
"s_waitcnt lgkmcnt(8)\n"
"buffer_load_dwordx4 v33, s[20:23], s41 offen offset:3072 lds\n"
"v_mfma_f32_16x16x32_bf16 v[192:195], a[128:131], a[216:219], v[192:195]\n"
"ds_read_b64_tr_b16 a[248:249], v1 offset:20736\n"
"v_mfma_f32_16x16x32_bf16 v[192:195], a[144:147], a[220:223], v[192:195]\n"
"ds_read_b64_tr_b16 a[250:251], v1 offset:25856\n"
"v_mfma_f32_16x16x32_bf16 v[196:199], a[132:135], a[216:219], v[196:199]\n"
"ds_read_b64_tr_b16 a[252:253], v1 offset:30976\n"
"v_mfma_f32_16x16x32_bf16 v[196:199], a[148:151], a[220:223], v[196:199]\n"
"ds_read_b64_tr_b16 a[254:255], v1 offset:36096\n"
"v_mfma_f32_16x16x32_bf16 v[200:203], a[136:139], a[216:219], v[200:203]\n"
"v_mfma_f32_16x16x32_bf16 v[200:203], a[152:155], a[220:223], v[200:203]\n"
"buffer_load_dwordx4 a[44:47], v12, s[24:27], s42 offen offset:3072 sc1\n"
"s_add_u32 s42, s42, 4096\n"
"v_mfma_f32_16x16x32_bf16 v[204:207], a[140:143], a[216:219], v[204:207]\n"
"s_add_u32 m0, s95, 4096\n"
"v_mfma_f32_16x16x32_bf16 v[204:207], a[156:159], a[220:223], v[204:207]\n"
"s_waitcnt lgkmcnt(8)\n"
"buffer_load_dwordx4 v34, s[20:23], s41 offen offset:0 lds\n"
"v_mfma_f32_16x16x32_bf16 v[208:211], a[128:131], a[224:227], v[208:211]\n"
"s_add_u32 s41, s41, 81920\n"
"s_cmp_eq_u32 s41, s29\n"
"s_cselect_b32 s41, s28, s41\n"
"ds_read_b64_tr_b16 a[216:217], v28 offset:41024\n"
"v_mfma_f32_16x16x32_bf16 v[208:211], a[144:147], a[228:231], v[208:211]\n"
"ds_read_b64_tr_b16 a[218:219], v28 offset:46144\n"
"v_mfma_f32_16x16x32_bf16 v[212:215], a[132:135], a[224:227], v[212:215]\n"
"ds_read_b64_tr_b16 a[220:221], v28 offset:51264\n"
"v_mfma_f32_16x16x32_bf16 v[212:215], a[148:151], a[228:231], v[212:215]\n"
"ds_read_b64_tr_b16 a[222:223], v28 offset:56384\n"
"v_mfma_f32_16x16x32_bf16 v[216:219], a[136:139], a[224:227], v[216:219]\n"
"v_and_b32 v46, s98, v12\n"
"v_mfma_f32_16x16x32_bf16 v[216:219], a[152:155], a[228:231], v[216:219]\n"
"buffer_load_dwordx2 v[46:47], v46, s[24:27], s90 offen sc1\n"
"v_mfma_f32_16x16x32_bf16 v[220:223], a[140:143], a[224:227], v[220:223]\n"
"v_mfma_f32_16x16x32_bf16 v[220:223], a[156:159], a[228:231], v[220:223]\n"
"s_waitcnt lgkmcnt(8)\n"
"buffer_load_dwordx4 a[48:51], v12, s[24:27], s42 offen offset:0 sc1\n"
"v_mfma_f32_16x16x32_bf16 v[224:227], a[128:131], a[240:243], v[224:227]\n"
"ds_read_b64_tr_b16 a[224:225], v29 offset:41024\n"
"v_mfma_f32_16x16x32_bf16 v[224:227], a[144:147], a[244:247], v[224:227]\n"
"ds_read_b64_tr_b16 a[226:227], v29 offset:46144\n"
"v_mfma_f32_16x16x32_bf16 v[228:231], a[132:135], a[240:243], v[228:231]\n"
"ds_read_b64_tr_b16 a[228:229], v29 offset:51264\n"
"v_mfma_f32_16x16x32_bf16 v[228:231], a[148:151], a[244:247], v[228:231]\n"
"ds_read_b64_tr_b16 a[230:231], v29 offset:56384\n"
"v_mfma_f32_16x16x32_bf16 v[232:235], a[136:139], a[240:243], v[232:235]\n"
"v_mfma_f32_16x16x32_bf16 v[232:235], a[152:155], a[244:247], v[232:235]\n"
"v_mfma_f32_16x16x32_bf16 v[236:239], a[140:143], a[240:243], v[236:239]\n"
"v_mfma_f32_16x16x32_bf16 v[236:239], a[156:159], a[244:247], v[236:239]\n"
"s_waitcnt lgkmcnt(8)\n"
"v_mfma_f32_16x16x32_bf16 v[240:243], a[128:131], a[248:251], v[240:243]\n"
"ds_read_b64_tr_b16 a[240:241], v28 offset:41088\n"
"v_mfma_f32_16x16x32_bf16 v[240:243], a[144:147], a[252:255], v[240:243]\n"
"ds_read_b64_tr_b16 a[242:243], v28 offset:46208\n"
"v_mfma_f32_16x16x32_bf16 v[244:247], a[132:135], a[248:251], v[244:247]\n"
"ds_read_b64_tr_b16 a[244:245], v28 offset:51328\n"
"v_mfma_f32_16x16x32_bf16 v[244:247], a[148:151], a[252:255], v[244:247]\n"
"s_waitcnt vmcnt(14)\n"
"s_waitcnt vmcnt(40)\n"
"ds_read_b64_tr_b16 a[246:247], v28 offset:56448\n"
"v_mfma_f32_16x16x32_bf16 v[248:251], a[136:139], a[248:251], v[248:251]\n"
"s_barrier\n"
"v_mfma_f32_16x16x32_bf16 v[248:251], a[152:155], a[252:255], v[248:251]\n"
"v_mfma_f32_16x16x32_bf16 v[252:255], a[140:143], a[248:251], v[252:255]\n"
"v_mfma_f32_16x16x32_bf16 v[252:255], a[156:159], a[252:255], v[252:255]\n"
"s_waitcnt lgkmcnt(8)\n"
"s_waitcnt vmcnt(24)\n"
"v_mfma_f32_16x16x32_bf16 v[96:99], a[160:163], a[216:219], v[96:99]\n"
"ds_read_b64_tr_b16 a[248:249], v29 offset:41088\n"
"v_mfma_f32_16x16x32_bf16 v[96:99], a[176:179], a[220:223], v[96:99]\n"
"ds_read_b64_tr_b16 a[250:251], v29 offset:46208\n"
"v_mfma_f32_16x16x32_bf16 v[100:103], a[164:167], a[216:219], v[100:103]\n"
"ds_read_b64_tr_b16 a[252:253], v29 offset:51328\n"
"v_mfma_f32_16x16x32_bf16 v[100:103], a[180:183], a[220:223], v[100:103]\n"
"ds_read_b64_tr_b16 a[254:255], v29 offset:56448\n"
"v_mfma_f32_16x16x32_bf16 v[104:107], a[168:171], a[216:219], v[104:107]\n"
"v_mfma_f32_16x16x32_bf16 v[104:107], a[184:187], a[220:223], v[104:107]\n"
"buffer_load_dwordx4 a[52:55], v12, s[24:27], s42 offen offset:1024 sc1\n"
"v_mfma_f32_16x16x32_bf16 v[108:111], a[172:175], a[216:219], v[108:111]\n"
"v_mfma_f32_16x16x32_bf16 v[108:111], a[188:191], a[220:223], v[108:111]\n"
"s_waitcnt lgkmcnt(8)\n"
"buffer_load_dwordx4 a[56:59], v12, s[24:27], s42 offen offset:2048 sc1\n"
"v_mfma_f32_16x16x32_bf16 v[112:115], a[160:163], a[224:227], v[112:115]\n"
"s_mul_i32 s47, s93, 196608\n"
"ds_read_b64_tr_b16 a[216:217], v28 offset:41152\n"
"v_mfma_f32_16x16x32_bf16 v[112:115], a[176:179], a[228:231], v[112:115]\n"
"ds_read_b64_tr_b16 a[218:219], v28 offset:46272\n"
"v_mfma_f32_16x16x32_bf16 v[116:119], a[164:167], a[224:227], v[116:119]\n"
"s_add_u32 s43, s92, s47\n"
"ds_read_b64_tr_b16 a[220:221], v28 offset:51392\n"
"v_mfma_f32_16x16x32_bf16 v[116:119], a[180:183], a[228:231], v[116:119]\n"
"ds_read_b64_tr_b16 a[222:223], v28 offset:56512\n"
"v_mfma_f32_16x16x32_bf16 v[120:123], a[168:171], a[224:227], v[120:123]\n"
"s_add_u32 s44, s43, 5120\n"
"v_mfma_f32_16x16x32_bf16 v[120:123], a[184:187], a[228:231], v[120:123]\n"
"buffer_load_dwordx4 a[60:63], v12, s[24:27], s42 offen offset:3072 sc1\n"
"s_add_u32 s42, s42, 4096\n"
"v_mfma_f32_16x16x32_bf16 v[124:127], a[172:175], a[224:227], v[124:127]\n"
"s_add_u32 s45, s43, 10240\n"
"v_mfma_f32_16x16x32_bf16 v[124:127], a[188:191], a[228:231], v[124:127]\n"
"s_waitcnt lgkmcnt(8)\n"
"buffer_load_dwordx4 v[48:51], v12, s[24:27], s42 offen offset:0 sc1\n"
"v_mfma_f32_16x16x32_bf16 v[160:163], a[160:163], a[240:243], v[160:163]\n"
"s_xor_b32 s47, s93, 1\n"
"ds_read_b64_tr_b16 a[224:225], v29 offset:41152\n"
"v_mfma_f32_16x16x32_bf16 v[160:163], a[176:179], a[244:247], v[160:163]\n"
"ds_read_b64_tr_b16 a[226:227], v29 offset:46272\n"
"v_mfma_f32_16x16x32_bf16 v[164:167], a[164:167], a[240:243], v[164:167]\n"
"s_mul_i32 s47, s47, 196608\n"
"ds_read_b64_tr_b16 a[228:229], v29 offset:51392\n"
"v_mfma_f32_16x16x32_bf16 v[164:167], a[180:183], a[244:247], v[164:167]\n"
"ds_read_b64_tr_b16 a[230:231], v29 offset:56512\n"
"v_mfma_f32_16x16x32_bf16 v[168:171], a[168:171], a[240:243], v[168:171]\n"
"s_add_u32 s8, s92, s47\n"
"v_mfma_f32_16x16x32_bf16 v[168:171], a[184:187], a[244:247], v[168:171]\n"
"buffer_load_dwordx4 v[52:55], v12, s[24:27], s42 offen offset:1024 sc1\n"
"v_mfma_f32_16x16x32_bf16 v[172:175], a[172:175], a[240:243], v[172:175]\n"
"s_xor_b32 s47, s93, 2\n"
"v_mfma_f32_16x16x32_bf16 v[172:175], a[188:191], a[244:247], v[172:175]\n"
"s_waitcnt lgkmcnt(8)\n"
"buffer_load_dwordx4 v[56:59], v12, s[24:27], s42 offen offset:2048 sc1\n"
"v_mfma_f32_16x16x32_bf16 v[176:179], a[160:163], a[248:251], v[176:179]\n"
"s_mul_i32 s47, s47, 196608\n"
"ds_read_b64_tr_b16 a[240:241], v28 offset:41216\n"
"v_mfma_f32_16x16x32_bf16 v[176:179], a[176:179], a[252:255], v[176:179]\n"
"ds_read_b64_tr_b16 a[242:243], v28 offset:46336\n"
"v_mfma_f32_16x16x32_bf16 v[180:183], a[164:167], a[248:251], v[180:183]\n"
"s_add_u32 s9, s92, s47\n"
"ds_read_b64_tr_b16 a[244:245], v28 offset:51456\n"
"v_mfma_f32_16x16x32_bf16 v[180:183], a[180:183], a[252:255], v[180:183]\n"
"ds_read_b64_tr_b16 a[246:247], v28 offset:56576\n"
"v_mfma_f32_16x16x32_bf16 v[184:187], a[168:171], a[248:251], v[184:187]\n"
"s_add_u32 s9, s9, 5120\n"
"v_mfma_f32_16x16x32_bf16 v[184:187], a[184:187], a[252:255], v[184:187]\n"
"buffer_load_dwordx4 v[60:63], v12, s[24:27], s42 offen offset:3072 sc1\n"
"s_add_u32 s42, s42, 4096\n"
"v_mfma_f32_16x16x32_bf16 v[188:191], a[172:175], a[248:251], v[188:191]\n"
"s_waitcnt vmcnt(48)\n"
"s_xor_b32 s47, s93, 3\n"
"v_mfma_f32_16x16x32_bf16 v[188:191], a[188:191], a[252:255], v[188:191]\n"
"s_waitcnt lgkmcnt(8)\n"
"buffer_load_dwordx4 v[18:21], v12, s[24:27], s42 offen offset:0 sc1\n"
"v_mfma_f32_16x16x32_bf16 v[192:195], a[160:163], a[216:219], v[192:195]\n"
"s_mul_i32 s47, s47, 196608\n"
"ds_read_b64_tr_b16 a[248:249], v29 offset:41216\n"
"v_mfma_f32_16x16x32_bf16 v[192:195], a[176:179], a[220:223], v[192:195]\n"
"ds_read_b64_tr_b16 a[250:251], v29 offset:46336\n"
"v_mfma_f32_16x16x32_bf16 v[196:199], a[164:167], a[216:219], v[196:199]\n"
"s_add_u32 s10, s92, s47\n"
"ds_read_b64_tr_b16 a[252:253], v29 offset:51456\n"
"v_mfma_f32_16x16x32_bf16 v[196:199], a[180:183], a[220:223], v[196:199]\n"
"ds_read_b64_tr_b16 a[254:255], v29 offset:56576\n"
"v_mfma_f32_16x16x32_bf16 v[200:203], a[168:171], a[216:219], v[200:203]\n"
"s_add_u32 s10, s10, 10240\n"
"v_mfma_f32_16x16x32_bf16 v[200:203], a[184:187], a[220:223], v[200:203]\n"
"buffer_load_dwordx4 v[22:25], v12, s[24:27], s42 offen offset:1024 sc1\n"
"v_mfma_f32_16x16x32_bf16 v[204:207], a[172:175], a[216:219], v[204:207]\n"
"s_mov_b64 s[16:17], s[74:75]\n"
"v_mfma_f32_16x16x32_bf16 v[204:207], a[188:191], a[220:223], v[204:207]\n"
"s_waitcnt lgkmcnt(8)\n"
"buffer_load_dwordx4 v[36:39], v12, s[24:27], s42 offen offset:2048 sc1\n"
"v_mfma_f32_16x16x32_bf16 v[208:211], a[160:163], a[224:227], v[208:211]\n"
"s_mov_b32 s18, 161600\n"
"ds_read_b64_tr_b16 a[216:217], v2 offset:256\n"
"s_mov_b64 exec, 0\n"
"s_cmp_eq_u32 s38, 0\n"
"s_cbranch_scc1 L_pg24\n"
"s_cmp_eq_u32 s38, 3072\n"
"s_cbranch_scc1 L_pg24\n"
"s_cmp_eq_u32 s38, 6144\n"
"s_cbranch_scc1 L_pg24\n"
"s_branch L_pg24\n"
"L_bk6:\n"
"s_mov_b64 exec, -1\n"
"v_mfma_f32_16x16x32_bf16 v[208:211], a[176:179], a[228:231], v[208:211]\n"
"ds_read_b64_tr_b16 a[218:219], v2 offset:5376\n"
"v_mfma_f32_16x16x32_bf16 v[212:215], a[164:167], a[224:227], v[212:215]\n"
"ds_read_b64_tr_b16 a[220:221], v2 offset:10496\n"
"v_mfma_f32_16x16x32_bf16 v[212:215], a[180:183], a[228:231], v[212:215]\n"
"ds_read_b64_tr_b16 a[222:223], v2 offset:15616\n"
"v_mfma_f32_16x16x32_bf16 v[216:219], a[168:171], a[224:227], v[216:219]\n"
"v_mfma_f32_16x16x32_bf16 v[216:219], a[184:187], a[228:231], v[216:219]\n"
"buffer_load_dwordx4 v[6:9], v12, s[24:27], s42 offen offset:3072 sc1\n"
"s_add_u32 s42, s42, 4096\n"
"s_cmp_eq_u32 s42, s31\n"
"s_cselect_b32 s42, s30, s42\n"
"v_mfma_f32_16x16x32_bf16 v[220:223], a[172:175], a[224:227], v[220:223]\n"
"v_mfma_f32_16x16x32_bf16 v[220:223], a[188:191], a[228:231], v[220:223]\n"
"s_waitcnt lgkmcnt(8)\n"
"v_mfma_f32_16x16x32_bf16 v[224:227], a[160:163], a[240:243], v[224:227]\n"
"ds_read_b64_tr_b16 a[224:225], v3 offset:256\n"
"v_mfma_f32_16x16x32_bf16 v[224:227], a[176:179], a[244:247], v[224:227]\n"
"ds_read_b64_tr_b16 a[226:227], v3 offset:5376\n"
"v_mfma_f32_16x16x32_bf16 v[228:231], a[164:167], a[240:243], v[228:231]\n"
"ds_read_b64_tr_b16 a[228:229], v3 offset:10496\n"
"v_mfma_f32_16x16x32_bf16 v[228:231], a[180:183], a[244:247], v[228:231]\n"
"ds_read_b64_tr_b16 a[230:231], v3 offset:15616\n"
"v_mfma_f32_16x16x32_bf16 v[232:235], a[168:171], a[240:243], v[232:235]\n"
"v_mfma_f32_16x16x32_bf16 v[232:235], a[184:187], a[244:247], v[232:235]\n"
"v_mfma_f32_16x16x32_bf16 v[236:239], a[172:175], a[240:243], v[236:239]\n"
"v_mfma_f32_16x16x32_bf16 v[236:239], a[188:191], a[244:247], v[236:239]\n"
"s_waitcnt lgkmcnt(8)\n"
"v_mfma_f32_16x16x32_bf16 v[240:243], a[160:163], a[248:251], v[240:243]\n"
"ds_read_b64_tr_b16 a[240:241], v2 offset:20736\n"
"v_mfma_f32_16x16x32_bf16 v[240:243], a[176:179], a[252:255], v[240:243]\n"
"ds_read_b64_tr_b16 a[242:243], v2 offset:25856\n"
"v_mfma_f32_16x16x32_bf16 v[244:247], a[164:167], a[248:251], v[244:247]\n"
"ds_read_b64_tr_b16 a[244:245], v2 offset:30976\n"
"v_mfma_f32_16x16x32_bf16 v[244:247], a[180:183], a[252:255], v[244:247]\n"
"s_waitcnt vmcnt(13)\n"
"s_waitcnt vmcnt(40)\n"
"ds_read_b64_tr_b16 a[246:247], v2 offset:36096\n"
"v_mfma_f32_16x16x32_bf16 v[248:251], a[168:171], a[248:251], v[248:251]\n"
"s_barrier\n"
"v_mfma_f32_16x16x32_bf16 v[248:251], a[184:187], a[252:255], v[248:251]\n"
"s_waitcnt vmcnt(12)\n"
"v_cmp_eq_u64 vcc, s[76:77], v[46:47]\n"
"v_mfma_f32_16x16x32_bf16 v[252:255], a[172:175], a[248:251], v[252:255]\n"
"v_mfma_f32_16x16x32_bf16 v[252:255], a[188:191], a[252:255], v[252:255]\n"
"s_waitcnt lgkmcnt(8)\n"
"s_waitcnt vmcnt(22)\n"
"s_cmp_eq_u64 vcc, -1\n"
"s_getpc_b64 s[52:53]\n"
"s_cbranch_scc0 L_poll\n"
"v_mfma_f32_16x16x32_bf16 v[224:227], a[0:3], a[216:219], v[224:227]\n"
"ds_read_b64_tr_b16 a[248:249], v3 offset:20736\n"
"v_mfma_f32_16x16x32_bf16 v[224:227], a[16:19], a[220:223], v[224:227]\n"
"ds_read_b64_tr_b16 a[250:251], v3 offset:25856\n"
"v_mfma_f32_16x16x32_bf16 v[228:231], a[4:7], a[216:219], v[228:231]\n"
"ds_read_b64_tr_b16 a[252:253], v3 offset:30976\n"
"v_mfma_f32_16x16x32_bf16 v[228:231], a[20:23], a[220:223], v[228:231]\n"
"ds_read_b64_tr_b16 a[254:255], v3 offset:36096\n"
"v_mfma_f32_16x16x32_bf16 v[232:235], a[8:11], a[216:219], v[232:235]\n"
"v_and_b32 v44, 0x70, v12\n"
"v_mfma_f32_16x16x32_bf16 v[232:235], a[24:27], a[220:223], v[232:235]\n"
"v_mfma_f32_16x16x32_bf16 v[236:239], a[12:15], a[216:219], v[236:239]\n"
"v_add_u32 v40, 0x1000, v12\n"
"v_mfma_f32_16x16x32_bf16 v[236:239], a[28:31], a[220:223], v[236:239]\n"
"s_waitcnt lgkmcnt(8)\n"
"v_mfma_f32_16x16x32_bf16 v[240:243], a[0:3], a[224:227], v[240:243]\n"
"s_and_b32 s90, s91, 0xffffff80\n"
"ds_read_b64_tr_b16 a[216:217], v28 offset:256\n"
"v_mfma_f32_16x16x32_bf16 v[240:243], a[16:19], a[228:231], v[240:243]\n"
"ds_read_b64_tr_b16 a[218:219], v28 offset:5376\n"
"v_mfma_f32_16x16x32_bf16 v[244:247], a[4:7], a[224:227], v[244:247]\n"
"s_lshl_b32 s11, s71, 1\n"
"ds_read_b64_tr_b16 a[220:221], v28 offset:10496\n"
"v_mfma_f32_16x16x32_bf16 v[244:247], a[20:23], a[228:231], v[244:247]\n"
"ds_read_b64_tr_b16 a[222:223], v28 offset:15616\n"
"v_mfma_f32_16x16x32_bf16 v[248:251], a[8:11], a[224:227], v[248:251]\n"
"s_lshl_b32 s50, s93, 6\n"
"v_mfma_f32_16x16x32_bf16 v[248:251], a[24:27], a[228:231], v[248:251]\n"
"v_mfma_f32_16x16x32_bf16 v[252:255], a[12:15], a[224:227], v[252:255]\n"
"s_lshl1_add_u32 s68, s68, s50\n"
"v_mfma_f32_16x16x32_bf16 v[252:255], a[28:31], a[228:231], v[252:255]\n"
"s_waitcnt lgkmcnt(8)\n"
"s_waitcnt vmcnt(8)\n"
"v_mfma_f32_16x16x32_bf16 v[224:227], a[32:35], a[240:243], v[224:227]\n"
"s_lshl1_add_u32 s69, s69, s50\n"
"ds_read_b64_tr_b16 a[224:225], v29 offset:256\n"
"v_mfma_f32_16x16x32_bf16 v[224:227], a[48:51], a[244:247], v[224:227]\n"
"ds_read_b64_tr_b16 a[226:227], v29 offset:5376\n"
"v_mfma_f32_16x16x32_bf16 v[228:231], a[36:39], a[240:243], v[228:231]\n"
"s_lshl1_add_u32 s70, s70, s50\n"
"ds_read_b64_tr_b16 a[228:229], v29 offset:10496\n"
"v_mfma_f32_16x16x32_bf16 v[228:231], a[52:55], a[244:247], v[228:231]\n"
"ds_read_b64_tr_b16 a[230:231], v29 offset:15616\n"
"v_mfma_f32_16x16x32_bf16 v[232:235], a[40:43], a[240:243], v[232:235]\n"
"s_lshl1_add_u32 s71, s71, s50\n"
"v_mfma_f32_16x16x32_bf16 v[232:235], a[56:59], a[244:247], v[232:235]\n"
"v_mfma_f32_16x16x32_bf16 v[236:239], a[44:47], a[240:243], v[236:239]\n"
"s_lshr_b32 s50, 0x80, s93\n"
"v_mfma_f32_16x16x32_bf16 v[236:239], a[60:63], a[244:247], v[236:239]\n"
"s_waitcnt lgkmcnt(8)\n"
"v_mfma_f32_16x16x32_bf16 v[240:243], a[32:35], a[248:251], v[240:243]\n"
"s_lshl_b32 s47, 1, s93\n"
"ds_read_b64_tr_b16 a[240:241], v2 offset:64\n"
"v_mfma_f32_16x16x32_bf16 v[240:243], a[48:51], a[252:255], v[240:243]\n"
"ds_read_b64_tr_b16 a[242:243], v2 offset:5184\n"
"v_mfma_f32_16x16x32_bf16 v[244:247], a[36:39], a[248:251], v[244:247]\n"
"s_andn2_b32 s50, 0xf0, s50\n"
"ds_read_b64_tr_b16 a[244:245], v2 offset:10304\n"
"v_mfma_f32_16x16x32_bf16 v[244:247], a[52:55], a[252:255], v[244:247]\n"
"ds_read_b64_tr_b16 a[246:247], v2 offset:15424\n"
"v_mfma_f32_16x16x32_bf16 v[248:251], a[40:43], a[248:251], v[248:251]\n"
"s_or_b32 s47, s47, s50\n"
"v_mfma_f32_16x16x32_bf16 v[248:251], a[56:59], a[252:255], v[248:251]\n"
"v_mfma_f32_16x16x32_bf16 v[252:255], a[44:47], a[248:251], v[252:255]\n"
"v_mfma_f32_16x16x32_bf16 v[252:255], a[60:63], a[252:255], v[252:255]\n"
"s_waitcnt lgkmcnt(8)\n"
"s_waitcnt vmcnt(0)\n"
"v_mfma_f32_16x16x32_bf16 v[224:227], v[48:51], a[216:219], v[224:227]\n"
"ds_read_b64_tr_b16 a[248:249], v2 offset:20544\n"
"v_mfma_f32_16x16x32_bf16 v[224:227], v[18:21], a[220:223], v[224:227]\n"
"ds_read_b64_tr_b16 a[250:251], v2 offset:25664\n"
"v_mfma_f32_16x16x32_bf16 v[228:231], v[52:55], a[216:219], v[228:231]\n"
"ds_read_b64_tr_b16 a[252:253], v2 offset:30784\n"
"v_mfma_f32_16x16x32_bf16 v[228:231], v[22:25], a[220:223], v[228:231]\n"
"ds_read_b64_tr_b16 a[254:255], v2 offset:35904\n"
"v_mfma_f32_16x16x32_bf16 v[232:235], v[56:59], a[216:219], v[232:235]\n"
"v_mfma_f32_16x16x32_bf16 v[232:235], v[36:39], a[220:223], v[232:235]\n"
"s_nop 0\n"
"v_sub_f32 v224, v224, v13\n"
"v_sub_f32 v225, v225, v13\n"
"v_mfma_f32_16x16x32_bf16 v[236:239], v[60:63], a[216:219], v[236:239]\n"
"v_sub_f32 v226, v226, v13\n"
"v_sub_f32 v227, v227, v13\n"
"v_mfma_f32_16x16x32_bf16 v[236:239], v[6:9], a[220:223], v[236:239]\n"
"s_waitcnt lgkmcnt(8)\n"
"v_cvt_pk_bf16_f32 v224, v224, v225\n"
"v_cvt_pk_bf16_f32 v225, v226, v227\n"
"v_mfma_f32_16x16x32_bf16 v[240:243], v[48:51], a[224:227], v[240:243]\n"
"ds_read_b64_tr_b16 a[216:217], v28 offset:64\n"
"v_sub_f32 v228, v228, v13\n"
"v_mfma_f32_16x16x32_bf16 v[240:243], v[18:21], a[228:231], v[240:243]\n"
"ds_read_b64_tr_b16 a[218:219], v28 offset:5184\n"
"v_sub_f32 v229, v229, v13\n"
"v_mfma_f32_16x16x32_bf16 v[244:247], v[52:55], a[224:227], v[244:247]\n"
"ds_read_b64_tr_b16 a[220:221], v28 offset:10304\n"
"v_sub_f32 v230, v230, v13\n"
"v_mfma_f32_16x16x32_bf16 v[244:247], v[22:25], a[228:231], v[244:247]\n"
"ds_read_b64_tr_b16 a[222:223], v28 offset:15424\n"
"v_sub_f32 v231, v231, v13\n"
"v_mfma_f32_16x16x32_bf16 v[248:251], v[56:59], a[224:227], v[248:251]\n"
"v_cvt_pk_bf16_f32 v228, v228, v229\n"
"v_cvt_pk_bf16_f32 v229, v230, v231\n"
"v_mfma_f32_16x16x32_bf16 v[248:251], v[36:39], a[228:231], v[248:251]\n"
"v_sub_f32 v232, v232, v13\n"
"v_sub_f32 v233, v233, v13\n"
"v_mfma_f32_16x16x32_bf16 v[252:255], v[60:63], a[224:227], v[252:255]\n"
"v_sub_f32 v234, v234, v13\n"
"v_sub_f32 v235, v235, v13\n"
"v_mfma_f32_16x16x32_bf16 v[252:255], v[6:9], a[228:231], v[252:255]\n"
"s_waitcnt lgkmcnt(8)\n"
"v_cvt_pk_bf16_f32 v232, v232, v233\n"
"v_cvt_pk_bf16_f32 v233, v234, v235\n"
"v_mfma_f32_16x16x32_bf16 v[96:99], a[0:3], a[240:243], v[96:99]\n"
"ds_read_b64_tr_b16 a[224:225], v3 offset:64\n"
"v_sub_f32 v236, v236, v13\n"
"v_mfma_f32_16x16x32_bf16 v[96:99], a[16:19], a[244:247], v[96:99]\n"
"ds_read_b64_tr_b16 a[226:227], v3 offset:5184\n"
"v_sub_f32 v237, v237, v13\n"
"v_mfma_f32_16x16x32_bf16 v[100:103], a[4:7], a[240:243], v[100:103]\n"
"ds_read_b64_tr_b16 a[228:229], v3 offset:10304\n"
"v_sub_f32 v238, v238, v13\n"
"v_mfma_f32_16x16x32_bf16 v[100:103], a[20:23], a[244:247], v[100:103]\n"
"ds_read_b64_tr_b16 a[230:231], v3 offset:15424\n"
"v_sub_f32 v239, v239, v13\n"
"v_mfma_f32_16x16x32_bf16 v[104:107], a[8:11], a[240:243], v[104:107]\n"
"v_sub_f32 v240, v240, v13\n"
"v_sub_f32 v241, v241, v13\n"
"v_mfma_f32_16x16x32_bf16 v[104:107], a[24:27], a[244:247], v[104:107]\n"
"v_sub_f32 v242, v242, v13\n"
"v_sub_f32 v243, v243, v13\n"
"v_mfma_f32_16x16x32_bf16 v[108:111], a[12:15], a[240:243], v[108:111]\n"
"v_cvt_pk_bf16_f32 v226, v240, v241\n"
"v_cvt_pk_bf16_f32 v227, v242, v243\n"
"v_mfma_f32_16x16x32_bf16 v[108:111], a[28:31], a[244:247], v[108:111]\n"
"s_waitcnt lgkmcnt(8)\n"
"buffer_store_dwordx4 v[224:227], v40, s[24:27], s45 offen offset:0 sc1\n"
"v_mfma_f32_16x16x32_bf16 v[96:99], a[32:35], a[248:251], v[96:99]\n"
"ds_read_b64_tr_b16 a[240:241], v3 offset:20544\n"
"v_sub_f32 v244, v244, v13\n"
"v_mfma_f32_16x16x32_bf16 v[96:99], a[48:51], a[252:255], v[96:99]\n"
"ds_read_b64_tr_b16 a[242:243], v3 offset:25664\n"
"v_sub_f32 v245, v245, v13\n"
"v_mfma_f32_16x16x32_bf16 v[100:103], a[36:39], a[248:251], v[100:103]\n"
"ds_read_b64_tr_b16 a[244:245], v3 offset:30784\n"
"v_sub_f32 v246, v246, v13\n"
"v_mfma_f32_16x16x32_bf16 v[100:103], a[52:55], a[252:255], v[100:103]\n"
"ds_read_b64_tr_b16 a[246:247], v3 offset:35904\n"
"v_sub_f32 v247, v247, v13\n"
"v_mfma_f32_16x16x32_bf16 v[104:107], a[40:43], a[248:251], v[104:107]\n"
"v_cvt_pk_bf16_f32 v230, v244, v245\n"
"v_cvt_pk_bf16_f32 v231, v246, v247\n"
"v_mfma_f32_16x16x32_bf16 v[104:107], a[56:59], a[252:255], v[104:107]\n"
"buffer_store_dwordx4 v[228:231], v40, s[24:27], s44 offen offset:0 sc1\n"
"v_mfma_f32_16x16x32_bf16 v[108:111], a[44:47], a[248:251], v[108:111]\n"
"v_sub_f32 v248, v248, v13\n"
"v_sub_f32 v249, v249, v13\n"
"v_mfma_f32_16x16x32_bf16 v[108:111], a[60:63], a[252:255], v[108:111]\n"
"s_waitcnt lgkmcnt(8)\n"
"v_sub_f32 v250, v250, v13\n"
"v_sub_f32 v251, v251, v13\n"
"v_mfma_f32_16x16x32_bf16 v[96:99], v[48:51], a[216:219], v[96:99]\n"
"ds_read_b64_tr_b16 a[248:249], v29 offset:64\n"
"v_cvt_pk_bf16_f32 v234, v248, v249\n"
"v_mfma_f32_16x16x32_bf16 v[96:99], v[18:21], a[220:223], v[96:99]\n"
"ds_read_b64_tr_b16 a[250:251], v29 offset:5184\n"
"v_cvt_pk_bf16_f32 v235, v250, v251\n"
"v_mfma_f32_16x16x32_bf16 v[100:103], v[52:55], a[216:219], v[100:103]\n"
"ds_read_b64_tr_b16 a[252:253], v29 offset:10304\n"
"v_sub_f32 v252, v252, v13\n"
"v_mfma_f32_16x16x32_bf16 v[100:103], v[22:25], a[220:223], v[100:103]\n"
"ds_read_b64_tr_b16 a[254:255], v29 offset:15424\n"
"v_sub_f32 v253, v253, v13\n"
"v_mfma_f32_16x16x32_bf16 v[104:107], v[56:59], a[216:219], v[104:107]\n"
"buffer_store_dwordx4 v[232:235], v40, s[24:27], s43 offen offset:0 sc1\n"
"v_mfma_f32_16x16x32_bf16 v[104:107], v[36:39], a[220:223], v[104:107]\n"
"v_sub_f32 v254, v254, v13\n"
"v_sub_f32 v255, v255, v13\n"
"v_mfma_f32_16x16x32_bf16 v[108:111], v[60:63], a[216:219], v[108:111]\n"
"v_sub_f32 v96, v96, v13\n"
"v_sub_f32 v97, v97, v13\n"
"v_mfma_f32_16x16x32_bf16 v[108:111], v[6:9], a[220:223], v[108:111]\n"
"s_waitcnt lgkmcnt(8)\n"
"v_sub_f32 v98, v98, v13\n"
"v_sub_f32 v99, v99, v13\n"
"v_mfma_f32_16x16x32_bf16 v[112:115], a[0:3], a[224:227], v[112:115]\n"
"ds_read_b64_tr_b16 a[216:217], v2 offset:128\n"
"v_cvt_pk_bf16_f32 v96, v96, v97\n"
"v_mfma_f32_16x16x32_bf16 v[112:115], a[16:19], a[228:231], v[112:115]\n"
"ds_read_b64_tr_b16 a[218:219], v2 offset:5248\n"
"v_cvt_pk_bf16_f32 v97, v98, v99\n"
"v_mfma_f32_16x16x32_bf16 v[116:119], a[4:7], a[224:227], v[116:119]\n"
"ds_read_b64_tr_b16 a[220:221], v2 offset:10368\n"
"v_sub_f32 v100, v100, v13\n"
"v_mfma_f32_16x16x32_bf16 v[116:119], a[20:23], a[228:231], v[116:119]\n"
"ds_read_b64_tr_b16 a[222:223], v2 offset:15488\n"
"v_sub_f32 v101, v101, v13\n"
"v_mfma_f32_16x16x32_bf16 v[120:123], a[8:11], a[224:227], v[120:123]\n"
"v_sub_f32 v102, v102, v13\n"
"v_sub_f32 v103, v103, v13\n"
"v_mfma_f32_16x16x32_bf16 v[120:123], a[24:27], a[228:231], v[120:123]\n"
"v_cvt_pk_bf16_f32 v100, v100, v101\n"
"v_cvt_pk_bf16_f32 v101, v102, v103\n"
"v_mfma_f32_16x16x32_bf16 v[124:127], a[12:15], a[224:227], v[124:127]\n"
"v_sub_f32 v104, v104, v13\n"
"v_sub_f32 v105, v105, v13\n"
"v_mfma_f32_16x16x32_bf16 v[124:127], a[28:31], a[228:231], v[124:127]\n"
"s_waitcnt lgkmcnt(8)\n"
"v_sub_f32 v106, v106, v13\n"
"v_sub_f32 v107, v107, v13\n"
"v_mfma_f32_16x16x32_bf16 v[112:115], a[32:35], a[240:243], v[112:115]\n"
"ds_read_b64_tr_b16 a[224:225], v2 offset:20608\n"
"v_cvt_pk_bf16_f32 v104, v104, v105\n"
"v_mfma_f32_16x16x32_bf16 v[112:115], a[48:51], a[244:247], v[112:115]\n"
"ds_read_b64_tr_b16 a[226:227], v2 offset:25728\n"
"v_cvt_pk_bf16_f32 v105, v106, v107\n"
"v_mfma_f32_16x16x32_bf16 v[116:119], a[36:39], a[240:243], v[116:119]\n"
"ds_read_b64_tr_b16 a[228:229], v2 offset:30848\n"
"v_sub_f32 v108, v108, v13\n"
"v_mfma_f32_16x16x32_bf16 v[116:119], a[52:55], a[244:247], v[116:119]\n"
"ds_read_b64_tr_b16 a[230:231], v2 offset:35968\n"
"v_sub_f32 v109, v109, v13\n"
"v_mfma_f32_16x16x32_bf16 v[120:123], a[40:43], a[240:243], v[120:123]\n"
"v_sub_f32 v110, v110, v13\n"
"v_sub_f32 v111, v111, v13\n"
"v_mfma_f32_16x16x32_bf16 v[120:123], a[56:59], a[244:247], v[120:123]\n"
"v_cvt_pk_bf16_f32 v108, v108, v109\n"
"v_cvt_pk_bf16_f32 v109, v110, v111\n"
"v_mfma_f32_16x16x32_bf16 v[124:127], a[44:47], a[240:243], v[124:127]\n"
"v_mfma_f32_16x16x32_bf16 v[124:127], a[60:63], a[244:247], v[124:127]\n"
"s_waitcnt lgkmcnt(8)\n"
"v_mfma_f32_16x16x32_bf16 v[112:115], v[48:51], a[248:251], v[112:115]\n"
"ds_read_b64_tr_b16 a[240:241], v28 offset:128\n"
"v_mfma_f32_16x16x32_bf16 v[112:115], v[18:21], a[252:255], v[112:115]\n"
"ds_read_b64_tr_b16 a[242:243], v28 offset:5248\n"
"v_mfma_f32_16x16x32_bf16 v[116:119], v[52:55], a[248:251], v[116:119]\n"
"ds_read_b64_tr_b16 a[244:245], v28 offset:10368\n"
"v_mfma_f32_16x16x32_bf16 v[116:119], v[22:25], a[252:255], v[116:119]\n"
"ds_read_b64_tr_b16 a[246:247], v28 offset:15488\n"
"v_mfma_f32_16x16x32_bf16 v[120:123], v[56:59], a[248:251], v[120:123]\n"
"v_mfma_f32_16x16x32_bf16 v[120:123], v[36:39], a[252:255], v[120:123]\n"
"s_nop 0\n"
"v_sub_f32 v112, v112, v13\n"
"v_sub_f32 v113, v113, v13\n"
"v_mfma_f32_16x16x32_bf16 v[124:127], v[60:63], a[248:251], v[124:127]\n"
"v_sub_f32 v114, v114, v13\n"
"v_sub_f32 v115, v115, v13\n"
"v_mfma_f32_16x16x32_bf16 v[124:127], v[6:9], a[252:255], v[124:127]\n"
"s_waitcnt lgkmcnt(8)\n"
"v_cvt_pk_bf16_f32 v98, v112, v113\n"
"v_cvt_pk_bf16_f32 v99, v114, v115\n"
"v_mfma_f32_16x16x32_bf16 v[160:163], a[0:3], a[216:219], v[160:163]\n"
"ds_read_b64_tr_b16 a[248:249], v3 offset:128\n"
"v_sub_f32 v116, v116, v13\n"
"v_mfma_f32_16x16x32_bf16 v[160:163], a[16:19], a[220:223], v[160:163]\n"
"ds_read_b64_tr_b16 a[250:251], v3 offset:5248\n"
"v_sub_f32 v117, v117, v13\n"
"v_mfma_f32_16x16x32_bf16 v[164:167], a[4:7], a[216:219], v[164:167]\n"
"ds_read_b64_tr_b16 a[252:253], v3 offset:10368\n"
"v_sub_f32 v118, v118, v13\n"
"v_mfma_f32_16x16x32_bf16 v[164:167], a[20:23], a[220:223], v[164:167]\n"
"ds_read_b64_tr_b16 a[254:255], v3 offset:15488\n"
"v_sub_f32 v119, v119, v13\n"
"v_mfma_f32_16x16x32_bf16 v[168:171], a[8:11], a[216:219], v[168:171]\n"
"s_mov_b32 vcc_lo, 0x00f0000f\n"
"s_mov_b32 vcc_hi, 0xf0000f00\n"
"buffer_store_dwordx4 v[96:99], v12, s[24:27], s43 offen offset:0 sc1\n"
"v_mfma_f32_16x16x32_bf16 v[168:171], a[24:27], a[220:223], v[168:171]\n"
"v_cvt_pk_bf16_f32 v102, v116, v117\n"
"v_cvt_pk_bf16_f32 v103, v118, v119\n"
"v_mfma_f32_16x16x32_bf16 v[172:175], a[12:15], a[216:219], v[172:175]\n"
"buffer_store_dwordx4 v[100:103], v12, s[24:27], s43 offen offset:1024 sc1\n"
"v_mfma_f32_16x16x32_bf16 v[172:175], a[28:31], a[220:223], v[172:175]\n"
"s_waitcnt lgkmcnt(8)\n"
"v_sub_f32 v120, v120, v13\n"
"v_sub_f32 v121, v121, v13\n"
"v_mfma_f32_16x16x32_bf16 v[160:163], a[32:35], a[224:227], v[160:163]\n"
"ds_read_b64_tr_b16 a[216:217], v3 offset:20608\n"
"v_sub_f32 v122, v122, v13\n"
"v_mfma_f32_16x16x32_bf16 v[160:163], a[48:51], a[228:231], v[160:163]\n"
"ds_read_b64_tr_b16 a[218:219], v3 offset:25728\n"
"v_sub_f32 v123, v123, v13\n"
"v_mfma_f32_16x16x32_bf16 v[164:167], a[36:39], a[224:227], v[164:167]\n"
"ds_read_b64_tr_b16 a[220:221], v3 offset:30848\n"
"v_cvt_pk_bf16_f32 v106, v120, v121\n"
"v_mfma_f32_16x16x32_bf16 v[164:167], a[52:55], a[228:231], v[164:167]\n"
"ds_read_b64_tr_b16 a[222:223], v3 offset:35968\n"
"v_cvt_pk_bf16_f32 v107, v122, v123\n"
"v_mfma_f32_16x16x32_bf16 v[168:171], a[40:43], a[224:227], v[168:171]\n"
"v_sub_f32 v124, v124, v13\n"
"v_sub_f32 v125, v125, v13\n"
"v_mfma_f32_16x16x32_bf16 v[168:171], a[56:59], a[228:231], v[168:171]\n"
"buffer_store_dwordx4 v[104:107], v12, s[24:27], s43 offen offset:2048 sc1\n"
"v_mfma_f32_16x16x32_bf16 v[172:175], a[44:47], a[224:227], v[172:175]\n"
"v_sub_f32 v126, v126, v13\n"
"v_sub_f32 v127, v127, v13\n"
"v_mfma_f32_16x16x32_bf16 v[172:175], a[60:63], a[228:231], v[172:175]\n"
"s_waitcnt lgkmcnt(8)\n"
"v_cvt_pk_bf16_f32 v110, v124, v125\n"
"v_cvt_pk_bf16_f32 v111, v126, v127\n"
"v_mfma_f32_16x16x32_bf16 v[160:163], v[48:51], a[240:243], v[160:163]\n"
"ds_read_b64_tr_b16 a[224:225], v29 offset:128\n"
"v_mov_b32 v224, 0\n"
"v_mfma_f32_16x16x32_bf16 v[160:163], v[18:21], a[244:247], v[160:163]\n"
"ds_read_b64_tr_b16 a[226:227], v29 offset:5248\n"
"v_mov_b32 v225, 0\n"
"v_mfma_f32_16x16x32_bf16 v[164:167], v[52:55], a[240:243], v[164:167]\n"
"ds_read_b64_tr_b16 a[228:229], v29 offset:10368\n"
"v_mov_b32 v227, 0\n"
"v_mfma_f32_16x16x32_bf16 v[164:167], v[22:25], a[244:247], v[164:167]\n"
"ds_read_b64_tr_b16 a[230:231], v29 offset:15488\n"
"v_mov_b32 v228, 0\n"
"v_mfma_f32_16x16x32_bf16 v[168:171], v[56:59], a[240:243], v[168:171]\n"
"v_mov_b32 v229, 0\n"
"v_mov_b32 v230, 0x3f80\n"
"v_mfma_f32_16x16x32_bf16 v[168:171], v[36:39], a[244:247], v[168:171]\n"
"buffer_store_dwordx4 v[108:111], v12, s[24:27], s43 offen offset:3072 sc1\n"
"v_mfma_f32_16x16x32_bf16 v[172:175], v[60:63], a[240:243], v[172:175]\n"
"L_pg24:\n"
"s_cbranch_execz L_bk6\n"
"v_cndmask_b32 v226, 0, v230\n"
"v_lshlrev_b64 v[226:227], v12, v[226:227]\n"
"v_mfma_f32_16x16x32_bf16 v[172:175], v[6:9], a[244:247], v[172:175]\n"
"s_waitcnt lgkmcnt(8)\n"
"v_sub_f32 v160, v160, v13\n"
"v_sub_f32 v161, v161, v13\n"
"v_mfma_f32_16x16x32_bf16 v[176:179], a[0:3], a[248:251], v[176:179]\n"
"ds_read_b64_tr_b16 a[240:241], v2 offset:192\n"
"v_sub_f32 v162, v162, v13\n"
"v_mfma_f32_16x16x32_bf16 v[176:179], a[16:19], a[252:255], v[176:179]\n"
"ds_read_b64_tr_b16 a[242:243], v2 offset:5312\n"
"v_sub_f32 v163, v163, v13\n"
"v_mfma_f32_16x16x32_bf16 v[180:183], a[4:7], a[248:251], v[180:183]\n"
"ds_read_b64_tr_b16 a[244:245], v2 offset:10432\n"
"v_cvt_pk_bf16_f32 v160, v160, v161\n"
"v_mfma_f32_16x16x32_bf16 v[180:183], a[20:23], a[252:255], v[180:183]\n"
"ds_read_b64_tr_b16 a[246:247], v2 offset:15552\n"
"v_cvt_pk_bf16_f32 v161, v162, v163\n"
"v_mfma_f32_16x16x32_bf16 v[184:187], a[8:11], a[248:251], v[184:187]\n"
"v_sub_f32 v164, v164, v13\n"
"v_sub_f32 v165, v165, v13\n"
"v_mfma_f32_16x16x32_bf16 v[184:187], a[24:27], a[252:255], v[184:187]\n"
"v_sub_f32 v166, v166, v13\n"
"v_sub_f32 v167, v167, v13\n"
"v_mfma_f32_16x16x32_bf16 v[188:191], a[12:15], a[248:251], v[188:191]\n"
"v_cvt_pk_bf16_f32 v164, v164, v165\n"
"v_cvt_pk_bf16_f32 v165, v166, v167\n"
"v_mfma_f32_16x16x32_bf16 v[188:191], a[28:31], a[252:255], v[188:191]\n"
"s_waitcnt lgkmcnt(8)\n"
"v_sub_f32 v168, v168, v13\n"
"v_sub_f32 v169, v169, v13\n"
"v_mfma_f32_16x16x32_bf16 v[176:179], a[32:35], a[216:219], v[176:179]\n"
"ds_read_b64_tr_b16 a[248:249], v2 offset:20672\n"
"v_sub_f32 v170, v170, v13\n"
"v_mfma_f32_16x16x32_bf16 v[176:179], a[48:51], a[220:223], v[176:179]\n"
"ds_read_b64_tr_b16 a[250:251], v2 offset:25792\n"
"v_sub_f32 v171, v171, v13\n"
"v_mfma_f32_16x16x32_bf16 v[180:183], a[36:39], a[216:219], v[180:183]\n"
"ds_read_b64_tr_b16 a[252:253], v2 offset:30912\n"
"v_cvt_pk_bf16_f32 v168, v168, v169\n"
"v_mfma_f32_16x16x32_bf16 v[180:183], a[52:55], a[220:223], v[180:183]\n"
"ds_read_b64_tr_b16 a[254:255], v2 offset:36032\n"
"v_cvt_pk_bf16_f32 v169, v170, v171\n"
"v_mfma_f32_16x16x32_bf16 v[184:187], a[40:43], a[216:219], v[184:187]\n"
"v_sub_f32 v172, v172, v13\n"
"v_sub_f32 v173, v173, v13\n"
"v_mfma_f32_16x16x32_bf16 v[184:187], a[56:59], a[220:223], v[184:187]\n"
"v_sub_f32 v174, v174, v13\n"
"v_sub_f32 v175, v175, v13\n"
"v_mfma_f32_16x16x32_bf16 v[188:191], a[44:47], a[216:219], v[188:191]\n"
"v_cvt_pk_bf16_f32 v172, v172, v173\n"
"v_cvt_pk_bf16_f32 v173, v174, v175\n"
"v_mfma_f32_16x16x32_bf16 v[188:191], a[60:63], a[220:223], v[188:191]\n"
"s_waitcnt lgkmcnt(8)\n"
"v_mfma_f32_16x16x32_bf16 v[176:179], v[48:51], a[224:227], v[176:179]\n"
"ds_read_b64_tr_b16 a[216:217], v28 offset:192\n"
"v_mfma_f32_16x16x32_bf16 v[176:179], v[18:21], a[228:231], v[176:179]\n"
"ds_read_b64_tr_b16 a[218:219], v28 offset:5312\n"
"v_mfma_f32_16x16x32_bf16 v[180:183], v[52:55], a[224:227], v[180:183]\n"
"ds_read_b64_tr_b16 a[220:221], v28 offset:10432\n"
"v_mfma_f32_16x16x32_bf16 v[180:183], v[22:25], a[228:231], v[180:183]\n"
"ds_read_b64_tr_b16 a[222:223], v28 offset:15552\n"
"v_mfma_f32_16x16x32_bf16 v[184:187], v[56:59], a[224:227], v[184:187]\n"
"v_mfma_f32_16x16x32_bf16 v[184:187], v[36:39], a[228:231], v[184:187]\n"
"s_nop 0\n"
"v_sub_f32 v176, v176, v13\n"
"v_sub_f32 v177, v177, v13\n"
"v_mfma_f32_16x16x32_bf16 v[188:191], v[60:63], a[224:227], v[188:191]\n"
"v_sub_f32 v178, v178, v13\n"
"v_sub_f32 v179, v179, v13\n"
"v_mfma_f32_16x16x32_bf16 v[188:191], v[6:9], a[228:231], v[188:191]\n"
"s_waitcnt lgkmcnt(8)\n"
"v_cvt_pk_bf16_f32 v162, v176, v177\n"
"v_cvt_pk_bf16_f32 v163, v178, v179\n"
"v_mfma_f32_16x16x32_bf16 v[192:195], a[0:3], a[240:243], v[192:195]\n"
"ds_read_b64_tr_b16 a[224:225], v3 offset:192\n"
"v_sub_f32 v180, v180, v13\n"
"v_mfma_f32_16x16x32_bf16 v[192:195], a[16:19], a[244:247], v[192:195]\n"
"ds_read_b64_tr_b16 a[226:227], v3 offset:5312\n"
"v_sub_f32 v181, v181, v13\n"
"v_mfma_f32_16x16x32_bf16 v[196:199], a[4:7], a[240:243], v[196:199]\n"
"ds_read_b64_tr_b16 a[228:229], v3 offset:10432\n"
"v_sub_f32 v182, v182, v13\n"
"v_mfma_f32_16x16x32_bf16 v[196:199], a[20:23], a[244:247], v[196:199]\n"
"ds_read_b64_tr_b16 a[230:231], v3 offset:15552\n"
"v_sub_f32 v183, v183, v13\n"
"v_mfma_f32_16x16x32_bf16 v[200:203], a[8:11], a[240:243], v[200:203]\n"
"buffer_store_dwordx4 v[160:163], v12, s[24:27], s44 offen offset:0 sc1\n"
"v_mfma_f32_16x16x32_bf16 v[200:203], a[24:27], a[244:247], v[200:203]\n"
"v_cvt_pk_bf16_f32 v166, v180, v181\n"
"v_cvt_pk_bf16_f32 v167, v182, v183\n"
"v_mfma_f32_16x16x32_bf16 v[204:207], a[12:15], a[240:243], v[204:207]\n"
"buffer_store_dwordx4 v[164:167], v12, s[24:27], s44 offen offset:1024 sc1\n"
"v_mfma_f32_16x16x32_bf16 v[204:207], a[28:31], a[244:247], v[204:207]\n"
"s_waitcnt lgkmcnt(8)\n"
"v_sub_f32 v184, v184, v13\n"
"v_sub_f32 v185, v185, v13\n"
"v_mfma_f32_16x16x32_bf16 v[192:195], a[32:35], a[248:251], v[192:195]\n"
"ds_read_b64_tr_b16 a[240:241], v3 offset:20672\n"
"v_sub_f32 v186, v186, v13\n"
"v_mfma_f32_16x16x32_bf16 v[192:195], a[48:51], a[252:255], v[192:195]\n"
"ds_read_b64_tr_b16 a[242:243], v3 offset:25792\n"
"v_sub_f32 v187, v187, v13\n"
"v_mfma_f32_16x16x32_bf16 v[196:199], a[36:39], a[248:251], v[196:199]\n"
"ds_read_b64_tr_b16 a[244:245], v3 offset:30912\n"
"v_cvt_pk_bf16_f32 v170, v184, v185\n"
"v_mfma_f32_16x16x32_bf16 v[196:199], a[52:55], a[252:255], v[196:199]\n"
"ds_read_b64_tr_b16 a[246:247], v3 offset:36032\n"
"v_cvt_pk_bf16_f32 v171, v186, v187\n"
"v_mfma_f32_16x16x32_bf16 v[200:203], a[40:43], a[248:251], v[200:203]\n"
"v_sub_f32 v188, v188, v13\n"
"v_sub_f32 v189, v189, v13\n"
"v_mfma_f32_16x16x32_bf16 v[200:203], a[56:59], a[252:255], v[200:203]\n"
"buffer_store_dwordx4 v[168:171], v12, s[24:27], s44 offen offset:2048 sc1\n"
"v_mfma_f32_16x16x32_bf16 v[204:207], a[44:47], a[248:251], v[204:207]\n"
"v_sub_f32 v190, v190, v13\n"
"v_sub_f32 v191, v191, v13\n"
"v_mfma_f32_16x16x32_bf16 v[204:207], a[60:63], a[252:255], v[204:207]\n"
"s_waitcnt lgkmcnt(8)\n"
"v_cvt_pk_bf16_f32 v174, v188, v189\n"
"v_cvt_pk_bf16_f32 v175, v190, v191\n"
"v_mfma_f32_16x16x32_bf16 v[192:195], v[48:51], a[216:219], v[192:195]\n"
"ds_read_b64_tr_b16 a[248:249], v29 offset:192\n"
"v_mfma_f32_16x16x32_bf16 v[192:195], v[18:21], a[220:223], v[192:195]\n"
"ds_read_b64_tr_b16 a[250:251], v29 offset:5312\n"
"v_mfma_f32_16x16x32_bf16 v[196:199], v[52:55], a[216:219], v[196:199]\n"
"ds_read_b64_tr_b16 a[252:253], v29 offset:10432\n"
"v_mfma_f32_16x16x32_bf16 v[196:199], v[22:25], a[220:223], v[196:199]\n"
"ds_read_b64_tr_b16 a[254:255], v29 offset:15552\n"
"v_mfma_f32_16x16x32_bf16 v[200:203], v[56:59], a[216:219], v[200:203]\n"
"v_mfma_f32_16x16x32_bf16 v[200:203], v[36:39], a[220:223], v[200:203]\n"
"buffer_store_dwordx4 v[172:175], v12, s[24:27], s44 offen offset:3072 sc1\n"
"v_mfma_f32_16x16x32_bf16 v[204:207], v[60:63], a[216:219], v[204:207]\n"
"v_sub_f32 v192, v192, v13\n"
"v_sub_f32 v193, v193, v13\n"
"v_mfma_f32_16x16x32_bf16 v[204:207], v[6:9], a[220:223], v[204:207]\n"
"s_waitcnt lgkmcnt(8)\n"
"v_sub_f32 v194, v194, v13\n"
"v_sub_f32 v195, v195, v13\n"
"v_mfma_f32_16x16x32_bf16 v[208:211], a[0:3], a[224:227], v[208:211]\n"
"ds_read_b64_tr_b16 a[216:217], v2 offset:40960\n"
"v_cvt_pk_bf16_f32 v192, v192, v193\n"
"v_mfma_f32_16x16x32_bf16 v[208:211], a[16:19], a[228:231], v[208:211]\n"
"ds_read_b64_tr_b16 a[218:219], v2 offset:46080\n"
"v_cvt_pk_bf16_f32 v193, v194, v195\n"
"v_mfma_f32_16x16x32_bf16 v[212:215], a[4:7], a[224:227], v[212:215]\n"
"ds_read_b64_tr_b16 a[220:221], v2 offset:51200\n"
"v_sub_f32 v196, v196, v13\n"
"v_mfma_f32_16x16x32_bf16 v[212:215], a[20:23], a[228:231], v[212:215]\n"
"ds_read_b64_tr_b16 a[222:223], v2 offset:56320\n"
"v_sub_f32 v197, v197, v13\n"
"v_mfma_f32_16x16x32_bf16 v[216:219], a[8:11], a[224:227], v[216:219]\n"
"v_sub_f32 v198, v198, v13\n"
"v_sub_f32 v199, v199, v13\n"
"v_mfma_f32_16x16x32_bf16 v[216:219], a[24:27], a[228:231], v[216:219]\n"
"v_cvt_pk_bf16_f32 v196, v196, v197\n"
"v_cvt_pk_bf16_f32 v197, v198, v199\n"
"v_mfma_f32_16x16x32_bf16 v[220:223], a[12:15], a[224:227], v[220:223]\n"
"v_sub_f32 v200, v200, v13\n"
"v_sub_f32 v201, v201, v13\n"
"v_mfma_f32_16x16x32_bf16 v[220:223], a[28:31], a[228:231], v[220:223]\n"
"s_waitcnt lgkmcnt(8)\n"
"v_sub_f32 v202, v202, v13\n"
"v_sub_f32 v203, v203, v13\n"
"v_mfma_f32_16x16x32_bf16 v[208:211], a[32:35], a[240:243], v[208:211]\n"
"ds_read_b64_tr_b16 a[224:225], v0 offset:0\n"
"v_cvt_pk_bf16_f32 v200, v200, v201\n"
"v_mfma_f32_16x16x32_bf16 v[208:211], a[48:51], a[244:247], v[208:211]\n"
"ds_read_b64_tr_b16 a[226:227], v0 offset:5120\n"
"v_cvt_pk_bf16_f32 v201, v202, v203\n"
"v_mfma_f32_16x16x32_bf16 v[212:215], a[36:39], a[240:243], v[212:215]\n"
"ds_read_b64_tr_b16 a[228:229], v0 offset:10240\n"
"v_sub_f32 v204, v204, v13\n"
"v_mfma_f32_16x16x32_bf16 v[212:215], a[52:55], a[244:247], v[212:215]\n"
"ds_read_b64_tr_b16 a[230:231], v0 offset:15360\n"
"v_sub_f32 v205, v205, v13\n"
"v_mfma_f32_16x16x32_bf16 v[216:219], a[40:43], a[240:243], v[216:219]\n"
"v_sub_f32 v206, v206, v13\n"
"v_sub_f32 v207, v207, v13\n"
"v_mfma_f32_16x16x32_bf16 v[216:219], a[56:59], a[244:247], v[216:219]\n"
"v_cvt_pk_bf16_f32 v204, v204, v205\n"
"v_cvt_pk_bf16_f32 v205, v206, v207\n"
"v_mfma_f32_16x16x32_bf16 v[220:223], a[44:47], a[240:243], v[220:223]\n"
"v_mfma_f32_16x16x32_bf16 v[220:223], a[60:63], a[244:247], v[220:223]\n"
"s_waitcnt lgkmcnt(8)\n"
"v_mfma_f32_16x16x32_bf16 v[208:211], v[48:51], a[248:251], v[208:211]\n"
"ds_read_b64_tr_b16 a[240:241], v0 offset:20480\n"
"v_mfma_f32_16x16x32_bf16 v[208:211], v[18:21], a[252:255], v[208:211]\n"
"ds_read_b64_tr_b16 a[242:243], v0 offset:25600\n"
"v_mfma_f32_16x16x32_bf16 v[212:215], v[52:55], a[248:251], v[212:215]\n"
"ds_read_b64_tr_b16 a[244:245], v0 offset:30720\n"
"v_mfma_f32_16x16x32_bf16 v[212:215], v[22:25], a[252:255], v[212:215]\n"
"ds_read_b64_tr_b16 a[246:247], v0 offset:35840\n"
"v_mfma_f32_16x16x32_bf16 v[216:219], v[56:59], a[248:251], v[216:219]\n"
"v_mfma_f32_16x16x32_bf16 v[216:219], v[36:39], a[252:255], v[216:219]\n"
"s_nop 0\n"
"v_sub_f32 v208, v208, v13\n"
"v_sub_f32 v209, v209, v13\n"
"v_mfma_f32_16x16x32_bf16 v[220:223], v[60:63], a[248:251], v[220:223]\n"
"v_sub_f32 v210, v210, v13\n"
"v_sub_f32 v211, v211, v13\n"
"v_mfma_f32_16x16x32_bf16 v[220:223], v[6:9], a[252:255], v[220:223]\n"
"s_waitcnt lgkmcnt(8)\n"
"v_cvt_pk_bf16_f32 v194, v208, v209\n"
"v_cvt_pk_bf16_f32 v195, v210, v211\n"
"v_mfma_f32_16x16x32_bf16 v[64:67], a[96:99], a[216:219], v[64:67]\n"
"ds_read_b64_tr_b16 a[248:249], v28 offset:40960\n"
"v_sub_f32 v212, v212, v13\n"
"v_mfma_f32_16x16x32_bf16 v[64:67], a[112:115], a[220:223], v[64:67]\n"
"ds_read_b64_tr_b16 a[250:251], v28 offset:46080\n"
"v_sub_f32 v213, v213, v13\n"
"v_mfma_f32_16x16x32_bf16 v[68:71], a[100:103], a[216:219], v[68:71]\n"
"ds_read_b64_tr_b16 a[252:253], v28 offset:51200\n"
"v_sub_f32 v214, v214, v13\n"
"v_mfma_f32_16x16x32_bf16 v[68:71], a[116:119], a[220:223], v[68:71]\n"
"ds_read_b64_tr_b16 a[254:255], v28 offset:56320\n"
"v_sub_f32 v215, v215, v13\n"
"v_mfma_f32_16x16x32_bf16 v[72:75], a[104:107], a[216:219], v[72:75]\n"
"buffer_store_dwordx4 v[192:195], v12, s[24:27], s45 offen offset:0 sc1\n"
"v_mfma_f32_16x16x32_bf16 v[72:75], a[120:123], a[220:223], v[72:75]\n"
"v_cvt_pk_bf16_f32 v198, v212, v213\n"
"v_cvt_pk_bf16_f32 v199, v214, v215\n"
"v_mfma_f32_16x16x32_bf16 v[76:79], a[108:111], a[216:219], v[76:79]\n"
"buffer_store_dwordx4 v[196:199], v12, s[24:27], s45 offen offset:1024 sc1\n"
"v_mfma_f32_16x16x32_bf16 v[76:79], a[124:127], a[220:223], v[76:79]\n"
"s_waitcnt lgkmcnt(8)\n"
"v_sub_f32 v216, v216, v13\n"
"v_sub_f32 v217, v217, v13\n"
"v_mfma_f32_16x16x32_bf16 v[64:67], v[128:131], a[224:227], v[64:67]\n"
"ds_read_b64_tr_b16 a[216:217], v2 offset:0\n"
"v_sub_f32 v218, v218, v13\n"
"v_mfma_f32_16x16x32_bf16 v[64:67], v[144:147], a[228:231], v[64:67]\n"
"ds_read_b64_tr_b16 a[218:219], v2 offset:5120\n"
"v_sub_f32 v219, v219, v13\n"
"v_mfma_f32_16x16x32_bf16 v[68:71], v[132:135], a[224:227], v[68:71]\n"
"ds_read_b64_tr_b16 a[220:221], v2 offset:10240\n"
"v_cvt_pk_bf16_f32 v202, v216, v217\n"
"v_mfma_f32_16x16x32_bf16 v[68:71], v[148:151], a[228:231], v[68:71]\n"
"ds_read_b64_tr_b16 a[222:223], v2 offset:15360\n"
"v_cvt_pk_bf16_f32 v203, v218, v219\n"
"v_mfma_f32_16x16x32_bf16 v[72:75], v[136:139], a[224:227], v[72:75]\n"
"buffer_store_dwordx4 v[200:203], v12, s[24:27], s45 offen offset:2048 sc1\n"
"v_mfma_f32_16x16x32_bf16 v[72:75], v[152:155], a[228:231], v[72:75]\n"
"v_sub_f32 v220, v220, v13\n"
"v_sub_f32 v221, v221, v13\n"
"v_mfma_f32_16x16x32_bf16 v[76:79], v[140:143], a[224:227], v[76:79]\n"
"v_sub_f32 v222, v222, v13\n"
"v_sub_f32 v223, v223, v13\n"
"v_mfma_f32_16x16x32_bf16 v[76:79], v[156:159], a[228:231], v[76:79]\n"
"s_waitcnt lgkmcnt(8)\n"
"v_cvt_pk_bf16_f32 v206, v220, v221\n"
"v_cvt_pk_bf16_f32 v207, v222, v223\n"
"v_mfma_f32_16x16x32_bf16 v[64:67], a[128:131], a[240:243], v[64:67]\n"
"ds_read_b64_tr_b16 a[224:225], v2 offset:20480\n"
"v_mfma_f32_16x16x32_bf16 v[64:67], a[144:147], a[244:247], v[64:67]\n"
"ds_read_b64_tr_b16 a[226:227], v2 offset:25600\n"
"v_mfma_f32_16x16x32_bf16 v[68:71], a[132:135], a[240:243], v[68:71]\n"
"ds_read_b64_tr_b16 a[228:229], v2 offset:30720\n"
"v_mfma_f32_16x16x32_bf16 v[68:71], a[148:151], a[244:247], v[68:71]\n"
"ds_read_b64_tr_b16 a[230:231], v2 offset:35840\n"
"v_mfma_f32_16x16x32_bf16 v[72:75], a[136:139], a[240:243], v[72:75]\n"
"buffer_store_dwordx4 v[204:207], v12, s[24:27], s45 offen offset:3072 sc1\n"
"v_mfma_f32_16x16x32_bf16 v[72:75], a[152:155], a[244:247], v[72:75]\n"
"v_mfma_f32_16x16x32_bf16 v[76:79], a[140:143], a[240:243], v[76:79]\n"
"v_mfma_f32_16x16x32_bf16 v[76:79], a[156:159], a[244:247], v[76:79]\n"
"s_waitcnt lgkmcnt(8)\n"
"v_mfma_f32_16x16x32_bf16 v[64:67], a[160:163], a[248:251], v[64:67]\n"
"ds_read_b64_tr_b16 a[240:241], v28 offset:0\n"
"v_mfma_f32_16x16x32_bf16 v[64:67], a[176:179], a[252:255], v[64:67]\n"
"ds_read_b64_tr_b16 a[242:243], v28 offset:5120\n"
"v_mfma_f32_16x16x32_bf16 v[68:71], a[164:167], a[248:251], v[68:71]\n"
"ds_read_b64_tr_b16 a[244:245], v28 offset:10240\n"
"v_mfma_f32_16x16x32_bf16 v[68:71], a[180:183], a[252:255], v[68:71]\n"
"ds_read_b64_tr_b16 a[246:247], v28 offset:15360\n"
"v_mfma_f32_16x16x32_bf16 v[72:75], a[168:171], a[248:251], v[72:75]\n"
"v_mfma_f32_16x16x32_bf16 v[72:75], a[184:187], a[252:255], v[72:75]\n"
"v_mfma_f32_16x16x32_bf16 v[76:79], a[172:175], a[248:251], v[76:79]\n"
"v_mfma_f32_16x16x32_bf16 v[76:79], a[188:191], a[252:255], v[76:79]\n"
"s_waitcnt lgkmcnt(8)\n"
"v_mfma_f32_16x16x32_bf16 v[64:67], a[0:3], a[216:219], v[64:67]\n"
"ds_read_b64_tr_b16 a[248:249], v3 offset:40960\n"
"v_mfma_f32_16x16x32_bf16 v[64:67], a[16:19], a[220:223], v[64:67]\n"
"ds_read_b64_tr_b16 a[250:251], v3 offset:46080\n"
"v_mfma_f32_16x16x32_bf16 v[68:71], a[4:7], a[216:219], v[68:71]\n"
"ds_read_b64_tr_b16 a[252:253], v3 offset:51200\n"
"v_mfma_f32_16x16x32_bf16 v[68:71], a[20:23], a[220:223], v[68:71]\n"
"ds_read_b64_tr_b16 a[254:255], v3 offset:56320\n"
"v_mfma_f32_16x16x32_bf16 v[72:75], a[8:11], a[216:219], v[72:75]\n"
"v_mfma_f32_16x16x32_bf16 v[72:75], a[24:27], a[220:223], v[72:75]\n"
"v_mfma_f32_16x16x32_bf16 v[76:79], a[12:15], a[216:219], v[76:79]\n"
"v_mfma_f32_16x16x32_bf16 v[76:79], a[28:31], a[220:223], v[76:79]\n"
"s_waitcnt lgkmcnt(8)\n"
"v_mfma_f32_16x16x32_bf16 v[64:67], a[32:35], a[224:227], v[64:67]\n"
"ds_read_b64_tr_b16 a[216:217], v1 offset:0\n"
"v_mfma_f32_16x16x32_bf16 v[64:67], a[48:51], a[228:231], v[64:67]\n"
"ds_read_b64_tr_b16 a[218:219], v1 offset:5120\n"
"v_mfma_f32_16x16x32_bf16 v[68:71], a[36:39], a[224:227], v[68:71]\n"
"ds_read_b64_tr_b16 a[220:221], v1 offset:10240\n"
"v_mfma_f32_16x16x32_bf16 v[68:71], a[52:55], a[228:231], v[68:71]\n"
"ds_read_b64_tr_b16 a[222:223], v1 offset:15360\n"
"v_mfma_f32_16x16x32_bf16 v[72:75], a[40:43], a[224:227], v[72:75]\n"
"v_mfma_f32_16x16x32_bf16 v[72:75], a[56:59], a[228:231], v[72:75]\n"
"v_mfma_f32_16x16x32_bf16 v[76:79], a[44:47], a[224:227], v[76:79]\n"
"v_mfma_f32_16x16x32_bf16 v[76:79], a[60:63], a[228:231], v[76:79]\n"
"s_waitcnt lgkmcnt(8)\n"
"v_mfma_f32_16x16x32_bf16 v[64:67], v[48:51], a[240:243], v[64:67]\n"
"ds_read_b64_tr_b16 a[224:225], v1 offset:20480\n"
"v_mfma_f32_16x16x32_bf16 v[64:67], v[18:21], a[244:247], v[64:67]\n"
"ds_read_b64_tr_b16 a[226:227], v1 offset:25600\n"
"v_mfma_f32_16x16x32_bf16 v[68:71], v[52:55], a[240:243], v[68:71]\n"
"ds_read_b64_tr_b16 a[228:229], v1 offset:30720\n"
"v_mfma_f32_16x16x32_bf16 v[68:71], v[22:25], a[244:247], v[68:71]\n"
"ds_read_b64_tr_b16 a[230:231], v1 offset:35840\n"
"v_mfma_f32_16x16x32_bf16 v[72:75], v[56:59], a[240:243], v[72:75]\n"
"v_mfma_f32_16x16x32_bf16 v[72:75], v[36:39], a[244:247], v[72:75]\n"
"v_mfma_f32_16x16x32_bf16 v[76:79], v[60:63], a[240:243], v[76:79]\n"
"v_mfma_f32_16x16x32_bf16 v[76:79], v[6:9], a[244:247], v[76:79]\n"
"s_waitcnt lgkmcnt(8)\n"
"v_mfma_f32_16x16x32_bf16 v[80:83], a[96:99], a[248:251], v[80:83]\n"
"s_waitcnt vmcnt(4)\n"
"v_mov_b32 v46, s76\n"
"v_mov_b32 v47, s77\n"
"s_mov_b64 exec, 1\n"
"buffer_store_dwordx2 v[46:47], off, s[24:27], s91 sc1\n"
"s_mov_b64 exec, -1\n"
"s_add_u32 s91, s91, 64\n"
"ds_read_b64_tr_b16 a[240:241], v29 offset:40960\n"
"v_mfma_f32_16x16x32_bf16 v[80:83], a[112:115], a[252:255], v[80:83]\n"
"ds_read_b64_tr_b16 a[242:243], v29 offset:46080\n"
"v_mfma_f32_16x16x32_bf16 v[84:87], a[100:103], a[248:251], v[84:87]\n"
"ds_read_b64_tr_b16 a[244:245], v29 offset:51200\n"
"v_mfma_f32_16x16x32_bf16 v[84:87], a[116:119], a[252:255], v[84:87]\n"
"ds_read_b64_tr_b16 a[246:247], v29 offset:56320\n"
"v_mfma_f32_16x16x32_bf16 v[88:91], a[104:107], a[248:251], v[88:91]\n"
"v_mfma_f32_16x16x32_bf16 v[88:91], a[120:123], a[252:255], v[88:91]\n"
"v_mfma_f32_16x16x32_bf16 v[92:95], a[108:111], a[248:251], v[92:95]\n"
"v_mfma_f32_16x16x32_bf16 v[92:95], a[124:127], a[252:255], v[92:95]\n"
"s_waitcnt lgkmcnt(8)\n"
"v_mfma_f32_16x16x32_bf16 v[80:83], v[128:131], a[216:219], v[80:83]\n"
"ds_read_b64_tr_b16 a[248:249], v3 offset:0\n"
"v_mfma_f32_16x16x32_bf16 v[80:83], v[144:147], a[220:223], v[80:83]\n"
"ds_read_b64_tr_b16 a[250:251], v3 offset:5120\n"
"v_mfma_f32_16x16x32_bf16 v[84:87], v[132:135], a[216:219], v[84:87]\n"
"ds_read_b64_tr_b16 a[252:253], v3 offset:10240\n"
"v_mfma_f32_16x16x32_bf16 v[84:87], v[148:151], a[220:223], v[84:87]\n"
"ds_read_b64_tr_b16 a[254:255], v3 offset:15360\n"
"v_mfma_f32_16x16x32_bf16 v[88:91], v[136:139], a[216:219], v[88:91]\n"
"v_mfma_f32_16x16x32_bf16 v[88:91], v[152:155], a[220:223], v[88:91]\n"
"v_mfma_f32_16x16x32_bf16 v[92:95], v[140:143], a[216:219], v[92:95]\n"
"v_mfma_f32_16x16x32_bf16 v[92:95], v[156:159], a[220:223], v[92:95]\n"
"s_waitcnt lgkmcnt(8)\n"
"v_mfma_f32_16x16x32_bf16 v[80:83], a[128:131], a[224:227], v[80:83]\n"
"ds_read_b64_tr_b16 a[216:217], v3 offset:20480\n"
"v_mfma_f32_16x16x32_bf16 v[80:83], a[144:147], a[228:231], v[80:83]\n"
"ds_read_b64_tr_b16 a[218:219], v3 offset:25600\n"
"v_mfma_f32_16x16x32_bf16 v[84:87], a[132:135], a[224:227], v[84:87]\n"
"ds_read_b64_tr_b16 a[220:221], v3 offset:30720\n"
"v_mfma_f32_16x16x32_bf16 v[84:87], a[148:151], a[228:231], v[84:87]\n"
"ds_read_b64_tr_b16 a[222:223], v3 offset:35840\n"
"v_mfma_f32_16x16x32_bf16 v[88:91], a[136:139], a[224:227], v[88:91]\n"
"v_mfma_f32_16x16x32_bf16 v[88:91], a[152:155], a[228:231], v[88:91]\n"
"v_mfma_f32_16x16x32_bf16 v[92:95], a[140:143], a[224:227], v[92:95]\n"
"v_mfma_f32_16x16x32_bf16 v[92:95], a[156:159], a[228:231], v[92:95]\n"
"s_waitcnt lgkmcnt(8)\n"
"v_mfma_f32_16x16x32_bf16 v[80:83], a[160:163], a[240:243], v[80:83]\n"
"ds_read_b64_tr_b16 a[224:225], v29 offset:0\n"
"v_mfma_f32_16x16x32_bf16 v[80:83], a[176:179], a[244:247], v[80:83]\n"
"ds_read_b64_tr_b16 a[226:227], v29 offset:5120\n"
"v_mfma_f32_16x16x32_bf16 v[84:87], a[164:167], a[240:243], v[84:87]\n"
"ds_read_b64_tr_b16 a[228:229], v29 offset:10240\n"
"v_mfma_f32_16x16x32_bf16 v[84:87], a[180:183], a[244:247], v[84:87]\n"
"ds_read_b64_tr_b16 a[230:231], v29 offset:15360\n"
"v_mfma_f32_16x16x32_bf16 v[88:91], a[168:171], a[240:243], v[88:91]\n"
"buffer_load_dwordx2 v[42:43], v44, s[24:27], s90 offen sc1\n"
"v_mfma_f32_16x16x32_bf16 v[88:91], a[184:187], a[244:247], v[88:91]\n"
"v_mfma_f32_16x16x32_bf16 v[92:95], a[172:175], a[240:243], v[92:95]\n"
"v_mfma_f32_16x16x32_bf16 v[92:95], a[188:191], a[244:247], v[92:95]\n"
"s_waitcnt lgkmcnt(8)\n"
"v_mfma_f32_16x16x32_bf16 v[80:83], a[0:3], a[248:251], v[80:83]\n"
"v_mfma_f32_16x16x32_bf16 v[80:83], a[16:19], a[252:255], v[80:83]\n"
"v_mfma_f32_16x16x32_bf16 v[84:87], a[4:7], a[248:251], v[84:87]\n"
"v_mfma_f32_16x16x32_bf16 v[84:87], a[20:23], a[252:255], v[84:87]\n"
"v_mfma_f32_16x16x32_bf16 v[88:91], a[8:11], a[248:251], v[88:91]\n"
"v_mfma_f32_16x16x32_bf16 v[88:91], a[24:27], a[252:255], v[88:91]\n"
"v_mfma_f32_16x16x32_bf16 v[92:95], a[12:15], a[248:251], v[92:95]\n"
"v_mfma_f32_16x16x32_bf16 v[92:95], a[28:31], a[252:255], v[92:95]\n"
"s_waitcnt lgkmcnt(4)\n"
"v_mfma_f32_16x16x32_bf16 v[80:83], a[32:35], a[216:219], v[80:83]\n"
"v_mfma_f32_16x16x32_bf16 v[80:83], a[48:51], a[220:223], v[80:83]\n"
"v_mfma_f32_16x16x32_bf16 v[84:87], a[36:39], a[216:219], v[84:87]\n"
"v_mfma_f32_16x16x32_bf16 v[84:87], a[52:55], a[220:223], v[84:87]\n"
"v_mfma_f32_16x16x32_bf16 v[88:91], a[40:43], a[216:219], v[88:91]\n"
"v_mfma_f32_16x16x32_bf16 v[88:91], a[56:59], a[220:223], v[88:91]\n"
"v_mfma_f32_16x16x32_bf16 v[92:95], a[44:47], a[216:219], v[92:95]\n"
"v_mfma_f32_16x16x32_bf16 v[92:95], a[60:63], a[220:223], v[92:95]\n"
"s_waitcnt lgkmcnt(0)\n"
"v_mfma_f32_16x16x32_bf16 v[80:83], v[48:51], a[224:227], v[80:83]\n"
"s_waitcnt vmcnt(1)\n"
"v_mov_b32 v46, s76\n"
"v_mov_b32 v47, s77\n"
"s_mov_b64 exec, 1\n"
"buffer_store_dwordx2 v[46:47], off, s[24:27], s91 sc1\n"
"s_mov_b64 exec, -1\n"
"s_add_u32 s91, s91, 64\n"
"v_mfma_f32_16x16x32_bf16 v[80:83], v[18:21], a[228:231], v[80:83]\n"
"v_mfma_f32_16x16x32_bf16 v[84:87], v[52:55], a[224:227], v[84:87]\n"
"v_mfma_f32_16x16x32_bf16 v[84:87], v[22:25], a[228:231], v[84:87]\n"
"v_mfma_f32_16x16x32_bf16 v[88:91], v[56:59], a[224:227], v[88:91]\n"
"v_mfma_f32_16x16x32_bf16 v[88:91], v[36:39], a[228:231], v[88:91]\n"
"v_mfma_f32_16x16x32_bf16 v[92:95], v[60:63], a[224:227], v[92:95]\n"
"v_mfma_f32_16x16x32_bf16 v[92:95], v[6:9], a[228:231], v[92:95]\n"
"s_waitcnt vmcnt(0)\n"
"v_cmp_eq_u64 vcc, s[76:77], v[42:43]\n"
"s_nop 3\n"
"s_or_b32 s50, vcc_lo, s47\n"
"s_and_b32 s51, s50, 0xf\n"
"s_cmp_eq_u32 s51, 0xf\n"
"s_cbranch_scc0 L_slow\n"
"buffer_load_dwordx4 v[240:243], v40, s[24:27], s8 offen offset:0 sc1\n"
"buffer_load_dwordx4 v[112:115], v12, s[24:27], s8 offen offset:1024 sc1\n"
"buffer_load_dwordx4 v[116:119], v12, s[24:27], s8 offen offset:0 sc1\n"
"buffer_load_dwordx4 v[120:123], v12, s[24:27], s8 offen offset:3072 sc1\n"
"buffer_load_dwordx4 v[124:127], v12, s[24:27], s8 offen offset:2048 sc1\n"
"buffer_load_dwordx4 v[244:247], v40, s[24:27], s9 offen offset:0 sc1\n"
"buffer_load_dwordx4 v[176:179], v12, s[24:27], s9 offen offset:2048 sc1\n"
"buffer_load_dwordx4 v[180:183], v12, s[24:27], s9 offen offset:3072 sc1\n"
"buffer_load_dwordx4 v[184:187], v12, s[24:27], s9 offen offset:0 sc1\n"
"buffer_load_dwordx4 v[188:191], v12, s[24:27], s9 offen offset:1024 sc1\n"
"buffer_load_dwordx4 v[248:251], v40, s[24:27], s10 offen offset:0 sc1\n"
"L_mid:\n"
"s_andn2_b32 s30, 0xf0, s50\n"
"s_cbranch_scc1 L_n\n"
"buffer_load_dwordx4 v[208:211], v12, s[24:27], s10 offen offset:3072 sc1\n"
"buffer_load_dwordx4 v[212:215], v12, s[24:27], s10 offen offset:2048 sc1\n"
"buffer_load_dwordx4 v[216:219], v12, s[24:27], s10 offen offset:1024 sc1\n"
"buffer_load_dwordx4 v[220:223], v12, s[24:27], s10 offen offset:0 sc1\n"
"L_n:\n"
"s_waitcnt vmcnt(10)\n"
"v_mfma_f32_16x16x32_bf16 v[236:239], v[226:229], v[240:243], v[236:239]\n"
"v_mfma_f32_16x16x32_bf16 v[252:255], v[224:227], v[240:243], v[252:255]\n"
"s_waitcnt vmcnt(9)\n"
"v_mfma_f32_16x16x32_bf16 v[64:67], v[226:229], v[112:115], v[64:67]\n"
"v_mfma_f32_16x16x32_bf16 v[80:83], v[224:227], v[112:115], v[80:83]\n"
"s_waitcnt vmcnt(8)\n"
"v_mfma_f32_16x16x32_bf16 v[68:71], v[226:229], v[116:119], v[68:71]\n"
"v_mfma_f32_16x16x32_bf16 v[84:87], v[224:227], v[116:119], v[84:87]\n"
"s_waitcnt vmcnt(7)\n"
"v_mfma_f32_16x16x32_bf16 v[72:75], v[226:229], v[120:123], v[72:75]\n"
"v_mfma_f32_16x16x32_bf16 v[88:91], v[224:227], v[120:123], v[88:91]\n"
"s_waitcnt vmcnt(6)\n"
"v_mfma_f32_16x16x32_bf16 v[76:79], v[226:229], v[124:127], v[76:79]\n"
"v_mfma_f32_16x16x32_bf16 v[92:95], v[224:227], v[124:127], v[92:95]\n"
"s_waitcnt vmcnt(5)\n"
"v_mfma_f32_16x16x32_bf16 v[236:239], v[226:229], v[244:247], v[236:239]\n"
"v_mfma_f32_16x16x32_bf16 v[252:255], v[224:227], v[244:247], v[252:255]\n"
"s_waitcnt vmcnt(4)\n"
"v_mfma_f32_16x16x32_bf16 v[64:67], v[226:229], v[176:179], v[64:67]\n"
"v_mfma_f32_16x16x32_bf16 v[80:83], v[224:227], v[176:179], v[80:83]\n"
"s_waitcnt vmcnt(3)\n"
"v_mfma_f32_16x16x32_bf16 v[68:71], v[226:229], v[180:183], v[68:71]\n"
"v_mfma_f32_16x16x32_bf16 v[84:87], v[224:227], v[180:183], v[84:87]\n"
"s_waitcnt vmcnt(2)\n"
"v_mfma_f32_16x16x32_bf16 v[72:75], v[226:229], v[184:187], v[72:75]\n"
"v_mfma_f32_16x16x32_bf16 v[88:91], v[224:227], v[184:187], v[88:91]\n"
"s_waitcnt vmcnt(1)\n"
"v_mfma_f32_16x16x32_bf16 v[76:79], v[226:229], v[188:191], v[76:79]\n"
"v_mfma_f32_16x16x32_bf16 v[92:95], v[224:227], v[188:191], v[92:95]\n"
"s_waitcnt vmcnt(0)\n"
"v_mfma_f32_16x16x32_bf16 v[236:239], v[226:229], v[248:251], v[236:239]\n"
"v_mfma_f32_16x16x32_bf16 v[252:255], v[224:227], v[248:251], v[252:255]\n"
"s_nop 6\n"
"v_cvt_pk_bf16_f32 v236, v236, v237\n"
"v_cvt_pk_bf16_f32 v237, v238, v239\n"
"v_cvt_pk_bf16_f32 v238, v252, v253\n"
"v_cvt_pk_bf16_f32 v239, v254, v255\n"
"ds_write2st64_b64 v4, v[236:237], v[238:239] offset0:8 offset1:9\n"
"ds_read_b64_tr_b16 v[252:253], v5 offset:4096\n"
"ds_read_b64_tr_b16 v[254:255], v5 offset:4128\n"
"s_cmp_eq_u32 s30, 0\n"
"s_cbranch_scc1 L_f\n"
"s_mov_b32 s32, 0x40000\n"
"L_spb:\n"
"buffer_load_dwordx2 v[42:43], v44, s[24:27], s90 offen sc1\n"
"s_waitcnt vmcnt(0)\n"
"v_cmp_eq_u64 vcc, s[76:77], v[42:43]\n"
"s_nop 3\n"
"s_or_b32 s50, vcc_lo, s47\n"
"s_and_b32 s51, s50, 0xf0\n"
"s_cmp_eq_u32 s51, 0xf0\n"
"s_cbranch_scc1 L_okb\n"
"s_sleep 1\n"
"s_sub_u32 s32, s32, 1\n"
"s_cmp_lg_u32 s32, 0\n"
"s_cbranch_scc1 L_spb\n"
"L_okb:\n"
"buffer_load_dwordx4 v[208:211], v12, s[24:27], s10 offen offset:3072 sc1\n"
"buffer_load_dwordx4 v[212:215], v12, s[24:27], s10 offen offset:2048 sc1\n"
"buffer_load_dwordx4 v[216:219], v12, s[24:27], s10 offen offset:1024 sc1\n"
"buffer_load_dwordx4 v[220:223], v12, s[24:27], s10 offen offset:0 sc1\n"
"L_f:\n"
"s_waitcnt vmcnt(3)\n"
"v_mfma_f32_16x16x32_bf16 v[64:67], v[226:229], v[208:211], v[64:67]\n"
"v_mfma_f32_16x16x32_bf16 v[80:83], v[224:227], v[208:211], v[80:83]\n"
"s_waitcnt vmcnt(2)\n"
"v_mfma_f32_16x16x32_bf16 v[68:71], v[226:229], v[212:215], v[68:71]\n"
"v_mfma_f32_16x16x32_bf16 v[84:87], v[224:227], v[212:215], v[84:87]\n"
"s_waitcnt vmcnt(1)\n"
"v_mfma_f32_16x16x32_bf16 v[72:75], v[226:229], v[216:219], v[72:75]\n"
"v_mfma_f32_16x16x32_bf16 v[88:91], v[224:227], v[216:219], v[88:91]\n"
"s_waitcnt vmcnt(0)\n"
"v_mfma_f32_16x16x32_bf16 v[76:79], v[226:229], v[220:223], v[76:79]\n"
"v_mfma_f32_16x16x32_bf16 v[92:95], v[224:227], v[220:223], v[92:95]\n"
"s_waitcnt lgkmcnt(0)\n"
"buffer_store_dwordx4 v[252:255], v35, s[16:19], s11 offen offset:256 sc1\n"
"v_cvt_pk_bf16_f32 v64, v64, v65\n"
"v_cvt_pk_bf16_f32 v65, v66, v67\n"
"v_cvt_pk_bf16_f32 v66, v80, v81\n"
"v_cvt_pk_bf16_f32 v67, v82, v83\n"
"ds_write2st64_b64 v4, v[64:65], v[66:67] offset0:0 offset1:1\n"
"ds_read_b64_tr_b16 v[80:81], v5 offset:0\n"
"ds_read_b64_tr_b16 v[82:83], v5 offset:32\n"
"v_cvt_pk_bf16_f32 v68, v68, v69\n"
"v_cvt_pk_bf16_f32 v69, v70, v71\n"
"v_cvt_pk_bf16_f32 v70, v84, v85\n"
"v_cvt_pk_bf16_f32 v71, v86, v87\n"
"ds_write2st64_b64 v4, v[68:69], v[70:71] offset0:2 offset1:3\n"
"ds_read_b64_tr_b16 v[84:85], v5 offset:1024\n"
"ds_read_b64_tr_b16 v[86:87], v5 offset:1056\n"
"v_cvt_pk_bf16_f32 v72, v72, v73\n"
"v_cvt_pk_bf16_f32 v73, v74, v75\n"
"v_cvt_pk_bf16_f32 v74, v88, v89\n"
"v_cvt_pk_bf16_f32 v75, v90, v91\n"
"ds_write2st64_b64 v4, v[72:73], v[74:75] offset0:4 offset1:5\n"
"ds_read_b64_tr_b16 v[88:89], v5 offset:2048\n"
"ds_read_b64_tr_b16 v[90:91], v5 offset:2080\n"
"v_cvt_pk_bf16_f32 v76, v76, v77\n"
"v_cvt_pk_bf16_f32 v77, v78, v79\n"
"v_cvt_pk_bf16_f32 v78, v92, v93\n"
"v_cvt_pk_bf16_f32 v79, v94, v95\n"
"ds_write2st64_b64 v4, v[76:77], v[78:79] offset0:6 offset1:7\n"
"ds_read_b64_tr_b16 v[92:93], v5 offset:3072\n"
"ds_read_b64_tr_b16 v[94:95], v5 offset:3104\n"
"s_waitcnt lgkmcnt(8)\n"
"buffer_store_dwordx4 v[80:83], v35, s[16:19], s68 offen offset:0 sc1\n"
"s_waitcnt lgkmcnt(6)\n"
"buffer_store_dwordx4 v[84:87], v35, s[16:19], s69 offen offset:0 sc1\n"
"s_waitcnt lgkmcnt(3)\n"
"buffer_store_dwordx4 v[88:91], v35, s[16:19], s70 offen offset:0 sc1\n"
"s_waitcnt lgkmcnt(0)\n"
"buffer_store_dwordx4 v[92:95], v35, s[16:19], s71 offen offset:0 sc1\n"
"s_endpgm\n"
"L_slow:\n"
"s_mov_b32 s32, 0x40000\n"
"L_spa:\n"
"buffer_load_dwordx2 v[42:43], v44, s[24:27], s90 offen sc1\n"
"s_waitcnt vmcnt(0)\n"
"v_cmp_eq_u64 vcc, s[76:77], v[42:43]\n"
"s_nop 3\n"
"s_or_b32 s50, vcc_lo, s47\n"
"s_and_b32 s51, s50, 0xf\n"
"s_cmp_eq_u32 s51, 0xf\n"
"s_cbranch_scc1 L_oka\n"
"s_sleep 1\n"
"s_sub_u32 s32, s32, 1\n"
"s_cmp_lg_u32 s32, 0\n"
"s_cbranch_scc1 L_spa\n"
"L_oka:\n"
"buffer_load_dwordx4 v[240:243], v40, s[24:27], s8 offen offset:0 sc1\n"
"buffer_load_dwordx4 v[112:115], v12, s[24:27], s8 offen offset:1024 sc1\n"
"buffer_load_dwordx4 v[116:119], v12, s[24:27], s8 offen offset:0 sc1\n"
"buffer_load_dwordx4 v[120:123], v12, s[24:27], s8 offen offset:3072 sc1\n"
"buffer_load_dwordx4 v[124:127], v12, s[24:27], s8 offen offset:2048 sc1\n"
"buffer_load_dwordx4 v[244:247], v40, s[24:27], s9 offen offset:0 sc1\n"
"buffer_load_dwordx4 v[176:179], v12, s[24:27], s9 offen offset:2048 sc1\n"
"buffer_load_dwordx4 v[180:183], v12, s[24:27], s9 offen offset:3072 sc1\n"
"buffer_load_dwordx4 v[184:187], v12, s[24:27], s9 offen offset:0 sc1\n"
"buffer_load_dwordx4 v[188:191], v12, s[24:27], s9 offen offset:1024 sc1\n"
"buffer_load_dwordx4 v[248:251], v40, s[24:27], s10 offen offset:0 sc1\n"
"s_branch L_mid\n"
"L_hot:\n"
"L_rare0:\n"
"s_nop 7\n"
"s_nop 7\n"
"s_add_u32 s50, s55, 0\n"
"s_add_u32 s50, s50, s63\n"
"s_and_b32 s50, s50, 31\n"
"s_mul_i32 s50, s50, 3072\n"
"s_add_u32 s50, s50, 1024\n"
"v_bfe_u32 v23, v12, 7, 1\n"
"v_lshl_add_u32 v22, v23, 10, s50\n"
"v_lshrrev_b32 v24, 15, v22\n"
"v_mul_u32_u24 v24, 0x18000, v24\n"
"v_add_u32 v22, v22, v24\n"
"v_lshl_add_u32 v22, v23, 15, v22\n"
"v_mul_u32_u24 v23, 5, v23\n"
"v_lshrrev_b32 v24, 4, v12\n"
"v_add3_u32 v23, v23, v24, 0\n"
"v_and_b32 v23, 7, v23\n"
"v_mul_u32_u24 v23, 0x180, v23\n"
"v_and_b32 v24, 48, v24\n"
"v_add3_u32 v22, v22, v23, v24\n"
"s_mov_b32 s50, 0x18000\n"
"buffer_load_dwordx4 v[40:43], v22, s[8:11], s50 offen offset:0\n"
"s_waitcnt vmcnt(0)\n"
"v_mfma_f32_16x16x32_bf16 v[96:99], v[40:43], v[160:163], v[48:51]\n"
"v_mfma_f32_16x16x32_bf16 v[100:103], v[40:43], v[164:167], v[52:55]\n"
"v_mfma_f32_16x16x32_bf16 v[104:107], v[40:43], v[168:171], v[56:59]\n"
"v_mfma_f32_16x16x32_bf16 v[108:111], v[40:43], v[172:175], v[60:63]\n"
"buffer_load_dwordx4 v[44:47], v22, s[8:11], s50 offen offset:64\n"
"s_waitcnt vmcnt(0)\n"
"v_mfma_f32_16x16x32_bf16 v[96:99], v[44:47], v[176:179], v[96:99]\n"
"v_mfma_f32_16x16x32_bf16 v[100:103], v[44:47], v[180:183], v[100:103]\n"
"v_mfma_f32_16x16x32_bf16 v[104:107], v[44:47], v[184:187], v[104:107]\n"
"v_mfma_f32_16x16x32_bf16 v[108:111], v[44:47], v[188:191], v[108:111]\n"
"buffer_load_dwordx4 v[40:43], v22, s[8:11], s50 offen offset:128\n"
"s_waitcnt vmcnt(0)\n"
"v_mfma_f32_16x16x32_bf16 v[96:99], v[40:43], v[192:195], v[96:99]\n"
"v_mfma_f32_16x16x32_bf16 v[100:103], v[40:43], v[196:199], v[100:103]\n"
"v_mfma_f32_16x16x32_bf16 v[104:107], v[40:43], v[200:203], v[104:107]\n"
"v_mfma_f32_16x16x32_bf16 v[108:111], v[40:43], v[204:207], v[108:111]\n"
"buffer_load_dwordx4 v[44:47], v22, s[8:11], s50 offen offset:192\n"
"s_waitcnt vmcnt(0)\n"
"v_mfma_f32_16x16x32_bf16 v[96:99], v[44:47], v[208:211], v[96:99]\n"
"v_mfma_f32_16x16x32_bf16 v[100:103], v[44:47], v[212:215], v[100:103]\n"
"v_mfma_f32_16x16x32_bf16 v[104:107], v[44:47], v[216:219], v[104:107]\n"
"v_mfma_f32_16x16x32_bf16 v[108:111], v[44:47], v[220:223], v[108:111]\n"
"buffer_load_dwordx4 v[40:43], v22, s[8:11], s50 offen offset:256\n"
"s_waitcnt vmcnt(0)\n"
"v_mfma_f32_16x16x32_bf16 v[96:99], v[40:43], v[224:227], v[96:99]\n"
"v_mfma_f32_16x16x32_bf16 v[100:103], v[40:43], v[228:231], v[100:103]\n"
"v_mfma_f32_16x16x32_bf16 v[104:107], v[40:43], v[232:235], v[104:107]\n"
"v_mfma_f32_16x16x32_bf16 v[108:111], v[40:43], v[236:239], v[108:111]\n"
"buffer_load_dwordx4 v[44:47], v22, s[8:11], s50 offen offset:320\n"
"s_waitcnt vmcnt(0)\n"
"v_mfma_f32_16x16x32_bf16 v[96:99], v[44:47], v[240:243], v[96:99]\n"
"v_mfma_f32_16x16x32_bf16 v[100:103], v[44:47], v[244:247], v[100:103]\n"
"v_mfma_f32_16x16x32_bf16 v[104:107], v[44:47], v[248:251], v[104:107]\n"
"v_mfma_f32_16x16x32_bf16 v[108:111], v[44:47], v[252:255], v[108:111]\n"
"s_add_u32 s50, s55, 0\n"
"s_add_u32 s50, s50, s63\n"
"s_and_b32 s50, s50, 31\n"
"s_mul_i32 s50, s50, 3072\n"
"s_add_u32 s50, s50, 0\n"
"v_bfe_u32 v23, v12, 7, 1\n"
"v_lshl_add_u32 v22, v23, 10, s50\n"
"v_lshrrev_b32 v24, 15, v22\n"
"v_mul_u32_u24 v24, 0x18000, v24\n"
"v_add_u32 v22, v22, v24\n"
"v_lshl_add_u32 v22, v23, 15, v22\n"
"v_mul_u32_u24 v23, 5, v23\n"
"v_lshrrev_b32 v24, 4, v12\n"
"v_add3_u32 v23, v23, v24, 3\n"
"v_and_b32 v23, 7, v23\n"
"v_mul_u32_u24 v23, 0x180, v23\n"
"v_and_b32 v24, 48, v24\n"
"v_add3_u32 v22, v22, v23, v24\n"
"s_mov_b32 s50, 0x28000\n"
"buffer_load_dwordx4 v[40:43], v22, s[8:11], s50 offen offset:0\n"
"s_waitcnt vmcnt(0)\n"
"v_mfma_f32_16x16x32_bf16 v[64:67], v[40:43], v[160:163], v[48:51]\n"
"v_mfma_f32_16x16x32_bf16 v[68:71], v[40:43], v[164:167], v[52:55]\n"
"v_mfma_f32_16x16x32_bf16 v[72:75], v[40:43], v[168:171], v[56:59]\n"
"v_mfma_f32_16x16x32_bf16 v[76:79], v[40:43], v[172:175], v[60:63]\n"
"buffer_load_dwordx4 v[44:47], v22, s[8:11], s50 offen offset:64\n"
"s_waitcnt vmcnt(0)\n"
"v_mfma_f32_16x16x32_bf16 v[64:67], v[44:47], v[176:179], v[64:67]\n"
"v_mfma_f32_16x16x32_bf16 v[68:71], v[44:47], v[180:183], v[68:71]\n"
"v_mfma_f32_16x16x32_bf16 v[72:75], v[44:47], v[184:187], v[72:75]\n"
"v_mfma_f32_16x16x32_bf16 v[76:79], v[44:47], v[188:191], v[76:79]\n"
"buffer_load_dwordx4 v[40:43], v22, s[8:11], s50 offen offset:128\n"
"s_waitcnt vmcnt(0)\n"
"v_mfma_f32_16x16x32_bf16 v[64:67], v[40:43], v[192:195], v[64:67]\n"
"v_mfma_f32_16x16x32_bf16 v[68:71], v[40:43], v[196:199], v[68:71]\n"
"v_mfma_f32_16x16x32_bf16 v[72:75], v[40:43], v[200:203], v[72:75]\n"
"v_mfma_f32_16x16x32_bf16 v[76:79], v[40:43], v[204:207], v[76:79]\n"
"buffer_load_dwordx4 v[44:47], v22, s[8:11], s50 offen offset:192\n"
"s_waitcnt vmcnt(0)\n"
"v_mfma_f32_16x16x32_bf16 v[64:67], v[44:47], v[208:211], v[64:67]\n"
"v_mfma_f32_16x16x32_bf16 v[68:71], v[44:47], v[212:215], v[68:71]\n"
"v_mfma_f32_16x16x32_bf16 v[72:75], v[44:47], v[216:219], v[72:75]\n"
"v_mfma_f32_16x16x32_bf16 v[76:79], v[44:47], v[220:223], v[76:79]\n"
"buffer_load_dwordx4 v[40:43], v22, s[8:11], s50 offen offset:256\n"
"s_waitcnt vmcnt(0)\n"
"v_mfma_f32_16x16x32_bf16 v[64:67], v[40:43], v[224:227], v[64:67]\n"
"v_mfma_f32_16x16x32_bf16 v[68:71], v[40:43], v[228:231], v[68:71]\n"
"v_mfma_f32_16x16x32_bf16 v[72:75], v[40:43], v[232:235], v[72:75]\n"
"v_mfma_f32_16x16x32_bf16 v[76:79], v[40:43], v[236:239], v[76:79]\n"
"buffer_load_dwordx4 v[44:47], v22, s[8:11], s50 offen offset:320\n"
"s_waitcnt vmcnt(0)\n"
"v_mfma_f32_16x16x32_bf16 v[64:67], v[44:47], v[240:243], v[64:67]\n"
"v_mfma_f32_16x16x32_bf16 v[68:71], v[44:47], v[244:247], v[68:71]\n"
"v_mfma_f32_16x16x32_bf16 v[72:75], v[44:47], v[248:251], v[72:75]\n"
"v_mfma_f32_16x16x32_bf16 v[76:79], v[44:47], v[252:255], v[76:79]\n"
"s_add_u32 s50, s55, 9\n"
"s_add_u32 s50, s50, s63\n"
"s_and_b32 s50, s50, 31\n"
"s_mul_i32 s50, s50, 3072\n"
"s_add_u32 s50, s50, 1024\n"
"v_bfe_u32 v23, v12, 7, 1\n"
"v_lshl_add_u32 v22, v23, 10, s50\n"
"v_lshrrev_b32 v24, 15, v22\n"
"v_mul_u32_u24 v24, 0x18000, v24\n"
"v_add_u32 v22, v22, v24\n"
"v_lshl_add_u32 v22, v23, 15, v22\n"
"v_mul_u32_u24 v23, 5, v23\n"
"v_lshrrev_b32 v24, 4, v12\n"
"v_add3_u32 v23, v23, v24, 0\n"
"v_and_b32 v23, 7, v23\n"
"v_mul_u32_u24 v23, 0x180, v23\n"
"v_and_b32 v24, 48, v24\n"
"v_add3_u32 v22, v22, v23, v24\n"
"s_mov_b32 s50, 0x18000\n"
"buffer_load_dwordx4 v[40:43], v22, s[8:11], s50 offen offset:0\n"
"s_waitcnt vmcnt(0)\n"
"v_mfma_f32_16x16x32_bf16 v[112:115], v[40:43], v[160:163], v[48:51]\n"
"v_mfma_f32_16x16x32_bf16 v[116:119], v[40:43], v[164:167], v[52:55]\n"
"v_mfma_f32_16x16x32_bf16 v[120:123], v[40:43], v[168:171], v[56:59]\n"
"v_mfma_f32_16x16x32_bf16 v[124:127], v[40:43], v[172:175], v[60:63]\n"
"buffer_load_dwordx4 v[44:47], v22, s[8:11], s50 offen offset:64\n"
"s_waitcnt vmcnt(0)\n"
"v_mfma_f32_16x16x32_bf16 v[112:115], v[44:47], v[176:179], v[112:115]\n"
"v_mfma_f32_16x16x32_bf16 v[116:119], v[44:47], v[180:183], v[116:119]\n"
"v_mfma_f32_16x16x32_bf16 v[120:123], v[44:47], v[184:187], v[120:123]\n"
"v_mfma_f32_16x16x32_bf16 v[124:127], v[44:47], v[188:191], v[124:127]\n"
"buffer_load_dwordx4 v[40:43], v22, s[8:11], s50 offen offset:128\n"
"s_waitcnt vmcnt(0)\n"
"v_mfma_f32_16x16x32_bf16 v[112:115], v[40:43], v[192:195], v[112:115]\n"
"v_mfma_f32_16x16x32_bf16 v[116:119], v[40:43], v[196:199], v[116:119]\n"
"v_mfma_f32_16x16x32_bf16 v[120:123], v[40:43], v[200:203], v[120:123]\n"
"v_mfma_f32_16x16x32_bf16 v[124:127], v[40:43], v[204:207], v[124:127]\n"
"buffer_load_dwordx4 v[44:47], v22, s[8:11], s50 offen offset:192\n"
"s_waitcnt vmcnt(0)\n"
"v_mfma_f32_16x16x32_bf16 v[112:115], v[44:47], v[208:211], v[112:115]\n"
"v_mfma_f32_16x16x32_bf16 v[116:119], v[44:47], v[212:215], v[116:119]\n"
"v_mfma_f32_16x16x32_bf16 v[120:123], v[44:47], v[216:219], v[120:123]\n"
"v_mfma_f32_16x16x32_bf16 v[124:127], v[44:47], v[220:223], v[124:127]\n"
"buffer_load_dwordx4 v[40:43], v22, s[8:11], s50 offen offset:256\n"
"s_waitcnt vmcnt(0)\n"
"v_mfma_f32_16x16x32_bf16 v[112:115], v[40:43], v[224:227], v[112:115]\n"
"v_mfma_f32_16x16x32_bf16 v[116:119], v[40:43], v[228:231], v[116:119]\n"
"v_mfma_f32_16x16x32_bf16 v[120:123], v[40:43], v[232:235], v[120:123]\n"
"v_mfma_f32_16x16x32_bf16 v[124:127], v[40:43], v[236:239], v[124:127]\n"
"buffer_load_dwordx4 v[44:47], v22, s[8:11], s50 offen offset:320\n"
"s_waitcnt vmcnt(0)\n"
"v_mfma_f32_16x16x32_bf16 v[112:115], v[44:47], v[240:243], v[112:115]\n"
"v_mfma_f32_16x16x32_bf16 v[116:119], v[44:47], v[244:247], v[116:119]\n"
"v_mfma_f32_16x16x32_bf16 v[120:123], v[44:47], v[248:251], v[120:123]\n"
"v_mfma_f32_16x16x32_bf16 v[124:127], v[44:47], v[252:255], v[124:127]\n"
"s_add_u32 s50, s55, 9\n"
"s_add_u32 s50, s50, s63\n"
"s_and_b32 s50, s50, 31\n"
"s_mul_i32 s50, s50, 3072\n"
"s_add_u32 s50, s50, 0\n"
"v_bfe_u32 v23, v12, 7, 1\n"
"v_lshl_add_u32 v22, v23, 10, s50\n"
"v_lshrrev_b32 v24, 15, v22\n"
"v_mul_u32_u24 v24, 0x18000, v24\n"
"v_add_u32 v22, v22, v24\n"
"v_lshl_add_u32 v22, v23, 15, v22\n"
"v_mul_u32_u24 v23, 5, v23\n"
"v_lshrrev_b32 v24, 4, v12\n"
"v_add3_u32 v23, v23, v24, 3\n"
"v_and_b32 v23, 7, v23\n"
"v_mul_u32_u24 v23, 0x180, v23\n"
"v_and_b32 v24, 48, v24\n"
"v_add3_u32 v22, v22, v23, v24\n"
"s_mov_b32 s50, 0x28000\n"
"buffer_load_dwordx4 v[40:43], v22, s[8:11], s50 offen offset:0\n"
"s_waitcnt vmcnt(0)\n"
"v_mfma_f32_16x16x32_bf16 v[80:83], v[40:43], v[160:163], v[48:51]\n"
"v_mfma_f32_16x16x32_bf16 v[84:87], v[40:43], v[164:167], v[52:55]\n"
"v_mfma_f32_16x16x32_bf16 v[88:91], v[40:43], v[168:171], v[56:59]\n"
"v_mfma_f32_16x16x32_bf16 v[92:95], v[40:43], v[172:175], v[60:63]\n"
"buffer_load_dwordx4 v[44:47], v22, s[8:11], s50 offen offset:64\n"
"s_waitcnt vmcnt(0)\n"
"v_mfma_f32_16x16x32_bf16 v[80:83], v[44:47], v[176:179], v[80:83]\n"
"v_mfma_f32_16x16x32_bf16 v[84:87], v[44:47], v[180:183], v[84:87]\n"
"v_mfma_f32_16x16x32_bf16 v[88:91], v[44:47], v[184:187], v[88:91]\n"
"v_mfma_f32_16x16x32_bf16 v[92:95], v[44:47], v[188:191], v[92:95]\n"
"buffer_load_dwordx4 v[40:43], v22, s[8:11], s50 offen offset:128\n"
"s_waitcnt vmcnt(0)\n"
"v_mfma_f32_16x16x32_bf16 v[80:83], v[40:43], v[192:195], v[80:83]\n"
"v_mfma_f32_16x16x32_bf16 v[84:87], v[40:43], v[196:199], v[84:87]\n"
"v_mfma_f32_16x16x32_bf16 v[88:91], v[40:43], v[200:203], v[88:91]\n"
"v_mfma_f32_16x16x32_bf16 v[92:95], v[40:43], v[204:207], v[92:95]\n"
"buffer_load_dwordx4 v[44:47], v22, s[8:11], s50 offen offset:192\n"
"s_waitcnt vmcnt(0)\n"
"v_mfma_f32_16x16x32_bf16 v[80:83], v[44:47], v[208:211], v[80:83]\n"
"v_mfma_f32_16x16x32_bf16 v[84:87], v[44:47], v[212:215], v[84:87]\n"
"v_mfma_f32_16x16x32_bf16 v[88:91], v[44:47], v[216:219], v[88:91]\n"
"v_mfma_f32_16x16x32_bf16 v[92:95], v[44:47], v[220:223], v[92:95]\n"
"buffer_load_dwordx4 v[40:43], v22, s[8:11], s50 offen offset:256\n"
"s_waitcnt vmcnt(0)\n"
"v_mfma_f32_16x16x32_bf16 v[80:83], v[40:43], v[224:227], v[80:83]\n"
"v_mfma_f32_16x16x32_bf16 v[84:87], v[40:43], v[228:231], v[84:87]\n"
"v_mfma_f32_16x16x32_bf16 v[88:91], v[40:43], v[232:235], v[88:91]\n"
"v_mfma_f32_16x16x32_bf16 v[92:95], v[40:43], v[236:239], v[92:95]\n"
"buffer_load_dwordx4 v[44:47], v22, s[8:11], s50 offen offset:320\n"
"s_waitcnt vmcnt(0)\n"
"v_mfma_f32_16x16x32_bf16 v[80:83], v[44:47], v[240:243], v[80:83]\n"
"v_mfma_f32_16x16x32_bf16 v[84:87], v[44:47], v[244:247], v[84:87]\n"
"v_mfma_f32_16x16x32_bf16 v[88:91], v[44:47], v[248:251], v[88:91]\n"
"v_mfma_f32_16x16x32_bf16 v[92:95], v[44:47], v[252:255], v[92:95]\n"
"s_nop 7\n"
"s_nop 7\n"
"s_lshr_b32 s50, s54, 23\n"
"s_sub_u32 s50, s50, 153\n"
"v_cvt_f32_i32 v24, s50\n"
"s_cmp_eq_u32 s55, 0\n"
"s_cselect_b32 s50, 0xff800000, 0\n"
"v_max_f32 v22, v96, v97\n"
"v_max_f32 v22, v22, v98\n"
"v_max_f32 v22, v22, v99\n"
"v_max_f32 v22, v22, v64\n"
"v_max_f32 v22, v22, v65\n"
"v_max_f32 v22, v22, v66\n"
"v_max_f32 v22, v22, v67\n"
"v_max_f32 v22, v22, v112\n"
"v_max_f32 v22, v22, v113\n"
"v_max_f32 v22, v22, v114\n"
"v_max_f32 v22, v22, v115\n"
"v_max_f32 v22, v22, v80\n"
"v_max_f32 v22, v22, v81\n"
"v_max_f32 v22, v22, v82\n"
"v_max_f32 v22, v22, v83\n"
"v_sub_f32 v22, v22, v24\n"
"v_max_f32 v22, s50, v22\n"
"v_mov_b32 v23, v22\n"
"s_nop 1\n"
"v_permlane32_swap_b32 v22, v23\n"
"v_max_f32 v22, v22, v23\n"
"v_mov_b32 v23, v22\n"
"s_nop 1\n"
"v_permlane16_swap_b32 v22, v23\n"
"v_max_f32 v22, v22, v23\n"
"v_sub_f32 v48, v48, v22\n"
"v_sub_f32 v49, v49, v22\n"
"v_sub_f32 v50, v50, v22\n"
"v_sub_f32 v51, v51, v22\n"
"v_sub_f32 v96, v96, v22\n"
"v_sub_f32 v97, v97, v22\n"
"v_sub_f32 v98, v98, v22\n"
"v_sub_f32 v99, v99, v22\n"
"v_sub_f32 v64, v64, v22\n"
"v_sub_f32 v65, v65, v22\n"
"v_sub_f32 v66, v66, v22\n"
"v_sub_f32 v67, v67, v22\n"
"v_sub_f32 v112, v112, v22\n"
"v_sub_f32 v113, v113, v22\n"
"v_sub_f32 v114, v114, v22\n"
"v_sub_f32 v115, v115, v22\n"
"v_sub_f32 v80, v80, v22\n"
"v_sub_f32 v81, v81, v22\n"
"v_sub_f32 v82, v82, v22\n"
"v_sub_f32 v83, v83, v22\n"
"v_sub_f32 v23, 0, v22\n"
"v_exp_f32 v23, v23\n"
"s_nop 0\n"
"v_mul_f32 v14, v14, v23\n"
"v_sub_f32 v22, 1.0, v23\n"
"v_accvgpr_read_b32 v40, a0\n"
"v_accvgpr_read_b32 v41, a1\n"
"v_accvgpr_read_b32 v42, a2\n"
"v_accvgpr_read_b32 v43, a3\n"
"v_fma_f32 v40, v40, v23, v22\n"
"v_fma_f32 v41, v41, v23, v22\n"
"v_fma_f32 v42, v42, v23, v22\n"
"v_fma_f32 v43, v43, v23, v22\n"
"v_accvgpr_write_b32 a0, v40\n"
"v_accvgpr_write_b32 a1, v41\n"
"v_accvgpr_write_b32 a2, v42\n"
"v_accvgpr_write_b32 a3, v43\n"
"v_accvgpr_read_b32 v40, a16\n"
"v_accvgpr_read_b32 v41, a17\n"
"v_accvgpr_read_b32 v42, a18\n"
"v_accvgpr_read_b32 v43, a19\n"
"v_fma_f32 v40, v40, v23, v22\n"
"v_fma_f32 v41, v41, v23, v22\n"
"v_fma_f32 v42, v42, v23, v22\n"
"v_fma_f32 v43, v43, v23, v22\n"
"v_accvgpr_write_b32 a16, v40\n"
"v_accvgpr_write_b32 a17, v41\n"
"v_accvgpr_write_b32 a18, v42\n"
"v_accvgpr_write_b32 a19, v43\n"
"v_accvgpr_read_b32 v40, a32\n"
"v_accvgpr_read_b32 v41, a33\n"
"v_accvgpr_read_b32 v42, a34\n"
"v_accvgpr_read_b32 v43, a35\n"
"v_fma_f32 v40, v40, v23, v22\n"
"v_fma_f32 v41, v41, v23, v22\n"
"v_fma_f32 v42, v42, v23, v22\n"
"v_fma_f32 v43, v43, v23, v22\n"
"v_accvgpr_write_b32 a32, v40\n"
"v_accvgpr_write_b32 a33, v41\n"
"v_accvgpr_write_b32 a34, v42\n"
"v_accvgpr_write_b32 a35, v43\n"
"v_accvgpr_read_b32 v40, a48\n"
"v_accvgpr_read_b32 v41, a49\n"
"v_accvgpr_read_b32 v42, a50\n"
"v_accvgpr_read_b32 v43, a51\n"
"v_fma_f32 v40, v40, v23, v22\n"
"v_fma_f32 v41, v41, v23, v22\n"
"v_fma_f32 v42, v42, v23, v22\n"
"v_fma_f32 v43, v43, v23, v22\n"
"v_accvgpr_write_b32 a48, v40\n"
"v_accvgpr_write_b32 a49, v41\n"
"v_accvgpr_write_b32 a50, v42\n"
"v_accvgpr_write_b32 a51, v43\n"
"v_accvgpr_read_b32 v40, a64\n"
"v_accvgpr_read_b32 v41, a65\n"
"v_accvgpr_read_b32 v42, a66\n"
"v_accvgpr_read_b32 v43, a67\n"
"v_fma_f32 v40, v40, v23, v22\n"
"v_fma_f32 v41, v41, v23, v22\n"
"v_fma_f32 v42, v42, v23, v22\n"
"v_fma_f32 v43, v43, v23, v22\n"
"v_accvgpr_write_b32 a64, v40\n"
"v_accvgpr_write_b32 a65, v41\n"
"v_accvgpr_write_b32 a66, v42\n"
"v_accvgpr_write_b32 a67, v43\n"
"v_accvgpr_read_b32 v40, a80\n"
"v_accvgpr_read_b32 v41, a81\n"
"v_accvgpr_read_b32 v42, a82\n"
"v_accvgpr_read_b32 v43, a83\n"
"v_fma_f32 v40, v40, v23, v22\n"
"v_fma_f32 v41, v41, v23, v22\n"
"v_fma_f32 v42, v42, v23, v22\n"
"v_fma_f32 v43, v43, v23, v22\n"
"v_accvgpr_write_b32 a80, v40\n"
"v_accvgpr_write_b32 a81, v41\n"
"v_accvgpr_write_b32 a82, v42\n"
"v_accvgpr_write_b32 a83, v43\n"
"v_accvgpr_read_b32 v40, a96\n"
"v_accvgpr_read_b32 v41, a97\n"
"v_accvgpr_read_b32 v42, a98\n"
"v_accvgpr_read_b32 v43, a99\n"
"v_fma_f32 v40, v40, v23, v22\n"
"v_fma_f32 v41, v41, v23, v22\n"
"v_fma_f32 v42, v42, v23, v22\n"
"v_fma_f32 v43, v43, v23, v22\n"
"v_accvgpr_write_b32 a96, v40\n"
"v_accvgpr_write_b32 a97, v41\n"
"v_accvgpr_write_b32 a98, v42\n"
"v_accvgpr_write_b32 a99, v43\n"
"v_accvgpr_read_b32 v40, a112\n"
"v_accvgpr_read_b32 v41, a113\n"
"v_accvgpr_read_b32 v42, a114\n"
"v_accvgpr_read_b32 v43, a115\n"
"v_fma_f32 v40, v40, v23, v22\n"
"v_fma_f32 v41, v41, v23, v22\n"
"v_fma_f32 v42, v42, v23, v22\n"
"v_fma_f32 v43, v43, v23, v22\n"
"v_accvgpr_write_b32 a112, v40\n"
"v_accvgpr_write_b32 a113, v41\n"
"v_accvgpr_write_b32 a114, v42\n"
"v_accvgpr_write_b32 a115, v43\n"
"v_accvgpr_read_b32 v40, a128\n"
"v_accvgpr_read_b32 v41, a129\n"
"v_accvgpr_read_b32 v42, a130\n"
"v_accvgpr_read_b32 v43, a131\n"
"v_fma_f32 v40, v40, v23, v22\n"
"v_fma_f32 v41, v41, v23, v22\n"
"v_fma_f32 v42, v42, v23, v22\n"
"v_fma_f32 v43, v43, v23, v22\n"
"v_accvgpr_write_b32 a128, v40\n"
"v_accvgpr_write_b32 a129, v41\n"
"v_accvgpr_write_b32 a130, v42\n"
"v_accvgpr_write_b32 a131, v43\n"
"v_accvgpr_read_b32 v40, a144\n"
"v_accvgpr_read_b32 v41, a145\n"
"v_accvgpr_read_b32 v42, a146\n"
"v_accvgpr_read_b32 v43, a147\n"
"v_fma_f32 v40, v40, v23, v22\n"
"v_fma_f32 v41, v41, v23, v22\n"
"v_fma_f32 v42, v42, v23, v22\n"
"v_fma_f32 v43, v43, v23, v22\n"
"v_accvgpr_write_b32 a144, v40\n"
"v_accvgpr_write_b32 a145, v41\n"
"v_accvgpr_write_b32 a146, v42\n"
"v_accvgpr_write_b32 a147, v43\n"
"v_accvgpr_read_b32 v40, a160\n"
"v_accvgpr_read_b32 v41, a161\n"
"v_accvgpr_read_b32 v42, a162\n"
"v_accvgpr_read_b32 v43, a163\n"
"v_fma_f32 v40, v40, v23, v22\n"
"v_fma_f32 v41, v41, v23, v22\n"
"v_fma_f32 v42, v42, v23, v22\n"
"v_fma_f32 v43, v43, v23, v22\n"
"v_accvgpr_write_b32 a160, v40\n"
"v_accvgpr_write_b32 a161, v41\n"
"v_accvgpr_write_b32 a162, v42\n"
"v_accvgpr_write_b32 a163, v43\n"
"v_accvgpr_read_b32 v40, a176\n"
"v_accvgpr_read_b32 v41, a177\n"
"v_accvgpr_read_b32 v42, a178\n"
"v_accvgpr_read_b32 v43, a179\n"
"v_fma_f32 v40, v40, v23, v22\n"
"v_fma_f32 v41, v41, v23, v22\n"
"v_fma_f32 v42, v42, v23, v22\n"
"v_fma_f32 v43, v43, v23, v22\n"
"v_accvgpr_write_b32 a176, v40\n"
"v_accvgpr_write_b32 a177, v41\n"
"v_accvgpr_write_b32 a178, v42\n"
"v_accvgpr_write_b32 a179, v43\n"
"v_max_f32 v22, v100, v101\n"
"v_max_f32 v22, v22, v102\n"
"v_max_f32 v22, v22, v103\n"
"v_max_f32 v22, v22, v68\n"
"v_max_f32 v22, v22, v69\n"
"v_max_f32 v22, v22, v70\n"
"v_max_f32 v22, v22, v71\n"
"v_max_f32 v22, v22, v116\n"
"v_max_f32 v22, v22, v117\n"
"v_max_f32 v22, v22, v118\n"
"v_max_f32 v22, v22, v119\n"
"v_max_f32 v22, v22, v84\n"
"v_max_f32 v22, v22, v85\n"
"v_max_f32 v22, v22, v86\n"
"v_max_f32 v22, v22, v87\n"
"v_sub_f32 v22, v22, v24\n"
"v_max_f32 v22, s50, v22\n"
"v_mov_b32 v23, v22\n"
"s_nop 1\n"
"v_permlane32_swap_b32 v22, v23\n"
"v_max_f32 v22, v22, v23\n"
"v_mov_b32 v23, v22\n"
"s_nop 1\n"
"v_permlane16_swap_b32 v22, v23\n"
"v_max_f32 v22, v22, v23\n"
"v_sub_f32 v52, v52, v22\n"
"v_sub_f32 v53, v53, v22\n"
"v_sub_f32 v54, v54, v22\n"
"v_sub_f32 v55, v55, v22\n"
"v_sub_f32 v100, v100, v22\n"
"v_sub_f32 v101, v101, v22\n"
"v_sub_f32 v102, v102, v22\n"
"v_sub_f32 v103, v103, v22\n"
"v_sub_f32 v68, v68, v22\n"
"v_sub_f32 v69, v69, v22\n"
"v_sub_f32 v70, v70, v22\n"
"v_sub_f32 v71, v71, v22\n"
"v_sub_f32 v116, v116, v22\n"
"v_sub_f32 v117, v117, v22\n"
"v_sub_f32 v118, v118, v22\n"
"v_sub_f32 v119, v119, v22\n"
"v_sub_f32 v84, v84, v22\n"
"v_sub_f32 v85, v85, v22\n"
"v_sub_f32 v86, v86, v22\n"
"v_sub_f32 v87, v87, v22\n"
"v_sub_f32 v23, 0, v22\n"
"v_exp_f32 v23, v23\n"
"s_nop 0\n"
"v_mul_f32 v15, v15, v23\n"
"v_sub_f32 v22, 1.0, v23\n"
"v_accvgpr_read_b32 v40, a4\n"
"v_accvgpr_read_b32 v41, a5\n"
"v_accvgpr_read_b32 v42, a6\n"
"v_accvgpr_read_b32 v43, a7\n"
"v_fma_f32 v40, v40, v23, v22\n"
"v_fma_f32 v41, v41, v23, v22\n"
"v_fma_f32 v42, v42, v23, v22\n"
"v_fma_f32 v43, v43, v23, v22\n"
"v_accvgpr_write_b32 a4, v40\n"
"v_accvgpr_write_b32 a5, v41\n"
"v_accvgpr_write_b32 a6, v42\n"
"v_accvgpr_write_b32 a7, v43\n"
"v_accvgpr_read_b32 v40, a20\n"
"v_accvgpr_read_b32 v41, a21\n"
"v_accvgpr_read_b32 v42, a22\n"
"v_accvgpr_read_b32 v43, a23\n"
"v_fma_f32 v40, v40, v23, v22\n"
"v_fma_f32 v41, v41, v23, v22\n"
"v_fma_f32 v42, v42, v23, v22\n"
"v_fma_f32 v43, v43, v23, v22\n"
"v_accvgpr_write_b32 a20, v40\n"
"v_accvgpr_write_b32 a21, v41\n"
"v_accvgpr_write_b32 a22, v42\n"
"v_accvgpr_write_b32 a23, v43\n"
"v_accvgpr_read_b32 v40, a36\n"
"v_accvgpr_read_b32 v41, a37\n"
"v_accvgpr_read_b32 v42, a38\n"
"v_accvgpr_read_b32 v43, a39\n"
"v_fma_f32 v40, v40, v23, v22\n"
"v_fma_f32 v41, v41, v23, v22\n"
"v_fma_f32 v42, v42, v23, v22\n"
"v_fma_f32 v43, v43, v23, v22\n"
"v_accvgpr_write_b32 a36, v40\n"
"v_accvgpr_write_b32 a37, v41\n"
"v_accvgpr_write_b32 a38, v42\n"
"v_accvgpr_write_b32 a39, v43\n"
"v_accvgpr_read_b32 v40, a52\n"
"v_accvgpr_read_b32 v41, a53\n"
"v_accvgpr_read_b32 v42, a54\n"
"v_accvgpr_read_b32 v43, a55\n"
"v_fma_f32 v40, v40, v23, v22\n"
"v_fma_f32 v41, v41, v23, v22\n"
"v_fma_f32 v42, v42, v23, v22\n"
"v_fma_f32 v43, v43, v23, v22\n"
"v_accvgpr_write_b32 a52, v40\n"
"v_accvgpr_write_b32 a53, v41\n"
"v_accvgpr_write_b32 a54, v42\n"
"v_accvgpr_write_b32 a55, v43\n"
"v_accvgpr_read_b32 v40, a68\n"
"v_accvgpr_read_b32 v41, a69\n"
"v_accvgpr_read_b32 v42, a70\n"
"v_accvgpr_read_b32 v43, a71\n"
"v_fma_f32 v40, v40, v23, v22\n"
"v_fma_f32 v41, v41, v23, v22\n"
"v_fma_f32 v42, v42, v23, v22\n"
"v_fma_f32 v43, v43, v23, v22\n"
"v_accvgpr_write_b32 a68, v40\n"
"v_accvgpr_write_b32 a69, v41\n"
"v_accvgpr_write_b32 a70, v42\n"
"v_accvgpr_write_b32 a71, v43\n"
"v_accvgpr_read_b32 v40, a84\n"
"v_accvgpr_read_b32 v41, a85\n"
"v_accvgpr_read_b32 v42, a86\n"
"v_accvgpr_read_b32 v43, a87\n"
"v_fma_f32 v40, v40, v23, v22\n"
"v_fma_f32 v41, v41, v23, v22\n"
"v_fma_f32 v42, v42, v23, v22\n"
"v_fma_f32 v43, v43, v23, v22\n"
"v_accvgpr_write_b32 a84, v40\n"
"v_accvgpr_write_b32 a85, v41\n"
"v_accvgpr_write_b32 a86, v42\n"
"v_accvgpr_write_b32 a87, v43\n"
"v_accvgpr_read_b32 v40, a100\n"
"v_accvgpr_read_b32 v41, a101\n"
"v_accvgpr_read_b32 v42, a102\n"
"v_accvgpr_read_b32 v43, a103\n"
"v_fma_f32 v40, v40, v23, v22\n"
"v_fma_f32 v41, v41, v23, v22\n"
"v_fma_f32 v42, v42, v23, v22\n"
"v_fma_f32 v43, v43, v23, v22\n"
"v_accvgpr_write_b32 a100, v40\n"
"v_accvgpr_write_b32 a101, v41\n"
"v_accvgpr_write_b32 a102, v42\n"
"v_accvgpr_write_b32 a103, v43\n"
"v_accvgpr_read_b32 v40, a116\n"
"v_accvgpr_read_b32 v41, a117\n"
"v_accvgpr_read_b32 v42, a118\n"
"v_accvgpr_read_b32 v43, a119\n"
"v_fma_f32 v40, v40, v23, v22\n"
"v_fma_f32 v41, v41, v23, v22\n"
"v_fma_f32 v42, v42, v23, v22\n"
"v_fma_f32 v43, v43, v23, v22\n"
"v_accvgpr_write_b32 a116, v40\n"
"v_accvgpr_write_b32 a117, v41\n"
"v_accvgpr_write_b32 a118, v42\n"
"v_accvgpr_write_b32 a119, v43\n"
"v_accvgpr_read_b32 v40, a132\n"
"v_accvgpr_read_b32 v41, a133\n"
"v_accvgpr_read_b32 v42, a134\n"
"v_accvgpr_read_b32 v43, a135\n"
"v_fma_f32 v40, v40, v23, v22\n"
"v_fma_f32 v41, v41, v23, v22\n"
"v_fma_f32 v42, v42, v23, v22\n"
"v_fma_f32 v43, v43, v23, v22\n"
"v_accvgpr_write_b32 a132, v40\n"
"v_accvgpr_write_b32 a133, v41\n"
"v_accvgpr_write_b32 a134, v42\n"
"v_accvgpr_write_b32 a135, v43\n"
"v_accvgpr_read_b32 v40, a148\n"
"v_accvgpr_read_b32 v41, a149\n"
"v_accvgpr_read_b32 v42, a150\n"
"v_accvgpr_read_b32 v43, a151\n"
"v_fma_f32 v40, v40, v23, v22\n"
"v_fma_f32 v41, v41, v23, v22\n"
"v_fma_f32 v42, v42, v23, v22\n"
"v_fma_f32 v43, v43, v23, v22\n"
"v_accvgpr_write_b32 a148, v40\n"
"v_accvgpr_write_b32 a149, v41\n"
"v_accvgpr_write_b32 a150, v42\n"
"v_accvgpr_write_b32 a151, v43\n"
"v_accvgpr_read_b32 v40, a164\n"
"v_accvgpr_read_b32 v41, a165\n"
"v_accvgpr_read_b32 v42, a166\n"
"v_accvgpr_read_b32 v43, a167\n"
"v_fma_f32 v40, v40, v23, v22\n"
"v_fma_f32 v41, v41, v23, v22\n"
"v_fma_f32 v42, v42, v23, v22\n"
"v_fma_f32 v43, v43, v23, v22\n"
"v_accvgpr_write_b32 a164, v40\n"
"v_accvgpr_write_b32 a165, v41\n"
"v_accvgpr_write_b32 a166, v42\n"
"v_accvgpr_write_b32 a167, v43\n"
"v_accvgpr_read_b32 v40, a180\n"
"v_accvgpr_read_b32 v41, a181\n"
"v_accvgpr_read_b32 v42, a182\n"
"v_accvgpr_read_b32 v43, a183\n"
"v_fma_f32 v40, v40, v23, v22\n"
"v_fma_f32 v41, v41, v23, v22\n"
"v_fma_f32 v42, v42, v23, v22\n"
"v_fma_f32 v43, v43, v23, v22\n"
"v_accvgpr_write_b32 a180, v40\n"
"v_accvgpr_write_b32 a181, v41\n"
"v_accvgpr_write_b32 a182, v42\n"
"v_accvgpr_write_b32 a183, v43\n"
"v_max_f32 v22, v104, v105\n"
"v_max_f32 v22, v22, v106\n"
"v_max_f32 v22, v22, v107\n"
"v_max_f32 v22, v22, v72\n"
"v_max_f32 v22, v22, v73\n"
"v_max_f32 v22, v22, v74\n"
"v_max_f32 v22, v22, v75\n"
"v_max_f32 v22, v22, v120\n"
"v_max_f32 v22, v22, v121\n"
"v_max_f32 v22, v22, v122\n"
"v_max_f32 v22, v22, v123\n"
"v_max_f32 v22, v22, v88\n"
"v_max_f32 v22, v22, v89\n"
"v_max_f32 v22, v22, v90\n"
"v_max_f32 v22, v22, v91\n"
"v_sub_f32 v22, v22, v24\n"
"v_max_f32 v22, s50, v22\n"
"v_mov_b32 v23, v22\n"
"s_nop 1\n"
"v_permlane32_swap_b32 v22, v23\n"
"v_max_f32 v22, v22, v23\n"
"v_mov_b32 v23, v22\n"
"s_nop 1\n"
"v_permlane16_swap_b32 v22, v23\n"
"v_max_f32 v22, v22, v23\n"
"v_sub_f32 v56, v56, v22\n"
"v_sub_f32 v57, v57, v22\n"
"v_sub_f32 v58, v58, v22\n"
"v_sub_f32 v59, v59, v22\n"
"v_sub_f32 v104, v104, v22\n"
"v_sub_f32 v105, v105, v22\n"
"v_sub_f32 v106, v106, v22\n"
"v_sub_f32 v107, v107, v22\n"
"v_sub_f32 v72, v72, v22\n"
"v_sub_f32 v73, v73, v22\n"
"v_sub_f32 v74, v74, v22\n"
"v_sub_f32 v75, v75, v22\n"
"v_sub_f32 v120, v120, v22\n"
"v_sub_f32 v121, v121, v22\n"
"v_sub_f32 v122, v122, v22\n"
"v_sub_f32 v123, v123, v22\n"
"v_sub_f32 v88, v88, v22\n"
"v_sub_f32 v89, v89, v22\n"
"v_sub_f32 v90, v90, v22\n"
"v_sub_f32 v91, v91, v22\n"
"v_sub_f32 v23, 0, v22\n"
"v_exp_f32 v23, v23\n"
"s_nop 0\n"
"v_mul_f32 v16, v16, v23\n"
"v_sub_f32 v22, 1.0, v23\n"
"v_accvgpr_read_b32 v40, a8\n"
"v_accvgpr_read_b32 v41, a9\n"
"v_accvgpr_read_b32 v42, a10\n"
"v_accvgpr_read_b32 v43, a11\n"
"v_fma_f32 v40, v40, v23, v22\n"
"v_fma_f32 v41, v41, v23, v22\n"
"v_fma_f32 v42, v42, v23, v22\n"
"v_fma_f32 v43, v43, v23, v22\n"
"v_accvgpr_write_b32 a8, v40\n"
"v_accvgpr_write_b32 a9, v41\n"
"v_accvgpr_write_b32 a10, v42\n"
"v_accvgpr_write_b32 a11, v43\n"
"v_accvgpr_read_b32 v40, a24\n"
"v_accvgpr_read_b32 v41, a25\n"
"v_accvgpr_read_b32 v42, a26\n"
"v_accvgpr_read_b32 v43, a27\n"
"v_fma_f32 v40, v40, v23, v22\n"
"v_fma_f32 v41, v41, v23, v22\n"
"v_fma_f32 v42, v42, v23, v22\n"
"v_fma_f32 v43, v43, v23, v22\n"
"v_accvgpr_write_b32 a24, v40\n"
"v_accvgpr_write_b32 a25, v41\n"
"v_accvgpr_write_b32 a26, v42\n"
"v_accvgpr_write_b32 a27, v43\n"
"v_accvgpr_read_b32 v40, a40\n"
"v_accvgpr_read_b32 v41, a41\n"
"v_accvgpr_read_b32 v42, a42\n"
"v_accvgpr_read_b32 v43, a43\n"
"v_fma_f32 v40, v40, v23, v22\n"
"v_fma_f32 v41, v41, v23, v22\n"
"v_fma_f32 v42, v42, v23, v22\n"
"v_fma_f32 v43, v43, v23, v22\n"
"v_accvgpr_write_b32 a40, v40\n"
"v_accvgpr_write_b32 a41, v41\n"
"v_accvgpr_write_b32 a42, v42\n"
"v_accvgpr_write_b32 a43, v43\n"
"v_accvgpr_read_b32 v40, a56\n"
"v_accvgpr_read_b32 v41, a57\n"
"v_accvgpr_read_b32 v42, a58\n"
"v_accvgpr_read_b32 v43, a59\n"
"v_fma_f32 v40, v40, v23, v22\n"
"v_fma_f32 v41, v41, v23, v22\n"
"v_fma_f32 v42, v42, v23, v22\n"
"v_fma_f32 v43, v43, v23, v22\n"
"v_accvgpr_write_b32 a56, v40\n"
"v_accvgpr_write_b32 a57, v41\n"
"v_accvgpr_write_b32 a58, v42\n"
"v_accvgpr_write_b32 a59, v43\n"
"v_accvgpr_read_b32 v40, a72\n"
"v_accvgpr_read_b32 v41, a73\n"
"v_accvgpr_read_b32 v42, a74\n"
"v_accvgpr_read_b32 v43, a75\n"
"v_fma_f32 v40, v40, v23, v22\n"
"v_fma_f32 v41, v41, v23, v22\n"
"v_fma_f32 v42, v42, v23, v22\n"
"v_fma_f32 v43, v43, v23, v22\n"
"v_accvgpr_write_b32 a72, v40\n"
"v_accvgpr_write_b32 a73, v41\n"
"v_accvgpr_write_b32 a74, v42\n"
"v_accvgpr_write_b32 a75, v43\n"
"v_accvgpr_read_b32 v40, a88\n"
"v_accvgpr_read_b32 v41, a89\n"
"v_accvgpr_read_b32 v42, a90\n"
"v_accvgpr_read_b32 v43, a91\n"
"v_fma_f32 v40, v40, v23, v22\n"
"v_fma_f32 v41, v41, v23, v22\n"
"v_fma_f32 v42, v42, v23, v22\n"
"v_fma_f32 v43, v43, v23, v22\n"
"v_accvgpr_write_b32 a88, v40\n"
"v_accvgpr_write_b32 a89, v41\n"
"v_accvgpr_write_b32 a90, v42\n"
"v_accvgpr_write_b32 a91, v43\n"
"v_accvgpr_read_b32 v40, a104\n"
"v_accvgpr_read_b32 v41, a105\n"
"v_accvgpr_read_b32 v42, a106\n"
"v_accvgpr_read_b32 v43, a107\n"
"v_fma_f32 v40, v40, v23, v22\n"
"v_fma_f32 v41, v41, v23, v22\n"
"v_fma_f32 v42, v42, v23, v22\n"
"v_fma_f32 v43, v43, v23, v22\n"
"v_accvgpr_write_b32 a104, v40\n"
"v_accvgpr_write_b32 a105, v41\n"
"v_accvgpr_write_b32 a106, v42\n"
"v_accvgpr_write_b32 a107, v43\n"
"v_accvgpr_read_b32 v40, a120\n"
"v_accvgpr_read_b32 v41, a121\n"
"v_accvgpr_read_b32 v42, a122\n"
"v_accvgpr_read_b32 v43, a123\n"
"v_fma_f32 v40, v40, v23, v22\n"
"v_fma_f32 v41, v41, v23, v22\n"
"v_fma_f32 v42, v42, v23, v22\n"
"v_fma_f32 v43, v43, v23, v22\n"
"v_accvgpr_write_b32 a120, v40\n"
"v_accvgpr_write_b32 a121, v41\n"
"v_accvgpr_write_b32 a122, v42\n"
"v_accvgpr_write_b32 a123, v43\n"
"v_accvgpr_read_b32 v40, a136\n"
"v_accvgpr_read_b32 v41, a137\n"
"v_accvgpr_read_b32 v42, a138\n"
"v_accvgpr_read_b32 v43, a139\n"
"v_fma_f32 v40, v40, v23, v22\n"
"v_fma_f32 v41, v41, v23, v22\n"
"v_fma_f32 v42, v42, v23, v22\n"
"v_fma_f32 v43, v43, v23, v22\n"
"v_accvgpr_write_b32 a136, v40\n"
"v_accvgpr_write_b32 a137, v41\n"
"v_accvgpr_write_b32 a138, v42\n"
"v_accvgpr_write_b32 a139, v43\n"
"v_accvgpr_read_b32 v40, a152\n"
"v_accvgpr_read_b32 v41, a153\n"
"v_accvgpr_read_b32 v42, a154\n"
"v_accvgpr_read_b32 v43, a155\n"
"v_fma_f32 v40, v40, v23, v22\n"
"v_fma_f32 v41, v41, v23, v22\n"
"v_fma_f32 v42, v42, v23, v22\n"
"v_fma_f32 v43, v43, v23, v22\n"
"v_accvgpr_write_b32 a152, v40\n"
"v_accvgpr_write_b32 a153, v41\n"
"v_accvgpr_write_b32 a154, v42\n"
"v_accvgpr_write_b32 a155, v43\n"
"v_accvgpr_read_b32 v40, a168\n"
"v_accvgpr_read_b32 v41, a169\n"
"v_accvgpr_read_b32 v42, a170\n"
"v_accvgpr_read_b32 v43, a171\n"
"v_fma_f32 v40, v40, v23, v22\n"
"v_fma_f32 v41, v41, v23, v22\n"
"v_fma_f32 v42, v42, v23, v22\n"
"v_fma_f32 v43, v43, v23, v22\n"
"v_accvgpr_write_b32 a168, v40\n"
"v_accvgpr_write_b32 a169, v41\n"
"v_accvgpr_write_b32 a170, v42\n"
"v_accvgpr_write_b32 a171, v43\n"
"v_accvgpr_read_b32 v40, a184\n"
"v_accvgpr_read_b32 v41, a185\n"
"v_accvgpr_read_b32 v42, a186\n"
"v_accvgpr_read_b32 v43, a187\n"
"v_fma_f32 v40, v40, v23, v22\n"
"v_fma_f32 v41, v41, v23, v22\n"
"v_fma_f32 v42, v42, v23, v22\n"
"v_fma_f32 v43, v43, v23, v22\n"
"v_accvgpr_write_b32 a184, v40\n"
"v_accvgpr_write_b32 a185, v41\n"
"v_accvgpr_write_b32 a186, v42\n"
"v_accvgpr_write_b32 a187, v43\n"
"v_max_f32 v22, v108, v109\n"
"v_max_f32 v22, v22, v110\n"
"v_max_f32 v22, v22, v111\n"
"v_max_f32 v22, v22, v76\n"
"v_max_f32 v22, v22, v77\n"
"v_max_f32 v22, v22, v78\n"
"v_max_f32 v22, v22, v79\n"
"v_max_f32 v22, v22, v124\n"
"v_max_f32 v22, v22, v125\n"
"v_max_f32 v22, v22, v126\n"
"v_max_f32 v22, v22, v127\n"
"v_max_f32 v22, v22, v92\n"
"v_max_f32 v22, v22, v93\n"
"v_max_f32 v22, v22, v94\n"
"v_max_f32 v22, v22, v95\n"
"v_sub_f32 v22, v22, v24\n"
"v_max_f32 v22, s50, v22\n"
"v_mov_b32 v23, v22\n"
"s_nop 1\n"
"v_permlane32_swap_b32 v22, v23\n"
"v_max_f32 v22, v22, v23\n"
"v_mov_b32 v23, v22\n"
"s_nop 1\n"
"v_permlane16_swap_b32 v22, v23\n"
"v_max_f32 v22, v22, v23\n"
"v_sub_f32 v60, v60, v22\n"
"v_sub_f32 v61, v61, v22\n"
"v_sub_f32 v62, v62, v22\n"
"v_sub_f32 v63, v63, v22\n"
"v_sub_f32 v108, v108, v22\n"
"v_sub_f32 v109, v109, v22\n"
"v_sub_f32 v110, v110, v22\n"
"v_sub_f32 v111, v111, v22\n"
"v_sub_f32 v76, v76, v22\n"
"v_sub_f32 v77, v77, v22\n"
"v_sub_f32 v78, v78, v22\n"
"v_sub_f32 v79, v79, v22\n"
"v_sub_f32 v124, v124, v22\n"
"v_sub_f32 v125, v125, v22\n"
"v_sub_f32 v126, v126, v22\n"
"v_sub_f32 v127, v127, v22\n"
"v_sub_f32 v92, v92, v22\n"
"v_sub_f32 v93, v93, v22\n"
"v_sub_f32 v94, v94, v22\n"
"v_sub_f32 v95, v95, v22\n"
"v_sub_f32 v23, 0, v22\n"
"v_exp_f32 v23, v23\n"
"s_nop 0\n"
"v_mul_f32 v17, v17, v23\n"
"v_sub_f32 v22, 1.0, v23\n"
"v_accvgpr_read_b32 v40, a12\n"
"v_accvgpr_read_b32 v41, a13\n"
"v_accvgpr_read_b32 v42, a14\n"
"v_accvgpr_read_b32 v43, a15\n"
"v_fma_f32 v40, v40, v23, v22\n"
"v_fma_f32 v41, v41, v23, v22\n"
"v_fma_f32 v42, v42, v23, v22\n"
"v_fma_f32 v43, v43, v23, v22\n"
"v_accvgpr_write_b32 a12, v40\n"
"v_accvgpr_write_b32 a13, v41\n"
"v_accvgpr_write_b32 a14, v42\n"
"v_accvgpr_write_b32 a15, v43\n"
"v_accvgpr_read_b32 v40, a28\n"
"v_accvgpr_read_b32 v41, a29\n"
"v_accvgpr_read_b32 v42, a30\n"
"v_accvgpr_read_b32 v43, a31\n"
"v_fma_f32 v40, v40, v23, v22\n"
"v_fma_f32 v41, v41, v23, v22\n"
"v_fma_f32 v42, v42, v23, v22\n"
"v_fma_f32 v43, v43, v23, v22\n"
"v_accvgpr_write_b32 a28, v40\n"
"v_accvgpr_write_b32 a29, v41\n"
"v_accvgpr_write_b32 a30, v42\n"
"v_accvgpr_write_b32 a31, v43\n"
"v_accvgpr_read_b32 v40, a44\n"
"v_accvgpr_read_b32 v41, a45\n"
"v_accvgpr_read_b32 v42, a46\n"
"v_accvgpr_read_b32 v43, a47\n"
"v_fma_f32 v40, v40, v23, v22\n"
"v_fma_f32 v41, v41, v23, v22\n"
"v_fma_f32 v42, v42, v23, v22\n"
"v_fma_f32 v43, v43, v23, v22\n"
"v_accvgpr_write_b32 a44, v40\n"
"v_accvgpr_write_b32 a45, v41\n"
"v_accvgpr_write_b32 a46, v42\n"
"v_accvgpr_write_b32 a47, v43\n"
"v_accvgpr_read_b32 v40, a60\n"
"v_accvgpr_read_b32 v41, a61\n"
"v_accvgpr_read_b32 v42, a62\n"
"v_accvgpr_read_b32 v43, a63\n"
"v_fma_f32 v40, v40, v23, v22\n"
"v_fma_f32 v41, v41, v23, v22\n"
"v_fma_f32 v42, v42, v23, v22\n"
"v_fma_f32 v43, v43, v23, v22\n"
"v_accvgpr_write_b32 a60, v40\n"
"v_accvgpr_write_b32 a61, v41\n"
"v_accvgpr_write_b32 a62, v42\n"
"v_accvgpr_write_b32 a63, v43\n"
"v_accvgpr_read_b32 v40, a76\n"
"v_accvgpr_read_b32 v41, a77\n"
"v_accvgpr_read_b32 v42, a78\n"
"v_accvgpr_read_b32 v43, a79\n"
"v_fma_f32 v40, v40, v23, v22\n"
"v_fma_f32 v41, v41, v23, v22\n"
"v_fma_f32 v42, v42, v23, v22\n"
"v_fma_f32 v43, v43, v23, v22\n"
"v_accvgpr_write_b32 a76, v40\n"
"v_accvgpr_write_b32 a77, v41\n"
"v_accvgpr_write_b32 a78, v42\n"
"v_accvgpr_write_b32 a79, v43\n"
"v_accvgpr_read_b32 v40, a92\n"
"v_accvgpr_read_b32 v41, a93\n"
"v_accvgpr_read_b32 v42, a94\n"
"v_accvgpr_read_b32 v43, a95\n"
"v_fma_f32 v40, v40, v23, v22\n"
"v_fma_f32 v41, v41, v23, v22\n"
"v_fma_f32 v42, v42, v23, v22\n"
"v_fma_f32 v43, v43, v23, v22\n"
"v_accvgpr_write_b32 a92, v40\n"
"v_accvgpr_write_b32 a93, v41\n"
"v_accvgpr_write_b32 a94, v42\n"
"v_accvgpr_write_b32 a95, v43\n"
"v_accvgpr_read_b32 v40, a108\n"
"v_accvgpr_read_b32 v41, a109\n"
"v_accvgpr_read_b32 v42, a110\n"
"v_accvgpr_read_b32 v43, a111\n"
"v_fma_f32 v40, v40, v23, v22\n"
"v_fma_f32 v41, v41, v23, v22\n"
"v_fma_f32 v42, v42, v23, v22\n"
"v_fma_f32 v43, v43, v23, v22\n"
"v_accvgpr_write_b32 a108, v40\n"
"v_accvgpr_write_b32 a109, v41\n"
"v_accvgpr_write_b32 a110, v42\n"
"v_accvgpr_write_b32 a111, v43\n"
"v_accvgpr_read_b32 v40, a124\n"
"v_accvgpr_read_b32 v41, a125\n"
"v_accvgpr_read_b32 v42, a126\n"
"v_accvgpr_read_b32 v43, a127\n"
"v_fma_f32 v40, v40, v23, v22\n"
"v_fma_f32 v41, v41, v23, v22\n"
"v_fma_f32 v42, v42, v23, v22\n"
"v_fma_f32 v43, v43, v23, v22\n"
"v_accvgpr_write_b32 a124, v40\n"
"v_accvgpr_write_b32 a125, v41\n"
"v_accvgpr_write_b32 a126, v42\n"
"v_accvgpr_write_b32 a127, v43\n"
"v_accvgpr_read_b32 v40, a140\n"
"v_accvgpr_read_b32 v41, a141\n"
"v_accvgpr_read_b32 v42, a142\n"
"v_accvgpr_read_b32 v43, a143\n"
"v_fma_f32 v40, v40, v23, v22\n"
"v_fma_f32 v41, v41, v23, v22\n"
"v_fma_f32 v42, v42, v23, v22\n"
"v_fma_f32 v43, v43, v23, v22\n"
"v_accvgpr_write_b32 a140, v40\n"
"v_accvgpr_write_b32 a141, v41\n"
"v_accvgpr_write_b32 a142, v42\n"
"v_accvgpr_write_b32 a143, v43\n"
"v_accvgpr_read_b32 v40, a156\n"
"v_accvgpr_read_b32 v41, a157\n"
"v_accvgpr_read_b32 v42, a158\n"
"v_accvgpr_read_b32 v43, a159\n"
"v_fma_f32 v40, v40, v23, v22\n"
"v_fma_f32 v41, v41, v23, v22\n"
"v_fma_f32 v42, v42, v23, v22\n"
"v_fma_f32 v43, v43, v23, v22\n"
"v_accvgpr_write_b32 a156, v40\n"
"v_accvgpr_write_b32 a157, v41\n"
"v_accvgpr_write_b32 a158, v42\n"
"v_accvgpr_write_b32 a159, v43\n"
"v_accvgpr_read_b32 v40, a172\n"
"v_accvgpr_read_b32 v41, a173\n"
"v_accvgpr_read_b32 v42, a174\n"
"v_accvgpr_read_b32 v43, a175\n"
"v_fma_f32 v40, v40, v23, v22\n"
"v_fma_f32 v41, v41, v23, v22\n"
"v_fma_f32 v42, v42, v23, v22\n"
"v_fma_f32 v43, v43, v23, v22\n"
"v_accvgpr_write_b32 a172, v40\n"
"v_accvgpr_write_b32 a173, v41\n"
"v_accvgpr_write_b32 a174, v42\n"
"v_accvgpr_write_b32 a175, v43\n"
"v_accvgpr_read_b32 v40, a188\n"
"v_accvgpr_read_b32 v41, a189\n"
"v_accvgpr_read_b32 v42, a190\n"
"v_accvgpr_read_b32 v43, a191\n"
"v_fma_f32 v40, v40, v23, v22\n"
"v_fma_f32 v41, v41, v23, v22\n"
"v_fma_f32 v42, v42, v23, v22\n"
"v_fma_f32 v43, v43, v23, v22\n"
"v_accvgpr_write_b32 a188, v40\n"
"v_accvgpr_write_b32 a189, v41\n"
"v_accvgpr_write_b32 a190, v42\n"
"v_accvgpr_write_b32 a191, v43\n"
"v_exp_f32 v96, v96\n"
"v_exp_f32 v97, v97\n"
"v_exp_f32 v98, v98\n"
"v_exp_f32 v99, v99\n"
"v_add_f32 v18, v96, v97\n"
"v_cvt_pk_bf16_f32 v96, v96, v97\n"
"v_add_f32 v18, v18, v98\n"
"v_add_f32 v18, v18, v99\n"
"v_cvt_pk_bf16_f32 v97, v98, v99\n"
"v_exp_f32 v100, v100\n"
"v_exp_f32 v101, v101\n"
"v_exp_f32 v102, v102\n"
"v_exp_f32 v103, v103\n"
"v_add_f32 v19, v100, v101\n"
"v_cvt_pk_bf16_f32 v100, v100, v101\n"
"v_add_f32 v19, v19, v102\n"
"v_add_f32 v19, v19, v103\n"
"v_cvt_pk_bf16_f32 v101, v102, v103\n"
"v_exp_f32 v104, v104\n"
"v_exp_f32 v105, v105\n"
"v_exp_f32 v106, v106\n"
"v_exp_f32 v107, v107\n"
"v_add_f32 v20, v104, v105\n"
"v_cvt_pk_bf16_f32 v104, v104, v105\n"
"v_add_f32 v20, v20, v106\n"
"v_add_f32 v20, v20, v107\n"
"v_cvt_pk_bf16_f32 v105, v106, v107\n"
"v_exp_f32 v108, v108\n"
"v_exp_f32 v109, v109\n"
"v_exp_f32 v110, v110\n"
"v_exp_f32 v111, v111\n"
"v_add_f32 v21, v108, v109\n"
"v_cvt_pk_bf16_f32 v108, v108, v109\n"
"v_add_f32 v21, v21, v110\n"
"v_add_f32 v21, v21, v111\n"
"v_cvt_pk_bf16_f32 v109, v110, v111\n"
"v_exp_f32 v64, v64\n"
"v_exp_f32 v65, v65\n"
"v_exp_f32 v66, v66\n"
"v_exp_f32 v67, v67\n"
"v_add_f32 v18, v18, v64\n"
"v_add_f32 v18, v18, v65\n"
"v_cvt_pk_bf16_f32 v98, v64, v65\n"
"v_add_f32 v18, v18, v66\n"
"v_add_f32 v18, v18, v67\n"
"v_cvt_pk_bf16_f32 v99, v66, v67\n"
"v_exp_f32 v68, v68\n"
"v_exp_f32 v69, v69\n"
"v_exp_f32 v70, v70\n"
"v_exp_f32 v71, v71\n"
"v_add_f32 v19, v19, v68\n"
"v_add_f32 v19, v19, v69\n"
"v_cvt_pk_bf16_f32 v102, v68, v69\n"
"v_add_f32 v19, v19, v70\n"
"v_add_f32 v19, v19, v71\n"
"v_cvt_pk_bf16_f32 v103, v70, v71\n"
"v_exp_f32 v72, v72\n"
"v_exp_f32 v73, v73\n"
"v_exp_f32 v74, v74\n"
"v_exp_f32 v75, v75\n"
"v_add_f32 v20, v20, v72\n"
"v_add_f32 v20, v20, v73\n"
"v_cvt_pk_bf16_f32 v106, v72, v73\n"
"v_add_f32 v20, v20, v74\n"
"v_add_f32 v20, v20, v75\n"
"v_cvt_pk_bf16_f32 v107, v74, v75\n"
"v_exp_f32 v76, v76\n"
"v_exp_f32 v77, v77\n"
"v_exp_f32 v78, v78\n"
"v_exp_f32 v79, v79\n"
"v_add_f32 v21, v21, v76\n"
"v_add_f32 v21, v21, v77\n"
"v_cvt_pk_bf16_f32 v110, v76, v77\n"
"v_add_f32 v21, v21, v78\n"
"v_add_f32 v21, v21, v79\n"
"v_cvt_pk_bf16_f32 v111, v78, v79\n"
"v_exp_f32 v112, v112\n"
"v_exp_f32 v113, v113\n"
"v_exp_f32 v114, v114\n"
"v_exp_f32 v115, v115\n"
"v_add_f32 v18, v18, v112\n"
"v_add_f32 v18, v18, v113\n"
"v_cvt_pk_bf16_f32 v112, v112, v113\n"
"v_add_f32 v18, v18, v114\n"
"v_add_f32 v18, v18, v115\n"
"v_cvt_pk_bf16_f32 v113, v114, v115\n"
"v_exp_f32 v116, v116\n"
"v_exp_f32 v117, v117\n"
"v_exp_f32 v118, v118\n"
"v_exp_f32 v119, v119\n"
"v_add_f32 v19, v19, v116\n"
"v_add_f32 v19, v19, v117\n"
"v_cvt_pk_bf16_f32 v116, v116, v117\n"
"v_add_f32 v19, v19, v118\n"
"v_add_f32 v19, v19, v119\n"
"v_cvt_pk_bf16_f32 v117, v118, v119\n"
"v_exp_f32 v120, v120\n"
"v_exp_f32 v121, v121\n"
"v_exp_f32 v122, v122\n"
"v_exp_f32 v123, v123\n"
"v_add_f32 v20, v20, v120\n"
"v_add_f32 v20, v20, v121\n"
"v_cvt_pk_bf16_f32 v120, v120, v121\n"
"v_add_f32 v20, v20, v122\n"
"v_add_f32 v20, v20, v123\n"
"v_cvt_pk_bf16_f32 v121, v122, v123\n"
"v_exp_f32 v124, v124\n"
"v_exp_f32 v125, v125\n"
"v_exp_f32 v126, v126\n"
"v_exp_f32 v127, v127\n"
"v_add_f32 v21, v21, v124\n"
"v_add_f32 v21, v21, v125\n"
"v_cvt_pk_bf16_f32 v124, v124, v125\n"
"v_add_f32 v21, v21, v126\n"
"v_add_f32 v21, v21, v127\n"
"v_cvt_pk_bf16_f32 v125, v126, v127\n"
"v_exp_f32 v80, v80\n"
"v_exp_f32 v81, v81\n"
"v_exp_f32 v82, v82\n"
"v_exp_f32 v83, v83\n"
"v_add_f32 v18, v18, v80\n"
"v_add_f32 v18, v18, v81\n"
"v_cvt_pk_bf16_f32 v114, v80, v81\n"
"v_add_f32 v18, v18, v82\n"
"v_add_f32 v18, v18, v83\n"
"v_cvt_pk_bf16_f32 v115, v82, v83\n"
"v_exp_f32 v84, v84\n"
"v_exp_f32 v85, v85\n"
"v_exp_f32 v86, v86\n"
"v_exp_f32 v87, v87\n"
"v_add_f32 v19, v19, v84\n"
"v_add_f32 v19, v19, v85\n"
"v_cvt_pk_bf16_f32 v118, v84, v85\n"
"v_add_f32 v19, v19, v86\n"
"v_add_f32 v19, v19, v87\n"
"v_cvt_pk_bf16_f32 v119, v86, v87\n"
"v_exp_f32 v88, v88\n"
"v_exp_f32 v89, v89\n"
"v_exp_f32 v90, v90\n"
"v_exp_f32 v91, v91\n"
"v_add_f32 v20, v20, v88\n"
"v_add_f32 v20, v20, v89\n"
"v_cvt_pk_bf16_f32 v122, v88, v89\n"
"v_add_f32 v20, v20, v90\n"
"v_add_f32 v20, v20, v91\n"
"v_cvt_pk_bf16_f32 v123, v90, v91\n"
"v_exp_f32 v92, v92\n"
"v_exp_f32 v93, v93\n"
"v_exp_f32 v94, v94\n"
"v_exp_f32 v95, v95\n"
"v_add_f32 v21, v21, v92\n"
"v_add_f32 v21, v21, v93\n"
"v_cvt_pk_bf16_f32 v126, v92, v93\n"
"v_add_f32 v21, v21, v94\n"
"v_add_f32 v21, v21, v95\n"
"v_cvt_pk_bf16_f32 v127, v94, v95\n"
"s_nop 3\n"
"s_add_u32 s52, s52, 4\n"
"s_addc_u32 s53, s53, 0\n"
"s_setpc_b64 s[52:53]\n"
"L_rare1:\n"
"s_nop 7\n"
"s_nop 7\n"
"s_add_u32 s50, s55, 0\n"
"s_add_u32 s50, s50, s63\n"
"s_and_b32 s50, s50, 31\n"
"s_mul_i32 s50, s50, 3072\n"
"s_add_u32 s50, s50, 1024\n"
"v_bfe_u32 v23, v12, 7, 1\n"
"v_lshl_add_u32 v22, v23, 10, s50\n"
"v_lshrrev_b32 v24, 15, v22\n"
"v_mul_u32_u24 v24, 0x18000, v24\n"
"v_add_u32 v22, v22, v24\n"
"v_lshl_add_u32 v22, v23, 15, v22\n"
"v_mul_u32_u24 v23, 5, v23\n"
"v_lshrrev_b32 v24, 4, v12\n"
"v_add3_u32 v23, v23, v24, 0\n"
"v_and_b32 v23, 7, v23\n"
"v_mul_u32_u24 v23, 0x180, v23\n"
"v_and_b32 v24, 48, v24\n"
"v_add3_u32 v22, v22, v23, v24\n"
"s_mov_b32 s50, 0x18000\n"
"buffer_load_dwordx4 v[40:43], v22, s[8:11], s50 offen offset:0\n"
"s_waitcnt vmcnt(0)\n"
"v_mfma_f32_16x16x32_bf16 v[128:131], v[40:43], v[160:163], v[48:51]\n"
"v_mfma_f32_16x16x32_bf16 v[132:135], v[40:43], v[164:167], v[52:55]\n"
"v_mfma_f32_16x16x32_bf16 v[136:139], v[40:43], v[168:171], v[56:59]\n"
"v_mfma_f32_16x16x32_bf16 v[140:143], v[40:43], v[172:175], v[60:63]\n"
"buffer_load_dwordx4 v[44:47], v22, s[8:11], s50 offen offset:64\n"
"s_waitcnt vmcnt(0)\n"
"v_mfma_f32_16x16x32_bf16 v[128:131], v[44:47], v[176:179], v[128:131]\n"
"v_mfma_f32_16x16x32_bf16 v[132:135], v[44:47], v[180:183], v[132:135]\n"
"v_mfma_f32_16x16x32_bf16 v[136:139], v[44:47], v[184:187], v[136:139]\n"
"v_mfma_f32_16x16x32_bf16 v[140:143], v[44:47], v[188:191], v[140:143]\n"
"buffer_load_dwordx4 v[40:43], v22, s[8:11], s50 offen offset:128\n"
"s_waitcnt vmcnt(0)\n"
"v_mfma_f32_16x16x32_bf16 v[128:131], v[40:43], v[192:195], v[128:131]\n"
"v_mfma_f32_16x16x32_bf16 v[132:135], v[40:43], v[196:199], v[132:135]\n"
"v_mfma_f32_16x16x32_bf16 v[136:139], v[40:43], v[200:203], v[136:139]\n"
"v_mfma_f32_16x16x32_bf16 v[140:143], v[40:43], v[204:207], v[140:143]\n"
"buffer_load_dwordx4 v[44:47], v22, s[8:11], s50 offen offset:192\n"
"s_waitcnt vmcnt(0)\n"
"v_mfma_f32_16x16x32_bf16 v[128:131], v[44:47], v[208:211], v[128:131]\n"
"v_mfma_f32_16x16x32_bf16 v[132:135], v[44:47], v[212:215], v[132:135]\n"
"v_mfma_f32_16x16x32_bf16 v[136:139], v[44:47], v[216:219], v[136:139]\n"
"v_mfma_f32_16x16x32_bf16 v[140:143], v[44:47], v[220:223], v[140:143]\n"
"buffer_load_dwordx4 v[40:43], v22, s[8:11], s50 offen offset:256\n"
"s_waitcnt vmcnt(0)\n"
"v_mfma_f32_16x16x32_bf16 v[128:131], v[40:43], v[224:227], v[128:131]\n"
"v_mfma_f32_16x16x32_bf16 v[132:135], v[40:43], v[228:231], v[132:135]\n"
"v_mfma_f32_16x16x32_bf16 v[136:139], v[40:43], v[232:235], v[136:139]\n"
"v_mfma_f32_16x16x32_bf16 v[140:143], v[40:43], v[236:239], v[140:143]\n"
"buffer_load_dwordx4 v[44:47], v22, s[8:11], s50 offen offset:320\n"
"s_waitcnt vmcnt(0)\n"
"v_mfma_f32_16x16x32_bf16 v[128:131], v[44:47], v[240:243], v[128:131]\n"
"v_mfma_f32_16x16x32_bf16 v[132:135], v[44:47], v[244:247], v[132:135]\n"
"v_mfma_f32_16x16x32_bf16 v[136:139], v[44:47], v[248:251], v[136:139]\n"
"v_mfma_f32_16x16x32_bf16 v[140:143], v[44:47], v[252:255], v[140:143]\n"
"s_add_u32 s50, s55, 0\n"
"s_add_u32 s50, s50, s63\n"
"s_and_b32 s50, s50, 31\n"
"s_mul_i32 s50, s50, 3072\n"
"s_add_u32 s50, s50, 0\n"
"v_bfe_u32 v23, v12, 7, 1\n"
"v_lshl_add_u32 v22, v23, 10, s50\n"
"v_lshrrev_b32 v24, 15, v22\n"
"v_mul_u32_u24 v24, 0x18000, v24\n"
"v_add_u32 v22, v22, v24\n"
"v_lshl_add_u32 v22, v23, 15, v22\n"
"v_mul_u32_u24 v23, 5, v23\n"
"v_lshrrev_b32 v24, 4, v12\n"
"v_add3_u32 v23, v23, v24, 3\n"
"v_and_b32 v23, 7, v23\n"
"v_mul_u32_u24 v23, 0x180, v23\n"
"v_and_b32 v24, 48, v24\n"
"v_add3_u32 v22, v22, v23, v24\n"
"s_mov_b32 s50, 0x28000\n"
"buffer_load_dwordx4 v[40:43], v22, s[8:11], s50 offen offset:0\n"
"s_waitcnt vmcnt(0)\n"
"v_mfma_f32_16x16x32_bf16 v[64:67], v[40:43], v[160:163], v[48:51]\n"
"v_mfma_f32_16x16x32_bf16 v[68:71], v[40:43], v[164:167], v[52:55]\n"
"v_mfma_f32_16x16x32_bf16 v[72:75], v[40:43], v[168:171], v[56:59]\n"
"v_mfma_f32_16x16x32_bf16 v[76:79], v[40:43], v[172:175], v[60:63]\n"
"buffer_load_dwordx4 v[44:47], v22, s[8:11], s50 offen offset:64\n"
"s_waitcnt vmcnt(0)\n"
"v_mfma_f32_16x16x32_bf16 v[64:67], v[44:47], v[176:179], v[64:67]\n"
"v_mfma_f32_16x16x32_bf16 v[68:71], v[44:47], v[180:183], v[68:71]\n"
"v_mfma_f32_16x16x32_bf16 v[72:75], v[44:47], v[184:187], v[72:75]\n"
"v_mfma_f32_16x16x32_bf16 v[76:79], v[44:47], v[188:191], v[76:79]\n"
"buffer_load_dwordx4 v[40:43], v22, s[8:11], s50 offen offset:128\n"
"s_waitcnt vmcnt(0)\n"
"v_mfma_f32_16x16x32_bf16 v[64:67], v[40:43], v[192:195], v[64:67]\n"
"v_mfma_f32_16x16x32_bf16 v[68:71], v[40:43], v[196:199], v[68:71]\n"
"v_mfma_f32_16x16x32_bf16 v[72:75], v[40:43], v[200:203], v[72:75]\n"
"v_mfma_f32_16x16x32_bf16 v[76:79], v[40:43], v[204:207], v[76:79]\n"
"buffer_load_dwordx4 v[44:47], v22, s[8:11], s50 offen offset:192\n"
"s_waitcnt vmcnt(0)\n"
"v_mfma_f32_16x16x32_bf16 v[64:67], v[44:47], v[208:211], v[64:67]\n"
"v_mfma_f32_16x16x32_bf16 v[68:71], v[44:47], v[212:215], v[68:71]\n"
"v_mfma_f32_16x16x32_bf16 v[72:75], v[44:47], v[216:219], v[72:75]\n"
"v_mfma_f32_16x16x32_bf16 v[76:79], v[44:47], v[220:223], v[76:79]\n"
"buffer_load_dwordx4 v[40:43], v22, s[8:11], s50 offen offset:256\n"
"s_waitcnt vmcnt(0)\n"
"v_mfma_f32_16x16x32_bf16 v[64:67], v[40:43], v[224:227], v[64:67]\n"
"v_mfma_f32_16x16x32_bf16 v[68:71], v[40:43], v[228:231], v[68:71]\n"
"v_mfma_f32_16x16x32_bf16 v[72:75], v[40:43], v[232:235], v[72:75]\n"
"v_mfma_f32_16x16x32_bf16 v[76:79], v[40:43], v[236:239], v[76:79]\n"
"buffer_load_dwordx4 v[44:47], v22, s[8:11], s50 offen offset:320\n"
"s_waitcnt vmcnt(0)\n"
"v_mfma_f32_16x16x32_bf16 v[64:67], v[44:47], v[240:243], v[64:67]\n"
"v_mfma_f32_16x16x32_bf16 v[68:71], v[44:47], v[244:247], v[68:71]\n"
"v_mfma_f32_16x16x32_bf16 v[72:75], v[44:47], v[248:251], v[72:75]\n"
"v_mfma_f32_16x16x32_bf16 v[76:79], v[44:47], v[252:255], v[76:79]\n"
"s_add_u32 s50, s55, 9\n"
"s_add_u32 s50, s50, s63\n"
"s_and_b32 s50, s50, 31\n"
"s_mul_i32 s50, s50, 3072\n"
"s_add_u32 s50, s50, 1024\n"
"v_bfe_u32 v23, v12, 7, 1\n"
"v_lshl_add_u32 v22, v23, 10, s50\n"
"v_lshrrev_b32 v24, 15, v22\n"
"v_mul_u32_u24 v24, 0x18000, v24\n"
"v_add_u32 v22, v22, v24\n"
"v_lshl_add_u32 v22, v23, 15, v22\n"
"v_mul_u32_u24 v23, 5, v23\n"
"v_lshrrev_b32 v24, 4, v12\n"
"v_add3_u32 v23, v23, v24, 0\n"
"v_and_b32 v23, 7, v23\n"
"v_mul_u32_u24 v23, 0x180, v23\n"
"v_and_b32 v24, 48, v24\n"
"v_add3_u32 v22, v22, v23, v24\n"
"s_mov_b32 s50, 0x18000\n"
"buffer_load_dwordx4 v[40:43], v22, s[8:11], s50 offen offset:0\n"
"s_waitcnt vmcnt(0)\n"
"v_mfma_f32_16x16x32_bf16 v[144:147], v[40:43], v[160:163], v[48:51]\n"
"v_mfma_f32_16x16x32_bf16 v[148:151], v[40:43], v[164:167], v[52:55]\n"
"v_mfma_f32_16x16x32_bf16 v[152:155], v[40:43], v[168:171], v[56:59]\n"
"v_mfma_f32_16x16x32_bf16 v[156:159], v[40:43], v[172:175], v[60:63]\n"
"buffer_load_dwordx4 v[44:47], v22, s[8:11], s50 offen offset:64\n"
"s_waitcnt vmcnt(0)\n"
"v_mfma_f32_16x16x32_bf16 v[144:147], v[44:47], v[176:179], v[144:147]\n"
"v_mfma_f32_16x16x32_bf16 v[148:151], v[44:47], v[180:183], v[148:151]\n"
"v_mfma_f32_16x16x32_bf16 v[152:155], v[44:47], v[184:187], v[152:155]\n"
"v_mfma_f32_16x16x32_bf16 v[156:159], v[44:47], v[188:191], v[156:159]\n"
"buffer_load_dwordx4 v[40:43], v22, s[8:11], s50 offen offset:128\n"
"s_waitcnt vmcnt(0)\n"
"v_mfma_f32_16x16x32_bf16 v[144:147], v[40:43], v[192:195], v[144:147]\n"
"v_mfma_f32_16x16x32_bf16 v[148:151], v[40:43], v[196:199], v[148:151]\n"
"v_mfma_f32_16x16x32_bf16 v[152:155], v[40:43], v[200:203], v[152:155]\n"
"v_mfma_f32_16x16x32_bf16 v[156:159], v[40:43], v[204:207], v[156:159]\n"
"buffer_load_dwordx4 v[44:47], v22, s[8:11], s50 offen offset:192\n"
"s_waitcnt vmcnt(0)\n"
"v_mfma_f32_16x16x32_bf16 v[144:147], v[44:47], v[208:211], v[144:147]\n"
"v_mfma_f32_16x16x32_bf16 v[148:151], v[44:47], v[212:215], v[148:151]\n"
"v_mfma_f32_16x16x32_bf16 v[152:155], v[44:47], v[216:219], v[152:155]\n"
"v_mfma_f32_16x16x32_bf16 v[156:159], v[44:47], v[220:223], v[156:159]\n"
"buffer_load_dwordx4 v[40:43], v22, s[8:11], s50 offen offset:256\n"
"s_waitcnt vmcnt(0)\n"
"v_mfma_f32_16x16x32_bf16 v[144:147], v[40:43], v[224:227], v[144:147]\n"
"v_mfma_f32_16x16x32_bf16 v[148:151], v[40:43], v[228:231], v[148:151]\n"
"v_mfma_f32_16x16x32_bf16 v[152:155], v[40:43], v[232:235], v[152:155]\n"
"v_mfma_f32_16x16x32_bf16 v[156:159], v[40:43], v[236:239], v[156:159]\n"
"buffer_load_dwordx4 v[44:47], v22, s[8:11], s50 offen offset:320\n"
"s_waitcnt vmcnt(0)\n"
"v_mfma_f32_16x16x32_bf16 v[144:147], v[44:47], v[240:243], v[144:147]\n"
"v_mfma_f32_16x16x32_bf16 v[148:151], v[44:47], v[244:247], v[148:151]\n"
"v_mfma_f32_16x16x32_bf16 v[152:155], v[44:47], v[248:251], v[152:155]\n"
"v_mfma_f32_16x16x32_bf16 v[156:159], v[44:47], v[252:255], v[156:159]\n"
"s_add_u32 s50, s55, 9\n"
"s_add_u32 s50, s50, s63\n"
"s_and_b32 s50, s50, 31\n"
"s_mul_i32 s50, s50, 3072\n"
"s_add_u32 s50, s50, 0\n"
"v_bfe_u32 v23, v12, 7, 1\n"
"v_lshl_add_u32 v22, v23, 10, s50\n"
"v_lshrrev_b32 v24, 15, v22\n"
"v_mul_u32_u24 v24, 0x18000, v24\n"
"v_add_u32 v22, v22, v24\n"
"v_lshl_add_u32 v22, v23, 15, v22\n"
"v_mul_u32_u24 v23, 5, v23\n"
"v_lshrrev_b32 v24, 4, v12\n"
"v_add3_u32 v23, v23, v24, 3\n"
"v_and_b32 v23, 7, v23\n"
"v_mul_u32_u24 v23, 0x180, v23\n"
"v_and_b32 v24, 48, v24\n"
"v_add3_u32 v22, v22, v23, v24\n"
"s_mov_b32 s50, 0x28000\n"
"buffer_load_dwordx4 v[40:43], v22, s[8:11], s50 offen offset:0\n"
"s_waitcnt vmcnt(0)\n"
"v_mfma_f32_16x16x32_bf16 v[80:83], v[40:43], v[160:163], v[48:51]\n"
"v_mfma_f32_16x16x32_bf16 v[84:87], v[40:43], v[164:167], v[52:55]\n"
"v_mfma_f32_16x16x32_bf16 v[88:91], v[40:43], v[168:171], v[56:59]\n"
"v_mfma_f32_16x16x32_bf16 v[92:95], v[40:43], v[172:175], v[60:63]\n"
"buffer_load_dwordx4 v[44:47], v22, s[8:11], s50 offen offset:64\n"
"s_waitcnt vmcnt(0)\n"
"v_mfma_f32_16x16x32_bf16 v[80:83], v[44:47], v[176:179], v[80:83]\n"
"v_mfma_f32_16x16x32_bf16 v[84:87], v[44:47], v[180:183], v[84:87]\n"
"v_mfma_f32_16x16x32_bf16 v[88:91], v[44:47], v[184:187], v[88:91]\n"
"v_mfma_f32_16x16x32_bf16 v[92:95], v[44:47], v[188:191], v[92:95]\n"
"buffer_load_dwordx4 v[40:43], v22, s[8:11], s50 offen offset:128\n"
"s_waitcnt vmcnt(0)\n"
"v_mfma_f32_16x16x32_bf16 v[80:83], v[40:43], v[192:195], v[80:83]\n"
"v_mfma_f32_16x16x32_bf16 v[84:87], v[40:43], v[196:199], v[84:87]\n"
"v_mfma_f32_16x16x32_bf16 v[88:91], v[40:43], v[200:203], v[88:91]\n"
"v_mfma_f32_16x16x32_bf16 v[92:95], v[40:43], v[204:207], v[92:95]\n"
"buffer_load_dwordx4 v[44:47], v22, s[8:11], s50 offen offset:192\n"
"s_waitcnt vmcnt(0)\n"
"v_mfma_f32_16x16x32_bf16 v[80:83], v[44:47], v[208:211], v[80:83]\n"
"v_mfma_f32_16x16x32_bf16 v[84:87], v[44:47], v[212:215], v[84:87]\n"
"v_mfma_f32_16x16x32_bf16 v[88:91], v[44:47], v[216:219], v[88:91]\n"
"v_mfma_f32_16x16x32_bf16 v[92:95], v[44:47], v[220:223], v[92:95]\n"
"buffer_load_dwordx4 v[40:43], v22, s[8:11], s50 offen offset:256\n"
"s_waitcnt vmcnt(0)\n"
"v_mfma_f32_16x16x32_bf16 v[80:83], v[40:43], v[224:227], v[80:83]\n"
"v_mfma_f32_16x16x32_bf16 v[84:87], v[40:43], v[228:231], v[84:87]\n"
"v_mfma_f32_16x16x32_bf16 v[88:91], v[40:43], v[232:235], v[88:91]\n"
"v_mfma_f32_16x16x32_bf16 v[92:95], v[40:43], v[236:239], v[92:95]\n"
"buffer_load_dwordx4 v[44:47], v22, s[8:11], s50 offen offset:320\n"
"s_waitcnt vmcnt(0)\n"
"v_mfma_f32_16x16x32_bf16 v[80:83], v[44:47], v[240:243], v[80:83]\n"
"v_mfma_f32_16x16x32_bf16 v[84:87], v[44:47], v[244:247], v[84:87]\n"
"v_mfma_f32_16x16x32_bf16 v[88:91], v[44:47], v[248:251], v[88:91]\n"
"v_mfma_f32_16x16x32_bf16 v[92:95], v[44:47], v[252:255], v[92:95]\n"
"s_nop 7\n"
"s_nop 7\n"
"s_lshr_b32 s50, s54, 23\n"
"s_sub_u32 s50, s50, 153\n"
"v_cvt_f32_i32 v24, s50\n"
"v_max_f32 v22, v128, v129\n"
"v_max_f32 v22, v22, v130\n"
"v_max_f32 v22, v22, v131\n"
"v_max_f32 v22, v22, v64\n"
"v_max_f32 v22, v22, v65\n"
"v_max_f32 v22, v22, v66\n"
"v_max_f32 v22, v22, v67\n"
"v_max_f32 v22, v22, v144\n"
"v_max_f32 v22, v22, v145\n"
"v_max_f32 v22, v22, v146\n"
"v_max_f32 v22, v22, v147\n"
"v_max_f32 v22, v22, v80\n"
"v_max_f32 v22, v22, v81\n"
"v_max_f32 v22, v22, v82\n"
"v_max_f32 v22, v22, v83\n"
"v_sub_f32 v22, v22, v24\n"
"v_max_f32 v22, 0, v22\n"
"v_mov_b32 v23, v22\n"
"s_nop 1\n"
"v_permlane32_swap_b32 v22, v23\n"
"v_max_f32 v22, v22, v23\n"
"v_mov_b32 v23, v22\n"
"s_nop 1\n"
"v_permlane16_swap_b32 v22, v23\n"
"v_max_f32 v22, v22, v23\n"
"v_sub_f32 v48, v48, v22\n"
"v_sub_f32 v49, v49, v22\n"
"v_sub_f32 v50, v50, v22\n"
"v_sub_f32 v51, v51, v22\n"
"v_sub_f32 v128, v128, v22\n"
"v_sub_f32 v129, v129, v22\n"
"v_sub_f32 v130, v130, v22\n"
"v_sub_f32 v131, v131, v22\n"
"v_sub_f32 v64, v64, v22\n"
"v_sub_f32 v65, v65, v22\n"
"v_sub_f32 v66, v66, v22\n"
"v_sub_f32 v67, v67, v22\n"
"v_sub_f32 v144, v144, v22\n"
"v_sub_f32 v145, v145, v22\n"
"v_sub_f32 v146, v146, v22\n"
"v_sub_f32 v147, v147, v22\n"
"v_sub_f32 v80, v80, v22\n"
"v_sub_f32 v81, v81, v22\n"
"v_sub_f32 v82, v82, v22\n"
"v_sub_f32 v83, v83, v22\n"
"v_sub_f32 v23, 0, v22\n"
"v_exp_f32 v23, v23\n"
"s_nop 0\n"
"v_mul_f32 v14, v14, v23\n"
"v_sub_f32 v22, 1.0, v23\n"
"v_accvgpr_read_b32 v40, a0\n"
"v_accvgpr_read_b32 v41, a1\n"
"v_accvgpr_read_b32 v42, a2\n"
"v_accvgpr_read_b32 v43, a3\n"
"v_fma_f32 v40, v40, v23, v22\n"
"v_fma_f32 v41, v41, v23, v22\n"
"v_fma_f32 v42, v42, v23, v22\n"
"v_fma_f32 v43, v43, v23, v22\n"
"v_accvgpr_write_b32 a0, v40\n"
"v_accvgpr_write_b32 a1, v41\n"
"v_accvgpr_write_b32 a2, v42\n"
"v_accvgpr_write_b32 a3, v43\n"
"v_accvgpr_read_b32 v40, a16\n"
"v_accvgpr_read_b32 v41, a17\n"
"v_accvgpr_read_b32 v42, a18\n"
"v_accvgpr_read_b32 v43, a19\n"
"v_fma_f32 v40, v40, v23, v22\n"
"v_fma_f32 v41, v41, v23, v22\n"
"v_fma_f32 v42, v42, v23, v22\n"
"v_fma_f32 v43, v43, v23, v22\n"
"v_accvgpr_write_b32 a16, v40\n"
"v_accvgpr_write_b32 a17, v41\n"
"v_accvgpr_write_b32 a18, v42\n"
"v_accvgpr_write_b32 a19, v43\n"
"v_accvgpr_read_b32 v40, a32\n"
"v_accvgpr_read_b32 v41, a33\n"
"v_accvgpr_read_b32 v42, a34\n"
"v_accvgpr_read_b32 v43, a35\n"
"v_fma_f32 v40, v40, v23, v22\n"
"v_fma_f32 v41, v41, v23, v22\n"
"v_fma_f32 v42, v42, v23, v22\n"
"v_fma_f32 v43, v43, v23, v22\n"
"v_accvgpr_write_b32 a32, v40\n"
"v_accvgpr_write_b32 a33, v41\n"
"v_accvgpr_write_b32 a34, v42\n"
"v_accvgpr_write_b32 a35, v43\n"
"v_accvgpr_read_b32 v40, a48\n"
"v_accvgpr_read_b32 v41, a49\n"
"v_accvgpr_read_b32 v42, a50\n"
"v_accvgpr_read_b32 v43, a51\n"
"v_fma_f32 v40, v40, v23, v22\n"
"v_fma_f32 v41, v41, v23, v22\n"
"v_fma_f32 v42, v42, v23, v22\n"
"v_fma_f32 v43, v43, v23, v22\n"
"v_accvgpr_write_b32 a48, v40\n"
"v_accvgpr_write_b32 a49, v41\n"
"v_accvgpr_write_b32 a50, v42\n"
"v_accvgpr_write_b32 a51, v43\n"
"v_accvgpr_read_b32 v40, a64\n"
"v_accvgpr_read_b32 v41, a65\n"
"v_accvgpr_read_b32 v42, a66\n"
"v_accvgpr_read_b32 v43, a67\n"
"v_fma_f32 v40, v40, v23, v22\n"
"v_fma_f32 v41, v41, v23, v22\n"
"v_fma_f32 v42, v42, v23, v22\n"
"v_fma_f32 v43, v43, v23, v22\n"
"v_accvgpr_write_b32 a64, v40\n"
"v_accvgpr_write_b32 a65, v41\n"
"v_accvgpr_write_b32 a66, v42\n"
"v_accvgpr_write_b32 a67, v43\n"
"v_accvgpr_read_b32 v40, a80\n"
"v_accvgpr_read_b32 v41, a81\n"
"v_accvgpr_read_b32 v42, a82\n"
"v_accvgpr_read_b32 v43, a83\n"
"v_fma_f32 v40, v40, v23, v22\n"
"v_fma_f32 v41, v41, v23, v22\n"
"v_fma_f32 v42, v42, v23, v22\n"
"v_fma_f32 v43, v43, v23, v22\n"
"v_accvgpr_write_b32 a80, v40\n"
"v_accvgpr_write_b32 a81, v41\n"
"v_accvgpr_write_b32 a82, v42\n"
"v_accvgpr_write_b32 a83, v43\n"
"v_accvgpr_read_b32 v40, a96\n"
"v_accvgpr_read_b32 v41, a97\n"
"v_accvgpr_read_b32 v42, a98\n"
"v_accvgpr_read_b32 v43, a99\n"
"v_fma_f32 v40, v40, v23, v22\n"
"v_fma_f32 v41, v41, v23, v22\n"
"v_fma_f32 v42, v42, v23, v22\n"
"v_fma_f32 v43, v43, v23, v22\n"
"v_accvgpr_write_b32 a96, v40\n"
"v_accvgpr_write_b32 a97, v41\n"
"v_accvgpr_write_b32 a98, v42\n"
"v_accvgpr_write_b32 a99, v43\n"
"v_accvgpr_read_b32 v40, a112\n"
"v_accvgpr_read_b32 v41, a113\n"
"v_accvgpr_read_b32 v42, a114\n"
"v_accvgpr_read_b32 v43, a115\n"
"v_fma_f32 v40, v40, v23, v22\n"
"v_fma_f32 v41, v41, v23, v22\n"
"v_fma_f32 v42, v42, v23, v22\n"
"v_fma_f32 v43, v43, v23, v22\n"
"v_accvgpr_write_b32 a112, v40\n"
"v_accvgpr_write_b32 a113, v41\n"
"v_accvgpr_write_b32 a114, v42\n"
"v_accvgpr_write_b32 a115, v43\n"
"v_accvgpr_read_b32 v40, a128\n"
"v_accvgpr_read_b32 v41, a129\n"
"v_accvgpr_read_b32 v42, a130\n"
"v_accvgpr_read_b32 v43, a131\n"
"v_fma_f32 v40, v40, v23, v22\n"
"v_fma_f32 v41, v41, v23, v22\n"
"v_fma_f32 v42, v42, v23, v22\n"
"v_fma_f32 v43, v43, v23, v22\n"
"v_accvgpr_write_b32 a128, v40\n"
"v_accvgpr_write_b32 a129, v41\n"
"v_accvgpr_write_b32 a130, v42\n"
"v_accvgpr_write_b32 a131, v43\n"
"v_accvgpr_read_b32 v40, a144\n"
"v_accvgpr_read_b32 v41, a145\n"
"v_accvgpr_read_b32 v42, a146\n"
"v_accvgpr_read_b32 v43, a147\n"
"v_fma_f32 v40, v40, v23, v22\n"
"v_fma_f32 v41, v41, v23, v22\n"
"v_fma_f32 v42, v42, v23, v22\n"
"v_fma_f32 v43, v43, v23, v22\n"
"v_accvgpr_write_b32 a144, v40\n"
"v_accvgpr_write_b32 a145, v41\n"
"v_accvgpr_write_b32 a146, v42\n"
"v_accvgpr_write_b32 a147, v43\n"
"v_accvgpr_read_b32 v40, a160\n"
"v_accvgpr_read_b32 v41, a161\n"
"v_accvgpr_read_b32 v42, a162\n"
"v_accvgpr_read_b32 v43, a163\n"
"v_fma_f32 v40, v40, v23, v22\n"
"v_fma_f32 v41, v41, v23, v22\n"
"v_fma_f32 v42, v42, v23, v22\n"
"v_fma_f32 v43, v43, v23, v22\n"
"v_accvgpr_write_b32 a160, v40\n"
"v_accvgpr_write_b32 a161, v41\n"
"v_accvgpr_write_b32 a162, v42\n"
"v_accvgpr_write_b32 a163, v43\n"
"v_accvgpr_read_b32 v40, a176\n"
"v_accvgpr_read_b32 v41, a177\n"
"v_accvgpr_read_b32 v42, a178\n"
"v_accvgpr_read_b32 v43, a179\n"
"v_fma_f32 v40, v40, v23, v22\n"
"v_fma_f32 v41, v41, v23, v22\n"
"v_fma_f32 v42, v42, v23, v22\n"
"v_fma_f32 v43, v43, v23, v22\n"
"v_accvgpr_write_b32 a176, v40\n"
"v_accvgpr_write_b32 a177, v41\n"
"v_accvgpr_write_b32 a178, v42\n"
"v_accvgpr_write_b32 a179, v43\n"
"v_max_f32 v22, v132, v133\n"
"v_max_f32 v22, v22, v134\n"
"v_max_f32 v22, v22, v135\n"
"v_max_f32 v22, v22, v68\n"
"v_max_f32 v22, v22, v69\n"
"v_max_f32 v22, v22, v70\n"
"v_max_f32 v22, v22, v71\n"
"v_max_f32 v22, v22, v148\n"
"v_max_f32 v22, v22, v149\n"
"v_max_f32 v22, v22, v150\n"
"v_max_f32 v22, v22, v151\n"
"v_max_f32 v22, v22, v84\n"
"v_max_f32 v22, v22, v85\n"
"v_max_f32 v22, v22, v86\n"
"v_max_f32 v22, v22, v87\n"
"v_sub_f32 v22, v22, v24\n"
"v_max_f32 v22, 0, v22\n"
"v_mov_b32 v23, v22\n"
"s_nop 1\n"
"v_permlane32_swap_b32 v22, v23\n"
"v_max_f32 v22, v22, v23\n"
"v_mov_b32 v23, v22\n"
"s_nop 1\n"
"v_permlane16_swap_b32 v22, v23\n"
"v_max_f32 v22, v22, v23\n"
"v_sub_f32 v52, v52, v22\n"
"v_sub_f32 v53, v53, v22\n"
"v_sub_f32 v54, v54, v22\n"
"v_sub_f32 v55, v55, v22\n"
"v_sub_f32 v132, v132, v22\n"
"v_sub_f32 v133, v133, v22\n"
"v_sub_f32 v134, v134, v22\n"
"v_sub_f32 v135, v135, v22\n"
"v_sub_f32 v68, v68, v22\n"
"v_sub_f32 v69, v69, v22\n"
"v_sub_f32 v70, v70, v22\n"
"v_sub_f32 v71, v71, v22\n"
"v_sub_f32 v148, v148, v22\n"
"v_sub_f32 v149, v149, v22\n"
"v_sub_f32 v150, v150, v22\n"
"v_sub_f32 v151, v151, v22\n"
"v_sub_f32 v84, v84, v22\n"
"v_sub_f32 v85, v85, v22\n"
"v_sub_f32 v86, v86, v22\n"
"v_sub_f32 v87, v87, v22\n"
"v_sub_f32 v23, 0, v22\n"
"v_exp_f32 v23, v23\n"
"s_nop 0\n"
"v_mul_f32 v15, v15, v23\n"
"v_sub_f32 v22, 1.0, v23\n"
"v_accvgpr_read_b32 v40, a4\n"
"v_accvgpr_read_b32 v41, a5\n"
"v_accvgpr_read_b32 v42, a6\n"
"v_accvgpr_read_b32 v43, a7\n"
"v_fma_f32 v40, v40, v23, v22\n"
"v_fma_f32 v41, v41, v23, v22\n"
"v_fma_f32 v42, v42, v23, v22\n"
"v_fma_f32 v43, v43, v23, v22\n"
"v_accvgpr_write_b32 a4, v40\n"
"v_accvgpr_write_b32 a5, v41\n"
"v_accvgpr_write_b32 a6, v42\n"
"v_accvgpr_write_b32 a7, v43\n"
"v_accvgpr_read_b32 v40, a20\n"
"v_accvgpr_read_b32 v41, a21\n"
"v_accvgpr_read_b32 v42, a22\n"
"v_accvgpr_read_b32 v43, a23\n"
"v_fma_f32 v40, v40, v23, v22\n"
"v_fma_f32 v41, v41, v23, v22\n"
"v_fma_f32 v42, v42, v23, v22\n"
"v_fma_f32 v43, v43, v23, v22\n"
"v_accvgpr_write_b32 a20, v40\n"
"v_accvgpr_write_b32 a21, v41\n"
"v_accvgpr_write_b32 a22, v42\n"
"v_accvgpr_write_b32 a23, v43\n"
"v_accvgpr_read_b32 v40, a36\n"
"v_accvgpr_read_b32 v41, a37\n"
"v_accvgpr_read_b32 v42, a38\n"
"v_accvgpr_read_b32 v43, a39\n"
"v_fma_f32 v40, v40, v23, v22\n"
"v_fma_f32 v41, v41, v23, v22\n"
"v_fma_f32 v42, v42, v23, v22\n"
"v_fma_f32 v43, v43, v23, v22\n"
"v_accvgpr_write_b32 a36, v40\n"
"v_accvgpr_write_b32 a37, v41\n"
"v_accvgpr_write_b32 a38, v42\n"
"v_accvgpr_write_b32 a39, v43\n"
"v_accvgpr_read_b32 v40, a52\n"
"v_accvgpr_read_b32 v41, a53\n"
"v_accvgpr_read_b32 v42, a54\n"
"v_accvgpr_read_b32 v43, a55\n"
"v_fma_f32 v40, v40, v23, v22\n"
"v_fma_f32 v41, v41, v23, v22\n"
"v_fma_f32 v42, v42, v23, v22\n"
"v_fma_f32 v43, v43, v23, v22\n"
"v_accvgpr_write_b32 a52, v40\n"
"v_accvgpr_write_b32 a53, v41\n"
"v_accvgpr_write_b32 a54, v42\n"
"v_accvgpr_write_b32 a55, v43\n"
"v_accvgpr_read_b32 v40, a68\n"
"v_accvgpr_read_b32 v41, a69\n"
"v_accvgpr_read_b32 v42, a70\n"
"v_accvgpr_read_b32 v43, a71\n"
"v_fma_f32 v40, v40, v23, v22\n"
"v_fma_f32 v41, v41, v23, v22\n"
"v_fma_f32 v42, v42, v23, v22\n"
"v_fma_f32 v43, v43, v23, v22\n"
"v_accvgpr_write_b32 a68, v40\n"
"v_accvgpr_write_b32 a69, v41\n"
"v_accvgpr_write_b32 a70, v42\n"
"v_accvgpr_write_b32 a71, v43\n"
"v_accvgpr_read_b32 v40, a84\n"
"v_accvgpr_read_b32 v41, a85\n"
"v_accvgpr_read_b32 v42, a86\n"
"v_accvgpr_read_b32 v43, a87\n"
"v_fma_f32 v40, v40, v23, v22\n"
"v_fma_f32 v41, v41, v23, v22\n"
"v_fma_f32 v42, v42, v23, v22\n"
"v_fma_f32 v43, v43, v23, v22\n"
"v_accvgpr_write_b32 a84, v40\n"
"v_accvgpr_write_b32 a85, v41\n"
"v_accvgpr_write_b32 a86, v42\n"
"v_accvgpr_write_b32 a87, v43\n"
"v_accvgpr_read_b32 v40, a100\n"
"v_accvgpr_read_b32 v41, a101\n"
"v_accvgpr_read_b32 v42, a102\n"
"v_accvgpr_read_b32 v43, a103\n"
"v_fma_f32 v40, v40, v23, v22\n"
"v_fma_f32 v41, v41, v23, v22\n"
"v_fma_f32 v42, v42, v23, v22\n"
"v_fma_f32 v43, v43, v23, v22\n"
"v_accvgpr_write_b32 a100, v40\n"
"v_accvgpr_write_b32 a101, v41\n"
"v_accvgpr_write_b32 a102, v42\n"
"v_accvgpr_write_b32 a103, v43\n"
"v_accvgpr_read_b32 v40, a116\n"
"v_accvgpr_read_b32 v41, a117\n"
"v_accvgpr_read_b32 v42, a118\n"
"v_accvgpr_read_b32 v43, a119\n"
"v_fma_f32 v40, v40, v23, v22\n"
"v_fma_f32 v41, v41, v23, v22\n"
"v_fma_f32 v42, v42, v23, v22\n"
"v_fma_f32 v43, v43, v23, v22\n"
"v_accvgpr_write_b32 a116, v40\n"
"v_accvgpr_write_b32 a117, v41\n"
"v_accvgpr_write_b32 a118, v42\n"
"v_accvgpr_write_b32 a119, v43\n"
"v_accvgpr_read_b32 v40, a132\n"
"v_accvgpr_read_b32 v41, a133\n"
"v_accvgpr_read_b32 v42, a134\n"
"v_accvgpr_read_b32 v43, a135\n"
"v_fma_f32 v40, v40, v23, v22\n"
"v_fma_f32 v41, v41, v23, v22\n"
"v_fma_f32 v42, v42, v23, v22\n"
"v_fma_f32 v43, v43, v23, v22\n"
"v_accvgpr_write_b32 a132, v40\n"
"v_accvgpr_write_b32 a133, v41\n"
"v_accvgpr_write_b32 a134, v42\n"
"v_accvgpr_write_b32 a135, v43\n"
"v_accvgpr_read_b32 v40, a148\n"
"v_accvgpr_read_b32 v41, a149\n"
"v_accvgpr_read_b32 v42, a150\n"
"v_accvgpr_read_b32 v43, a151\n"
"v_fma_f32 v40, v40, v23, v22\n"
"v_fma_f32 v41, v41, v23, v22\n"
"v_fma_f32 v42, v42, v23, v22\n"
"v_fma_f32 v43, v43, v23, v22\n"
"v_accvgpr_write_b32 a148, v40\n"
"v_accvgpr_write_b32 a149, v41\n"
"v_accvgpr_write_b32 a150, v42\n"
"v_accvgpr_write_b32 a151, v43\n"
"v_accvgpr_read_b32 v40, a164\n"
"v_accvgpr_read_b32 v41, a165\n"
"v_accvgpr_read_b32 v42, a166\n"
"v_accvgpr_read_b32 v43, a167\n"
"v_fma_f32 v40, v40, v23, v22\n"
"v_fma_f32 v41, v41, v23, v22\n"
"v_fma_f32 v42, v42, v23, v22\n"
"v_fma_f32 v43, v43, v23, v22\n"
"v_accvgpr_write_b32 a164, v40\n"
"v_accvgpr_write_b32 a165, v41\n"
"v_accvgpr_write_b32 a166, v42\n"
"v_accvgpr_write_b32 a167, v43\n"
"v_accvgpr_read_b32 v40, a180\n"
"v_accvgpr_read_b32 v41, a181\n"
"v_accvgpr_read_b32 v42, a182\n"
"v_accvgpr_read_b32 v43, a183\n"
"v_fma_f32 v40, v40, v23, v22\n"
"v_fma_f32 v41, v41, v23, v22\n"
"v_fma_f32 v42, v42, v23, v22\n"
"v_fma_f32 v43, v43, v23, v22\n"
"v_accvgpr_write_b32 a180, v40\n"
"v_accvgpr_write_b32 a181, v41\n"
"v_accvgpr_write_b32 a182, v42\n"
"v_accvgpr_write_b32 a183, v43\n"
"v_max_f32 v22, v136, v137\n"
"v_max_f32 v22, v22, v138\n"
"v_max_f32 v22, v22, v139\n"
"v_max_f32 v22, v22, v72\n"
"v_max_f32 v22, v22, v73\n"
"v_max_f32 v22, v22, v74\n"
"v_max_f32 v22, v22, v75\n"
"v_max_f32 v22, v22, v152\n"
"v_max_f32 v22, v22, v153\n"
"v_max_f32 v22, v22, v154\n"
"v_max_f32 v22, v22, v155\n"
"v_max_f32 v22, v22, v88\n"
"v_max_f32 v22, v22, v89\n"
"v_max_f32 v22, v22, v90\n"
"v_max_f32 v22, v22, v91\n"
"v_sub_f32 v22, v22, v24\n"
"v_max_f32 v22, 0, v22\n"
"v_mov_b32 v23, v22\n"
"s_nop 1\n"
"v_permlane32_swap_b32 v22, v23\n"
"v_max_f32 v22, v22, v23\n"
"v_mov_b32 v23, v22\n"
"s_nop 1\n"
"v_permlane16_swap_b32 v22, v23\n"
"v_max_f32 v22, v22, v23\n"
"v_sub_f32 v56, v56, v22\n"
"v_sub_f32 v57, v57, v22\n"
"v_sub_f32 v58, v58, v22\n"
"v_sub_f32 v59, v59, v22\n"
"v_sub_f32 v136, v136, v22\n"
"v_sub_f32 v137, v137, v22\n"
"v_sub_f32 v138, v138, v22\n"
"v_sub_f32 v139, v139, v22\n"
"v_sub_f32 v72, v72, v22\n"
"v_sub_f32 v73, v73, v22\n"
"v_sub_f32 v74, v74, v22\n"
"v_sub_f32 v75, v75, v22\n"
"v_sub_f32 v152, v152, v22\n"
"v_sub_f32 v153, v153, v22\n"
"v_sub_f32 v154, v154, v22\n"
"v_sub_f32 v155, v155, v22\n"
"v_sub_f32 v88, v88, v22\n"
"v_sub_f32 v89, v89, v22\n"
"v_sub_f32 v90, v90, v22\n"
"v_sub_f32 v91, v91, v22\n"
"v_sub_f32 v23, 0, v22\n"
"v_exp_f32 v23, v23\n"
"s_nop 0\n"
"v_mul_f32 v16, v16, v23\n"
"v_sub_f32 v22, 1.0, v23\n"
"v_accvgpr_read_b32 v40, a8\n"
"v_accvgpr_read_b32 v41, a9\n"
"v_accvgpr_read_b32 v42, a10\n"
"v_accvgpr_read_b32 v43, a11\n"
"v_fma_f32 v40, v40, v23, v22\n"
"v_fma_f32 v41, v41, v23, v22\n"
"v_fma_f32 v42, v42, v23, v22\n"
"v_fma_f32 v43, v43, v23, v22\n"
"v_accvgpr_write_b32 a8, v40\n"
"v_accvgpr_write_b32 a9, v41\n"
"v_accvgpr_write_b32 a10, v42\n"
"v_accvgpr_write_b32 a11, v43\n"
"v_accvgpr_read_b32 v40, a24\n"
"v_accvgpr_read_b32 v41, a25\n"
"v_accvgpr_read_b32 v42, a26\n"
"v_accvgpr_read_b32 v43, a27\n"
"v_fma_f32 v40, v40, v23, v22\n"
"v_fma_f32 v41, v41, v23, v22\n"
"v_fma_f32 v42, v42, v23, v22\n"
"v_fma_f32 v43, v43, v23, v22\n"
"v_accvgpr_write_b32 a24, v40\n"
"v_accvgpr_write_b32 a25, v41\n"
"v_accvgpr_write_b32 a26, v42\n"
"v_accvgpr_write_b32 a27, v43\n"
"v_accvgpr_read_b32 v40, a40\n"
"v_accvgpr_read_b32 v41, a41\n"
"v_accvgpr_read_b32 v42, a42\n"
"v_accvgpr_read_b32 v43, a43\n"
"v_fma_f32 v40, v40, v23, v22\n"
"v_fma_f32 v41, v41, v23, v22\n"
"v_fma_f32 v42, v42, v23, v22\n"
"v_fma_f32 v43, v43, v23, v22\n"
"v_accvgpr_write_b32 a40, v40\n"
"v_accvgpr_write_b32 a41, v41\n"
"v_accvgpr_write_b32 a42, v42\n"
"v_accvgpr_write_b32 a43, v43\n"
"v_accvgpr_read_b32 v40, a56\n"
"v_accvgpr_read_b32 v41, a57\n"
"v_accvgpr_read_b32 v42, a58\n"
"v_accvgpr_read_b32 v43, a59\n"
"v_fma_f32 v40, v40, v23, v22\n"
"v_fma_f32 v41, v41, v23, v22\n"
"v_fma_f32 v42, v42, v23, v22\n"
"v_fma_f32 v43, v43, v23, v22\n"
"v_accvgpr_write_b32 a56, v40\n"
"v_accvgpr_write_b32 a57, v41\n"
"v_accvgpr_write_b32 a58, v42\n"
"v_accvgpr_write_b32 a59, v43\n"
"v_accvgpr_read_b32 v40, a72\n"
"v_accvgpr_read_b32 v41, a73\n"
"v_accvgpr_read_b32 v42, a74\n"
"v_accvgpr_read_b32 v43, a75\n"
"v_fma_f32 v40, v40, v23, v22\n"
"v_fma_f32 v41, v41, v23, v22\n"
"v_fma_f32 v42, v42, v23, v22\n"
"v_fma_f32 v43, v43, v23, v22\n"
"v_accvgpr_write_b32 a72, v40\n"
"v_accvgpr_write_b32 a73, v41\n"
"v_accvgpr_write_b32 a74, v42\n"
"v_accvgpr_write_b32 a75, v43\n"
"v_accvgpr_read_b32 v40, a88\n"
"v_accvgpr_read_b32 v41, a89\n"
"v_accvgpr_read_b32 v42, a90\n"
"v_accvgpr_read_b32 v43, a91\n"
"v_fma_f32 v40, v40, v23, v22\n"
"v_fma_f32 v41, v41, v23, v22\n"
"v_fma_f32 v42, v42, v23, v22\n"
"v_fma_f32 v43, v43, v23, v22\n"
"v_accvgpr_write_b32 a88, v40\n"
"v_accvgpr_write_b32 a89, v41\n"
"v_accvgpr_write_b32 a90, v42\n"
"v_accvgpr_write_b32 a91, v43\n"
"v_accvgpr_read_b32 v40, a104\n"
"v_accvgpr_read_b32 v41, a105\n"
"v_accvgpr_read_b32 v42, a106\n"
"v_accvgpr_read_b32 v43, a107\n"
"v_fma_f32 v40, v40, v23, v22\n"
"v_fma_f32 v41, v41, v23, v22\n"
"v_fma_f32 v42, v42, v23, v22\n"
"v_fma_f32 v43, v43, v23, v22\n"
"v_accvgpr_write_b32 a104, v40\n"
"v_accvgpr_write_b32 a105, v41\n"
"v_accvgpr_write_b32 a106, v42\n"
"v_accvgpr_write_b32 a107, v43\n"
"v_accvgpr_read_b32 v40, a120\n"
"v_accvgpr_read_b32 v41, a121\n"
"v_accvgpr_read_b32 v42, a122\n"
"v_accvgpr_read_b32 v43, a123\n"
"v_fma_f32 v40, v40, v23, v22\n"
"v_fma_f32 v41, v41, v23, v22\n"
"v_fma_f32 v42, v42, v23, v22\n"
"v_fma_f32 v43, v43, v23, v22\n"
"v_accvgpr_write_b32 a120, v40\n"
"v_accvgpr_write_b32 a121, v41\n"
"v_accvgpr_write_b32 a122, v42\n"
"v_accvgpr_write_b32 a123, v43\n"
"v_accvgpr_read_b32 v40, a136\n"
"v_accvgpr_read_b32 v41, a137\n"
"v_accvgpr_read_b32 v42, a138\n"
"v_accvgpr_read_b32 v43, a139\n"
"v_fma_f32 v40, v40, v23, v22\n"
"v_fma_f32 v41, v41, v23, v22\n"
"v_fma_f32 v42, v42, v23, v22\n"
"v_fma_f32 v43, v43, v23, v22\n"
"v_accvgpr_write_b32 a136, v40\n"
"v_accvgpr_write_b32 a137, v41\n"
"v_accvgpr_write_b32 a138, v42\n"
"v_accvgpr_write_b32 a139, v43\n"
"v_accvgpr_read_b32 v40, a152\n"
"v_accvgpr_read_b32 v41, a153\n"
"v_accvgpr_read_b32 v42, a154\n"
"v_accvgpr_read_b32 v43, a155\n"
"v_fma_f32 v40, v40, v23, v22\n"
"v_fma_f32 v41, v41, v23, v22\n"
"v_fma_f32 v42, v42, v23, v22\n"
"v_fma_f32 v43, v43, v23, v22\n"
"v_accvgpr_write_b32 a152, v40\n"
"v_accvgpr_write_b32 a153, v41\n"
"v_accvgpr_write_b32 a154, v42\n"
"v_accvgpr_write_b32 a155, v43\n"
"v_accvgpr_read_b32 v40, a168\n"
"v_accvgpr_read_b32 v41, a169\n"
"v_accvgpr_read_b32 v42, a170\n"
"v_accvgpr_read_b32 v43, a171\n"
"v_fma_f32 v40, v40, v23, v22\n"
"v_fma_f32 v41, v41, v23, v22\n"
"v_fma_f32 v42, v42, v23, v22\n"
"v_fma_f32 v43, v43, v23, v22\n"
"v_accvgpr_write_b32 a168, v40\n"
"v_accvgpr_write_b32 a169, v41\n"
"v_accvgpr_write_b32 a170, v42\n"
"v_accvgpr_write_b32 a171, v43\n"
"v_accvgpr_read_b32 v40, a184\n"
"v_accvgpr_read_b32 v41, a185\n"
"v_accvgpr_read_b32 v42, a186\n"
"v_accvgpr_read_b32 v43, a187\n"
"v_fma_f32 v40, v40, v23, v22\n"
"v_fma_f32 v41, v41, v23, v22\n"
"v_fma_f32 v42, v42, v23, v22\n"
"v_fma_f32 v43, v43, v23, v22\n"
"v_accvgpr_write_b32 a184, v40\n"
"v_accvgpr_write_b32 a185, v41\n"
"v_accvgpr_write_b32 a186, v42\n"
"v_accvgpr_write_b32 a187, v43\n"
"v_max_f32 v22, v140, v141\n"
"v_max_f32 v22, v22, v142\n"
"v_max_f32 v22, v22, v143\n"
"v_max_f32 v22, v22, v76\n"
"v_max_f32 v22, v22, v77\n"
"v_max_f32 v22, v22, v78\n"
"v_max_f32 v22, v22, v79\n"
"v_max_f32 v22, v22, v156\n"
"v_max_f32 v22, v22, v157\n"
"v_max_f32 v22, v22, v158\n"
"v_max_f32 v22, v22, v159\n"
"v_max_f32 v22, v22, v92\n"
"v_max_f32 v22, v22, v93\n"
"v_max_f32 v22, v22, v94\n"
"v_max_f32 v22, v22, v95\n"
"v_sub_f32 v22, v22, v24\n"
"v_max_f32 v22, 0, v22\n"
"v_mov_b32 v23, v22\n"
"s_nop 1\n"
"v_permlane32_swap_b32 v22, v23\n"
"v_max_f32 v22, v22, v23\n"
"v_mov_b32 v23, v22\n"
"s_nop 1\n"
"v_permlane16_swap_b32 v22, v23\n"
"v_max_f32 v22, v22, v23\n"
"v_sub_f32 v60, v60, v22\n"
"v_sub_f32 v61, v61, v22\n"
"v_sub_f32 v62, v62, v22\n"
"v_sub_f32 v63, v63, v22\n"
"v_sub_f32 v140, v140, v22\n"
"v_sub_f32 v141, v141, v22\n"
"v_sub_f32 v142, v142, v22\n"
"v_sub_f32 v143, v143, v22\n"
"v_sub_f32 v76, v76, v22\n"
"v_sub_f32 v77, v77, v22\n"
"v_sub_f32 v78, v78, v22\n"
"v_sub_f32 v79, v79, v22\n"
"v_sub_f32 v156, v156, v22\n"
"v_sub_f32 v157, v157, v22\n"
"v_sub_f32 v158, v158, v22\n"
"v_sub_f32 v159, v159, v22\n"
"v_sub_f32 v92, v92, v22\n"
"v_sub_f32 v93, v93, v22\n"
"v_sub_f32 v94, v94, v22\n"
"v_sub_f32 v95, v95, v22\n"
"v_sub_f32 v23, 0, v22\n"
"v_exp_f32 v23, v23\n"
"s_nop 0\n"
"v_mul_f32 v17, v17, v23\n"
"v_sub_f32 v22, 1.0, v23\n"
"v_accvgpr_read_b32 v40, a12\n"
"v_accvgpr_read_b32 v41, a13\n"
"v_accvgpr_read_b32 v42, a14\n"
"v_accvgpr_read_b32 v43, a15\n"
"v_fma_f32 v40, v40, v23, v22\n"
"v_fma_f32 v41, v41, v23, v22\n"
"v_fma_f32 v42, v42, v23, v22\n"
"v_fma_f32 v43, v43, v23, v22\n"
"v_accvgpr_write_b32 a12, v40\n"
"v_accvgpr_write_b32 a13, v41\n"
"v_accvgpr_write_b32 a14, v42\n"
"v_accvgpr_write_b32 a15, v43\n"
"v_accvgpr_read_b32 v40, a28\n"
"v_accvgpr_read_b32 v41, a29\n"
"v_accvgpr_read_b32 v42, a30\n"
"v_accvgpr_read_b32 v43, a31\n"
"v_fma_f32 v40, v40, v23, v22\n"
"v_fma_f32 v41, v41, v23, v22\n"
"v_fma_f32 v42, v42, v23, v22\n"
"v_fma_f32 v43, v43, v23, v22\n"
"v_accvgpr_write_b32 a28, v40\n"
"v_accvgpr_write_b32 a29, v41\n"
"v_accvgpr_write_b32 a30, v42\n"
"v_accvgpr_write_b32 a31, v43\n"
"v_accvgpr_read_b32 v40, a44\n"
"v_accvgpr_read_b32 v41, a45\n"
"v_accvgpr_read_b32 v42, a46\n"
"v_accvgpr_read_b32 v43, a47\n"
"v_fma_f32 v40, v40, v23, v22\n"
"v_fma_f32 v41, v41, v23, v22\n"
"v_fma_f32 v42, v42, v23, v22\n"
"v_fma_f32 v43, v43, v23, v22\n"
"v_accvgpr_write_b32 a44, v40\n"
"v_accvgpr_write_b32 a45, v41\n"
"v_accvgpr_write_b32 a46, v42\n"
"v_accvgpr_write_b32 a47, v43\n"
"v_accvgpr_read_b32 v40, a60\n"
"v_accvgpr_read_b32 v41, a61\n"
"v_accvgpr_read_b32 v42, a62\n"
"v_accvgpr_read_b32 v43, a63\n"
"v_fma_f32 v40, v40, v23, v22\n"
"v_fma_f32 v41, v41, v23, v22\n"
"v_fma_f32 v42, v42, v23, v22\n"
"v_fma_f32 v43, v43, v23, v22\n"
"v_accvgpr_write_b32 a60, v40\n"
"v_accvgpr_write_b32 a61, v41\n"
"v_accvgpr_write_b32 a62, v42\n"
"v_accvgpr_write_b32 a63, v43\n"
"v_accvgpr_read_b32 v40, a76\n"
"v_accvgpr_read_b32 v41, a77\n"
"v_accvgpr_read_b32 v42, a78\n"
"v_accvgpr_read_b32 v43, a79\n"
"v_fma_f32 v40, v40, v23, v22\n"
"v_fma_f32 v41, v41, v23, v22\n"
"v_fma_f32 v42, v42, v23, v22\n"
"v_fma_f32 v43, v43, v23, v22\n"
"v_accvgpr_write_b32 a76, v40\n"
"v_accvgpr_write_b32 a77, v41\n"
"v_accvgpr_write_b32 a78, v42\n"
"v_accvgpr_write_b32 a79, v43\n"
"v_accvgpr_read_b32 v40, a92\n"
"v_accvgpr_read_b32 v41, a93\n"
"v_accvgpr_read_b32 v42, a94\n"
"v_accvgpr_read_b32 v43, a95\n"
"v_fma_f32 v40, v40, v23, v22\n"
"v_fma_f32 v41, v41, v23, v22\n"
"v_fma_f32 v42, v42, v23, v22\n"
"v_fma_f32 v43, v43, v23, v22\n"
"v_accvgpr_write_b32 a92, v40\n"
"v_accvgpr_write_b32 a93, v41\n"
"v_accvgpr_write_b32 a94, v42\n"
"v_accvgpr_write_b32 a95, v43\n"
"v_accvgpr_read_b32 v40, a108\n"
"v_accvgpr_read_b32 v41, a109\n"
"v_accvgpr_read_b32 v42, a110\n"
"v_accvgpr_read_b32 v43, a111\n"
"v_fma_f32 v40, v40, v23, v22\n"
"v_fma_f32 v41, v41, v23, v22\n"
"v_fma_f32 v42, v42, v23, v22\n"
"v_fma_f32 v43, v43, v23, v22\n"
"v_accvgpr_write_b32 a108, v40\n"
"v_accvgpr_write_b32 a109, v41\n"
"v_accvgpr_write_b32 a110, v42\n"
"v_accvgpr_write_b32 a111, v43\n"
"v_accvgpr_read_b32 v40, a124\n"
"v_accvgpr_read_b32 v41, a125\n"
"v_accvgpr_read_b32 v42, a126\n"
"v_accvgpr_read_b32 v43, a127\n"
"v_fma_f32 v40, v40, v23, v22\n"
"v_fma_f32 v41, v41, v23, v22\n"
"v_fma_f32 v42, v42, v23, v22\n"
"v_fma_f32 v43, v43, v23, v22\n"
"v_accvgpr_write_b32 a124, v40\n"
"v_accvgpr_write_b32 a125, v41\n"
"v_accvgpr_write_b32 a126, v42\n"
"v_accvgpr_write_b32 a127, v43\n"
"v_accvgpr_read_b32 v40, a140\n"
"v_accvgpr_read_b32 v41, a141\n"
"v_accvgpr_read_b32 v42, a142\n"
"v_accvgpr_read_b32 v43, a143\n"
"v_fma_f32 v40, v40, v23, v22\n"
"v_fma_f32 v41, v41, v23, v22\n"
"v_fma_f32 v42, v42, v23, v22\n"
"v_fma_f32 v43, v43, v23, v22\n"
"v_accvgpr_write_b32 a140, v40\n"
"v_accvgpr_write_b32 a141, v41\n"
"v_accvgpr_write_b32 a142, v42\n"
"v_accvgpr_write_b32 a143, v43\n"
"v_accvgpr_read_b32 v40, a156\n"
"v_accvgpr_read_b32 v41, a157\n"
"v_accvgpr_read_b32 v42, a158\n"
"v_accvgpr_read_b32 v43, a159\n"
"v_fma_f32 v40, v40, v23, v22\n"
"v_fma_f32 v41, v41, v23, v22\n"
"v_fma_f32 v42, v42, v23, v22\n"
"v_fma_f32 v43, v43, v23, v22\n"
"v_accvgpr_write_b32 a156, v40\n"
"v_accvgpr_write_b32 a157, v41\n"
"v_accvgpr_write_b32 a158, v42\n"
"v_accvgpr_write_b32 a159, v43\n"
"v_accvgpr_read_b32 v40, a172\n"
"v_accvgpr_read_b32 v41, a173\n"
"v_accvgpr_read_b32 v42, a174\n"
"v_accvgpr_read_b32 v43, a175\n"
"v_fma_f32 v40, v40, v23, v22\n"
"v_fma_f32 v41, v41, v23, v22\n"
"v_fma_f32 v42, v42, v23, v22\n"
"v_fma_f32 v43, v43, v23, v22\n"
"v_accvgpr_write_b32 a172, v40\n"
"v_accvgpr_write_b32 a173, v41\n"
"v_accvgpr_write_b32 a174, v42\n"
"v_accvgpr_write_b32 a175, v43\n"
"v_accvgpr_read_b32 v40, a188\n"
"v_accvgpr_read_b32 v41, a189\n"
"v_accvgpr_read_b32 v42, a190\n"
"v_accvgpr_read_b32 v43, a191\n"
"v_fma_f32 v40, v40, v23, v22\n"
"v_fma_f32 v41, v41, v23, v22\n"
"v_fma_f32 v42, v42, v23, v22\n"
"v_fma_f32 v43, v43, v23, v22\n"
"v_accvgpr_write_b32 a188, v40\n"
"v_accvgpr_write_b32 a189, v41\n"
"v_accvgpr_write_b32 a190, v42\n"
"v_accvgpr_write_b32 a191, v43\n"
"v_exp_f32 v128, v128\n"
"v_exp_f32 v129, v129\n"
"v_exp_f32 v130, v130\n"
"v_exp_f32 v131, v131\n"
"v_add_f32 v18, v128, v129\n"
"v_cvt_pk_bf16_f32 v128, v128, v129\n"
"v_add_f32 v18, v18, v130\n"
"v_add_f32 v18, v18, v131\n"
"v_cvt_pk_bf16_f32 v129, v130, v131\n"
"v_exp_f32 v132, v132\n"
"v_exp_f32 v133, v133\n"
"v_exp_f32 v134, v134\n"
"v_exp_f32 v135, v135\n"
"v_add_f32 v19, v132, v133\n"
"v_cvt_pk_bf16_f32 v132, v132, v133\n"
"v_add_f32 v19, v19, v134\n"
"v_add_f32 v19, v19, v135\n"
"v_cvt_pk_bf16_f32 v133, v134, v135\n"
"v_exp_f32 v136, v136\n"
"v_exp_f32 v137, v137\n"
"v_exp_f32 v138, v138\n"
"v_exp_f32 v139, v139\n"
"v_add_f32 v20, v136, v137\n"
"v_cvt_pk_bf16_f32 v136, v136, v137\n"
"v_add_f32 v20, v20, v138\n"
"v_add_f32 v20, v20, v139\n"
"v_cvt_pk_bf16_f32 v137, v138, v139\n"
"v_exp_f32 v140, v140\n"
"v_exp_f32 v141, v141\n"
"v_exp_f32 v142, v142\n"
"v_exp_f32 v143, v143\n"
"v_add_f32 v21, v140, v141\n"
"v_cvt_pk_bf16_f32 v140, v140, v141\n"
"v_add_f32 v21, v21, v142\n"
"v_add_f32 v21, v21, v143\n"
"v_cvt_pk_bf16_f32 v141, v142, v143\n"
"v_exp_f32 v64, v64\n"
"v_exp_f32 v65, v65\n"
"v_exp_f32 v66, v66\n"
"v_exp_f32 v67, v67\n"
"v_add_f32 v18, v18, v64\n"
"v_add_f32 v18, v18, v65\n"
"v_cvt_pk_bf16_f32 v130, v64, v65\n"
"v_add_f32 v18, v18, v66\n"
"v_add_f32 v18, v18, v67\n"
"v_cvt_pk_bf16_f32 v131, v66, v67\n"
"v_exp_f32 v68, v68\n"
"v_exp_f32 v69, v69\n"
"v_exp_f32 v70, v70\n"
"v_exp_f32 v71, v71\n"
"v_add_f32 v19, v19, v68\n"
"v_add_f32 v19, v19, v69\n"
"v_cvt_pk_bf16_f32 v134, v68, v69\n"
"v_add_f32 v19, v19, v70\n"
"v_add_f32 v19, v19, v71\n"
"v_cvt_pk_bf16_f32 v135, v70, v71\n"
"v_exp_f32 v72, v72\n"
"v_exp_f32 v73, v73\n"
"v_exp_f32 v74, v74\n"
"v_exp_f32 v75, v75\n"
"v_add_f32 v20, v20, v72\n"
"v_add_f32 v20, v20, v73\n"
"v_cvt_pk_bf16_f32 v138, v72, v73\n"
"v_add_f32 v20, v20, v74\n"
"v_add_f32 v20, v20, v75\n"
"v_cvt_pk_bf16_f32 v139, v74, v75\n"
"v_exp_f32 v76, v76\n"
"v_exp_f32 v77, v77\n"
"v_exp_f32 v78, v78\n"
"v_exp_f32 v79, v79\n"
"v_add_f32 v21, v21, v76\n"
"v_add_f32 v21, v21, v77\n"
"v_cvt_pk_bf16_f32 v142, v76, v77\n"
"v_add_f32 v21, v21, v78\n"
"v_add_f32 v21, v21, v79\n"
"v_cvt_pk_bf16_f32 v143, v78, v79\n"
"v_exp_f32 v144, v144\n"
"v_exp_f32 v145, v145\n"
"v_exp_f32 v146, v146\n"
"v_exp_f32 v147, v147\n"
"v_add_f32 v18, v18, v144\n"
"v_add_f32 v18, v18, v145\n"
"v_cvt_pk_bf16_f32 v144, v144, v145\n"
"v_add_f32 v18, v18, v146\n"
"v_add_f32 v18, v18, v147\n"
"v_cvt_pk_bf16_f32 v145, v146, v147\n"
"v_exp_f32 v148, v148\n"
"v_exp_f32 v149, v149\n"
"v_exp_f32 v150, v150\n"
"v_exp_f32 v151, v151\n"
"v_add_f32 v19, v19, v148\n"
"v_add_f32 v19, v19, v149\n"
"v_cvt_pk_bf16_f32 v148, v148, v149\n"
"v_add_f32 v19, v19, v150\n"
"v_add_f32 v19, v19, v151\n"
"v_cvt_pk_bf16_f32 v149, v150, v151\n"
"v_exp_f32 v152, v152\n"
"v_exp_f32 v153, v153\n"
"v_exp_f32 v154, v154\n"
"v_exp_f32 v155, v155\n"
"v_add_f32 v20, v20, v152\n"
"v_add_f32 v20, v20, v153\n"
"v_cvt_pk_bf16_f32 v152, v152, v153\n"
"v_add_f32 v20, v20, v154\n"
"v_add_f32 v20, v20, v155\n"
"v_cvt_pk_bf16_f32 v153, v154, v155\n"
"v_exp_f32 v156, v156\n"
"v_exp_f32 v157, v157\n"
"v_exp_f32 v158, v158\n"
"v_exp_f32 v159, v159\n"
"v_add_f32 v21, v21, v156\n"
"v_add_f32 v21, v21, v157\n"
"v_cvt_pk_bf16_f32 v156, v156, v157\n"
"v_add_f32 v21, v21, v158\n"
"v_add_f32 v21, v21, v159\n"
"v_cvt_pk_bf16_f32 v157, v158, v159\n"
"v_exp_f32 v80, v80\n"
"v_exp_f32 v81, v81\n"
"v_exp_f32 v82, v82\n"
"v_exp_f32 v83, v83\n"
"v_add_f32 v18, v18, v80\n"
"v_add_f32 v18, v18, v81\n"
"v_cvt_pk_bf16_f32 v146, v80, v81\n"
"v_add_f32 v18, v18, v82\n"
"v_add_f32 v18, v18, v83\n"
"v_cvt_pk_bf16_f32 v147, v82, v83\n"
"v_exp_f32 v84, v84\n"
"v_exp_f32 v85, v85\n"
"v_exp_f32 v86, v86\n"
"v_exp_f32 v87, v87\n"
"v_add_f32 v19, v19, v84\n"
"v_add_f32 v19, v19, v85\n"
"v_cvt_pk_bf16_f32 v150, v84, v85\n"
"v_add_f32 v19, v19, v86\n"
"v_add_f32 v19, v19, v87\n"
"v_cvt_pk_bf16_f32 v151, v86, v87\n"
"v_exp_f32 v88, v88\n"
"v_exp_f32 v89, v89\n"
"v_exp_f32 v90, v90\n"
"v_exp_f32 v91, v91\n"
"v_add_f32 v20, v20, v88\n"
"v_add_f32 v20, v20, v89\n"
"v_cvt_pk_bf16_f32 v154, v88, v89\n"
"v_add_f32 v20, v20, v90\n"
"v_add_f32 v20, v20, v91\n"
"v_cvt_pk_bf16_f32 v155, v90, v91\n"
"v_exp_f32 v92, v92\n"
"v_exp_f32 v93, v93\n"
"v_exp_f32 v94, v94\n"
"v_exp_f32 v95, v95\n"
"v_add_f32 v21, v21, v92\n"
"v_add_f32 v21, v21, v93\n"
"v_cvt_pk_bf16_f32 v158, v92, v93\n"
"v_add_f32 v21, v21, v94\n"
"v_add_f32 v21, v21, v95\n"
"v_cvt_pk_bf16_f32 v159, v94, v95\n"
"s_nop 3\n"
"s_add_u32 s52, s52, 4\n"
"s_addc_u32 s53, s53, 0\n"
"s_setpc_b64 s[52:53]\n"
"L_poll:\n"
"s_mov_b32 s32, 0x40000\n"
"L_poll1:\n"
"v_and_b32 v46, s98, v12\n"
"buffer_load_dwordx2 v[46:47], v46, s[24:27], s90 offen sc1\n"
"s_waitcnt vmcnt(0)\n"
"v_cmp_eq_u64 vcc, s[76:77], v[46:47]\n"
"s_nop 3\n"
"s_cmp_eq_u64 vcc, -1\n"
"s_cbranch_scc1 L_poll2\n"
"s_sleep 2\n"
"s_sub_u32 s32, s32, 1\n"
"s_cmp_lg_u32 s32, 0\n"
"s_cbranch_scc1 L_poll1\n"
"L_poll2:\n"
"s_add_u32 s52, s52, 4\n"
"s_addc_u32 s53, s53, 0\n"
"s_setpc_b64 s[52:53]\n"
"L_rc0:\n"
"s_getpc_b64 s[52:53]\n"
"s_branch L_rare0\n"
"s_branch L_rr0\n"
"L_rc1:\n"
"s_getpc_b64 s[52:53]\n"
"s_branch L_rare1\n"
"s_branch L_rr1\n"
"L_rc2:\n"
"s_getpc_b64 s[52:53]\n"
"s_branch L_rare0\n"
"s_branch L_rr2\n"
"L_rc3:\n"
"s_getpc_b64 s[52:53]\n"
"s_branch L_rare1\n"
"s_branch L_rr3\n"
"L_rc4:\n"
"s_getpc_b64 s[52:53]\n"
"s_branch L_rare0\n"
"s_branch L_rr4\n"
"L_rc5:\n"
"s_getpc_b64 s[52:53]\n"
"s_branch L_rare1\n"
"s_branch L_rr5\n"
"L_rc6:\n"
"s_getpc_b64 s[52:53]\n"
"s_branch L_rare0\n"
"s_branch L_rr6\n"
"L_rc7:\n"
"s_getpc_b64 s[52:53]\n"
"s_branch L_rare1\n"
"s_branch L_rr7\n"
"L_rc8:\n"
"s_getpc_b64 s[52:53]\n"
"s_branch L_rare1\n"
"s_branch L_rr8\n"
"L_end:\n"
"s_waitcnt vmcnt(0)\n"
  :: "{v0}"(__builtin_amdgcn_workitem_id_x()), "{s[0:1]}"(__builtin_amdgcn_dispatch_ptr()), "{s[2:3]}"(__builtin_amdgcn_kernarg_segment_ptr()), "{s[4:5]}"(launch_index()), "{s6}"(__builtin_amdgcn_workgroup_id_x())
  : "v1","v2","v3","v4","v5","v6","v7","v8","v9","v10","v11","v12","v13","v14","v15","v16","v17","v18","v19","v20","v21","v22","v23","v24","v25","v26","v27","v28","v29","v30","v31","v32","v33","v34","v35","v36","v37","v38","v39","v40","v41","v42","v43","v44","v45","v46","v47","v48","v49","v50","v51","v52","v53","v54","v55","v56","v57","v58","v59","v60","v61","v62","v63","v64","v65","v66","v67","v68","v69","v70","v71","v72","v73","v74","v75","v76","v77","v78","v79","v80","v81","v82","v83","v84","v85","v86","v87","v88","v89","v90","v91","v92","v93","v94","v95","v96","v97","v98","v99","v100","v101","v102","v103","v104","v105","v106","v107","v108","v109","v110","v111","v112","v113","v114","v115","v116","v117","v118","v119","v120","v121","v122","v123","v124","v125","v126","v127","v128","v129","v130","v131","v132","v133","v134","v135","v136","v137","v138","v139","v140","v141","v142","v143","v144","v145","v146","v147","v148","v149","v150","v151","v152","v153","v154","v155","v156","v157","v158","v159","v160","v161","v162","v163","v164","v165","v166","v167","v168","v169","v170","v171","v172","v173","v174","v175","v176","v177","v178","v179","v180","v181","v182","v183","v184","v185","v186","v187","v188","v189","v190","v191","v192","v193","v194","v195","v196","v197","v198","v199","v200","v201","v202","v203","v204","v205","v206","v207","v208","v209","v210","v211","v212","v213","v214","v215","v216","v217","v218","v219","v220","v221","v222","v223","v224","v225","v226","v227","v228","v229","v230","v231","v232","v233","v234","v235","v236","v237","v238","v239","v240","v241","v242","v243","v244","v245","v246","v247","v248","v249","v250","v251","v252","v253","v254","v255","a0","a1","a2","a3","a4","a5","a6","a7","a8","a9","a10","a11","a12","a13","a14","a15","a16","a17","a18","a19","a20","a21","a22","a23","a24","a25","a26","a27","a28","a29","a30","a31","a32","a33","a34","a35","a36","a37","a38","a39","a40","a41","a42","a43","a44","a45","a46","a47","a48","a49","a50","a51","a52","a53","a54","a55","a56","a57","a58","a59","a60","a61","a62","a63","a64","a65","a66","a67","a68","a69","a70","a71","a72","a73","a74","a75","a76","a77","a78","a79","a80","a81","a82","a83","a84","a85","a86","a87","a88","a89","a90","a91","a92","a93","a94","a95","a96","a97","a98","a99","a100","a101","a102","a103","a104","a105","a106","a107","a108","a109","a110","a111","a112","a113","a114","a115","a116","a117","a118","a119","a120","a121","a122","a123","a124","a125","a126","a127","a128","a129","a130","a131","a132","a133","a134","a135","a136","a137","a138","a139","a140","a141","a142","a143","a144","a145","a146","a147","a148","a149","a150","a151","a152","a153","a154","a155","a156","a157","a158","a159","a160","a161","a162","a163","a164","a165","a166","a167","a168","a169","a170","a171","a172","a173","a174","a175","a176","a177","a178","a179","a180","a181","a182","a183","a184","a185","a186","a187","a188","a189","a190","a191","a192","a193","a194","a195","a196","a197","a198","a199","a200","a201","a202","a203","a204","a205","a206","a207","a208","a209","a210","a211","a212","a213","a214","a215","a216","a217","a218","a219","a220","a221","a222","a223","a224","a225","a226","a227","a228","a229","a230","a231","a232","a233","a234","a235","a236","a237","a238","a239","a240","a241","a242","a243","a244","a245","a246","a247","a248","a249","a250","a251","a252","a253","a254","a255","s7","s8","s9","s10","s11","s12","s13","s14","s15","s16","s17","s18","s19","s20","s21","s22","s23","s24","s25","s26","s27","s28","s29","s30","s31","s33","s34","s35","s36","s37","s38","s39","s40","s41","s42","s43","s44","s45","s46","s47","s48","s49","s50","s51","s52","s53","s54","s55","s56","s57","s58","s59","s60","s61","s62","s63","s64","s65","s66","s67","s68","s69","s70","s71","s72","s73","s74","s75","s76","s77","s78","s79","s80","s81","s82","s83","s84","s85","s86","s87","s88","s89","s90","s91","s92","s93","s94","s95","s96","s97","s98","s99","vcc","scc","memory");
}

extern "C" void kernel_launch(void* const* d_in, const int* in_sizes, int n_in, void* d_out, int out_size, void* d_ws, size_t ws_size, hipStream_t stream) {
  Ptrs P;
  for (int i = 0; i < 2; ++i) {
    P.x[i] = (const char*)d_in[5 * i + 0]; P.k[i] = (const char*)d_in[5 * i + 1]; P.v[i] = (const char*)d_in[5 * i + 2];
    P.wq[i] = (const char*)d_in[5 * i + 3]; P.wo[i] = (const char*)d_in[5 * i + 4];
  }
  (void)hipFuncSetAttribute((const void*)k1, hipFuncAttributeMaxDynamicSharedMemorySize, 163840);
  hipLaunchKernelGGL(k1, dim3(256), dim3(256), 163840, stream, P, (char*)d_ws, (char*)d_out);
}
```
